```python
import torch

def _softmax(x, axis=-1):
    return torch.softmax(x, dim=axis)

def _gelu(x):
    return torch.nn.functional.gelu(x, approximate='tanh')

def _silu(x):
    return torch.nn.functional.silu(x)

def _sigmoid(x):
    return torch.sigmoid(x)

def _layernorm(x, axis=-1, eps=1e-05):
    m = x.mean(dim=axis, keepdim=True)
    v = x.var(dim=axis, keepdim=True, unbiased=False)
    return (x - m) / torch.sqrt(v + eps)

def _scatter_add(tgt, idx, src, axis=0):
    return tgt.clone().index_add_(axis, idx, src)

def _segment_sum(src, seg, n):
    out = torch.zeros((n,) + src.shape[1:], dtype=src.dtype)
    return out.index_add_(0, seg, src)

def _pad(x, pw):
    flat = tuple((v for pair in reversed(pw) for v in pair))
    return torch.nn.functional.pad(x, flat)
kernel_name = 'adaLN_cond__compressed_kv_cla__mixer_s_mlp__prompt'

def setup_inputs():

    _w = lambda a: (lambda t: t.to(torch.bfloat16) if t.is_floating_point() else t)(torch.as_tensor(a))

    def _A_setup():
        B, H, Sq, Sk, D, M = (1, 32, 2048, 2048, 128, 640)
        Dc = 4 * D
        return {'x': _w((lambda *s: torch.randn(*s, dtype=torch.bfloat16) * 0.05)(B, Sq, M)), 'c_kv': _w((lambda *s: torch.randn(*s, dtype=torch.bfloat16) * 1.6)(B, Sk, Dc)), 'w_q': _w((lambda *s: torch.randn(*s, dtype=torch.bfloat16) * 0.8)(M, H, D) * M ** (-0.5)), 'w_uk': _w((lambda *s: torch.randn(*s, dtype=torch.bfloat16) * 0.8)(Dc, H, D) * Dc ** (-0.5)), 'w_uv': _w((lambda *s: torch.randn(*s, dtype=torch.bfloat16) * 1.6)(Dc, H, D) * Dc ** (-0.5)), 'w_o': _w((lambda *s: torch.randn(*s, dtype=torch.bfloat16) * 1.6)(H, D, M) * (H * D) ** (-0.5))}
    _A_in = _A_setup()

    def _B_setup():
        B, S, D, F = (1, 2048, 640, 1728)
        return {'x': _w((lambda *s: torch.randn(*s, dtype=torch.bfloat16) * 0.05)(B, S, D)), 'w1': _w((lambda *s: torch.randn(*s, dtype=torch.bfloat16) * 3.2)(D, D) * D ** (-0.5)), 'w2': _w((lambda *s: torch.randn(*s, dtype=torch.bfloat16) * 3.2)(D, D) * D ** (-0.5)), 'w3': _w((lambda *s: torch.randn(*s, dtype=torch.bfloat16) * 3.2)(D, F) * D ** (-0.5)), 'w4': _w((lambda *s: torch.randn(*s, dtype=torch.bfloat16) * 3.2)(F, D) * F ** (-0.5))}
    _B_in = _B_setup()
    return {'A_x': _A_in['x'], 'A_c_kv': _A_in['c_kv'], 'A_w_q': _A_in['w_q'], 'A_w_uk': _A_in['w_uk'], 'A_w_uv': _A_in['w_uv'], 'A_w_o': _A_in['w_o'], 'B_x': _B_in['x'], 'B_w1': _B_in['w1'], 'B_w2': _B_in['w2'], 'B_w3': _B_in['w3'], 'B_w4': _B_in['w4'], 'c': _w((lambda *s: torch.randn(*s, dtype=torch.bfloat16) * 0.1)(1, 640) * 8.0), 'w_mod': _w((lambda *s: torch.randn(*s, dtype=torch.bfloat16) * 0.1)(640, 6 * 640) * 640 ** (-0.5) * 8.0)}

def reference(A_x, A_c_kv, A_w_q, A_w_uk, A_w_uv, A_w_o, B_x, B_w1, B_w2, B_w3, B_w4, c, w_mod):

    def _A_ref(x, c_kv, w_q, w_uk, w_uv, w_o):
        B, Sq, M = (x.shape[0], x.shape[1], x.shape[2])
        Sk = c_kv.shape[1]
        H, D = (w_q.shape[1], w_q.shape[2])
        q = torch.einsum('bqm,mhd->bhqd', x, w_q)
        k = torch.einsum('bkc,chd->bhkd', c_kv, w_uk)
        v = torch.einsum('bkc,chd->bhkd', c_kv, w_uv)
        scores = torch.einsum('bhqd,bhkd->bhqk', q, k) * D ** (-0.5)
        off = Sk - Sq
        qi = torch.arange(Sq)[:, None]
        kj = torch.arange(Sk)[None, :]
        cmask = kj <= qi + off
        scores = torch.where(cmask[None, None], scores, -1000000000.0)
        p = _softmax(scores, axis=-1)
        ctx = torch.einsum('bhqk,bhkd->bhqd', p, v)
        return torch.einsum('bhqd,hdm->bqm', ctx, w_o)

    def _B_ref(x, w1, w2, w3, w4):
        B, S, D = x.shape
        h = _gelu(_layernorm(x, axis=-1) @ w1)
        c = D // 4
        g0 = _pad(h[:, :, 0 * c:1 * c], ((0, 0), (1, 0), (0, 0)))[:, :S, :]
        g1 = _pad(h[:, :, 1 * c:2 * c], ((0, 0), (0, 1), (0, 0)))[:, 1:, :]
        g2 = _pad(h[:, :, 2 * c:3 * c], ((0, 0), (2, 0), (0, 0)))[:, :S, :]
        g3 = _pad(h[:, :, 3 * c:], ((0, 0), (0, 2), (0, 0)))[:, 2:, :]
        hs = torch.concatenate([g0, g1, g2, g3], axis=-1) @ w2
        x1 = x + hs
        h2 = _gelu(_layernorm(x1, axis=-1) @ w3) @ w4
        return x1 + h2
    x = A_x
    mod = (_silu(c) @ w_mod).reshape(c.shape[0], 6, 1, -1)
    sa, ca, ga, sb, cb, gb = (mod[:, 0].clone(), mod[:, 1].clone(), mod[:, 2].clone(), mod[:, 3].clone(), mod[:, 4].clone(), mod[:, 5].clone())
    h = _layernorm(x, axis=-1) * (1 + ca) + sa
    x = x + ga * _A_ref(h, A_c_kv, A_w_q, A_w_uk, A_w_uv, A_w_o)
    h = _layernorm(x, axis=-1) * (1 + cb) + sb
    x = x + gb * _B_ref(h, B_w1, B_w2, B_w3, B_w4)
    return x
```

```cpp
#include <hip/hip_runtime.h>
#include <hip/hip_bf16.h>
typedef unsigned short u16;
__device__ __forceinline__ float bf2f(u16 b) { return __uint_as_float(((unsigned)b) << 16); }
__device__ __forceinline__ u16 f2bf(float f) { unsigned u = __float_as_uint(f); u += 0x7FFFu + ((u >> 16) & 1u); return (u16)(u >> 16); }
__device__ __forceinline__ float rb(float f) { return bf2f(f2bf(f)); }

constexpr int S = 2048, M = 640, H = 32, D = 128, DC = 512, F = 1728;

__global__ void k_mod(const u16* c, const u16* w, u16* mod) {
    int n = blockIdx.x * blockDim.x + threadIdx.x;
    if (n >= 6 * M) return;
    float acc = 0.f;
    for (int k = 0; k < M; ++k) { float x = bf2f(c[k]); float s = rb(x / (1.f + __expf(-x))); acc += s * bf2f(w[(size_t)k * 6 * M + n]); }
    mod[n] = f2bf(acc);
}
__global__ void k_ln(const u16* x, u16* out, const u16* sc, const u16* sh) {
    int row = blockIdx.x, t = threadIdx.x;
    float v[10]; float s = 0.f;
    for (int i = 0; i < 10; ++i) { v[i] = bf2f(x[(size_t)row * M + t + 64 * i]); s += v[i]; }
    for (int o = 32; o >= 1; o >>= 1) s += __shfl_xor(s, o);
    float mean = s / M; float q = 0.f;
    for (int i = 0; i < 10; ++i) { float d = v[i] - mean; q += d * d; }
    for (int o = 32; o >= 1; o >>= 1) q += __shfl_xor(q, o);
    float rstd = rsqrtf(q / M + 1e-5f);
    for (int i = 0; i < 10; ++i) {
        int col = t + 64 * i; float y = (v[i] - mean) * rstd;
        if (sc) y = y * (1.f + bf2f(sc[col])) + bf2f(sh[col]);
        out[(size_t)row * M + col] = f2bf(y);
    }
}
__device__ __forceinline__ float gelu_t(float x) { float u = 0.7978845608028654f * (x + 0.044715f * x * x * x); return 0.5f * x * (1.f + tanhf(u)); }
template <int MODE>
__global__ void k_gemm(const u16* A, const u16* B, u16* C, int N, int K, const u16* res, const u16* g, const u16* res2) {
    __shared__ float As[16][65], Bs[16][65];
    int tx = threadIdx.x & 15, ty = threadIdx.x >> 4;
    int r0 = blockIdx.y * 64, c0 = blockIdx.x * 64;
    float acc[4][4] = {};
    for (int k0 = 0; k0 < K; k0 += 16) {
        for (int i = threadIdx.x; i < 64 * 16; i += 256) { int r = i >> 4, k = i & 15; As[k][r] = bf2f(A[(size_t)(r0 + r) * K + k0 + k]); }
        for (int i = threadIdx.x; i < 64 * 16; i += 256) { int k = i >> 6, c = i & 63; Bs[k][c] = bf2f(B[(size_t)(k0 + k) * N + c0 + c]); }
        __syncthreads();
        for (int k = 0; k < 16; ++k)
            for (int i = 0; i < 4; ++i) for (int j = 0; j < 4; ++j) acc[i][j] += As[k][ty * 4 + i] * Bs[k][tx * 4 + j];
        __syncthreads();
    }
    for (int i = 0; i < 4; ++i) for (int j = 0; j < 4; ++j) {
        int r = r0 + ty * 4 + i, c = c0 + tx * 4 + j; float v = rb(acc[i][j]); size_t o = (size_t)r * N + c;
        if (MODE == 1) v = gelu_t(v);
        if (MODE == 2) v = bf2f(res[o]) + v;
        if (MODE == 3) v = bf2f(res[o]) + rb(bf2f(g[c]) * v);
        if (MODE == 4) v = bf2f(res2[o]) + rb(bf2f(g[c]) * rb(bf2f(res[o]) + v));
        C[o] = f2bf(v);
    }
}
__global__ void k_attn(const u16* Q, const u16* Kk, const u16* V, u16* O) {
    __shared__ float sc[S]; __shared__ float qs[D]; __shared__ float red[4];
    int q = blockIdx.x, h = blockIdx.y, t = threadIdx.x;
    qs[t] = bf2f(Q[(size_t)q * H * D + h * D + t]);
    __syncthreads();
    float mx = -1e30f;
    for (int j = t; j <= q; j += 128) {
        float s = 0.f; const u16* kp = Kk + (size_t)j * H * D + h * D;
        for (int d = 0; d < D; ++d) s += qs[d] * bf2f(kp[d]);
        s = s * 0.08838834764831845f; sc[j] = s; mx = fmaxf(mx, s);
    }
    for (int o = 32; o >= 1; o >>= 1) mx = fmaxf(mx, __shfl_xor(mx, o));
    if ((t & 63) == 0) red[t >> 6] = mx;
    __syncthreads();
    mx = fmaxf(red[0], red[1]);
    float l = 0.f;
    for (int j = t; j <= q; j += 128) { float e = __expf(sc[j] - mx); sc[j] = e; l += e; }
    for (int o = 32; o >= 1; o >>= 1) l += __shfl_xor(l, o);
    if ((t & 63) == 0) red[2 + (t >> 6)] = l;
    __syncthreads();
    l = red[2] + red[3];
    float acc = 0.f;
    for (int j = 0; j <= q; ++j) acc += sc[j] * bf2f(V[(size_t)j * H * D + h * D + t]);
    O[(size_t)q * H * D + h * D + t] = f2bf(acc / l);
}
__global__ void k_shift(const u16* h, u16* o) {
    int i = blockIdx.x * blockDim.x + threadIdx.x; if (i >= S * M) return;
    int r = i / M, c = i % M, g = c / 160; int sr = g == 0 ? r - 1 : g == 1 ? r + 1 : g == 2 ? r - 2 : r + 2;
    o[i] = (sr >= 0 && sr < S) ? h[(size_t)sr * M + c] : (u16)0;
}

extern "C" void kernel_launch(void* const* d_in, const int* in_sizes, int n_in, void* d_out, int out_size, void* d_ws, size_t ws_size, hipStream_t stream) {
    const u16 *x = (const u16*)d_in[0], *ckv = (const u16*)d_in[1], *wq = (const u16*)d_in[2], *wuk = (const u16*)d_in[3], *wuv = (const u16*)d_in[4], *wo = (const u16*)d_in[5];
    const u16 *w1 = (const u16*)d_in[7], *w2 = (const u16*)d_in[8], *w3 = (const u16*)d_in[9], *w4 = (const u16*)d_in[10], *c = (const u16*)d_in[11], *wmod = (const u16*)d_in[12];
    u16* ws = (u16*)d_ws;
    u16* mod = ws; u16* h = ws + 4096; u16* Q = h + S * M; u16* K = Q + S * H * D; u16* V = K + S * H * D; u16* O = V + S * H * D;
    u16* x1 = O + S * H * D; u16* h2 = x1 + S * M; u16* a = h2 + S * M; u16* g1 = a + S * M; u16* sh = g1 + S * M; u16* y1 = sh + S * M; u16* g3 = y1 + S * M;
    k_mod<<<15, 256, 0, stream>>>(c, wmod, mod);
    k_ln<<<S, 64, 0, stream>>>(x, h, mod + M, mod);
    k_gemm<0><<<dim3(4096 / 64, S / 64), 256, 0, stream>>>(h, wq, Q, 4096, M, 0, 0, 0);
    k_gemm<0><<<dim3(4096 / 64, S / 64), 256, 0, stream>>>(ckv, wuk, K, 4096, DC, 0, 0, 0);
    k_gemm<0><<<dim3(4096 / 64, S / 64), 256, 0, stream>>>(ckv, wuv, V, 4096, DC, 0, 0, 0);
    k_attn<<<dim3(S, H), 128, 0, stream>>>(Q, K, V, O);
    k_gemm<3><<<dim3(M / 64, S / 64), 256, 0, stream>>>(O, wo, x1, M, 4096, x, mod + 2 * M, 0);
    k_ln<<<S, 64, 0, stream>>>(x1, h2, mod + 4 * M, mod + 3 * M);
    k_ln<<<S, 64, 0, stream>>>(h2, a, 0, 0);
    k_gemm<1><<<dim3(M / 64, S / 64), 256, 0, stream>>>(a, w1, g1, M, M, 0, 0, 0);
    k_shift<<<S * M / 256, 256, 0, stream>>>(g1, sh);
    k_gemm<2><<<dim3(M / 64, S / 64), 256, 0, stream>>>(sh, w2, y1, M, M, h2, 0, 0);
    k_ln<<<S, 64, 0, stream>>>(y1, a, 0, 0);
    k_gemm<1><<<dim3(F / 64, S / 64), 256, 0, stream>>>(a, w3, g3, F, M, 0, 0, 0);
    k_gemm<4><<<dim3(M / 64, S / 64), 256, 0, stream>>>(g3, w4, (u16*)d_out, M, F, y1, mod + 5 * M, x1);
}
```

```cpp
#include <hip/hip_runtime.h>
constexpr size_t WS_flags = 0;
constexpr size_t WS_mod = 65536;
constexpr size_t WS_cs = 86016;
constexpr size_t WS_kv = 1048576;
constexpr size_t WS_ctx = 35651584;
constexpr size_t WS_h = 54525952;
constexpr size_t WS_part = 58720256;
constexpr size_t WS_x1 = 81788928;
constexpr size_t WS_h2 = 84934656;
constexpr size_t WS_af = 88080384;
constexpr size_t WS_g1 = 91226112;
constexpr size_t WS_y1 = 94371840;
constexpr size_t WS_stats = 96993280;
constexpr size_t WS_g3 = 97517568;
constexpr size_t WS_wo = 106954752;
constexpr size_t WS_w1 = 113246208;
constexpr size_t WS_w2 = 114294784;
constexpr size_t WS_w3 = 115343360;
constexpr size_t WS_w4 = 118489088;
constexpr size_t WS_stamps = 125829120;

typedef unsigned short u16;
typedef unsigned u32x2 __attribute__((ext_vector_type(2)));
typedef unsigned u32x4 __attribute__((ext_vector_type(4)));
typedef float f32x4 __attribute__((ext_vector_type(4)));
__device__ __forceinline__ float bflo(unsigned w) { return __uint_as_float(w << 16); }
__device__ __forceinline__ float bfhi(unsigned w) { return __uint_as_float(w & 0xffff0000u); }
__device__ __forceinline__ unsigned pkbf(float lo, float hi) {
  typedef float f2 __attribute__((ext_vector_type(2))); typedef __bf16 b2 __attribute__((ext_vector_type(2)));
  f2 v = {lo, hi}; return __builtin_bit_cast(unsigned, __builtin_convertvector(v, b2)); }

__device__ __forceinline__ float gelu_t(float x) {
  const float e = __builtin_amdgcn_exp2f(x * (-2.302208198f - 0.1029432397f * x * x));
  return x * __builtin_amdgcn_rcpf(1.f + e); }
__device__ __forceinline__ u32x2 pk4(f32x4 v) { return (u32x2){pkbf(v[0], v[1]), pkbf(v[2], v[3])}; }
__device__ __forceinline__ f32x4 up4(u32x2 w) { return (f32x4){bflo(w[0]), bfhi(w[0]), bflo(w[1]), bfhi(w[1])}; }
__device__ __forceinline__ float sumg(float v) {
  auto a = __builtin_amdgcn_permlane16_swap(__float_as_uint(v), __float_as_uint(v), false, false); v = __uint_as_float(a[0]) + __uint_as_float(a[1]);
  auto c = __builtin_amdgcn_permlane32_swap(__float_as_uint(v), __float_as_uint(v), false, false); return __uint_as_float(c[0]) + __uint_as_float(c[1]); }

typedef __attribute__((address_space(3))) float* ldsf;
__device__ __forceinline__ float dot4(f32x4 a) { return (a[0] * a[0] + a[1] * a[1]) + (a[2] * a[2] + a[3] * a[3]); }
__device__ __forceinline__ float sum4(f32x4 a) { return (a[0] + a[1]) + (a[2] + a[3]); }
__device__ __forceinline__ float sum_t(float v) { v += __builtin_bit_cast(float, __builtin_amdgcn_update_dpp(0, __builtin_bit_cast(int, v), 0x128, 15, 15, false)); return sumg(v); }
typedef float f32x2 __attribute__((ext_vector_type(2)));
#define LO(v) __builtin_shufflevector(v, v, 0, 1)
#define HI(v) __builtin_shufflevector(v, v, 2, 3)
__device__ __forceinline__ void rowstat(unsigned tid, const f32x4 (&v)[6], bool third, ldsf red, float& mean, float& rstd) {
  const f32x4 s4 = (v[0] + v[1]) + (v[2] + v[3]), s5 = v[4] + v[5]; const f32x2 s2 = LO(s4) + HI(s4), t2 = LO(s5) + HI(s5);
  const float mw = sum_t((s2[0] + s2[1]) + (third ? t2[0] + t2[1] : 0.f)) * (third ? 1.f / 192.f : 1.f / 128.f);
  const f32x2 nm = {-mw, -mw}; f32x2 qa = {0.f, 0.f}, qb = {0.f, 0.f};
#pragma unroll
  for (int k = 0; k < 6; ++k) { const f32x2 d0 = LO(v[k]) + nm, d1 = HI(v[k]) + nm;
    if (k < 4) { qa = __builtin_elementwise_fma(d0, d0, qa); qa = __builtin_elementwise_fma(d1, d1, qa); } else { qb = __builtin_elementwise_fma(d0, d0, qb); qb = __builtin_elementwise_fma(d1, d1, qb); } }
  const float q = sum_t((qa[0] + qa[1]) + (third ? qb[0] + qb[1] : 0.f));
  const int j = tid & 7;
  if ((tid & 63) < 8) { red[(tid >> 6) * 8 + j] = mw; red[32 + (tid >> 6) * 8 + j] = q; }
  asm volatile("s_waitcnt lgkmcnt(0)\n s_barrier" ::: "memory");
  const float m0 = red[j], m1 = red[8 + j], m2 = red[16 + j], m3 = red[24 + j];
  mean = (m0 + m1) * 0.3f + (m2 + m3) * 0.2f;
  const float d0 = m0 - mean, d1 = m1 - mean, d2 = m2 - mean, d3 = m3 - mean;
  rstd = __builtin_amdgcn_rsqf((((red[32 + j] + red[40 + j]) + (red[48 + j] + red[56 + j])) + 192.f * (d0 * d0 + d1 * d1) + 128.f * (d2 * d2 + d3 * d3)) * (1.f / 640.f) + 1e-5f); }
typedef unsigned long long u64;
__device__ __forceinline__ f32x4 ldf4(__amdgpu_buffer_rsrc_t rs, unsigned off) { return __builtin_bit_cast(f32x4, __builtin_amdgcn_raw_buffer_load_b128(rs, off, 0, 16)); }
#define F_E 0
#define F_M (320 << 10)
#define F_H (328 << 10)
#define F_D (F_3 + 32768)
#define MODU 24576
#define F_R1 32768
#define F_N 38912
#define FS 128
#define F_O (1 << 17)
#define F_R (F_O + 32768)
#define F_1 (F_R + 32768)
#define F_2 (F_1 + 32768)
#define F_3 (F_2 + 32768)
__device__ __forceinline__ f32x4 ldm4(__amdgpu_buffer_rsrc_t rs, unsigned off) { return up4(pk4(ldf4(rs, off))); }
__device__ __forceinline__ void wait_once(char* ws, unsigned off, u64 tag) {
  unsigned spins = 0;
  while (__any(__hip_atomic_load((u64*)(ws + off), __ATOMIC_RELAXED, __HIP_MEMORY_SCOPE_AGENT) != tag) && ++spins < 131072u) {} }
struct Polls {};
#define KEEP(p)
template <int DELAY> __device__ __forceinline__ Polls wait_tag_(char* ws, unsigned off, u64 tag, unsigned skip) {
  u64 a; unsigned n;
  asm volatile("s_cmp_lg_u32 %5, 0\n s_cbranch_scc1 2f\n s_mov_b32 %1, 0x20000\n s_sleep %6\n"
               "1:\n global_load_dwordx2 %0, %2, %3 sc1\n s_sub_u32 %1, %1, 1\n s_waitcnt vmcnt(0)\n s_cbranch_scc1 2f\n v_cmp_ne_u64 vcc, %4, %0\n s_cbranch_vccnz 1b\n2:\n"
               : "=&v"(a), "=&s"(n) : "v"(off), "s"(ws), "s"(tag), "s"(skip), "i"(DELAY) : "vcc", "scc", "memory");
  return Polls{}; }
#define wait_tag(ws, off, tag, skip, d) wait_tag_<d>(ws, off, tag, skip)
#define DPP_ADD(v, ctl) v = v + __int_as_float(__builtin_amdgcn_update_dpp(0, __float_as_int(v), ctl, 15, 15, false))
__device__ __forceinline__ float sum32(float v) {
  DPP_ADD(v, 0x121); DPP_ADD(v, 0x122); DPP_ADD(v, 0x124); DPP_ADD(v, 0x128);
  auto a = __builtin_amdgcn_permlane16_swap(__float_as_uint(v), __float_as_uint(v), false, false); return __uint_as_float(a[0]) + __uint_as_float(a[1]); }
template <int NI> __device__ __forceinline__ float gemv_sum(int t, const u32x4 (&wv)[10], const unsigned (&cv)[10], bool ones, ldsf red) {
  f32x4 a0 = {}, a1 = {};
#pragma unroll
  for (int i = 0; i < NI; ++i) { const float c = bflo(cv[i]); float s = c * __builtin_amdgcn_rcpf(1.f + __builtin_amdgcn_exp2f(c * -1.44269504089f)); s = ones ? 1.f : bflo(pkbf(s, 0.f));
    a0 = __builtin_elementwise_fma(up4((u32x2){wv[i][0], wv[i][1]}), (f32x4)(s), a0); a1 = __builtin_elementwise_fma(up4((u32x2){wv[i][2], wv[i][3]}), (f32x4)(s), a1); }
#pragma unroll
  for (int e = 0; e < 4; ++e) { DPP_ADD(a0[e], 0x124); DPP_ADD(a1[e], 0x124); }
#pragma unroll
  for (int e = 0; e < 4; ++e) { DPP_ADD(a0[e], 0x128); DPP_ADD(a1[e], 0x128); }
  if ((t & 15) < 4) { typedef __attribute__((address_space(3))) f32x4* ldsv; ldsv p = (ldsv)(red + (t >> 4) * 32 + (t & 3) * 8); p[0] = a0; p[1] = a1; }
  asm volatile("s_waitcnt lgkmcnt(0)\n s_barrier" ::: "memory");
  const int c = t & 31, r = (t >> 5) & 1; float s = 0.f;
#pragma unroll
  for (int k = 0; k < 8; ++k) s += red[(r * 8 + k) * 32 + c];
  auto x = __builtin_amdgcn_permlane32_swap(__float_as_uint(s), __float_as_uint(s), false, false);
  return __uint_as_float(x[0]) + __uint_as_float(x[1]); }
template <int NT> __device__ __forceinline__ void store_tiles(const u32x2 (&y)[NT], int nt0, int nv, __amdgpu_buffer_rsrc_t rs, unsigned base, unsigned lane8) {
  const bool odd = nt0 & 1; const unsigned p = base + ((nt0 + 1) >> 1) * 1024;
#pragma unroll
  for (int k = 0; k < NT / 2; ++k) { const u32x2 lo = odd ? y[2 * k + 1] : y[2 * k], hi = odd ? y[2 * k + 2] : y[2 * k + 1];
    __builtin_amdgcn_raw_buffer_store_b128((u32x4){lo[0], lo[1], hi[0], hi[1]}, rs, p + k * 1024, 0, 16); }
  __builtin_amdgcn_raw_buffer_store_b64(odd ? y[0] : y[NT - 1], rs, odd ? p - 512 - lane8 : nv == NT ? p + (NT / 2) * 1024 - lane8 : 0x10000000u, 0, 16); }
__device__ __forceinline__ unsigned gelu2(f32x2 x) {
  const f32x2 u = x * __builtin_elementwise_fma(x * x, (f32x2)(-0.1029432397f), (f32x2)(-2.302208198f));
  const f32x2 d = (f32x2){__builtin_amdgcn_exp2f(u[0]), __builtin_amdgcn_exp2f(u[1])} + 1.f;
  const f32x2 y = x * (f32x2){__builtin_amdgcn_rcpf(d[0]), __builtin_amdgcn_rcpf(d[1])};
  return pkbf(y[0], y[1]); }
__device__ __forceinline__ unsigned rot1(unsigned g) { return 0 ? (g * 5) >> 1 : 0; }
__device__ __forceinline__ unsigned rot2(unsigned g) { return 0 ? (g & 3) * 5 : 0; }
__device__ __forceinline__ unsigned rot3(unsigned g) { return 0 ? (g & 3) * 5 : 0; }
__device__ __forceinline__ unsigned rot4a(unsigned g) { return 0 ? (g >= 6 ? g - 5 : g) * 7 : 0; }
__device__ __forceinline__ unsigned rot4b(unsigned g) { return 0 ? (g * 3) >> 1 : 0; }
#define ACC4(at, w0, w1) do { if (0) { asm("v_dot2c_f32_bf16 %0, %1, %2" : "+v"(at[0]) : "v"(w0), "v"(0x00003f80u)); asm("v_dot2c_f32_bf16 %0, %1, %2" : "+v"(at[1]) : "v"(w0), "v"(0x3f800000u)); \
    asm("v_dot2c_f32_bf16 %0, %1, %2" : "+v"(at[2]) : "v"(w1), "v"(0x00003f80u)); asm("v_dot2c_f32_bf16 %0, %1, %2" : "+v"(at[3]) : "v"(w1), "v"(0x3f800000u)); } \
  else at += up4((u32x2){w0, w1}); } while (0)
struct Args { char* ws; const char *ckv, *wuk, *wuv, *wq; const u16 *x, *cvec, *wmod, *wo, *w1, *w2, *w3, *w4; u16* out; };
typedef __attribute__((address_space(4))) const Args* kargs;
__device__ __forceinline__ void tail_part(int b, u64 tag);
extern "C" __global__ __launch_bounds__(256, 1) void k_all(Args A) {
  const int b = blockIdx.x, tid = threadIdx.x, i = b >> 3, w = tid >> 6, lane = tid & 63;
  const kargs kp = (kargs)__builtin_amdgcn_kernarg_segment_ptr();
  char* const ws = A.ws; const u16 *w1 = A.w1, *w2 = A.w2, *w3 = A.w3, *w4 = A.w4;
  const unsigned hq = (b & 7) * 4 + (i >> 3), um = (unsigned)b, sj = ((7 - (i & 7)) * 32 + hq) * 4 + w;
  asm volatile(R"(
.macro mm t,d,ta,a,tb,b
v_mfma_f32_16x16x32_bf16 \t[\d:\d+3], \ta[\a:\a+3], \tb[\b:\b+3], \t[\d:\d+3]
.endm
.macro mz t,d,ta,a,tb,b
v_mfma_f32_16x16x32_bf16 \t[\d:\d+3], \ta[\a:\a+3], \tb[\b:\b+3], 0
.endm
.macro mc t,d,ta,a,tb,b,c
v_mfma_f32_16x16x32_bf16 \t[\d:\d+3], \ta[\a:\a+3], \tb[\b:\b+3], \t[\c:\c+3]
.endm
.macro mp d,ta,a,tb,b
v_mfma_f32_16x16x32_bf16 \d, \ta[\a:\a+3], \tb[\b:\b+3], \d
.endm
.macro mq d,ta,a,tb,b
v_mfma_f32_16x16x32_bf16 \d, \ta[\a:\a+3], \tb[\b:\b+3], 0
.endm
.macro rd t,d,a,o
ds_read_b128 \t[\d:\d+3], v\a offset:\o
.endm
.macro rt t,d,a,o
ds_read_b64_tr_b16 \t[\d:\d+1], v\a offset:\o
.endm
.macro wv n
s_waitcnt vmcnt(\n)
.endm
.macro wl n
s_waitcnt lgkmcnt(\n)
.endm
.macro dl a,r,o
buffer_load_dwordx4 v\a, s[\r:\r+3], s\o offen lds
.endm
.macro dc a,r,o,i=0
buffer_load_dwordx4 v\a, s[\r:\r+3], s\o offen offset:\i sc1 lds
.endm
.macro cv d,a,b
v_cvt_pk_bf16_f32 v\d, v\a, v\b
.endm
.macro ex d,a
v_exp_f32 v\d, v\a
.endm
.macro ad d,a,b
v_add_f32 v\d, v\a, v\b
.endm
.macro sb d,a,b
v_sub_f32 v\d, v\a, v\b
.endm
.macro ml d,a,b
v_mul_f32 v\d, v\a, v\b
.endm
.macro pm d,a
v_pk_mul_f32 v[\d:\d+1], v[\d:\d+1], v[\a:\a+1] op_sel_hi:[1,0]
.endm
.macro sa d,a,b
s_add_u32 \d, \a, \b
.endm
)");
  const int lnoff = F_H + ((((b >> 4) < 8 ? (b >> 4) * 32 : (15 - (b >> 4)) * 32 + 16) + (b & 15)) << 3);
  asm volatile(
"s_load_dwordx8 s[20:27], %1, 0\ns_load_dwordx2 s[28:29], %1, 32\ns_load_dwordx4 s[40:43], %1, 40\ns_load_dwordx2 s[48:49], %1, 56\ns_getpc_b64 s[44:45]\nCT0:\n"
"s_lshr_b32 s46, %2, 3\ns_lshl_b32 s46, s46, 13\nv_lshrrev_b32 v166, 6, %0\nv_and_b32 v167, 15, %0\nv_lshlrev_b32 v166, 11, v166\nv_lshl_add_u32 v167, v167, 7, v166\n"
"v_add_u32 v167, s46, v167\nv_min_u32 v167, CODE_END-CT0, v167\nglobal_load_dword v167, v167, s[44:45]\ns_lshr_b32 s30, %2, 3\ns_lshr_b32 s31, s30, 3\ns_and_b32 s30, s30, 7\n"
"s_and_b32 s36, %2, 7\ns_lshl_b32 s36, s36, 2\nsa s31,s31,s36\ns_lshl_b32 s36, s31, 8\nv_and_b32 v230, 63, %0\nv_lshrrev_b32 v234, 6, %0\n"
"v_lshlrev_b32 v230, 4, v230\nv_readfirstlane_b32 s52, v234\nwl 0\nsa s24,s24,s36\ns_addc_u32 s25, s25, 0\nsa s26,s26,s36\n"
"s_addc_u32 s27, s27, 0\nsa s28,s28,s36\ns_addc_u32 s29, s29, 0\ns_lshl_b32 s56, s52, 13\ns_and_b32 s37, s31, 3\ns_lshl_b32 s37, s37, 1\n"
"sa s37,s37,s30\ns_and_b32 s37, s37, 7\nv_lshrrev_b32 v122, 5, %0\nv_and_b32 v123, 31, %0\nv_mul_u32_u24 v122, 1280, v122\nv_lshl_add_u32 v122, v123, 3, v122\n"
"s_mul_i32 s85, %2, 10240\nv_mov_b32 v123, 0\nsa s40,s40,s85\ns_addc_u32 s41, s41, 0\nglobal_load_dwordx2 v[110:111], v123, s[20:21] sc1\nglobal_load_dwordx2 v[100:101], v122, s[40:41] offset:0\n"
"global_load_dwordx2 v[102:103], v122, s[40:41] offset:256\nglobal_load_dwordx2 v[104:105], v122, s[40:41] offset:512\nglobal_load_dwordx2 v[106:107], v122, s[40:41] offset:768\nglobal_load_dwordx2 v[108:109], v122, s[40:41] offset:1024\ns_cmp_lt_u32 %4, 40\ns_cbranch_scc0 NOMOD\n"
"s_lshl_b32 s50, %4, 6\nsa s48,s48,s50\ns_addc_u32 s49, s49, 0\ns_mov_b32 s50, 7680\ns_lshl_b32 s51, s50, 6\nv_lshrrev_b32 v122, 2, %0\n"
"v_and_b32 v124, 3, %0\nv_lshlrev_b32 v123, 1, v122\nv_mul_u32_u24 v122, s50, v122\nv_lshl_add_u32 v122, v124, 4, v122\nglobal_load_dwordx4 v[60:63], v122, s[48:49]\nsa s48,s48,s51\n"
"s_addc_u32 s49, s49, 0\nglobal_load_ushort v112, v123, s[42:43] offset:0\nglobal_load_dwordx4 v[64:67], v122, s[48:49]\nsa s48,s48,s51\ns_addc_u32 s49, s49, 0\nglobal_load_ushort v113, v123, s[42:43] offset:128\n"
"global_load_dwordx4 v[68:71], v122, s[48:49]\nsa s48,s48,s51\ns_addc_u32 s49, s49, 0\nglobal_load_ushort v114, v123, s[42:43] offset:256\nglobal_load_dwordx4 v[72:75], v122, s[48:49]\nsa s48,s48,s51\n"
"s_addc_u32 s49, s49, 0\nglobal_load_ushort v115, v123, s[42:43] offset:384\nglobal_load_dwordx4 v[76:79], v122, s[48:49]\nsa s48,s48,s51\ns_addc_u32 s49, s49, 0\nglobal_load_ushort v116, v123, s[42:43] offset:512\n"
"global_load_dwordx4 v[80:83], v122, s[48:49]\nsa s48,s48,s51\ns_addc_u32 s49, s49, 0\nglobal_load_ushort v117, v123, s[42:43] offset:640\nglobal_load_dwordx4 v[84:87], v122, s[48:49]\nsa s48,s48,s51\n"
"s_addc_u32 s49, s49, 0\nglobal_load_ushort v118, v123, s[42:43] offset:768\nglobal_load_dwordx4 v[88:91], v122, s[48:49]\nsa s48,s48,s51\ns_addc_u32 s49, s49, 0\nglobal_load_ushort v119, v123, s[42:43] offset:896\n"
"global_load_dwordx4 v[92:95], v122, s[48:49]\nsa s48,s48,s51\ns_addc_u32 s49, s49, 0\nglobal_load_ushort v120, v123, s[42:43] offset:1024\nglobal_load_dwordx4 v[96:99], v122, s[48:49]\nsa s48,s48,s51\n"
"s_addc_u32 s49, s49, 0\nglobal_load_ushort v121, v123, s[42:43] offset:1152\nNOMOD:\ns_mov_b64 s[40:41], s[20:21]\ns_and_b32 s41, s41, 0xffff\ns_mov_b32 s42, 134217728\n"
"s_mov_b32 s43, 0x20000\ns_mov_b64 s[68:69], s[22:23]\ns_and_b32 s69, s69, 0xffff\ns_mov_b32 s70, 2097152\ns_mov_b32 s71, 0x20000\ns_cmp_lt_u32 s52, 2\n"
"s_cselect_b64 s[72:73], s[24:25], s[26:27]\ns_and_b32 s73, s73, 0xffff\ns_lshl_b32 s85, s31, 8\ns_sub_u32 s74, 4194304, s85\ns_mov_b32 s75, 0x20000\ns_lshl_b32 s84, s30, 17\n"
"s_lshl_b32 s85, s52, 15\nsa s84,s84,s85\nv_lshrrev_b32 v234, 4, v230\nv_lshrrev_b32 v235, 4, v234\nv_and_b32 v236, 15, v234\nv_lshrrev_b32 v237, 1, v236\n"
"v_xor_b32 v237, v237, v235\nv_lshlrev_b32 v237, 4, v237\nv_lshl_add_u32 v237, v236, 7, v237\nv_add_u32 v196, 98304, v237\nv_xor_b32 v197, 64, v196\nv_lshrrev_b32 v237, 5, v234\n"
"v_lshlrev_b32 v237, 10, v237\nv_lshrrev_b32 v238, 2, v234\nv_and_b32 v238, 7, v238\nv_lshl_add_u32 v237, v238, 6, v237\nv_and_b32 v239, 3, v234\nv_lshl_add_u32 v237, v239, 3, v237\n"
"v_and_b32 v239, 1, v235\nv_lshl_add_u32 v204, v239, 5, v237\nv_xor_b32 v205, 32, v204\ns_mul_i32 s85, s52, 24576\nv_add_u32 v200, s85, v204\nv_xor_b32 v201, 32, v200\n"
"v_and_b32 v237, 7, v234\nv_xor_b32 v237, v237, v235\nv_lshlrev_b32 v237, 4, v237\nv_lshrrev_b32 v239, 3, v234\nv_lshl_add_u32 v198, v239, 10, v237\nv_xor_b32 v199, 64, v198\n"
"v_lshrrev_b32 v239, 2, v238\nv_lshlrev_b32 v237, 1, v239\nv_and_b32 v236, 3, v234\nv_xor_b32 v237, v237, v236\nv_lshlrev_b32 v237, 4, v237\nv_lshrrev_b32 v236, 5, v234\n"
"v_lshl_add_u32 v237, v236, 6, v237\ns_and_b32 s85, s52, 1\ns_lshl_b32 s85, s85, 7\nv_add_u32 v237, s85, v237\nv_lshl_add_u32 v203, v238, 13, v237\nv_lshl_add_u32 v238, v239, 2, v238\n"
"v_lshl_add_u32 v202, v238, 13, v237\ns_lshl_b32 s94, s52, 12\ns_mul_i32 s65, s52, 24576\nsa m0,s65,0\ns_mov_b32 s85, 0\ndl 202,72,85\n"
"sa m0,s65,1024\ns_mov_b32 s85, 131072\ndl 202,72,85\nsa m0,s65,2048\ns_mov_b32 s85, 32768\ndl 202,72,85\n"
"sa m0,s65,3072\ns_mov_b32 s85, 163840\ndl 202,72,85\nsa m0,s65,4096\ns_mov_b32 s85, 262144\ndl 202,72,85\n"
"sa m0,s65,5120\ns_mov_b32 s85, 393216\ndl 202,72,85\nsa m0,s65,6144\ns_mov_b32 s85, 294912\ndl 202,72,85\n"
"sa m0,s65,7168\ns_mov_b32 s85, 425984\ndl 202,72,85\nsa m0,s94,98304\nsa s85,s84,0\ndl 198,68,85\n"
"sa m0,s94,99328\nsa s85,s84,8192\ndl 199,68,85\nsa m0,s94,100352\nsa s85,s84,16384\ndl 198,68,85\n"
"sa m0,s94,101376\nsa s85,s84,24576\ndl 199,68,85\ns_cmp_lt_u32 %4, 40\ns_cbranch_scc1 A_END\nsa m0,s65,8192\n"
"s_mov_b32 s85, 524288\ndl 202,72,85\nsa m0,s65,9216\ns_mov_b32 s85, 655360\ndl 202,72,85\nsa m0,s65,10240\n"
"s_mov_b32 s85, 557056\ndl 202,72,85\nsa m0,s65,11264\ns_mov_b32 s85, 688128\ndl 202,72,85\nsa m0,s65,12288\n"
"s_mov_b32 s85, 786432\ndl 202,72,85\nsa m0,s65,13312\ns_mov_b32 s85, 917504\ndl 202,72,85\nsa m0,s65,14336\n"
"s_mov_b32 s85, 819200\ndl 202,72,85\nsa m0,s65,15360\ns_mov_b32 s85, 950272\ndl 202,72,85\nsa m0,s94,114688\n"
"sa s85,s84,128\ndl 198,68,85\nsa m0,s94,115712\nsa s85,s84,8320\ndl 199,68,85\nsa m0,s94,116736\n"
"sa s85,s84,16512\ndl 198,68,85\nsa m0,s94,117760\nsa s85,s84,24704\ndl 199,68,85\nA_END:\n"
  :: "v"(tid), "s"(kp), "s"(b), "s"(lnoff), "s"(um) : "s20", "s21", "s22", "s23", "s24", "s25", "s26", "s27", "s28", "s29", "s30", "s31", "s36", "s37", "s40", "s41", "s42", "s43", "s44", "s45", "s46", "s48", "s49", "s50", "s51", "s52", "s56", "s65", "s68", "s69", "s70", "s71", "s72", "s73", "s74", "s75", "s84", "s85", "s94", "v100", "v101", "v102", "v103", "v104", "v105", "v106", "v107", "v108", "v109", "v110", "v111", "v112", "v113", "v114", "v115", "v116", "v117", "v118", "v119", "v120", "v121", "v122", "v123", "v124", "v166", "v167", "v196", "v197", "v198", "v199", "v200", "v201", "v202", "v203", "v204", "v205", "v230", "v234", "v235", "v236", "v237", "v238", "v239", "v60", "v61", "v62", "v63", "v64", "v65", "v66", "v67", "v68", "v69", "v70", "v71", "v72", "v73", "v74", "v75", "v76", "v77", "v78", "v79", "v80", "v81", "v82", "v83", "v84", "v85", "v86", "v87", "v88", "v89", "v90", "v91", "v92", "v93", "v94", "v95", "v96", "v97", "v98", "v99", "scc", "m0", "memory");
  const __amdgpu_buffer_rsrc_t rs = __builtin_amdgcn_make_buffer_rsrc(ws, 0, 128 << 20, 0x00020000);
  u32x4 xa, xb; u32x2 xd; unsigned hoff, desc; u64 tagv;
  {
    const unsigned u = sj * 2 + ((lane >> 3) & 1), d = lane & 7;
    const bool i3 = u >= 400 && u < 940, i4 = u >= 940, ok = u < 1480;
    const unsigned q = i4 ? u - 940 : i3 ? u - 400 : u >= 200 ? u - 200 : u, ks = i3 ? q / 27u : q / 10u, cgr = q - ks * (i3 ? 27u : 10u), N = i3 ? 1728u : 640u;
    const unsigned e0 = ks * 32 * N + cgr * 64, nt = cgr * 4 + (d & 3);
    const unsigned grp = !i3 ? nt / 5u : nt < 84 ? nt / 7u : 12 + (nt - 84) / 6u, nl = !i3 ? nt - grp * 5 : nt < 84 ? nt - grp * 7 : nt - 84 - (grp - 12) * 6;
    const u64 a = (u64)((i4 ? w4 : i3 ? w3 : u >= 200 ? w2 : w1) + (ok ? e0 : 0));
    const unsigned per = i4 ? (ks < 42 ? 42u : 12u) : 20u, k0 = i4 && ks >= 42 ? 42u : 0u;
    const unsigned rt = i4 ? (ks < 42 ? rot4a(grp) : rot4b(grp)) : i3 ? rot3(grp) : u >= 200 ? rot2(grp) : rot1(grp), kr0 = ks - k0 + per - rt, kr = k0 + (kr0 >= per ? kr0 - per : kr0);
    const unsigned dst = (unsigned)(i4 ? WS_w4 : i3 ? WS_w3 : u >= 200 ? WS_w2 : WS_w1) + ((grp * (i4 ? 54u : 20u) + kr) * (i3 ? 7u : 5u) + nl) * 1024;
    desc = d == 0 ? (unsigned)a : d == 1 ? (unsigned)(a >> 32) : d == 2 ? (ok ? ((i3 || i4 ? 1105920u : 409600u) - e0) * 2 : 0u) : d == 3 ? N * 2 : ok ? dst : 0x10000000u;
  }
  { const unsigned o = min(sj * 6912u, 5242880u); const u64 a = (u64)A.wo + o;
    if (lane >= 40) desc = lane == 40 ? (unsigned)a : lane == 41 ? (unsigned)(a >> 32) & 0xffffu : min(6912u, 5242880u - o); }
  asm volatile("" : "+v"(desc));
  {
    const int row = b * 8 + w * 2 + (lane >> 5), t = lane & 31; f32x4 f[5]; u32x2 xr[5]; float s = 0.f;
    if (um < 40) {
      u32x4 wv[10]; unsigned cv[10];
      asm volatile("s_waitcnt vmcnt(12)" : "={v[110:111]}"(tagv), "={v[100:101]}"(xr[0]), "={v[102:103]}"(xr[1]), "={v[104:105]}"(xr[2]), "={v[106:107]}"(xr[3]), "={v[108:109]}"(xr[4]),
        "={v[60:63]}"(wv[0]), "={v112}"(cv[0]), "={v[64:67]}"(wv[1]), "={v113}"(cv[1]), "={v[68:71]}"(wv[2]), "={v114}"(cv[2]), "={v[72:75]}"(wv[3]), "={v115}"(cv[3]), "={v[76:79]}"(wv[4]), "={v116}"(cv[4]), "={v[80:83]}"(wv[5]), "={v117}"(cv[5]), "={v[84:87]}"(wv[6]), "={v118}"(cv[6]), "={v[88:91]}"(wv[7]), "={v119}"(cv[7]), "={v[92:95]}"(wv[8]), "={v120}"(cv[8]), "={v[96:99]}"(wv[9]), "={v121}"(cv[9]) :: "memory");
      const float v0 = gemv_sum<10>(tid, wv, cv, false, (ldsf)(0x24000)), v = bflo(pkbf(v0, 0.f)), C = 0.12751743082f;
      if (tid < 32) __builtin_amdgcn_raw_buffer_store_b32(__float_as_uint(um < 20 ? v * C : bflo(pkbf(1.f + v, 0.f)) * C), rs, (um < 20 ? MODU + 2560 + um * 128 : MODU + (um - 20) * 128) + tid * 4, 0, 16);
      if (tid < 64) __builtin_amdgcn_raw_buffer_store_b32(0u, rs, WS_mod + um * 256 + tid * 4, 0, 16);
      asm volatile("s_waitcnt vmcnt(0)" ::: "memory");
      if (tid == 0) __hip_atomic_store((u64*)(ws + F_M + (um << 3)), tagv + 1, __ATOMIC_RELAXED, __HIP_MEMORY_SCOPE_AGENT);
    } else
      asm volatile("s_waitcnt vmcnt(24)" : "={v[110:111]}"(tagv), "={v[100:101]}"(xr[0]), "={v[102:103]}"(xr[1]), "={v[104:105]}"(xr[2]), "={v[106:107]}"(xr[3]), "={v[108:109]}"(xr[4]) :: "memory");
#pragma unroll
    for (int k = 0; k < 5; ++k) { f[k] = up4(xr[k]); s += sum4(f[k]); }
    const float mean = sum32(s) * (1.f / 640.f); s = 0.f;
#pragma unroll
    for (int k = 0; k < 5; ++k) { f[k] -= mean; s += dot4(f[k]); }
    const float rstd = __builtin_amdgcn_rsqf(sum32(s) * (1.f / 640.f) + 1e-5f);
    const u32x2 p0 = pk4(f[0] * rstd), p1 = pk4(f[1] * rstd), p2 = pk4(f[2] * rstd), p3 = pk4(f[3] * rstd);
    xa = (u32x4){p0[0], p0[1], p1[0], p1[1]}; xb = (u32x4){p2[0], p2[1], p3[0], p3[1]}; xd = pk4(f[4] * rstd);
    hoff = (((row >> 4) * 20 + (t >> 3)) * 64 + (t & 3) * 16 + (row & 15)) * 16 + ((t >> 2) & 1) * 8;
  }
  tagv += 1;
  const u64 tag = ((u64)(unsigned)__builtin_amdgcn_readfirstlane((unsigned)(tagv >> 32)) << 32) | (u64)(unsigned)__builtin_amdgcn_readfirstlane((unsigned)tagv);
  asm volatile(
"s_load_dwordx8 s[20:27], %1, 0\ns_load_dwordx2 s[28:29], %1, 32\ns_lshr_b32 s30, %2, 3\ns_lshr_b32 s31, s30, 3\ns_and_b32 s30, s30, 7\ns_and_b32 s36, %2, 7\n"
"s_lshl_b32 s36, s36, 2\nsa s31,s31,s36\ns_lshl_b32 s36, s31, 8\nv_and_b32 v230, 63, %0\nv_lshrrev_b32 v234, 6, %0\nv_lshlrev_b32 v230, 4, v230\n"
"v_readfirstlane_b32 s52, v234\nwl 0\nsa s24,s24,s36\ns_addc_u32 s25, s25, 0\nsa s26,s26,s36\ns_addc_u32 s27, s27, 0\n"
"sa s28,s28,s36\ns_addc_u32 s29, s29, 0\ns_lshl_b32 s56, s52, 13\ns_and_b32 s37, s31, 3\ns_lshl_b32 s37, s37, 1\nsa s37,s37,s30\n"
"s_and_b32 s37, s37, 7\ns_mov_b64 s[40:41], s[20:21]\ns_and_b32 s41, s41, 0xffff\ns_mov_b32 s42, 134217728\ns_mov_b32 s43, 0x20000\ns_mov_b64 s[68:69], s[22:23]\n"
"s_and_b32 s69, s69, 0xffff\ns_mov_b32 s70, 2097152\ns_mov_b32 s71, 0x20000\ns_cmp_lt_u32 s52, 2\ns_cselect_b64 s[72:73], s[24:25], s[26:27]\ns_and_b32 s73, s73, 0xffff\n"
"s_lshl_b32 s85, s31, 8\ns_sub_u32 s74, 4194304, s85\ns_mov_b32 s75, 0x20000\ns_lshl_b32 s84, s30, 17\ns_lshl_b32 s85, s52, 15\nsa s84,s84,s85\n"
"s_mov_b64 s[76:77], s[28:29]\ns_and_b32 s77, s77, 0xffff\ns_mov_b32 s78, 0\ns_mov_b32 s79, 0x20000\ns_lshl_b32 s85, s31, 8\ns_sub_u32 s78, 5242880, s85\n"
"s_mov_b64 s[80:81], s[20:21]\nsa s80,s80,54525952\ns_addc_u32 s81, s81, 0\ns_and_b32 s81, s81, 0xffff\ns_mov_b32 s82, 2621440\ns_mov_b32 s83, 0x20000\n"
"s_mov_b64 s[48:49], s[20:21]\nsa s48,s48,35651584\ns_addc_u32 s49, s49, 0\ns_and_b32 s49, s49, 0xffff\ns_mov_b32 s50, 16777216\ns_mov_b32 s51, 0x20000\n"
"s_mov_b32 s63, s30\ns_mov_b32 s57, s56\ns_lshl_b32 s85, s31, 20\nsa s85,s85,1048576\nsa s56,s56,s85\ns_mov_b32 s53, s56\n"
"s_mov_b32 s54, s56\ns_sub_u32 s58, 15, s63\nsa s64,s58,1\ns_lshl_b32 s64, s64, 16\nsa s55,s56,s64\ns_lshl_b32 s60, s52, 1\n"
"s_sub_u32 s60, 7, s60\ns_lshl_b32 s87, s58, 3\nsa s87,s87,s52\nsa s66,s87,s60\ns_lshl_b32 s61, s87, 5\nsa s61,s61,s31\n"
"s_lshl_b32 s61, s61, 12\ns_mul_i32 s87, s87, 20480\ns_mul_i32 s66, s66, 20480\ns_lshl_b32 s88, s63, 3\nsa s88,s88,s52\nsa s59,s88,s60\n"
"s_lshl_b32 s62, s88, 5\nsa s62,s62,s31\ns_lshl_b32 s62, s62, 12\ns_mul_i32 s88, s88, 20480\ns_mul_i32 s59, s59, 20480\ns_lshl_b32 s60, s60, 17\n"
"s_and_b32 s89, s52, 1\ns_lshl_b32 s89, s89, 13\ns_lshr_b32 s93, s52, 1\ns_lshl_b32 s85, s93, 12\nsa s89,s89,s85\ns_lshl_b32 s93, s93, 18\n"
"s_mov_b64 s[96:97], s[20:21]\ns_and_b32 s97, s97, 0xffff\ns_mov_b32 s98, 655360\ns_mov_b32 s99, 0x20000\ns_mov_b64 s[46:47], %5\ns_lshl_b32 s90, s30, 8\n"
"sa s90,s90,335872\ns_lshl_b32 s91, s31, 8\nsa s91,s91,393216\nsa s95,s91,131072\ns_mov_b32 s35, %3\ns_cmp_eq_u32 s52, 0\n"
"s_cselect_b64 s[38:39], -1, 0\ns_cselect_b32 s85, 0, 0x100000\nv_lshrrev_b32 v179, 4, v230\nv_min_u32 v180, 39, v179\nv_lshlrev_b32 v180, 3, v180\nv_add_u32 v180, 327680, v180\n"
"v_add_u32 v180, s85, v180\nv_and_b32 v244, 31, v179\nv_lshlrev_b32 v244, 3, v244\nv_add_u32 v244, s85, v244\nv_and_b32 v179, 511, v230\nv_mov_b32 v245, 0\n"
"s_lshl_b32 s86, s52, 11\ns_lshl_b32 s85, s52, 14\ns_cmp_lt_u32 s52, 2\ns_cselect_b32 s86, s86, s85\ns_cselect_b32 s92, 2, 1\ns_lshl_b32 s92, s92, 11\n"
"s_lshl_b32 s64, s92, 3\ns_sub_u32 s64, 524288, s64\ns_lshl_b32 s85, s30, 16\nsa s86,s86,s85\ns_lshl_b32 s85, s31, 20\nsa s85,s85,1048576\n"
"sa s86,s86,s85\nv_lshrrev_b32 v234, 4, v230\nv_lshrrev_b32 v235, 4, v234\nv_and_b32 v236, 15, v234\nv_lshrrev_b32 v237, 1, v236\nv_xor_b32 v237, v237, v235\n"
"v_lshlrev_b32 v237, 4, v237\nv_lshl_add_u32 v237, v236, 7, v237\nv_add_u32 v196, 98304, v237\nv_xor_b32 v197, 64, v196\nv_lshrrev_b32 v237, 5, v234\nv_lshlrev_b32 v237, 10, v237\n"
"v_lshrrev_b32 v238, 2, v234\nv_and_b32 v238, 7, v238\nv_lshl_add_u32 v237, v238, 6, v237\nv_and_b32 v239, 3, v234\nv_lshl_add_u32 v237, v239, 3, v237\nv_and_b32 v239, 1, v235\n"
"v_lshl_add_u32 v204, v239, 5, v237\nv_xor_b32 v205, 32, v204\ns_mul_i32 s85, s52, 24576\nv_add_u32 v200, s85, v204\nv_xor_b32 v201, 32, v200\nv_and_b32 v237, 7, v234\n"
"v_xor_b32 v237, v237, v235\nv_lshlrev_b32 v237, 4, v237\nv_lshrrev_b32 v239, 3, v234\nv_lshl_add_u32 v198, v239, 10, v237\nv_xor_b32 v199, 64, v198\nv_lshrrev_b32 v239, 2, v238\n"
"v_lshlrev_b32 v237, 1, v239\nv_and_b32 v236, 3, v234\nv_xor_b32 v237, v237, v236\nv_lshlrev_b32 v237, 4, v237\nv_lshrrev_b32 v236, 5, v234\nv_lshl_add_u32 v237, v236, 6, v237\n"
"s_and_b32 s85, s52, 1\ns_lshl_b32 s85, s85, 7\nv_add_u32 v237, s85, v237\nv_lshl_add_u32 v203, v238, 13, v237\nv_lshl_add_u32 v238, v239, 2, v238\nv_lshl_add_u32 v202, v238, 13, v237\n"
"s_lshl_b32 s94, s52, 12\ns_mul_i32 s65, s52, 24576\ns_cmp_lt_u32 %4, 40\ns_cbranch_scc0 B_GO\nsa m0,s65,8192\ns_mov_b32 s85, 524288\n"
"dl 202,72,85\nsa m0,s65,9216\ns_mov_b32 s85, 655360\ndl 202,72,85\nsa m0,s65,10240\ns_mov_b32 s85, 557056\n"
"dl 202,72,85\nsa m0,s65,11264\ns_mov_b32 s85, 688128\ndl 202,72,85\nsa m0,s65,12288\ns_mov_b32 s85, 786432\n"
"dl 202,72,85\nsa m0,s65,13312\ns_mov_b32 s85, 917504\ndl 202,72,85\nsa m0,s65,14336\ns_mov_b32 s85, 819200\n"
"dl 202,72,85\nsa m0,s65,15360\ns_mov_b32 s85, 950272\ndl 202,72,85\nsa m0,s94,114688\nsa s85,s84,128\n"
"dl 198,68,85\nsa m0,s94,115712\nsa s85,s84,8320\ndl 199,68,85\nsa m0,s94,116736\nsa s85,s84,16512\n"
"dl 198,68,85\nsa m0,s94,117760\nsa s85,s84,24704\ndl 199,68,85\nB_GO:\nsa m0,s65,16384\n"
"s_mov_b32 s85, 1048576\ndl 202,72,85\nsa m0,s65,17408\ns_mov_b32 s85, 1179648\ndl 202,72,85\nsa m0,s65,18432\n"
"s_mov_b32 s85, 1081344\ndl 202,72,85\nsa m0,s65,19456\ns_mov_b32 s85, 1212416\ndl 202,72,85\nsa m0,s65,20480\n"
"s_mov_b32 s85, 1310720\ndl 202,72,85\nsa m0,s65,21504\ns_mov_b32 s85, 1441792\ndl 202,72,85\nsa m0,s65,22528\n"
"s_mov_b32 s85, 1343488\ndl 202,72,85\nsa m0,s65,23552\ns_mov_b32 s85, 1474560\ndl 202,72,85\nsa m0,s94,131072\n"
"sa s85,s84,256\ndl 198,68,85\nsa m0,s94,132096\nsa s85,s84,8448\ndl 199,68,85\nsa m0,s94,133120\n"
"sa s85,s84,16640\ndl 198,68,85\nsa m0,s94,134144\nsa s85,s84,24832\ndl 199,68,85\nv_lshlrev_b32 v160, 3, v230\n"
"v_lshrrev_b32 v161, 5, v230\nv_lshlrev_b32 v161, 13, v161\nv_and_b32 v162, 128, v160\nv_add_u32 v161, v161, v162\ns_and_b32 s34, s31, 3\ns_lshl_b32 s34, s34, 3\n"
"sa s34,s34,s30\ns_lshl_b32 s34, s34, 2\nsa s34,s34,s52\ns_and_b32 s33, s52, 1\ns_lshl_b32 s85, s30, 1\nsa s33,s33,s85\n"
"s_lshl_b32 s36, s30, 2\nsa s36,s36,s52\ns_lshl_b32 s85, s34, 13\nbuffer_load_dword v163, v160, s[68:71], s85 offen\nv_add_u32 v231, 0x1000, v230\nv_add_u32 v232, 0x8000, v230\n"
"v_add_u32 v233, 0x9000, v230\nv_lshlrev_b32 v160, 3, v230\nv_lshrrev_b32 v161, 5, v230\nv_lshlrev_b32 v161, 13, v161\nv_and_b32 v162, 128, v160\nv_add_u32 v161, v161, v162\n"
"s_and_b32 s34, s31, 3\ns_lshl_b32 s34, s34, 3\nsa s34,s34,s30\ns_lshl_b32 s34, s34, 2\nsa s34,s34,s52\ns_and_b32 s33, s52, 1\n"
"s_lshl_b32 s85, s30, 1\nsa s33,s33,s85\ns_lshl_b32 s36, s30, 2\nsa s36,s36,s52\ns_cmp_lt_u32 s52, 2\ns_cbranch_scc0 KV_V\n"
"wv 29\nrt a,0,200,0\nrt a,2,200,2048\nrt a,4,200,4096\nrt a,6,200,6144\nrt a,8,201,0\n"
"rt a,10,201,2048\nrt a,12,201,4096\nrt a,14,201,6144\nrt a,16,200,512\nrt a,18,200,2560\nrt a,20,200,4608\n"
"rt a,22,200,6656\nrt a,24,201,512\nrt a,26,201,2560\nrt a,28,201,4608\nrt a,30,201,6656\nwv 25\n"
"s_barrier\nrd v,128,196,0\nrd v,132,197,0\nrd v,136,196,2048\nrd v,140,197,2048\nwl 3\n"
"mz v,0,a,0,v,128\nwl 2\nmm v,0,a,4,v,132\nrd v,144,196,4096\nmz v,4,a,8,v,128\nmm v,4,a,12,v,132\n"
"mz v,8,a,16,v,128\nrd v,148,197,4096\nmm v,8,a,20,v,132\nmz v,12,a,24,v,128\nmm v,12,a,28,v,132\nwl 3\n"
"mz v,16,a,0,v,136\nwl 2\nmm v,16,a,4,v,140\nsa m0,s94,147456\nsa s85,s84,384\ndl 198,68,85\n"
"rd v,152,196,6144\nmz v,20,a,8,v,136\nmm v,20,a,12,v,140\nmz v,24,a,16,v,136\nrd v,156,197,6144\nmm v,24,a,20,v,140\n"
"sa m0,s94,148480\nsa s85,s84,8576\ndl 199,68,85\nmz v,28,a,24,v,136\nmm v,28,a,28,v,140\nwl 3\n"
"mz v,32,a,0,v,144\nwl 2\nmm v,32,a,4,v,148\nsa m0,s94,149504\nsa s85,s84,16768\ndl 198,68,85\n"
"rd v,128,196,8192\nmz v,36,a,8,v,144\nmm v,36,a,12,v,148\nmz v,40,a,16,v,144\nwv 20\nrd v,132,197,8192\n"
"mm v,40,a,20,v,148\nsa m0,s94,150528\nsa s85,s84,24960\ndl 199,68,85\nrt a,32,200,8192\nmz v,44,a,24,v,144\n"
"mm v,44,a,28,v,148\nrt a,34,200,10240\nwl 5\nmz v,48,a,0,v,152\nwl 4\nmm v,48,a,4,v,156\n"
"sa m0,s65,0\ns_mov_b32 s85, 1572864\ndl 202,72,85\nrt a,36,200,12288\nrd v,136,196,10240\nmz v,52,a,8,v,152\n"
"mm v,52,a,12,v,156\nmz v,56,a,16,v,152\nrt a,38,200,14336\nrd v,140,197,10240\nmm v,56,a,20,v,156\nsa m0,s65,1024\n"
"s_mov_b32 s85, 1703936\ndl 202,72,85\nmz v,60,a,24,v,152\nrt a,40,201,8192\nmm v,60,a,28,v,156\nwl 8\n"
"mz v,64,a,0,v,128\nwl 7\nmm v,64,a,4,v,132\nsa m0,s65,2048\ns_mov_b32 s85, 1605632\ndl 202,72,85\n"
"rt a,42,201,10240\nrd v,144,196,12288\nmz v,68,a,8,v,128\nmm v,68,a,12,v,132\nrt a,44,201,12288\nmz v,72,a,16,v,128\n"
"rd v,148,197,12288\nmm v,72,a,20,v,132\nsa m0,s65,3072\ns_mov_b32 s85, 1736704\ndl 202,72,85\nmz v,76,a,24,v,128\n"
"rt a,46,201,14336\nmm v,76,a,28,v,132\nwl 8\nmz v,80,a,0,v,136\nrt a,48,200,8704\nwl 7\n"
"mm v,80,a,4,v,140\nsa m0,s65,4096\ns_mov_b32 s85, 1835008\ndl 202,72,85\nmz v,84,a,8,v,136\nrd v,152,196,14336\n"
"rt a,50,200,10752\nmm v,84,a,12,v,140\nmz v,88,a,16,v,136\nrd v,156,197,14336\nmm v,88,a,20,v,140\nsa m0,s65,5120\n"
"s_mov_b32 s85, 1966080\ndl 202,72,85\nmz v,92,a,24,v,136\nrt a,52,200,12800\nmm v,92,a,28,v,140\nrt a,54,200,14848\n"
"wl 9\nmz v,96,a,0,v,144\nwl 7\nmm v,96,a,4,v,148\nsa m0,s65,6144\ns_mov_b32 s85, 1867776\n"
"dl 202,72,85\nmz v,100,a,8,v,144\ns_waitcnt vmcnt(24) lgkmcnt(2)\ns_barrier\nrt a,56,201,8704\nmm v,100,a,12,v,148\n"
"rd v,128,196,16384\nmz v,104,a,16,v,144\nrt a,58,201,10752\nmm v,104,a,20,v,148\nsa m0,s65,7168\ns_mov_b32 s85, 1998848\n"
"dl 202,72,85\nmz v,108,a,24,v,144\nrd v,132,197,16384\nmm v,108,a,28,v,148\nrd v,136,196,18432\nrt a,60,201,12800\n"
"mz v,112,a,0,v,152\nmm v,112,a,4,v,156\ns_lshl_b32 s85, s33, 18\nbuffer_load_dword v165, v161, s[72:75], s85 offen\nmz v,116,a,8,v,152\nrd v,140,197,18432\n"
"rt a,62,201,14848\nmm v,116,a,12,v,156\nmz v,120,a,16,v,152\nmm v,120,a,20,v,156\nmz v,124,a,24,v,152\nmm v,124,a,28,v,156\n"
"wl 6\nmm v,0,a,32,v,128\nwl 4\nmm v,0,a,36,v,132\nrd v,144,196,20480\nmm v,4,a,40,v,128\n"
"mm v,4,a,44,v,132\nmm v,8,a,48,v,128\nrd v,148,197,20480\nmm v,8,a,52,v,132\nmm v,12,a,56,v,128\nwl 2\n"
"mm v,12,a,60,v,132\nmm v,16,a,32,v,136\nmm v,16,a,36,v,140\nsa m0,s94,98304\nsa s85,s84,512\ndl 198,68,85\n"
"rd v,152,196,22528\nmm v,20,a,40,v,136\nmm v,20,a,44,v,140\nmm v,24,a,48,v,136\nrd v,156,197,22528\nmm v,24,a,52,v,140\n"
"sa m0,s94,99328\nsa s85,s84,8704\ndl 199,68,85\nmm v,28,a,56,v,136\nmm v,28,a,60,v,140\nwl 3\n"
"mm v,32,a,32,v,144\nwl 2\nmm v,32,a,36,v,148\nsa m0,s94,100352\nsa s85,s84,16896\ndl 198,68,85\n"
"rd v,128,196,24576\nmm v,36,a,40,v,144\nmm v,36,a,44,v,148\nmm v,40,a,48,v,144\nwv 21\nrd v,132,197,24576\n"
"mm v,40,a,52,v,148\nrt a,64,200,16384\nmm v,44,a,56,v,144\nsa m0,s94,101376\nsa s85,s84,25088\ndl 199,68,85\n"
"mm v,44,a,60,v,148\nrt a,66,200,18432\nwl 5\nmm v,48,a,32,v,152\nwl 4\nmm v,48,a,36,v,156\n"
"rt a,68,200,20480\nrd v,136,196,26624\nmm v,52,a,40,v,152\nsa m0,s65,8192\ns_mov_b32 s85, 2097152\ndl 202,72,85\n"
"mm v,52,a,44,v,156\nmm v,56,a,48,v,152\nrt a,70,200,22528\nrd v,140,197,26624\nmm v,56,a,52,v,156\nmm v,60,a,56,v,152\n"
"rt a,72,201,16384\nmm v,60,a,60,v,156\nsa m0,s65,9216\ns_mov_b32 s85, 2228224\ndl 202,72,85\nwl 8\n"
"mm v,64,a,32,v,128\nwl 7\nmm v,64,a,36,v,132\nrt a,74,201,18432\nrd v,144,196,28672\nmm v,68,a,40,v,128\n"
"mm v,68,a,44,v,132\nsa m0,s65,10240\ns_mov_b32 s85, 2129920\ndl 202,72,85\nrt a,76,201,20480\nmm v,72,a,48,v,128\n"
"rd v,148,197,28672\nmm v,72,a,52,v,132\nmm v,76,a,56,v,128\nrt a,78,201,22528\nmm v,76,a,60,v,132\nsa m0,s65,11264\n"
"s_mov_b32 s85, 2260992\ndl 202,72,85\nwl 8\nmm v,80,a,32,v,136\nrt a,80,200,16896\nwl 7\n"
"mm v,80,a,36,v,140\nrd v,152,196,30720\nmm v,84,a,40,v,136\nrt a,82,200,18944\nmm v,84,a,44,v,140\nmm v,88,a,48,v,136\n"
"sa m0,s65,12288\ns_mov_b32 s85, 2359296\ndl 202,72,85\nmm v,88,a,52,v,140\nrd v,156,197,30720\nrt a,84,200,20992\n"
"mm v,92,a,56,v,136\nmm v,92,a,60,v,140\nrt a,86,200,23040\nwl 9\nmm v,96,a,32,v,144\nsa m0,s65,13312\n"
"s_mov_b32 s85, 2490368\ndl 202,72,85\nwl 7\nmm v,96,a,36,v,148\nmm v,100,a,40,v,144\ns_waitcnt vmcnt(24) lgkmcnt(2)\n"
"s_barrier\nrt a,88,201,16896\nmm v,100,a,44,v,148\nrd v,128,196,32768\nmm v,104,a,48,v,144\nrt a,90,201,18944\n"
"mm v,104,a,52,v,148\nsa m0,s65,14336\ns_mov_b32 s85, 2392064\ndl 202,72,85\nmm v,108,a,56,v,144\nrd v,132,197,32768\n"
"mm v,108,a,60,v,148\nrd v,136,196,34816\nrt a,92,201,20992\nmm v,112,a,32,v,152\nmm v,112,a,36,v,156\nsa m0,s65,15360\n"
"s_mov_b32 s85, 2523136\ndl 202,72,85\nmm v,116,a,40,v,152\nrd v,140,197,34816\nrt a,94,201,23040\nmm v,116,a,44,v,156\n"
"mm v,120,a,48,v,152\nmm v,120,a,52,v,156\nmm v,124,a,56,v,152\nmm v,124,a,60,v,156\nwl 6\nmm v,0,a,64,v,128\n"
"wl 4\nmm v,0,a,68,v,132\nrd v,144,196,36864\nmm v,4,a,72,v,128\nmm v,4,a,76,v,132\nmm v,8,a,80,v,128\n"
"rd v,148,197,36864\nmm v,8,a,84,v,132\nmm v,12,a,88,v,128\nwl 2\nmm v,12,a,92,v,132\nmm v,16,a,64,v,136\n"
"mm v,16,a,68,v,140\nsa m0,s94,114688\nsa s85,s84,640\ndl 198,68,85\nrd v,152,196,38912\nmm v,20,a,72,v,136\n"
"mm v,20,a,76,v,140\nmm v,24,a,80,v,136\nrd v,156,197,38912\nmm v,24,a,84,v,140\nsa m0,s94,115712\nsa s85,s84,8832\n"
"dl 199,68,85\nmm v,28,a,88,v,136\nmm v,28,a,92,v,140\nwl 3\nmm v,32,a,64,v,144\nwl 2\n"
"mm v,32,a,68,v,148\nsa m0,s94,116736\nsa s85,s84,17024\ndl 198,68,85\nrd v,128,196,40960\nmm v,36,a,72,v,144\n"
"mm v,36,a,76,v,148\nmm v,40,a,80,v,144\nwv 16\nrd v,132,197,40960\nmm v,40,a,84,v,148\nsa m0,s94,117760\n"
"sa s85,s84,25216\ndl 199,68,85\nrt a,96,200,0\nmm v,44,a,88,v,144\nmm v,44,a,92,v,148\nrt a,98,200,2048\n"
"wl 5\nmm v,48,a,64,v,152\nwl 4\nmm v,48,a,68,v,156\nsa m0,s65,16384\ns_mov_b32 s85, 2621440\n"
"dl 202,72,85\nrt a,100,200,4096\nrd v,136,196,43008\nmm v,52,a,72,v,152\nmm v,52,a,76,v,156\nmm v,56,a,80,v,152\n"
"rt a,102,200,6144\nrd v,140,197,43008\nmm v,56,a,84,v,156\nsa m0,s65,17408\ns_mov_b32 s85, 2752512\ndl 202,72,85\n"
"mm v,60,a,88,v,152\nrt a,104,201,0\nmm v,60,a,92,v,156\nwl 8\nmm v,64,a,64,v,128\nwl 7\n"
"mm v,64,a,68,v,132\nsa m0,s65,18432\ns_mov_b32 s85, 2654208\ndl 202,72,85\nrt a,106,201,2048\nrd v,144,196,45056\n"
"mm v,68,a,72,v,128\nmm v,68,a,76,v,132\nrt a,108,201,4096\nmm v,72,a,80,v,128\nrd v,148,197,45056\nmm v,72,a,84,v,132\n"
"sa m0,s65,19456\ns_mov_b32 s85, 2785280\ndl 202,72,85\nmm v,76,a,88,v,128\nrt a,110,201,6144\nmm v,76,a,92,v,132\n"
"wl 8\nmm v,80,a,64,v,136\nrt a,112,200,512\nwl 7\nmm v,80,a,68,v,140\nsa m0,s65,20480\n"
"s_mov_b32 s85, 2883584\ndl 202,72,85\nmm v,84,a,72,v,136\nrd v,152,196,47104\nrt a,114,200,2560\nmm v,84,a,76,v,140\n"
"mm v,88,a,80,v,136\nrd v,156,197,47104\nmm v,88,a,84,v,140\nsa m0,s65,21504\ns_mov_b32 s85, 3014656\ndl 202,72,85\n"
"mm v,92,a,88,v,136\nrt a,116,200,4608\nmm v,92,a,92,v,140\nrt a,118,200,6656\nwl 9\nmm v,96,a,64,v,144\n"
"wl 7\nmm v,96,a,68,v,148\nsa m0,s65,22528\ns_mov_b32 s85, 2916352\ndl 202,72,85\nmm v,100,a,72,v,144\n"
"wl 2\ns_barrier\nrt a,120,201,512\nmm v,100,a,76,v,148\nrd v,128,196,49152\nmm v,104,a,80,v,144\n"
"rt a,122,201,2560\nmm v,104,a,84,v,148\nsa m0,s65,23552\ns_mov_b32 s85, 3047424\ndl 202,72,85\nmm v,108,a,88,v,144\n"
"rd v,132,197,49152\nmm v,108,a,92,v,148\nrd v,136,196,51200\nrt a,124,201,4608\nmm v,112,a,64,v,152\nmm v,112,a,68,v,156\n"
"rd v,140,197,51200\nrt a,126,201,6656\nmm v,116,a,72,v,152\nbuffer_load_dwordx2 v[242:243], v180, s[96:99], 0 offen offset:0 sc1\nmm v,116,a,76,v,156\nmm v,120,a,80,v,152\n"
"mm v,120,a,84,v,156\nmm v,124,a,88,v,152\nmm v,124,a,92,v,156\nwl 6\nmm v,0,a,96,v,128\nwl 4\n"
"mm v,0,a,100,v,132\nrd v,144,196,53248\nmm v,4,a,104,v,128\nmm v,4,a,108,v,132\nmm v,8,a,112,v,128\nrd v,148,197,53248\n"
"mm v,8,a,116,v,132\nmm v,12,a,120,v,128\nwl 2\nmm v,12,a,124,v,132\nmm v,16,a,96,v,136\nmm v,16,a,100,v,140\n"
"wv 0\nv_cmp_ne_u64 vcc, s[46:47], v[242:243]\ns_nop 0\ns_and_b64 vcc, vcc, s[38:39]\ns_cbranch_scc1 SPIN_MK\nSPUN_MK:\n"
"rd v,152,196,55296\nmm v,20,a,104,v,136\nmm v,20,a,108,v,140\nmm v,24,a,112,v,136\nsa m0,s94,131072\nsa s85,s84,768\n"
"dl 198,68,85\nrd v,156,197,55296\nmm v,24,a,116,v,140\nmm v,28,a,120,v,136\nmm v,28,a,124,v,140\nwl 3\n"
"mm v,32,a,96,v,144\nsa m0,s94,132096\nsa s85,s84,8960\ndl 199,68,85\nwl 2\nmm v,32,a,100,v,148\n"
"rd v,128,196,57344\nmm v,36,a,104,v,144\nmm v,36,a,108,v,148\nmm v,40,a,112,v,144\nsa m0,s94,133120\nsa s85,s84,17152\n"
"dl 198,68,85\nrd v,132,197,57344\nmm v,40,a,116,v,148\nrt a,128,200,8192\nmm v,44,a,120,v,144\nmm v,44,a,124,v,148\n"
"rt a,130,200,10240\nwl 5\nmm v,48,a,96,v,152\nsa m0,s94,134144\nsa s85,s84,25344\ndl 199,68,85\n"
"wl 4\nmm v,48,a,100,v,156\nrt a,132,200,12288\nrd v,136,196,59392\nmm v,52,a,104,v,152\nmm v,52,a,108,v,156\n"
"sa m0,s65,0\ns_mov_b32 s85, 3145728\ndl 202,72,85\nmm v,56,a,112,v,152\nrt a,134,200,14336\nrd v,140,197,59392\n"
"mm v,56,a,116,v,156\nmm v,60,a,120,v,152\nrt a,136,201,8192\nmm v,60,a,124,v,156\nsa m0,s65,1024\ns_mov_b32 s85, 3276800\n"
"dl 202,72,85\nwl 8\nmm v,64,a,96,v,128\nwl 7\nmm v,64,a,100,v,132\nrt a,138,201,10240\n"
"rd v,144,196,61440\nmm v,68,a,104,v,128\nmm v,68,a,108,v,132\nsa m0,s65,2048\ns_mov_b32 s85, 3178496\ndl 202,72,85\n"
"rt a,140,201,12288\nmm v,72,a,112,v,128\nrd v,148,197,61440\nmm v,72,a,116,v,132\nmm v,76,a,120,v,128\nrt a,142,201,14336\n"
"mm v,76,a,124,v,132\nsa m0,s65,3072\ns_mov_b32 s85, 3309568\ndl 202,72,85\nwl 8\nmm v,80,a,96,v,136\n"
"rt a,144,200,8704\nwl 7\nmm v,80,a,100,v,140\nrd v,152,196,63488\nmm v,84,a,104,v,136\nrt a,146,200,10752\n"
"mm v,84,a,108,v,140\nsa m0,s65,4096\ns_mov_b32 s85, 3407872\ndl 202,72,85\nmm v,88,a,112,v,136\nrd v,156,197,63488\n"
"mm v,88,a,116,v,140\nrt a,148,200,12800\nmm v,92,a,120,v,136\nsa m0,s65,5120\ns_mov_b32 s85, 3538944\ndl 202,72,85\n"
"mm v,92,a,124,v,140\nrt a,150,200,14848\nwl 9\nmm v,96,a,96,v,144\nwl 7\nmm v,96,a,100,v,148\n"
"mm v,100,a,104,v,144\nwl 2\ns_barrier\nsa m0,s65,6144\ns_mov_b32 s85, 3440640\ndl 202,72,85\n"
"mm v,100,a,108,v,148\nrd v,128,196,0\nrt a,152,201,8704\nmm v,104,a,112,v,144\nrt a,154,201,10752\nmm v,104,a,116,v,148\n"
"rd v,132,197,0\nmm v,108,a,120,v,144\nsa m0,s65,7168\ns_mov_b32 s85, 3571712\ndl 202,72,85\nmm v,108,a,124,v,148\n"
"rd v,136,196,2048\nrt a,156,201,12800\nmm v,112,a,96,v,152\nmm v,112,a,100,v,156\nrd v,140,197,2048\nrt a,158,201,14848\n"
"mm v,116,a,104,v,152\ns_lshl_b32 s85, s34, 13\nsa s85,s85,0x100000\nbuffer_load_dword v164, v160, s[68:71], s85 offen\nmm v,116,a,108,v,156\nmm v,120,a,112,v,152\n"
"mm v,120,a,116,v,156\nmm v,124,a,120,v,152\nmm v,124,a,124,v,156\nwl 7\nmm v,0,a,128,v,128\nwl 4\n"
"mm v,0,a,132,v,132\nrd v,144,196,4096\nmm v,4,a,136,v,128\nmm v,4,a,140,v,132\nmm v,8,a,144,v,128\nrd v,148,197,4096\n"
"mm v,8,a,148,v,132\nmm v,12,a,152,v,128\nwl 2\nmm v,12,a,156,v,132\nmm v,16,a,128,v,136\nmm v,16,a,132,v,140\n"
"s_mov_b32 s85, 24576\nbuffer_load_dwordx4 v[180:183], v179, s[96:99], s85 offen offset:0 sc1\nrd v,152,196,6144\nmm v,20,a,136,v,136\nmm v,20,a,140,v,140\ns_mov_b32 s85, 27136\n"
"buffer_load_dwordx4 v[184:187], v179, s[96:99], s85 offen offset:0 sc1\nmm v,24,a,144,v,136\nrd v,156,197,6144\nmm v,24,a,148,v,140\ns_mov_b32 s85, 24576\nbuffer_load_dwordx4 v[188:191], v179, s[96:99], s85 offen offset:512 sc1\n"
"mm v,28,a,152,v,136\nmm v,28,a,156,v,140\nwl 3\nmm v,32,a,128,v,144\ns_mov_b32 s85, 27136\nbuffer_load_dwordx4 v[192:195], v179, s[96:99], s85 offen offset:512 sc1\n"
"wl 2\nmm v,32,a,132,v,148\nrd v,128,196,8192\nmm v,36,a,136,v,144\ns_mov_b32 s85, 24576\nbuffer_load_dwordx4 v[206:209], v179, s[96:99], s85 offen offset:1024 sc1\n"
"mm v,36,a,140,v,148\nmm v,40,a,144,v,144\nrd v,132,197,8192\nmm v,40,a,148,v,148\ns_mov_b32 s85, 27136\nbuffer_load_dwordx4 v[210:213], v179, s[96:99], s85 offen offset:1024 sc1\n"
"rt a,160,200,16384\nmm v,44,a,152,v,144\nmm v,44,a,156,v,148\ns_mov_b32 s85, 24576\nbuffer_load_dwordx4 v[214:217], v179, s[96:99], s85 offen offset:1536 sc1\nrt a,162,200,18432\n"
"wl 5\nmm v,48,a,128,v,152\nwl 4\nmm v,48,a,132,v,156\ns_mov_b32 s85, 27136\nbuffer_load_dwordx4 v[218:221], v179, s[96:99], s85 offen offset:1536 sc1\n"
"rt a,164,200,20480\nrd v,136,196,10240\nmm v,52,a,136,v,152\nmm v,52,a,140,v,156\nmm v,56,a,144,v,152\ns_mov_b32 s85, 24576\n"
"buffer_load_dwordx4 v[222:225], v179, s[96:99], s85 offen offset:2048 sc1\nrt a,166,200,22528\nrd v,140,197,10240\nmm v,56,a,148,v,156\nmm v,60,a,152,v,152\ns_mov_b32 s85, 27136\n"
"buffer_load_dwordx4 v[226:229], v179, s[96:99], s85 offen offset:2048 sc1\nrt a,168,201,16384\nmm v,60,a,156,v,156\nwl 8\nmm v,64,a,128,v,128\nwl 7\n"
"mm v,64,a,132,v,132\nsa m0,s94,147456\nsa s85,s84,896\ndl 198,68,85\nrt a,170,201,18432\nrd v,144,196,12288\n"
"mm v,68,a,136,v,128\nmm v,68,a,140,v,132\nsa m0,s94,148480\nsa s85,s84,9088\ndl 199,68,85\nrt a,172,201,20480\n"
"mm v,72,a,144,v,128\nrd v,148,197,12288\nmm v,72,a,148,v,132\nsa m0,s94,149504\nsa s85,s84,17280\ndl 198,68,85\n"
"mm v,76,a,152,v,128\nrt a,174,201,22528\nmm v,76,a,156,v,132\nwl 8\nmm v,80,a,128,v,136\nsa m0,s94,150528\n"
"sa s85,s84,25472\ndl 199,68,85\nrt a,176,200,16896\nwl 7\nmm v,80,a,132,v,140\nrd v,152,196,14336\n"
"mm v,84,a,136,v,136\nsa m0,s65,8192\ns_mov_b32 s85, 3670016\ndl 202,72,85\nrt a,178,200,18944\nmm v,84,a,140,v,140\n"
"mm v,88,a,144,v,136\nrd v,156,197,14336\nmm v,88,a,148,v,140\nsa m0,s65,9216\ns_mov_b32 s85, 3801088\ndl 202,72,85\n"
"rt a,180,200,20992\nmm v,92,a,152,v,136\nmm v,92,a,156,v,140\nsa m0,s65,10240\ns_mov_b32 s85, 3702784\ndl 202,72,85\n"
"rt a,182,200,23040\nwl 9\nmm v,96,a,128,v,144\nwl 7\nmm v,96,a,132,v,148\nsa m0,s65,11264\n"
"s_mov_b32 s85, 3833856\ndl 202,72,85\nmm v,100,a,136,v,144\nwl 2\ns_barrier\nrt a,184,201,16896\n"
"mm v,100,a,140,v,148\nrd v,128,196,16384\nmm v,104,a,144,v,144\nsa m0,s65,12288\ns_mov_b32 s85, 3932160\ndl 202,72,85\n"
"mm v,104,a,148,v,148\nrd v,132,197,16384\nrt a,186,201,18944\nmm v,108,a,152,v,144\nsa m0,s65,13312\ns_mov_b32 s85, 4063232\n"
"dl 202,72,85\nmm v,108,a,156,v,148\nrd v,136,196,18432\nrt a,188,201,20992\nmm v,112,a,128,v,152\nmm v,112,a,132,v,156\n"
"sa m0,s65,14336\ns_mov_b32 s85, 3964928\ndl 202,72,85\nmm v,116,a,136,v,152\nrd v,140,197,18432\nrt a,190,201,23040\n"
"mm v,116,a,140,v,156\nsa m0,s65,15360\ns_mov_b32 s85, 4096000\ndl 202,72,85\nmm v,120,a,144,v,152\nmm v,120,a,148,v,156\n"
"mm v,124,a,152,v,152\nmm v,124,a,156,v,156\nwl 6\nmm v,0,a,160,v,128\nwl 5\nmm v,0,a,164,v,132\n"
"rd v,144,196,20480\nsa m0,s94,98304\nsa s85,s84,1048576\ndl 198,68,85\nmm v,4,a,168,v,128\nmm v,4,a,172,v,132\n"
"mm v,8,a,176,v,128\nrd v,148,197,20480\nmm v,8,a,180,v,132\nwl 6\nmm v,12,a,184,v,128\nwl 2\n"
"mm v,12,a,188,v,132\nmm v,16,a,160,v,136\nv_lshlrev_b32 v234, 16, v168\nv_and_b32 v235, 0xffff0000, v168\nv_lshlrev_b32 v236, 16, v169\nv_and_b32 v237, 0xffff0000, v169\n"
"wv 21\nv_fma_f32 v180, v234, v180, v184\nv_fma_f32 v181, v235, v181, v185\nv_fma_f32 v182, v236, v182, v186\nv_fma_f32 v183, v237, v183, v187\ncv 180,180,181\n"
"cv 181,182,183\nmm v,16,a,164,v,140\ns_mov_b32 s85, 0\nbuffer_store_dwordx2 v[180:181], v178, s[80:83], s85 offen sc1\nmm v,20,a,168,v,136\nrd v,152,196,22528\n"
"v_lshlrev_b32 v234, 16, v170\nmm v,20,a,172,v,140\nv_and_b32 v235, 0xffff0000, v170\nv_lshlrev_b32 v236, 16, v171\nmm v,24,a,176,v,136\nv_and_b32 v237, 0xffff0000, v171\n"
"wv 20\nv_fma_f32 v188, v234, v188, v192\nmm v,24,a,180,v,140\nrd v,156,197,22528\nv_fma_f32 v189, v235, v189, v193\nmm v,28,a,184,v,136\n"
"v_fma_f32 v190, v236, v190, v194\nv_fma_f32 v191, v237, v191, v195\nmm v,28,a,188,v,140\ncv 188,188,189\ncv 189,190,191\nwl 3\n"
"mm v,32,a,160,v,144\ns_mov_b32 s85, 4096\nbuffer_store_dwordx2 v[188:189], v178, s[80:83], s85 offen sc1\nwl 2\nmm v,32,a,164,v,148\nv_lshlrev_b32 v234, 16, v172\n"
"v_and_b32 v235, 0xffff0000, v172\nmm v,36,a,168,v,144\nrd v,128,196,24576\nsa m0,s94,99328\nsa s85,s84,1056768\ndl 199,68,85\n"
"mm v,36,a,172,v,148\nv_lshlrev_b32 v236, 16, v173\nv_and_b32 v237, 0xffff0000, v173\nmm v,40,a,176,v,144\nwv 20\nv_fma_f32 v206, v234, v206, v210\n"
"mm v,40,a,180,v,148\nrd v,132,197,24576\nv_fma_f32 v207, v235, v207, v211\nmm v,44,a,184,v,144\nv_fma_f32 v208, v236, v208, v212\nv_fma_f32 v209, v237, v209, v213\n"
"mm v,44,a,188,v,148\nrt a,192,200,0\ncv 206,206,207\nwl 4\nmm v,48,a,160,v,152\ncv 207,208,209\n"
"s_mov_b32 s85, 8192\nbuffer_store_dwordx2 v[206:207], v178, s[80:83], s85 offen sc1\nwl 3\nmm v,48,a,164,v,156\nrt a,194,200,2048\nv_lshlrev_b32 v234, 16, v174\n"
"mm v,52,a,168,v,152\nrd v,136,196,26624\nv_and_b32 v235, 0xffff0000, v174\nmm v,52,a,172,v,156\nrt a,196,200,4096\nv_lshlrev_b32 v236, 16, v175\n"
"mm v,56,a,176,v,152\nv_and_b32 v237, 0xffff0000, v175\nwv 19\nv_fma_f32 v214, v234, v214, v218\nmm v,56,a,180,v,156\nrd v,140,197,26624\n"
"v_fma_f32 v215, v235, v215, v219\nmm v,60,a,184,v,152\nrt a,198,200,6144\nv_fma_f32 v216, v236, v216, v220\nmm v,60,a,188,v,156\nv_fma_f32 v217, v237, v217, v221\n"
"cv 214,214,215\nwl 7\nmm v,64,a,160,v,128\nrt a,200,201,0\ncv 215,216,217\nwl 7\n"
"mm v,64,a,164,v,132\nsa m0,s94,100352\nsa s85,s84,1064960\ndl 198,68,85\nmm v,68,a,168,v,128\nrd v,144,196,28672\n"
"s_mov_b32 s85, 12288\nbuffer_store_dwordx2 v[214:215], v178, s[80:83], s85 offen sc1\nmm v,68,a,172,v,132\nrt a,202,201,2048\nv_lshlrev_b32 v234, 16, v176\nmm v,72,a,176,v,128\n"
"rt a,204,201,4096\nv_and_b32 v235, 0xffff0000, v176\nmm v,72,a,180,v,132\nrd v,148,197,28672\nv_lshlrev_b32 v236, 16, v177\nmm v,76,a,184,v,128\n"
"rt a,206,201,6144\nv_and_b32 v237, 0xffff0000, v177\nmm v,76,a,188,v,132\nwv 19\nv_fma_f32 v222, v234, v222, v226\nv_fma_f32 v223, v235, v223, v227\n"
"wl 9\nmm v,80,a,160,v,136\nrt a,208,200,512\nv_fma_f32 v224, v236, v224, v228\nwl 8\nmm v,80,a,164,v,140\n"
"rd v,152,196,30720\nv_fma_f32 v225, v237, v225, v229\nmm v,84,a,168,v,136\nrt a,210,200,2560\ncv 222,222,223\nmm v,84,a,172,v,140\n"
"cv 223,224,225\ns_mov_b32 s85, 16384\nbuffer_store_dwordx2 v[222:223], v178, s[80:83], s85 offen sc1\nmm v,88,a,176,v,136\nrd v,156,197,30720\nmm v,88,a,180,v,140\n"
"rt a,212,200,4608\nmm v,92,a,184,v,136\nsa m0,s94,101376\nsa s85,s84,1073152\ndl 199,68,85\nmm v,92,a,188,v,140\n"
"rt a,214,200,6656\nwl 10\nmm v,96,a,160,v,144\nwl 7\nmm v,96,a,164,v,148\nmm v,100,a,168,v,144\n"
"wl 2\ns_barrier\nrt a,216,201,512\nmm v,100,a,172,v,148\nrd v,128,196,32768\nmm v,104,a,176,v,144\n"
"rt a,218,201,2560\nmm v,104,a,180,v,148\nrd v,132,197,32768\nmm v,108,a,184,v,144\nmm v,108,a,188,v,148\nrd v,136,196,34816\n"
"rt a,220,201,4608\nmm v,112,a,160,v,152\nmm v,112,a,164,v,156\nrd v,140,197,34816\nrt a,222,201,6656\nmm v,116,a,168,v,152\n"
"mm v,116,a,172,v,156\nmm v,120,a,176,v,152\nmm v,120,a,180,v,156\nmm v,124,a,184,v,152\nmm v,124,a,188,v,156\nwl 6\n"
"mm v,0,a,192,v,128\nwl 4\nmm v,0,a,196,v,132\nrd v,144,196,36864\nsa m0,s94,114688\nsa s85,s84,1048704\n"
"dl 198,68,85\nmm v,4,a,200,v,128\nmm v,4,a,204,v,132\nmm v,8,a,208,v,128\nrd v,148,197,36864\nmm v,8,a,212,v,132\n"
"mm v,12,a,216,v,128\nwl 2\nmm v,12,a,220,v,132\nmm v,16,a,192,v,136\nmm v,16,a,196,v,140\nrd v,152,196,38912\n"
"mm v,20,a,200,v,136\nmm v,20,a,204,v,140\nmm v,24,a,208,v,136\nrd v,156,197,38912\nmm v,24,a,212,v,140\nsa m0,s94,115712\n"
"sa s85,s84,1056896\ndl 199,68,85\nmm v,28,a,216,v,136\nmm v,28,a,220,v,140\nwl 3\nmm v,32,a,192,v,144\n"
"wl 2\nmm v,32,a,196,v,148\nrd v,128,196,40960\nmm v,36,a,200,v,144\nmm v,36,a,204,v,148\nmm v,40,a,208,v,144\n"
"wv 11\nrd v,132,197,40960\nmm v,40,a,212,v,148\nrt a,224,200,8192\nmm v,44,a,216,v,144\nmm v,44,a,220,v,148\n"
"rt a,226,200,10240\nwl 5\nmm v,48,a,192,v,152\nwl 4\nmm v,48,a,196,v,156\nrt a,228,200,12288\n"
"rd v,136,196,43008\nsa m0,s94,116736\nsa s85,s84,1065088\ndl 198,68,85\nmm v,52,a,200,v,152\nmm v,52,a,204,v,156\n"
"mm v,56,a,208,v,152\nrt a,230,200,14336\nrd v,140,197,43008\nmm v,56,a,212,v,156\nmm v,60,a,216,v,152\nrt a,232,201,8192\n"
"mm v,60,a,220,v,156\nwl 8\nmm v,64,a,192,v,128\nwl 7\nmm v,64,a,196,v,132\nrt a,234,201,10240\n"
"rd v,144,196,45056\nmm v,68,a,200,v,128\nmm v,68,a,204,v,132\nrt a,236,201,12288\nmm v,72,a,208,v,128\nrd v,148,197,45056\n"
"mm v,72,a,212,v,132\nsa m0,s94,117760\nsa s85,s84,1073280\ndl 199,68,85\nmm v,76,a,216,v,128\nrt a,238,201,14336\n"
"mm v,76,a,220,v,132\nwl 8\nmm v,80,a,192,v,136\nrt a,240,200,8704\nwl 7\nmm v,80,a,196,v,140\n"
"rd v,152,196,47104\nmm v,84,a,200,v,136\nrt a,242,200,10752\nmm v,84,a,204,v,140\nmm v,88,a,208,v,136\nrd v,156,197,47104\n"
"mm v,88,a,212,v,140\nrt a,244,200,12800\nmm v,92,a,216,v,136\nmm v,92,a,220,v,140\nrt a,246,200,14848\nwl 9\n"
"mm v,96,a,192,v,144\nwl 7\nmm v,96,a,196,v,148\ns_mul_i32 s85, s36, 163840\nbuffer_load_dword v166, v161, s[76:79], s85 offen\nmm v,100,a,200,v,144\n"
"s_waitcnt vmcnt(6) lgkmcnt(2)\ns_barrier\nrt a,248,201,8704\nmm v,100,a,204,v,148\nrd v,128,196,49152\nmm v,104,a,208,v,144\n"
"rt a,250,201,10752\nmm v,104,a,212,v,148\nrd v,132,197,49152\nmm v,108,a,216,v,144\nmm v,108,a,220,v,148\nrd v,136,196,51200\n"
"rt a,252,201,12800\nmm v,112,a,192,v,152\nmm v,112,a,196,v,156\nrd v,140,197,51200\nrt a,254,201,14848\nmm v,116,a,200,v,152\n"
"mm v,116,a,204,v,156\nmm v,120,a,208,v,152\nmm v,120,a,212,v,156\nmm v,124,a,216,v,152\nmm v,124,a,220,v,156\nwl 6\n"
"mm v,0,a,224,v,128\nwl 4\nmm v,0,a,228,v,132\ns_cmp_eq_u32 s52, 0\ns_cbranch_scc0 NOLN_K\nv_mov_b32 v240, s46\n"
"v_mov_b32 v241, s47\ns_mov_b64 exec, 1\nbuffer_store_dwordx2 v[240:241], v245, s[96:99], s35 offen sc1\ns_mov_b64 exec, -1\nNOLN_K:\nmm v,4,a,232,v,128\n"
"rd v,144,196,53248\nmm v,4,a,236,v,132\nmm v,8,a,240,v,128\nrd v,148,197,53248\nmm v,8,a,244,v,132\nmm v,12,a,248,v,128\n"
"wl 2\nmm v,12,a,252,v,132\nmm v,16,a,224,v,136\nmm v,16,a,228,v,140\nrd v,152,196,55296\nmm v,20,a,232,v,136\n"
"mm v,20,a,236,v,140\nsa m0,s94,131072\nsa s85,s84,1048832\ndl 198,68,85\nmm v,24,a,240,v,136\nrd v,156,197,55296\n"
"mm v,24,a,244,v,140\nmm v,28,a,248,v,136\nmm v,28,a,252,v,140\nwl 3\nmm v,32,a,224,v,144\nwl 2\n"
"mm v,32,a,228,v,148\nrd v,128,196,57344\nmm v,36,a,232,v,144\nmm v,36,a,236,v,148\nmm v,40,a,240,v,144\nrd v,132,197,57344\n"
"mm v,40,a,244,v,148\nsa m0,s94,132096\nsa s85,s84,1057024\ndl 199,68,85\nmm v,44,a,248,v,144\nmm v,44,a,252,v,148\n"
"wl 3\nmm v,48,a,224,v,152\nwl 2\nmm v,48,a,228,v,156\nrd v,136,196,59392\nmm v,52,a,232,v,152\n"
"mm v,52,a,236,v,156\nmm v,56,a,240,v,152\nrd v,140,197,59392\nmm v,56,a,244,v,156\nmm v,60,a,248,v,152\nmm v,60,a,252,v,156\n"
"sa m0,s94,133120\nsa s85,s84,1065216\ndl 198,68,85\nwl 3\nmm v,64,a,224,v,128\nwl 2\n"
"mm v,64,a,228,v,132\nrd v,144,196,61440\nmm v,68,a,232,v,128\nmm v,68,a,236,v,132\nmm v,72,a,240,v,128\nrd v,148,197,61440\n"
"mm v,72,a,244,v,132\nmm v,76,a,248,v,128\nmm v,76,a,252,v,132\nwl 3\nmm v,80,a,224,v,136\nwl 2\n"
"mm v,80,a,228,v,140\nsa m0,s94,134144\nsa s85,s84,1073408\ndl 199,68,85\nmm v,84,a,232,v,136\nrd v,152,196,63488\n"
"mm v,84,a,236,v,140\nmm v,88,a,240,v,136\nrd v,156,197,63488\nmm v,88,a,244,v,140\nmm v,92,a,248,v,136\nmm v,92,a,252,v,140\n"
"wl 3\nmm v,96,a,224,v,144\nwl 2\nmm v,96,a,228,v,148\nmm v,100,a,232,v,144\ns_waitcnt vmcnt(9) lgkmcnt(0)\n"
"s_barrier\nmm v,100,a,236,v,148\ns_lshl_b32 s34, s36, 3\nsa s34,s34,s91\nsa s33,s34,131072\nmm v,104,a,240,v,144\n"
"rd v,128,196,0\nmm v,104,a,244,v,148\nrd v,132,197,0\nmm v,108,a,248,v,144\nmm v,108,a,252,v,148\nrd v,136,196,2048\n"
"mm v,112,a,224,v,152\nmm v,112,a,228,v,156\nrd v,140,197,2048\nmm v,116,a,232,v,152\nmm v,116,a,236,v,156\nmm v,120,a,240,v,152\n"
"mm v,120,a,244,v,156\nmm v,124,a,248,v,152\nmm v,124,a,252,v,156\ns_nop 2\ncv 160,0,1\ncv 161,2,3\n"
"cv 162,4,5\ncv 163,6,7\nwl 3\nmz v,0,a,0,v,128\ncv 164,8,9\ncv 165,10,11\n"
"wl 2\nmm v,0,a,4,v,132\nwv 4\ncv 166,12,13\ncv 167,14,15\nmz v,4,a,8,v,128\n"
"rd v,144,196,4096\ncv 168,16,17\nmm v,4,a,12,v,132\nsa m0,s94,147456\nsa s85,s84,1048960\ndl 198,68,85\n"
"mz v,8,a,16,v,128\ncv 169,18,19\ncv 170,20,21\nmm v,8,a,20,v,132\nrd v,148,197,4096\ncv 171,22,23\n"
"mz v,12,a,24,v,128\ncv 172,24,25\ncv 173,26,27\nmm v,12,a,28,v,132\ncv 174,28,29\ncv 175,30,31\n"
"wl 3\nmz v,16,a,0,v,136\ncv 176,32,33\ncv 177,34,35\nwl 2\nmm v,16,a,4,v,140\n"
"cv 178,36,37\ncv 179,38,39\nmz v,20,a,8,v,136\nrd v,152,196,6144\ncv 180,40,41\nmm v,20,a,12,v,140\n"
"cv 181,42,43\ncv 182,44,45\nmz v,24,a,16,v,136\ncv 183,46,47\ncv 184,48,49\nmm v,24,a,20,v,140\n"
"rd v,156,197,6144\ncv 185,50,51\nmz v,28,a,24,v,136\nsa m0,s94,148480\nsa s85,s84,1057152\ndl 199,68,85\n"
"mm v,28,a,28,v,140\ncv 186,52,53\ncv 187,54,55\nwl 3\nmz v,32,a,0,v,144\ncv 188,56,57\n"
"cv 189,58,59\nwl 2\nmm v,32,a,4,v,148\ncv 190,60,61\ncv 191,62,63\nmz v,36,a,8,v,144\n"
"rd v,128,196,8192\ncv 192,64,65\nmm v,36,a,12,v,148\ncv 193,66,67\ncv 194,68,69\nmz v,40,a,16,v,144\n"
"cv 195,70,71\ncv 206,72,73\nmm v,40,a,20,v,148\nrd v,132,197,8192\ncv 207,74,75\nmz v,44,a,24,v,144\n"
"cv 208,76,77\ncv 209,78,79\nmm v,44,a,28,v,148\nsa m0,s94,149504\nsa s85,s84,1065344\ndl 198,68,85\n"
"wl 3\nmz v,48,a,0,v,152\ncv 210,80,81\ncv 211,82,83\nwl 2\nmm v,48,a,4,v,156\n"
"cv 212,84,85\ncv 213,86,87\nmz v,52,a,8,v,152\nrd v,136,196,10240\ncv 214,88,89\nmm v,52,a,12,v,156\n"
"cv 215,90,91\ncv 216,92,93\nmz v,56,a,16,v,152\nsa m0,s94,150528\nsa s85,s84,1073536\ndl 199,68,85\n"
"mm v,56,a,20,v,156\nrd v,140,197,10240\ncv 217,94,95\nmz v,60,a,24,v,152\ncv 218,96,97\ncv 219,98,99\n"
"mm v,60,a,28,v,156\ncv 220,100,101\ncv 221,102,103\nwl 3\nmz v,64,a,0,v,128\ncv 222,104,105\n"
"cv 223,106,107\nwl 2\nmm v,64,a,4,v,132\ncv 224,108,109\nrd v,144,196,12288\nmz v,68,a,8,v,128\n"
"sa m0,s89,0\nsa s85,s93,0\ndl 203,76,85\nmm v,68,a,12,v,132\ncv 225,110,111\ncv 226,112,113\n"
"mz v,72,a,16,v,128\nrd v,148,197,12288\ncv 227,114,115\nmm v,72,a,20,v,132\ncv 228,116,117\ncv 229,118,119\n"
"mz v,76,a,24,v,128\ncv 246,120,121\ncv 247,122,123\nmm v,76,a,28,v,132\nsa m0,s89,1024\nsa s85,s93,65536\n"
"dl 203,76,85\nwl 3\nmz v,80,a,0,v,136\nbuffer_store_dwordx4 v[160:163], v230, s[40:43], s86 offen offset:0 sc1\nbuffer_store_dwordx4 v[164:167], v230, s[40:43], s86 offen offset:1024 sc1\nsa s86,s86,s92\n"
"wl 2\nmm v,80,a,4,v,140\nrd v,152,196,14336\ncv 248,124,125\nmz v,84,a,8,v,136\ncv 249,126,127\n"
"mm v,84,a,12,v,140\nmz v,88,a,16,v,136\nrd v,156,197,14336\nmm v,88,a,20,v,140\nsa m0,s89,2048\nsa s85,s93,131072\n"
"dl 203,76,85\nmz v,92,a,24,v,136\nmm v,92,a,28,v,140\nwl 3\nmz v,96,a,0,v,144\nwl 2\n"
"mm v,96,a,4,v,148\nmz v,100,a,8,v,144\nwl 0\ns_barrier\nsa m0,s89,3072\nsa s85,s93,196608\n"
"dl 203,76,85\nmm v,100,a,12,v,148\nrd v,128,196,16384\nmz v,104,a,16,v,144\nmm v,104,a,20,v,148\nrd v,132,197,16384\n"
"mz v,108,a,24,v,144\nmm v,108,a,28,v,148\nrd v,136,196,18432\nmz v,112,a,0,v,152\nsa m0,s89,16384\nsa s85,s93,524288\n"
"dl 203,76,85\nmm v,112,a,4,v,156\nrd v,140,197,18432\nmz v,116,a,8,v,152\nmm v,116,a,12,v,156\nmz v,120,a,16,v,152\n"
"mm v,120,a,20,v,156\nmz v,124,a,24,v,152\nmm v,124,a,28,v,156\nwl 3\nmm v,0,a,32,v,128\nwl 2\n"
"mm v,0,a,36,v,132\nsa m0,s94,98304\nsa s85,s84,1049088\ndl 198,68,85\nmm v,4,a,40,v,128\nrd v,144,196,20480\n"
"mm v,4,a,44,v,132\nmm v,8,a,48,v,128\nrd v,148,197,20480\nmm v,8,a,52,v,132\nmm v,12,a,56,v,128\nbuffer_store_dwordx4 v[168:171], v230, s[40:43], s86 offen offset:0 sc1\n"
"mm v,12,a,60,v,132\nwl 3\nmm v,16,a,32,v,136\nwl 2\nmm v,16,a,36,v,140\nrd v,152,196,22528\n"
"mm v,20,a,40,v,136\nmm v,20,a,44,v,140\nmm v,24,a,48,v,136\nsa m0,s94,99328\nsa s85,s84,1057280\ndl 199,68,85\n"
"mm v,24,a,52,v,140\nrd v,156,197,22528\nmm v,28,a,56,v,136\nmm v,28,a,60,v,140\nwl 3\nmm v,32,a,32,v,144\n"
"wl 2\nmm v,32,a,36,v,148\nbuffer_store_dwordx4 v[172:175], v230, s[40:43], s86 offen offset:1024 sc1\nsa s86,s86,s92\nmm v,36,a,40,v,144\nrd v,128,196,24576\n"
"mm v,36,a,44,v,148\nmm v,40,a,48,v,144\nrd v,132,197,24576\nmm v,40,a,52,v,148\nmm v,44,a,56,v,144\nmm v,44,a,60,v,148\n"
"sa m0,s94,100352\nsa s85,s84,1065472\ndl 198,68,85\nwl 3\nmm v,48,a,32,v,152\nwl 2\n"
"mm v,48,a,36,v,156\nrd v,136,196,26624\nmm v,52,a,40,v,152\nmm v,52,a,44,v,156\nmm v,56,a,48,v,152\nsa m0,s94,101376\n"
"sa s85,s84,1073664\ndl 199,68,85\nmm v,56,a,52,v,156\nrd v,140,197,26624\nmm v,60,a,56,v,152\nmm v,60,a,60,v,156\n"
"wl 3\nmm v,64,a,32,v,128\nwl 2\nmm v,64,a,36,v,132\nrd v,144,196,28672\nmm v,68,a,40,v,128\n"
"sa m0,s89,17408\nsa s85,s93,589824\ndl 203,76,85\nmm v,68,a,44,v,132\nmm v,72,a,48,v,128\nrd v,148,197,28672\n"
"mm v,72,a,52,v,132\nmm v,76,a,56,v,128\nmm v,76,a,60,v,132\nsa m0,s89,18432\nsa s85,s93,655360\ndl 203,76,85\n"
"wl 3\nmm v,80,a,32,v,136\nwl 2\nmm v,80,a,36,v,140\nrd v,152,196,30720\nmm v,84,a,40,v,136\n"
"mm v,84,a,44,v,140\nmm v,88,a,48,v,136\nrd v,156,197,30720\nmm v,88,a,52,v,140\nsa m0,s89,19456\nsa s85,s93,720896\n"
"dl 203,76,85\nmm v,92,a,56,v,136\nmm v,92,a,60,v,140\nwl 3\nmm v,96,a,32,v,144\nwl 2\n"
"mm v,96,a,36,v,148\nmm v,100,a,40,v,144\ns_waitcnt vmcnt(20) lgkmcnt(0)\ns_barrier\nsa m0,s89,32768\nsa s85,s93,1048576\n"
"dl 203,76,85\nmm v,100,a,44,v,148\nrd v,128,196,32768\nmm v,104,a,48,v,144\nmm v,104,a,52,v,148\nrd v,132,197,32768\n"
"mm v,108,a,56,v,144\nmm v,108,a,60,v,148\nrd v,136,196,34816\nmm v,112,a,32,v,152\nsa m0,s89,33792\nsa s85,s93,1114112\n"
"dl 203,76,85\nmm v,112,a,36,v,156\nrd v,140,197,34816\nmm v,116,a,40,v,152\nmm v,116,a,44,v,156\nmm v,120,a,48,v,152\n"
"mm v,120,a,52,v,156\nmm v,124,a,56,v,152\nmm v,124,a,60,v,156\nwl 3\nmm v,0,a,64,v,128\nwl 2\n"
"mm v,0,a,68,v,132\nsa m0,s94,114688\nsa s85,s84,1049216\ndl 198,68,85\nmm v,4,a,72,v,128\nrd v,144,196,36864\n"
"mm v,4,a,76,v,132\nmm v,8,a,80,v,128\nrd v,148,197,36864\nmm v,8,a,84,v,132\nmm v,12,a,88,v,128\nbuffer_store_dwordx4 v[176:179], v230, s[40:43], s86 offen offset:0 sc1\n"
"mm v,12,a,92,v,132\nwl 3\nmm v,16,a,64,v,136\nwl 2\nmm v,16,a,68,v,140\nrd v,152,196,38912\n"
"mm v,20,a,72,v,136\nmm v,20,a,76,v,140\nmm v,24,a,80,v,136\nsa m0,s94,115712\nsa s85,s84,1057408\ndl 199,68,85\n"
"mm v,24,a,84,v,140\nrd v,156,197,38912\nmm v,28,a,88,v,136\nmm v,28,a,92,v,140\nwl 3\nmm v,32,a,64,v,144\n"
"wl 2\nmm v,32,a,68,v,148\nbuffer_store_dwordx4 v[180:183], v230, s[40:43], s86 offen offset:1024 sc1\nsa s86,s86,s92\nmm v,36,a,72,v,144\nrd v,128,196,40960\n"
"mm v,36,a,76,v,148\nmm v,40,a,80,v,144\nrd v,132,197,40960\nmm v,40,a,84,v,148\nmm v,44,a,88,v,144\nmm v,44,a,92,v,148\n"
"sa m0,s94,116736\nsa s85,s84,1065600\ndl 198,68,85\nwl 3\nmm v,48,a,64,v,152\nwl 2\n"
"mm v,48,a,68,v,156\nrd v,136,196,43008\nmm v,52,a,72,v,152\nmm v,52,a,76,v,156\nmm v,56,a,80,v,152\nsa m0,s94,117760\n"
"sa s85,s84,1073792\ndl 199,68,85\nmm v,56,a,84,v,156\nrd v,140,197,43008\nmm v,60,a,88,v,152\nmm v,60,a,92,v,156\n"
"wl 3\nmm v,64,a,64,v,128\nwl 2\nmm v,64,a,68,v,132\nrd v,144,196,45056\nmm v,68,a,72,v,128\n"
"sa m0,s89,34816\nsa s85,s93,1179648\ndl 203,76,85\nmm v,68,a,76,v,132\nmm v,72,a,80,v,128\nrd v,148,197,45056\n"
"mm v,72,a,84,v,132\nmm v,76,a,88,v,128\nmm v,76,a,92,v,132\nsa m0,s89,35840\nsa s85,s93,1245184\ndl 203,76,85\n"
"wl 3\nmm v,80,a,64,v,136\nwl 2\nmm v,80,a,68,v,140\nrd v,152,196,47104\nmm v,84,a,72,v,136\n"
"mm v,84,a,76,v,140\nmm v,88,a,80,v,136\nrd v,156,197,47104\nmm v,88,a,84,v,140\nsa m0,s89,49152\nsa s85,s93,1572864\n"
"dl 203,76,85\nmm v,92,a,88,v,136\nmm v,92,a,92,v,140\nwl 3\nmm v,96,a,64,v,144\nwl 2\n"
"mm v,96,a,68,v,148\nmm v,100,a,72,v,144\ns_waitcnt vmcnt(27) lgkmcnt(0)\ns_barrier\nsa m0,s89,50176\nsa s85,s93,1638400\n"
"dl 203,76,85\nmm v,100,a,76,v,148\nrd v,128,196,49152\nmm v,104,a,80,v,144\nmm v,104,a,84,v,148\nrd v,132,197,49152\n"
"mm v,108,a,88,v,144\nmm v,108,a,92,v,148\nrd v,136,196,51200\nmm v,112,a,64,v,152\nsa m0,s89,51200\nsa s85,s93,1703936\n"
"dl 203,76,85\nmm v,112,a,68,v,156\nrd v,140,197,51200\nmm v,116,a,72,v,152\nmm v,116,a,76,v,156\nmm v,120,a,80,v,152\n"
"mm v,120,a,84,v,156\nmm v,124,a,88,v,152\nmm v,124,a,92,v,156\nwl 3\nmm v,0,a,96,v,128\nwl 2\n"
"mm v,0,a,100,v,132\nsa m0,s94,131072\nsa s85,s84,1049344\ndl 198,68,85\nmm v,4,a,104,v,128\nrd v,144,196,53248\n"
"mm v,4,a,108,v,132\nmm v,8,a,112,v,128\nrd v,148,197,53248\nmm v,8,a,116,v,132\nmm v,12,a,120,v,128\nbuffer_store_dwordx4 v[184:187], v230, s[40:43], s86 offen offset:0 sc1\n"
"mm v,12,a,124,v,132\nwl 3\nmm v,16,a,96,v,136\nwl 2\nmm v,16,a,100,v,140\nrd v,152,196,55296\n"
"mm v,20,a,104,v,136\nmm v,20,a,108,v,140\nsa m0,s94,132096\nsa s85,s84,1057536\ndl 199,68,85\nmm v,24,a,112,v,136\n"
"rd v,156,197,55296\nmm v,24,a,116,v,140\nmm v,28,a,120,v,136\nmm v,28,a,124,v,140\nwl 3\nmm v,32,a,96,v,144\n"
"buffer_store_dwordx4 v[188:191], v230, s[40:43], s86 offen offset:1024 sc1\nsa s86,s86,s92\nwl 2\nmm v,32,a,100,v,148\nrd v,128,196,57344\nmm v,36,a,104,v,144\n"
"mm v,36,a,108,v,148\nmm v,40,a,112,v,144\nrd v,132,197,57344\nmm v,40,a,116,v,148\nsa m0,s94,133120\nsa s85,s84,1065728\n"
"dl 198,68,85\nmm v,44,a,120,v,144\nmm v,44,a,124,v,148\nwl 3\nmm v,48,a,96,v,152\nwl 2\n"
"mm v,48,a,100,v,156\nrd v,136,196,59392\nmm v,52,a,104,v,152\nsa m0,s94,134144\nsa s85,s84,1073920\ndl 199,68,85\n"
"mm v,52,a,108,v,156\nmm v,56,a,112,v,152\nrd v,140,197,59392\nmm v,56,a,116,v,156\nmm v,60,a,120,v,152\nmm v,60,a,124,v,156\n"
"sa m0,s89,52224\nsa s85,s93,1769472\ndl 203,76,85\nwl 3\nmm v,64,a,96,v,128\nwl 2\n"
"mm v,64,a,100,v,132\nrd v,144,196,61440\nmm v,68,a,104,v,128\nmm v,68,a,108,v,132\nmm v,72,a,112,v,128\nsa m0,s89,65536\n"
"sa s85,s93,2097152\ndl 203,76,85\nmm v,72,a,116,v,132\nrd v,148,197,61440\nmm v,76,a,120,v,128\nmm v,76,a,124,v,132\n"
"wl 3\nmm v,80,a,96,v,136\nwl 2\nmm v,80,a,100,v,140\nsa m0,s89,66560\nsa s85,s93,2162688\n"
"dl 203,76,85\nmm v,84,a,104,v,136\nrd v,152,196,63488\nmm v,84,a,108,v,140\nmm v,88,a,112,v,136\nrd v,156,197,63488\n"
"mm v,88,a,116,v,140\nmm v,92,a,120,v,136\nsa m0,s89,67584\nsa s85,s93,2228224\ndl 203,76,85\nmm v,92,a,124,v,140\n"
"wl 3\nmm v,96,a,96,v,144\nwl 2\nmm v,96,a,100,v,148\nmm v,100,a,104,v,144\ns_waitcnt vmcnt(26) lgkmcnt(0)\n"
"s_barrier\nmm v,100,a,108,v,148\nrd v,128,196,0\nsa m0,s89,68608\nsa s85,s93,2293760\ndl 203,76,85\n"
"mm v,104,a,112,v,144\nmm v,104,a,116,v,148\nrd v,132,197,0\nmm v,108,a,120,v,144\nmm v,108,a,124,v,148\nrd v,136,196,2048\n"
"mm v,112,a,96,v,152\nbuffer_load_dwordx2 v[242:243], v244, s[96:99], s90 offen sc1\nmm v,112,a,100,v,156\nrd v,140,197,2048\nmm v,116,a,104,v,152\nmm v,116,a,108,v,156\n"
"mm v,120,a,112,v,152\nmm v,120,a,116,v,156\nmm v,124,a,120,v,152\nmm v,124,a,124,v,156\nwl 3\nmm v,0,a,128,v,128\n"
"wl 2\nmm v,0,a,132,v,132\nwv 0\nv_cmp_ne_u64 vcc, s[46:47], v[242:243]\ns_nop 0\ns_and_b64 vcc, vcc, s[38:39]\n"
"s_cbranch_scc1 SPIN_HK\nSPUN_HK:\nmm v,4,a,136,v,128\nrd v,144,196,4096\nmm v,4,a,140,v,132\nmm v,8,a,144,v,128\n"
"rd v,148,197,4096\nmm v,8,a,148,v,132\nmm v,12,a,152,v,128\nbuffer_store_dwordx4 v[192:195], v230, s[40:43], s86 offen offset:0 sc1\nmm v,12,a,156,v,132\nwl 3\n"
"mm v,16,a,128,v,136\nwl 2\nmm v,16,a,132,v,140\nrd v,152,196,6144\nmm v,20,a,136,v,136\nmm v,20,a,140,v,140\n"
"mm v,24,a,144,v,136\nsa m0,s94,147456\nsa s85,s84,1049472\ndl 198,68,85\nmm v,24,a,148,v,140\nrd v,156,197,6144\n"
"mm v,28,a,152,v,136\nmm v,28,a,156,v,140\nwl 3\nmm v,32,a,128,v,144\nwl 2\nmm v,32,a,132,v,148\n"
"buffer_store_dwordx4 v[206:209], v230, s[40:43], s86 offen offset:1024 sc1\nsa s86,s86,s92\nmm v,36,a,136,v,144\nrd v,128,196,8192\nmm v,36,a,140,v,148\nmm v,40,a,144,v,144\n"
"rd v,132,197,8192\nmm v,40,a,148,v,148\nmm v,44,a,152,v,144\nmm v,44,a,156,v,148\nsa m0,s94,148480\nsa s85,s84,1057664\n"
"dl 199,68,85\nwl 3\nmm v,48,a,128,v,152\nwl 2\nmm v,48,a,132,v,156\nrd v,136,196,10240\n"
"mm v,52,a,136,v,152\nmm v,52,a,140,v,156\nmm v,56,a,144,v,152\nsa m0,s94,149504\nsa s85,s84,1065856\ndl 198,68,85\n"
"mm v,56,a,148,v,156\nrd v,140,197,10240\nmm v,60,a,152,v,152\nmm v,60,a,156,v,156\nwl 3\nmm v,64,a,128,v,128\n"
"wl 2\nmm v,64,a,132,v,132\nrd v,144,196,12288\nmm v,68,a,136,v,128\nsa m0,s94,150528\nsa s85,s84,1074048\n"
"dl 199,68,85\nmm v,68,a,140,v,132\nmm v,72,a,144,v,128\nrd v,148,197,12288\nmm v,72,a,148,v,132\nmm v,76,a,152,v,128\n"
"mm v,76,a,156,v,132\nsa m0,s89,81920\nsa s85,s93,2621440\ndl 203,76,85\nwl 3\nmm v,80,a,128,v,136\n"
"wl 2\nmm v,80,a,132,v,140\nrd v,152,196,14336\nmm v,84,a,136,v,136\nmm v,84,a,140,v,140\nmm v,88,a,144,v,136\n"
"rd v,156,197,14336\nmm v,88,a,148,v,140\nsa m0,s89,82944\nsa s85,s93,2686976\ndl 203,76,85\nmm v,92,a,152,v,136\n"
"mm v,92,a,156,v,140\nwl 3\nmm v,96,a,128,v,144\nwl 2\nmm v,96,a,132,v,148\nmm v,100,a,136,v,144\n"
"wl 0\ns_barrier\nsa m0,s89,83968\nsa s85,s93,2752512\ndl 203,76,85\nmm v,100,a,140,v,148\n"
"rd v,128,196,16384\nmm v,104,a,144,v,144\nmm v,104,a,148,v,148\nrd v,132,197,16384\nmm v,108,a,152,v,144\nmm v,108,a,156,v,148\n"
"rd v,136,196,18432\nmm v,112,a,128,v,152\nsa m0,s89,84992\nsa s85,s93,2818048\ndl 203,76,85\nmm v,112,a,132,v,156\n"
"rd v,140,197,18432\nmm v,116,a,136,v,152\nmm v,116,a,140,v,156\nmm v,120,a,144,v,152\nmm v,120,a,148,v,156\nmm v,124,a,152,v,152\n"
"mm v,124,a,156,v,156\nwl 3\nmm v,0,a,160,v,128\nwl 2\nmm v,0,a,164,v,132\nsa s85,s87,0\n"
"buffer_load_dwordx4 a[0:3], v230, s[80:83], s85 offen sc1\nmm v,4,a,168,v,128\nrd v,144,196,20480\nmm v,4,a,172,v,132\nmm v,8,a,176,v,128\nrd v,148,197,20480\n"
"mm v,8,a,180,v,132\nmm v,12,a,184,v,128\nbuffer_store_dwordx4 v[210:213], v230, s[40:43], s86 offen offset:0 sc1\nmm v,12,a,188,v,132\nwl 3\nmm v,16,a,160,v,136\n"
"wl 2\nmm v,16,a,164,v,140\nrd v,152,196,22528\nmm v,20,a,168,v,136\nmm v,20,a,172,v,140\nmm v,24,a,176,v,136\n"
"sa s85,s87,1024\nbuffer_load_dwordx4 a[4:7], v230, s[80:83], s85 offen sc1\nmm v,24,a,180,v,140\nrd v,156,197,22528\nmm v,28,a,184,v,136\nmm v,28,a,188,v,140\n"
"wl 3\nmm v,32,a,160,v,144\nwl 2\nmm v,32,a,164,v,148\nbuffer_store_dwordx4 v[214:217], v230, s[40:43], s86 offen offset:1024 sc1\nsa s86,s86,s92\n"
"mm v,36,a,168,v,144\nrd v,128,196,24576\nmm v,36,a,172,v,148\nmm v,40,a,176,v,144\nrd v,132,197,24576\nmm v,40,a,180,v,148\n"
"mm v,44,a,184,v,144\nmm v,44,a,188,v,148\nsa s85,s66,0\nbuffer_load_dwordx4 a[8:11], v230, s[80:83], s85 offen sc1\nwl 3\nmm v,48,a,160,v,152\n"
"wl 2\nmm v,48,a,164,v,156\nrd v,136,196,26624\nmm v,52,a,168,v,152\nmm v,52,a,172,v,156\nmm v,56,a,176,v,152\n"
"sa s85,s66,1024\nbuffer_load_dwordx4 a[12:15], v230, s[80:83], s85 offen sc1\nmm v,56,a,180,v,156\nrd v,140,197,26624\nmm v,60,a,184,v,152\nmm v,60,a,188,v,156\n"
"wl 3\nmm v,64,a,160,v,128\nwl 2\nmm v,64,a,164,v,132\nrd v,144,196,28672\nmm v,68,a,168,v,128\n"
"sa s85,s88,0\nbuffer_load_dwordx4 a[16:19], v230, s[80:83], s85 offen sc1\nmm v,68,a,172,v,132\nmm v,72,a,176,v,128\nrd v,148,197,28672\nmm v,72,a,180,v,132\n"
"mm v,76,a,184,v,128\nmm v,76,a,188,v,132\nsa s85,s88,1024\nbuffer_load_dwordx4 a[20:23], v230, s[80:83], s85 offen sc1\nwl 3\nmm v,80,a,160,v,136\n"
"wl 2\nmm v,80,a,164,v,140\nrd v,152,196,30720\nmm v,84,a,168,v,136\nmm v,84,a,172,v,140\nmm v,88,a,176,v,136\n"
"rd v,156,197,30720\nmm v,88,a,180,v,140\nsa s85,s59,0\nbuffer_load_dwordx4 a[24:27], v230, s[80:83], s85 offen sc1\nmm v,92,a,184,v,136\nmm v,92,a,188,v,140\n"
"wl 3\nmm v,96,a,160,v,144\nwl 2\nmm v,96,a,164,v,148\nmm v,100,a,168,v,144\nwl 0\n"
"s_barrier\nsa s85,s59,1024\nbuffer_load_dwordx4 a[28:31], v230, s[80:83], s85 offen sc1\nmm v,100,a,172,v,148\nrd v,128,196,32768\nmm v,104,a,176,v,144\n"
"mm v,104,a,180,v,148\nrd v,132,197,32768\nmm v,108,a,184,v,144\nmm v,108,a,188,v,148\nrd v,136,196,34816\nmm v,112,a,160,v,152\n"
"sa s85,s87,2048\nbuffer_load_dwordx4 a[32:35], v230, s[80:83], s85 offen sc1\nmm v,112,a,164,v,156\nrd v,140,197,34816\nmm v,116,a,168,v,152\nmm v,116,a,172,v,156\n"
"mm v,120,a,176,v,152\nmm v,120,a,180,v,156\nmm v,124,a,184,v,152\nmm v,124,a,188,v,156\nwl 3\nmm v,0,a,192,v,128\n"
"wl 2\nmm v,0,a,196,v,132\nsa s85,s87,3072\nbuffer_load_dwordx4 a[36:39], v230, s[80:83], s85 offen sc1\nmm v,4,a,200,v,128\nrd v,144,196,36864\n"
"mm v,4,a,204,v,132\nmm v,8,a,208,v,128\nrd v,148,197,36864\nmm v,8,a,212,v,132\nmm v,12,a,216,v,128\nbuffer_store_dwordx4 v[218:221], v230, s[40:43], s86 offen offset:0 sc1\n"
"mm v,12,a,220,v,132\nwl 3\nmm v,16,a,192,v,136\nwl 2\nmm v,16,a,196,v,140\nrd v,152,196,38912\n"
"mm v,20,a,200,v,136\nmm v,20,a,204,v,140\nmm v,24,a,208,v,136\nsa s85,s66,2048\nbuffer_load_dwordx4 a[40:43], v230, s[80:83], s85 offen sc1\nmm v,24,a,212,v,140\n"
"rd v,156,197,38912\nmm v,28,a,216,v,136\nmm v,28,a,220,v,140\nwl 3\nmm v,32,a,192,v,144\nwl 2\n"
"mm v,32,a,196,v,148\nbuffer_store_dwordx4 v[222:225], v230, s[40:43], s86 offen offset:1024 sc1\nsa s86,s86,s92\nmm v,36,a,200,v,144\nrd v,128,196,40960\nmm v,36,a,204,v,148\n"
"mm v,40,a,208,v,144\nrd v,132,197,40960\nmm v,40,a,212,v,148\nmm v,44,a,216,v,144\nmm v,44,a,220,v,148\nsa s85,s66,3072\n"
"buffer_load_dwordx4 a[44:47], v230, s[80:83], s85 offen sc1\nwl 3\nmm v,48,a,192,v,152\nwl 2\nmm v,48,a,196,v,156\nrd v,136,196,43008\n"
"mm v,52,a,200,v,152\nmm v,52,a,204,v,156\nmm v,56,a,208,v,152\nsa s85,s88,2048\nbuffer_load_dwordx4 a[48:51], v230, s[80:83], s85 offen sc1\nmm v,56,a,212,v,156\n"
"rd v,140,197,43008\nmm v,60,a,216,v,152\nmm v,60,a,220,v,156\nwl 3\nmm v,64,a,192,v,128\nwl 2\n"
"mm v,64,a,196,v,132\nrd v,144,196,45056\nmm v,68,a,200,v,128\nsa s85,s88,3072\nbuffer_load_dwordx4 a[52:55], v230, s[80:83], s85 offen sc1\nmm v,68,a,204,v,132\n"
"mm v,72,a,208,v,128\nrd v,148,197,45056\nmm v,72,a,212,v,132\nmm v,76,a,216,v,128\nmm v,76,a,220,v,132\nsa s85,s59,2048\n"
"buffer_load_dwordx4 a[56:59], v230, s[80:83], s85 offen sc1\nwl 3\nmm v,80,a,192,v,136\nwl 2\nmm v,80,a,196,v,140\nrd v,152,196,47104\n"
"mm v,84,a,200,v,136\nmm v,84,a,204,v,140\nmm v,88,a,208,v,136\nrd v,156,197,47104\nmm v,88,a,212,v,140\nsa s85,s59,3072\n"
"buffer_load_dwordx4 a[60:63], v230, s[80:83], s85 offen sc1\nmm v,92,a,216,v,136\nmm v,92,a,220,v,140\nwl 3\nmm v,96,a,192,v,144\nwl 2\n"
"mm v,96,a,196,v,148\nmm v,100,a,200,v,144\ns_waitcnt vmcnt(24) lgkmcnt(0)\ns_barrier\nsa s85,s87,4096\nbuffer_load_dwordx4 a[64:67], v230, s[80:83], s85 offen sc1\n"
"mm v,100,a,204,v,148\nrd v,128,196,49152\nmm v,104,a,208,v,144\nmm v,104,a,212,v,148\nrd v,132,197,49152\nmm v,108,a,216,v,144\n"
"mm v,108,a,220,v,148\nrd v,136,196,51200\nmm v,112,a,192,v,152\nsa s85,s87,5120\nbuffer_load_dwordx4 a[68:71], v230, s[80:83], s85 offen sc1\nmm v,112,a,196,v,156\n"
"rd v,140,197,51200\nmm v,116,a,200,v,152\nmm v,116,a,204,v,156\nmm v,120,a,208,v,152\nmm v,120,a,212,v,156\nmm v,124,a,216,v,152\n"
"mm v,124,a,220,v,156\nwl 3\nmm v,0,a,224,v,128\nwl 2\nmm v,0,a,228,v,132\nsa s85,s66,4096\n"
"buffer_load_dwordx4 a[72:75], v230, s[80:83], s85 offen sc1\nmm v,4,a,232,v,128\nrd v,144,196,53248\nmm v,4,a,236,v,132\nmm v,8,a,240,v,128\nrd v,148,197,53248\n"
"mm v,8,a,244,v,132\nmm v,12,a,248,v,128\nbuffer_store_dwordx4 v[226:229], v230, s[40:43], s86 offen offset:0 sc1\nmm v,12,a,252,v,132\nwl 3\nmm v,16,a,224,v,136\n"
"wl 2\nmm v,16,a,228,v,140\nrd v,152,196,55296\nmm v,20,a,232,v,136\nmm v,20,a,236,v,140\nmm v,24,a,240,v,136\n"
"sa s85,s66,5120\nbuffer_load_dwordx4 a[76:79], v230, s[80:83], s85 offen sc1\nmm v,24,a,244,v,140\nrd v,156,197,55296\nmm v,28,a,248,v,136\nmm v,28,a,252,v,140\n"
"wl 3\nmm v,32,a,224,v,144\nwl 2\nmm v,32,a,228,v,148\nbuffer_store_dwordx4 v[246:249], v230, s[40:43], s86 offen offset:1024 sc1\nsa s86,s86,s92\n"
"mm v,36,a,232,v,144\nrd v,128,196,57344\nmm v,36,a,236,v,148\nmm v,40,a,240,v,144\nrd v,132,197,57344\nmm v,40,a,244,v,148\n"
"mm v,44,a,248,v,144\nmm v,44,a,252,v,148\nsa s85,s88,4096\nbuffer_load_dwordx4 a[80:83], v230, s[80:83], s85 offen sc1\nwl 3\nmm v,48,a,224,v,152\n"
"wl 2\nmm v,48,a,228,v,156\nrd v,136,196,59392\nmm v,52,a,232,v,152\nmm v,52,a,236,v,156\nmm v,56,a,240,v,152\n"
"sa s85,s88,5120\nbuffer_load_dwordx4 a[84:87], v230, s[80:83], s85 offen sc1\nmm v,56,a,244,v,156\nrd v,140,197,59392\nmm v,60,a,248,v,152\nmm v,60,a,252,v,156\n"
"wl 3\nmm v,64,a,224,v,128\nwl 2\nmm v,64,a,228,v,132\nrd v,144,196,61440\nmm v,68,a,232,v,128\n"
"sa s85,s59,4096\nbuffer_load_dwordx4 a[88:91], v230, s[80:83], s85 offen sc1\nmm v,68,a,236,v,132\nmm v,72,a,240,v,128\nrd v,148,197,61440\nmm v,72,a,244,v,132\n"
"mm v,76,a,248,v,128\nmm v,76,a,252,v,132\nsa s85,s59,5120\nbuffer_load_dwordx4 a[92:95], v230, s[80:83], s85 offen sc1\nwl 3\nmm v,80,a,224,v,136\n"
"wl 2\nmm v,80,a,228,v,140\nrd v,152,196,63488\nmm v,84,a,232,v,136\nmm v,84,a,236,v,140\nmm v,88,a,240,v,136\n"
"rd v,156,197,63488\nmm v,88,a,244,v,140\nsa s85,s87,6144\nbuffer_load_dwordx4 a[96:99], v230, s[80:83], s85 offen sc1\nmm v,92,a,248,v,136\nmm v,92,a,252,v,140\n"
"wl 3\nmm v,96,a,224,v,144\nwl 2\nmm v,96,a,228,v,148\nmm v,100,a,232,v,144\nsa s85,s87,7168\n"
"buffer_load_dwordx4 a[100:103], v230, s[80:83], s85 offen sc1\nmm v,100,a,236,v,148\nmm v,104,a,240,v,144\nmm v,104,a,244,v,148\nmm v,108,a,248,v,144\nmm v,108,a,252,v,148\n"
"wl 1\nmm v,112,a,224,v,152\nsa s85,s66,6144\nbuffer_load_dwordx4 a[104:107], v230, s[80:83], s85 offen sc1\nwl 0\nmm v,112,a,228,v,156\n"
"mm v,116,a,232,v,152\nmm v,116,a,236,v,156\nmm v,120,a,240,v,152\nmm v,120,a,244,v,156\nmm v,124,a,248,v,152\nmm v,124,a,252,v,156\n"
"s_branch KV_END\nKV_V:\nwv 29\nrt a,0,200,0\nrt a,2,200,2048\nrt a,4,200,4096\n"
"rt a,6,200,6144\nrt a,8,201,0\nrt a,10,201,2048\nrt a,12,201,4096\nrt a,14,201,6144\nrt a,16,200,512\n"
"rt a,18,200,2560\nrt a,20,200,4608\nrt a,22,200,6656\nrt a,24,201,512\nrt a,26,201,2560\nrt a,28,201,4608\n"
"rt a,30,201,6656\nwv 25\ns_barrier\nrd v,128,196,0\nrd v,132,197,0\nrd v,136,196,2048\n"
"rd v,140,197,2048\nwl 3\nmz v,0,v,128,a,0\nwl 2\nmm v,0,v,132,a,4\nrd v,144,196,4096\n"
"mz v,32,v,128,a,8\nmm v,32,v,132,a,12\nmz v,64,v,128,a,16\nrd v,148,197,4096\nmm v,64,v,132,a,20\nmz v,96,v,128,a,24\n"
"mm v,96,v,132,a,28\nwl 3\nmz v,4,v,136,a,0\nwl 2\nmm v,4,v,140,a,4\nsa m0,s94,147456\n"
"sa s85,s84,384\ndl 198,68,85\nrd v,152,196,6144\nmz v,36,v,136,a,8\nmm v,36,v,140,a,12\nmz v,68,v,136,a,16\n"
"rd v,156,197,6144\nmm v,68,v,140,a,20\nsa m0,s94,148480\nsa s85,s84,8576\ndl 199,68,85\nmz v,100,v,136,a,24\n"
"mm v,100,v,140,a,28\nwl 3\nmz v,8,v,144,a,0\nwl 2\nmm v,8,v,148,a,4\nsa m0,s94,149504\n"
"sa s85,s84,16768\ndl 198,68,85\nrd v,128,196,8192\nmz v,40,v,144,a,8\nmm v,40,v,148,a,12\nmz v,72,v,144,a,16\n"
"wv 20\nrd v,132,197,8192\nmm v,72,v,148,a,20\nsa m0,s94,150528\nsa s85,s84,24960\ndl 199,68,85\n"
"rt a,32,200,8192\nmz v,104,v,144,a,24\nmm v,104,v,148,a,28\nrt a,34,200,10240\nwl 5\nmz v,12,v,152,a,0\n"
"wl 4\nmm v,12,v,156,a,4\nsa m0,s65,0\ns_mov_b32 s85, 1572864\ndl 202,72,85\nrt a,36,200,12288\n"
"rd v,136,196,10240\nmz v,44,v,152,a,8\nmm v,44,v,156,a,12\nmz v,76,v,152,a,16\nrt a,38,200,14336\nrd v,140,197,10240\n"
"mm v,76,v,156,a,20\nsa m0,s65,1024\ns_mov_b32 s85, 1703936\ndl 202,72,85\nmz v,108,v,152,a,24\nrt a,40,201,8192\n"
"mm v,108,v,156,a,28\nwl 8\nmz v,16,v,128,a,0\nwl 7\nmm v,16,v,132,a,4\nsa m0,s65,2048\n"
"s_mov_b32 s85, 1605632\ndl 202,72,85\nrt a,42,201,10240\nrd v,144,196,12288\nmz v,48,v,128,a,8\nmm v,48,v,132,a,12\n"
"rt a,44,201,12288\nmz v,80,v,128,a,16\nrd v,148,197,12288\nmm v,80,v,132,a,20\nsa m0,s65,3072\ns_mov_b32 s85, 1736704\n"
"dl 202,72,85\nmz v,112,v,128,a,24\nrt a,46,201,14336\nmm v,112,v,132,a,28\nwl 8\nmz v,20,v,136,a,0\n"
"rt a,48,200,8704\nwl 7\nmm v,20,v,140,a,4\nsa m0,s65,4096\ns_mov_b32 s85, 1835008\ndl 202,72,85\n"
"mz v,52,v,136,a,8\nrd v,152,196,14336\nrt a,50,200,10752\nmm v,52,v,140,a,12\nmz v,84,v,136,a,16\nrd v,156,197,14336\n"
"mm v,84,v,140,a,20\nsa m0,s65,5120\ns_mov_b32 s85, 1966080\ndl 202,72,85\nmz v,116,v,136,a,24\nrt a,52,200,12800\n"
"mm v,116,v,140,a,28\nrt a,54,200,14848\nwl 9\nmz v,24,v,144,a,0\nwl 7\nmm v,24,v,148,a,4\n"
"sa m0,s65,6144\ns_mov_b32 s85, 1867776\ndl 202,72,85\nmz v,56,v,144,a,8\ns_waitcnt vmcnt(24) lgkmcnt(2)\ns_barrier\n"
"rt a,56,201,8704\nmm v,56,v,148,a,12\nrd v,128,196,16384\nmz v,88,v,144,a,16\nrt a,58,201,10752\nmm v,88,v,148,a,20\n"
"sa m0,s65,7168\ns_mov_b32 s85, 1998848\ndl 202,72,85\nmz v,120,v,144,a,24\nrd v,132,197,16384\nmm v,120,v,148,a,28\n"
"rd v,136,196,18432\nrt a,60,201,12800\nmz v,28,v,152,a,0\nmm v,28,v,156,a,4\ns_lshl_b32 s85, s33, 18\nbuffer_load_dword v165, v161, s[72:75], s85 offen\n"
"mz v,60,v,152,a,8\nrd v,140,197,18432\nrt a,62,201,14848\nmm v,60,v,156,a,12\nmz v,92,v,152,a,16\nmm v,92,v,156,a,20\n"
"mz v,124,v,152,a,24\nmm v,124,v,156,a,28\nwl 6\nmm v,0,v,128,a,32\nwl 4\nmm v,0,v,132,a,36\n"
"rd v,144,196,20480\nmm v,32,v,128,a,40\nmm v,32,v,132,a,44\nmm v,64,v,128,a,48\nrd v,148,197,20480\nmm v,64,v,132,a,52\n"
"mm v,96,v,128,a,56\nwl 2\nmm v,96,v,132,a,60\nmm v,4,v,136,a,32\nmm v,4,v,140,a,36\nsa m0,s94,98304\n"
"sa s85,s84,512\ndl 198,68,85\nrd v,152,196,22528\nmm v,36,v,136,a,40\nmm v,36,v,140,a,44\nmm v,68,v,136,a,48\n"
"rd v,156,197,22528\nmm v,68,v,140,a,52\nsa m0,s94,99328\nsa s85,s84,8704\ndl 199,68,85\nmm v,100,v,136,a,56\n"
"mm v,100,v,140,a,60\nwl 3\nmm v,8,v,144,a,32\nwl 2\nmm v,8,v,148,a,36\nsa m0,s94,100352\n"
"sa s85,s84,16896\ndl 198,68,85\nrd v,128,196,24576\nmm v,40,v,144,a,40\nmm v,40,v,148,a,44\nmm v,72,v,144,a,48\n"
"wv 21\nrd v,132,197,24576\nmm v,72,v,148,a,52\nrt a,64,200,16384\nmm v,104,v,144,a,56\nsa m0,s94,101376\n"
"sa s85,s84,25088\ndl 199,68,85\nmm v,104,v,148,a,60\nrt a,66,200,18432\nwl 5\nmm v,12,v,152,a,32\n"
"wl 4\nmm v,12,v,156,a,36\nrt a,68,200,20480\nrd v,136,196,26624\nmm v,44,v,152,a,40\nsa m0,s65,8192\n"
"s_mov_b32 s85, 2097152\ndl 202,72,85\nmm v,44,v,156,a,44\nmm v,76,v,152,a,48\nrt a,70,200,22528\nrd v,140,197,26624\n"
"mm v,76,v,156,a,52\nmm v,108,v,152,a,56\nrt a,72,201,16384\nmm v,108,v,156,a,60\nsa m0,s65,9216\ns_mov_b32 s85, 2228224\n"
"dl 202,72,85\nwl 8\nmm v,16,v,128,a,32\nwl 7\nmm v,16,v,132,a,36\nrt a,74,201,18432\n"
"rd v,144,196,28672\nmm v,48,v,128,a,40\nmm v,48,v,132,a,44\nsa m0,s65,10240\ns_mov_b32 s85, 2129920\ndl 202,72,85\n"
"rt a,76,201,20480\nmm v,80,v,128,a,48\nrd v,148,197,28672\nmm v,80,v,132,a,52\nmm v,112,v,128,a,56\nrt a,78,201,22528\n"
"mm v,112,v,132,a,60\nsa m0,s65,11264\ns_mov_b32 s85, 2260992\ndl 202,72,85\nwl 8\nmm v,20,v,136,a,32\n"
"rt a,80,200,16896\nwl 7\nmm v,20,v,140,a,36\nrd v,152,196,30720\nmm v,52,v,136,a,40\nrt a,82,200,18944\n"
"mm v,52,v,140,a,44\nmm v,84,v,136,a,48\nsa m0,s65,12288\ns_mov_b32 s85, 2359296\ndl 202,72,85\nmm v,84,v,140,a,52\n"
"rd v,156,197,30720\nrt a,84,200,20992\nmm v,116,v,136,a,56\nmm v,116,v,140,a,60\nrt a,86,200,23040\nwl 9\n"
"mm v,24,v,144,a,32\nsa m0,s65,13312\ns_mov_b32 s85, 2490368\ndl 202,72,85\nwl 7\nmm v,24,v,148,a,36\n"
"mm v,56,v,144,a,40\ns_waitcnt vmcnt(24) lgkmcnt(2)\ns_barrier\nrt a,88,201,16896\nmm v,56,v,148,a,44\nrd v,128,196,32768\n"
"mm v,88,v,144,a,48\nrt a,90,201,18944\nmm v,88,v,148,a,52\nsa m0,s65,14336\ns_mov_b32 s85, 2392064\ndl 202,72,85\n"
"mm v,120,v,144,a,56\nrd v,132,197,32768\nmm v,120,v,148,a,60\nrd v,136,196,34816\nrt a,92,201,20992\nmm v,28,v,152,a,32\n"
"mm v,28,v,156,a,36\nsa m0,s65,15360\ns_mov_b32 s85, 2523136\ndl 202,72,85\nmm v,60,v,152,a,40\nrd v,140,197,34816\n"
"rt a,94,201,23040\nmm v,60,v,156,a,44\nmm v,92,v,152,a,48\nmm v,92,v,156,a,52\nmm v,124,v,152,a,56\nmm v,124,v,156,a,60\n"
"wl 6\nmm v,0,v,128,a,64\nwl 4\nmm v,0,v,132,a,68\nrd v,144,196,36864\nmm v,32,v,128,a,72\n"
"mm v,32,v,132,a,76\nmm v,64,v,128,a,80\nrd v,148,197,36864\nmm v,64,v,132,a,84\nmm v,96,v,128,a,88\nwl 2\n"
"mm v,96,v,132,a,92\nmm v,4,v,136,a,64\nmm v,4,v,140,a,68\nsa m0,s94,114688\nsa s85,s84,640\ndl 198,68,85\n"
"rd v,152,196,38912\nmm v,36,v,136,a,72\nmm v,36,v,140,a,76\nmm v,68,v,136,a,80\nrd v,156,197,38912\nmm v,68,v,140,a,84\n"
"sa m0,s94,115712\nsa s85,s84,8832\ndl 199,68,85\nmm v,100,v,136,a,88\nmm v,100,v,140,a,92\nwl 3\n"
"mm v,8,v,144,a,64\nwl 2\nmm v,8,v,148,a,68\nsa m0,s94,116736\nsa s85,s84,17024\ndl 198,68,85\n"
"rd v,128,196,40960\nmm v,40,v,144,a,72\nmm v,40,v,148,a,76\nmm v,72,v,144,a,80\nwv 16\nrd v,132,197,40960\n"
"mm v,72,v,148,a,84\nsa m0,s94,117760\nsa s85,s84,25216\ndl 199,68,85\nrt a,96,200,0\nmm v,104,v,144,a,88\n"
"mm v,104,v,148,a,92\nrt a,98,200,2048\nwl 5\nmm v,12,v,152,a,64\nwl 4\nmm v,12,v,156,a,68\n"
"sa m0,s65,16384\ns_mov_b32 s85, 2621440\ndl 202,72,85\nrt a,100,200,4096\nrd v,136,196,43008\nmm v,44,v,152,a,72\n"
"mm v,44,v,156,a,76\nmm v,76,v,152,a,80\nrt a,102,200,6144\nrd v,140,197,43008\nmm v,76,v,156,a,84\nsa m0,s65,17408\n"
"s_mov_b32 s85, 2752512\ndl 202,72,85\nmm v,108,v,152,a,88\nrt a,104,201,0\nmm v,108,v,156,a,92\nwl 8\n"
"mm v,16,v,128,a,64\nwl 7\nmm v,16,v,132,a,68\nsa m0,s65,18432\ns_mov_b32 s85, 2654208\ndl 202,72,85\n"
"rt a,106,201,2048\nrd v,144,196,45056\nmm v,48,v,128,a,72\nmm v,48,v,132,a,76\nrt a,108,201,4096\nmm v,80,v,128,a,80\n"
"rd v,148,197,45056\nmm v,80,v,132,a,84\nsa m0,s65,19456\ns_mov_b32 s85, 2785280\ndl 202,72,85\nmm v,112,v,128,a,88\n"
"rt a,110,201,6144\nmm v,112,v,132,a,92\nwl 8\nmm v,20,v,136,a,64\nrt a,112,200,512\nwl 7\n"
"mm v,20,v,140,a,68\nsa m0,s65,20480\ns_mov_b32 s85, 2883584\ndl 202,72,85\nmm v,52,v,136,a,72\nrd v,152,196,47104\n"
"rt a,114,200,2560\nmm v,52,v,140,a,76\nmm v,84,v,136,a,80\nrd v,156,197,47104\nmm v,84,v,140,a,84\nsa m0,s65,21504\n"
"s_mov_b32 s85, 3014656\ndl 202,72,85\nmm v,116,v,136,a,88\nrt a,116,200,4608\nmm v,116,v,140,a,92\nrt a,118,200,6656\n"
"wl 9\nmm v,24,v,144,a,64\nwl 7\nmm v,24,v,148,a,68\nsa m0,s65,22528\ns_mov_b32 s85, 2916352\n"
"dl 202,72,85\nmm v,56,v,144,a,72\nwl 2\ns_barrier\nrt a,120,201,512\nmm v,56,v,148,a,76\n"
"rd v,128,196,49152\nmm v,88,v,144,a,80\nrt a,122,201,2560\nmm v,88,v,148,a,84\nsa m0,s65,23552\ns_mov_b32 s85, 3047424\n"
"dl 202,72,85\nmm v,120,v,144,a,88\nrd v,132,197,49152\nmm v,120,v,148,a,92\nrd v,136,196,51200\nrt a,124,201,4608\n"
"mm v,28,v,152,a,64\nmm v,28,v,156,a,68\nrd v,140,197,51200\nrt a,126,201,6656\nmm v,60,v,152,a,72\nbuffer_load_dwordx2 v[242:243], v180, s[96:99], 0 offen offset:0 sc1\n"
"mm v,60,v,156,a,76\nmm v,92,v,152,a,80\nmm v,92,v,156,a,84\nmm v,124,v,152,a,88\nmm v,124,v,156,a,92\nwl 6\n"
"mm v,0,v,128,a,96\nwl 4\nmm v,0,v,132,a,100\nrd v,144,196,53248\nmm v,32,v,128,a,104\nmm v,32,v,132,a,108\n"
"mm v,64,v,128,a,112\nrd v,148,197,53248\nmm v,64,v,132,a,116\nmm v,96,v,128,a,120\nwl 2\nmm v,96,v,132,a,124\n"
"mm v,4,v,136,a,96\nmm v,4,v,140,a,100\nwv 0\nv_cmp_ne_u64 vcc, s[46:47], v[242:243]\ns_nop 0\ns_and_b64 vcc, vcc, s[38:39]\n"
"s_cbranch_scc1 SPIN_MV\nSPUN_MV:\nrd v,152,196,55296\nmm v,36,v,136,a,104\nmm v,36,v,140,a,108\nmm v,68,v,136,a,112\n"
"sa m0,s94,131072\nsa s85,s84,768\ndl 198,68,85\nrd v,156,197,55296\nmm v,68,v,140,a,116\nmm v,100,v,136,a,120\n"
"mm v,100,v,140,a,124\nwl 3\nmm v,8,v,144,a,96\nsa m0,s94,132096\nsa s85,s84,8960\ndl 199,68,85\n"
"wl 2\nmm v,8,v,148,a,100\nrd v,128,196,57344\nmm v,40,v,144,a,104\nmm v,40,v,148,a,108\nmm v,72,v,144,a,112\n"
"sa m0,s94,133120\nsa s85,s84,17152\ndl 198,68,85\nrd v,132,197,57344\nmm v,72,v,148,a,116\nrt a,128,200,8192\n"
"mm v,104,v,144,a,120\nmm v,104,v,148,a,124\nrt a,130,200,10240\nwl 5\nmm v,12,v,152,a,96\nsa m0,s94,134144\n"
"sa s85,s84,25344\ndl 199,68,85\nwl 4\nmm v,12,v,156,a,100\nrt a,132,200,12288\nrd v,136,196,59392\n"
"mm v,44,v,152,a,104\nmm v,44,v,156,a,108\nsa m0,s65,0\ns_mov_b32 s85, 3145728\ndl 202,72,85\nmm v,76,v,152,a,112\n"
"rt a,134,200,14336\nrd v,140,197,59392\nmm v,76,v,156,a,116\nmm v,108,v,152,a,120\nrt a,136,201,8192\nmm v,108,v,156,a,124\n"
"sa m0,s65,1024\ns_mov_b32 s85, 3276800\ndl 202,72,85\nwl 8\nmm v,16,v,128,a,96\nwl 7\n"
"mm v,16,v,132,a,100\nrt a,138,201,10240\nrd v,144,196,61440\nmm v,48,v,128,a,104\nmm v,48,v,132,a,108\nsa m0,s65,2048\n"
"s_mov_b32 s85, 3178496\ndl 202,72,85\nrt a,140,201,12288\nmm v,80,v,128,a,112\nrd v,148,197,61440\nmm v,80,v,132,a,116\n"
"mm v,112,v,128,a,120\nrt a,142,201,14336\nmm v,112,v,132,a,124\nsa m0,s65,3072\ns_mov_b32 s85, 3309568\ndl 202,72,85\n"
"wl 8\nmm v,20,v,136,a,96\nrt a,144,200,8704\nwl 7\nmm v,20,v,140,a,100\nrd v,152,196,63488\n"
"mm v,52,v,136,a,104\nrt a,146,200,10752\nmm v,52,v,140,a,108\nsa m0,s65,4096\ns_mov_b32 s85, 3407872\ndl 202,72,85\n"
"mm v,84,v,136,a,112\nrd v,156,197,63488\nmm v,84,v,140,a,116\nrt a,148,200,12800\nmm v,116,v,136,a,120\nsa m0,s65,5120\n"
"s_mov_b32 s85, 3538944\ndl 202,72,85\nmm v,116,v,140,a,124\nrt a,150,200,14848\nwl 9\nmm v,24,v,144,a,96\n"
"wl 7\nmm v,24,v,148,a,100\nmm v,56,v,144,a,104\nwl 2\ns_barrier\nsa m0,s65,6144\n"
"s_mov_b32 s85, 3440640\ndl 202,72,85\nmm v,56,v,148,a,108\nrd v,128,196,0\nrt a,152,201,8704\nmm v,88,v,144,a,112\n"
"rt a,154,201,10752\nmm v,88,v,148,a,116\nrd v,132,197,0\nmm v,120,v,144,a,120\nsa m0,s65,7168\ns_mov_b32 s85, 3571712\n"
"dl 202,72,85\nmm v,120,v,148,a,124\nrd v,136,196,2048\nrt a,156,201,12800\nmm v,28,v,152,a,96\nmm v,28,v,156,a,100\n"
"rd v,140,197,2048\nrt a,158,201,14848\nmm v,60,v,152,a,104\ns_lshl_b32 s85, s34, 13\nsa s85,s85,0x100000\nbuffer_load_dword v164, v160, s[68:71], s85 offen\n"
"mm v,60,v,156,a,108\nmm v,92,v,152,a,112\nmm v,92,v,156,a,116\nmm v,124,v,152,a,120\nmm v,124,v,156,a,124\nwl 7\n"
"mm v,0,v,128,a,128\nwl 4\nmm v,0,v,132,a,132\nrd v,144,196,4096\nmm v,32,v,128,a,136\nmm v,32,v,132,a,140\n"
"mm v,64,v,128,a,144\nrd v,148,197,4096\nmm v,64,v,132,a,148\nmm v,96,v,128,a,152\nwl 2\nmm v,96,v,132,a,156\n"
"mm v,4,v,136,a,128\nmm v,4,v,140,a,132\ns_mov_b32 s85, 24576\nbuffer_load_dwordx4 v[180:183], v179, s[96:99], s85 offen offset:0 sc1\nrd v,152,196,6144\nmm v,36,v,136,a,136\n"
"mm v,36,v,140,a,140\ns_mov_b32 s85, 27136\nbuffer_load_dwordx4 v[184:187], v179, s[96:99], s85 offen offset:0 sc1\nmm v,68,v,136,a,144\nrd v,156,197,6144\nmm v,68,v,140,a,148\n"
"s_mov_b32 s85, 24576\nbuffer_load_dwordx4 v[188:191], v179, s[96:99], s85 offen offset:512 sc1\nmm v,100,v,136,a,152\nmm v,100,v,140,a,156\nwl 3\nmm v,8,v,144,a,128\n"
"s_mov_b32 s85, 27136\nbuffer_load_dwordx4 v[192:195], v179, s[96:99], s85 offen offset:512 sc1\nwl 2\nmm v,8,v,148,a,132\nrd v,128,196,8192\nmm v,40,v,144,a,136\n"
"s_mov_b32 s85, 24576\nbuffer_load_dwordx4 v[206:209], v179, s[96:99], s85 offen offset:1024 sc1\nmm v,40,v,148,a,140\nmm v,72,v,144,a,144\nrd v,132,197,8192\nmm v,72,v,148,a,148\n"
"s_mov_b32 s85, 27136\nbuffer_load_dwordx4 v[210:213], v179, s[96:99], s85 offen offset:1024 sc1\nrt a,160,200,16384\nmm v,104,v,144,a,152\nmm v,104,v,148,a,156\ns_mov_b32 s85, 24576\n"
"buffer_load_dwordx4 v[214:217], v179, s[96:99], s85 offen offset:1536 sc1\nrt a,162,200,18432\nwl 5\nmm v,12,v,152,a,128\nwl 4\nmm v,12,v,156,a,132\n"
"s_mov_b32 s85, 27136\nbuffer_load_dwordx4 v[218:221], v179, s[96:99], s85 offen offset:1536 sc1\nrt a,164,200,20480\nrd v,136,196,10240\nmm v,44,v,152,a,136\nmm v,44,v,156,a,140\n"
"mm v,76,v,152,a,144\ns_mov_b32 s85, 24576\nbuffer_load_dwordx4 v[222:225], v179, s[96:99], s85 offen offset:2048 sc1\nrt a,166,200,22528\nrd v,140,197,10240\nmm v,76,v,156,a,148\n"
"mm v,108,v,152,a,152\ns_mov_b32 s85, 27136\nbuffer_load_dwordx4 v[226:229], v179, s[96:99], s85 offen offset:2048 sc1\nrt a,168,201,16384\nmm v,108,v,156,a,156\nwl 8\n"
"mm v,16,v,128,a,128\nwl 7\nmm v,16,v,132,a,132\nsa m0,s94,147456\nsa s85,s84,896\ndl 198,68,85\n"
"rt a,170,201,18432\nrd v,144,196,12288\nmm v,48,v,128,a,136\nmm v,48,v,132,a,140\nsa m0,s94,148480\nsa s85,s84,9088\n"
"dl 199,68,85\nrt a,172,201,20480\nmm v,80,v,128,a,144\nrd v,148,197,12288\nmm v,80,v,132,a,148\nsa m0,s94,149504\n"
"sa s85,s84,17280\ndl 198,68,85\nmm v,112,v,128,a,152\nrt a,174,201,22528\nmm v,112,v,132,a,156\nwl 8\n"
"mm v,20,v,136,a,128\nsa m0,s94,150528\nsa s85,s84,25472\ndl 199,68,85\nrt a,176,200,16896\nwl 7\n"
"mm v,20,v,140,a,132\nrd v,152,196,14336\nmm v,52,v,136,a,136\nsa m0,s65,8192\ns_mov_b32 s85, 3670016\ndl 202,72,85\n"
"rt a,178,200,18944\nmm v,52,v,140,a,140\nmm v,84,v,136,a,144\nrd v,156,197,14336\nmm v,84,v,140,a,148\nsa m0,s65,9216\n"
"s_mov_b32 s85, 3801088\ndl 202,72,85\nrt a,180,200,20992\nmm v,116,v,136,a,152\nmm v,116,v,140,a,156\nsa m0,s65,10240\n"
"s_mov_b32 s85, 3702784\ndl 202,72,85\nrt a,182,200,23040\nwl 9\nmm v,24,v,144,a,128\nwl 7\n"
"mm v,24,v,148,a,132\nsa m0,s65,11264\ns_mov_b32 s85, 3833856\ndl 202,72,85\nmm v,56,v,144,a,136\nwl 2\n"
"s_barrier\nrt a,184,201,16896\nmm v,56,v,148,a,140\nrd v,128,196,16384\nmm v,88,v,144,a,144\nsa m0,s65,12288\n"
"s_mov_b32 s85, 3932160\ndl 202,72,85\nmm v,88,v,148,a,148\nrd v,132,197,16384\nrt a,186,201,18944\nmm v,120,v,144,a,152\n"
"sa m0,s65,13312\ns_mov_b32 s85, 4063232\ndl 202,72,85\nmm v,120,v,148,a,156\nrd v,136,196,18432\nrt a,188,201,20992\n"
"mm v,28,v,152,a,128\nmm v,28,v,156,a,132\nsa m0,s65,14336\ns_mov_b32 s85, 3964928\ndl 202,72,85\nmm v,60,v,152,a,136\n"
"rd v,140,197,18432\nrt a,190,201,23040\nmm v,60,v,156,a,140\nsa m0,s65,15360\ns_mov_b32 s85, 4096000\ndl 202,72,85\n"
"mm v,92,v,152,a,144\nmm v,92,v,156,a,148\nmm v,124,v,152,a,152\nmm v,124,v,156,a,156\nwl 6\nmm v,0,v,128,a,160\n"
"wl 5\nmm v,0,v,132,a,164\nrd v,144,196,20480\nsa m0,s94,98304\nsa s85,s84,1048576\ndl 198,68,85\n"
"mm v,32,v,128,a,168\nmm v,32,v,132,a,172\nmm v,64,v,128,a,176\nrd v,148,197,20480\nmm v,64,v,132,a,180\nwl 6\n"
"mm v,96,v,128,a,184\nwl 2\nmm v,96,v,132,a,188\nmm v,4,v,136,a,160\nv_lshlrev_b32 v234, 16, v168\nv_and_b32 v235, 0xffff0000, v168\n"
"v_lshlrev_b32 v236, 16, v169\nv_and_b32 v237, 0xffff0000, v169\nwv 21\nv_fma_f32 v180, v234, v180, v184\nv_fma_f32 v181, v235, v181, v185\nv_fma_f32 v182, v236, v182, v186\n"
"v_fma_f32 v183, v237, v183, v187\ncv 180,180,181\ncv 181,182,183\nmm v,4,v,140,a,164\ns_mov_b32 s85, 0\nbuffer_store_dwordx2 v[180:181], v178, s[80:83], s85 offen sc1\n"
"mm v,36,v,136,a,168\nrd v,152,196,22528\nv_lshlrev_b32 v234, 16, v170\nmm v,36,v,140,a,172\nv_and_b32 v235, 0xffff0000, v170\nv_lshlrev_b32 v236, 16, v171\n"
"mm v,68,v,136,a,176\nv_and_b32 v237, 0xffff0000, v171\nwv 20\nv_fma_f32 v188, v234, v188, v192\nmm v,68,v,140,a,180\nrd v,156,197,22528\n"
"v_fma_f32 v189, v235, v189, v193\nmm v,100,v,136,a,184\nv_fma_f32 v190, v236, v190, v194\nv_fma_f32 v191, v237, v191, v195\nmm v,100,v,140,a,188\ncv 188,188,189\n"
"cv 189,190,191\nwl 3\nmm v,8,v,144,a,160\ns_mov_b32 s85, 4096\nbuffer_store_dwordx2 v[188:189], v178, s[80:83], s85 offen sc1\nwl 2\n"
"mm v,8,v,148,a,164\nv_lshlrev_b32 v234, 16, v172\nv_and_b32 v235, 0xffff0000, v172\nmm v,40,v,144,a,168\nrd v,128,196,24576\nsa m0,s94,99328\n"
"sa s85,s84,1056768\ndl 199,68,85\nmm v,40,v,148,a,172\nv_lshlrev_b32 v236, 16, v173\nv_and_b32 v237, 0xffff0000, v173\nmm v,72,v,144,a,176\n"
"wv 20\nv_fma_f32 v206, v234, v206, v210\nmm v,72,v,148,a,180\nrd v,132,197,24576\nv_fma_f32 v207, v235, v207, v211\nmm v,104,v,144,a,184\n"
"v_fma_f32 v208, v236, v208, v212\nv_fma_f32 v209, v237, v209, v213\nmm v,104,v,148,a,188\nrt a,192,200,0\ncv 206,206,207\nwl 4\n"
"mm v,12,v,152,a,160\ncv 207,208,209\ns_mov_b32 s85, 8192\nbuffer_store_dwordx2 v[206:207], v178, s[80:83], s85 offen sc1\nwl 3\nmm v,12,v,156,a,164\n"
"rt a,194,200,2048\nv_lshlrev_b32 v234, 16, v174\nmm v,44,v,152,a,168\nrd v,136,196,26624\nv_and_b32 v235, 0xffff0000, v174\nmm v,44,v,156,a,172\n"
"rt a,196,200,4096\nv_lshlrev_b32 v236, 16, v175\nmm v,76,v,152,a,176\nv_and_b32 v237, 0xffff0000, v175\nwv 19\nv_fma_f32 v214, v234, v214, v218\n"
"mm v,76,v,156,a,180\nrd v,140,197,26624\nv_fma_f32 v215, v235, v215, v219\nmm v,108,v,152,a,184\nrt a,198,200,6144\nv_fma_f32 v216, v236, v216, v220\n"
"mm v,108,v,156,a,188\nv_fma_f32 v217, v237, v217, v221\ncv 214,214,215\nwl 7\nmm v,16,v,128,a,160\nrt a,200,201,0\n"
"cv 215,216,217\nwl 7\nmm v,16,v,132,a,164\nsa m0,s94,100352\nsa s85,s84,1064960\ndl 198,68,85\n"
"mm v,48,v,128,a,168\nrd v,144,196,28672\ns_mov_b32 s85, 12288\nbuffer_store_dwordx2 v[214:215], v178, s[80:83], s85 offen sc1\nmm v,48,v,132,a,172\nrt a,202,201,2048\n"
"v_lshlrev_b32 v234, 16, v176\nmm v,80,v,128,a,176\nrt a,204,201,4096\nv_and_b32 v235, 0xffff0000, v176\nmm v,80,v,132,a,180\nrd v,148,197,28672\n"
"v_lshlrev_b32 v236, 16, v177\nmm v,112,v,128,a,184\nrt a,206,201,6144\nv_and_b32 v237, 0xffff0000, v177\nmm v,112,v,132,a,188\nwv 19\n"
"v_fma_f32 v222, v234, v222, v226\nv_fma_f32 v223, v235, v223, v227\nwl 9\nmm v,20,v,136,a,160\nrt a,208,200,512\nv_fma_f32 v224, v236, v224, v228\n"
"wl 8\nmm v,20,v,140,a,164\nrd v,152,196,30720\nv_fma_f32 v225, v237, v225, v229\nmm v,52,v,136,a,168\nrt a,210,200,2560\n"
"cv 222,222,223\nmm v,52,v,140,a,172\ncv 223,224,225\ns_mov_b32 s85, 16384\nbuffer_store_dwordx2 v[222:223], v178, s[80:83], s85 offen sc1\nmm v,84,v,136,a,176\n"
"rd v,156,197,30720\nmm v,84,v,140,a,180\nrt a,212,200,4608\nmm v,116,v,136,a,184\nsa m0,s94,101376\nsa s85,s84,1073152\n"
"dl 199,68,85\nmm v,116,v,140,a,188\nrt a,214,200,6656\nwl 10\nmm v,24,v,144,a,160\nwl 7\n"
"mm v,24,v,148,a,164\nmm v,56,v,144,a,168\nwl 2\ns_barrier\nrt a,216,201,512\nmm v,56,v,148,a,172\n"
"rd v,128,196,32768\nmm v,88,v,144,a,176\nrt a,218,201,2560\nmm v,88,v,148,a,180\nrd v,132,197,32768\nmm v,120,v,144,a,184\n"
"mm v,120,v,148,a,188\nrd v,136,196,34816\nrt a,220,201,4608\nmm v,28,v,152,a,160\nmm v,28,v,156,a,164\nrd v,140,197,34816\n"
"rt a,222,201,6656\nmm v,60,v,152,a,168\nmm v,60,v,156,a,172\nmm v,92,v,152,a,176\nmm v,92,v,156,a,180\nmm v,124,v,152,a,184\n"
"mm v,124,v,156,a,188\nwl 6\nmm v,0,v,128,a,192\nwl 4\nmm v,0,v,132,a,196\nrd v,144,196,36864\n"
"sa m0,s94,114688\nsa s85,s84,1048704\ndl 198,68,85\nmm v,32,v,128,a,200\nmm v,32,v,132,a,204\nmm v,64,v,128,a,208\n"
"rd v,148,197,36864\nmm v,64,v,132,a,212\nmm v,96,v,128,a,216\nwl 2\nmm v,96,v,132,a,220\nmm v,4,v,136,a,192\n"
"mm v,4,v,140,a,196\nrd v,152,196,38912\nmm v,36,v,136,a,200\nmm v,36,v,140,a,204\nmm v,68,v,136,a,208\nrd v,156,197,38912\n"
"mm v,68,v,140,a,212\nsa m0,s94,115712\nsa s85,s84,1056896\ndl 199,68,85\nmm v,100,v,136,a,216\nmm v,100,v,140,a,220\n"
"wl 3\nmm v,8,v,144,a,192\nwl 2\nmm v,8,v,148,a,196\nrd v,128,196,40960\nmm v,40,v,144,a,200\n"
"mm v,40,v,148,a,204\nmm v,72,v,144,a,208\nwv 11\nrd v,132,197,40960\nmm v,72,v,148,a,212\nrt a,224,200,8192\n"
"mm v,104,v,144,a,216\nmm v,104,v,148,a,220\nrt a,226,200,10240\nwl 5\nmm v,12,v,152,a,192\nwl 4\n"
"mm v,12,v,156,a,196\nrt a,228,200,12288\nrd v,136,196,43008\nsa m0,s94,116736\nsa s85,s84,1065088\ndl 198,68,85\n"
"mm v,44,v,152,a,200\nmm v,44,v,156,a,204\nmm v,76,v,152,a,208\nrt a,230,200,14336\nrd v,140,197,43008\nmm v,76,v,156,a,212\n"
"mm v,108,v,152,a,216\nrt a,232,201,8192\nmm v,108,v,156,a,220\nwl 8\nmm v,16,v,128,a,192\nwl 7\n"
"mm v,16,v,132,a,196\nrt a,234,201,10240\nrd v,144,196,45056\nmm v,48,v,128,a,200\nmm v,48,v,132,a,204\nrt a,236,201,12288\n"
"mm v,80,v,128,a,208\nrd v,148,197,45056\nmm v,80,v,132,a,212\nsa m0,s94,117760\nsa s85,s84,1073280\ndl 199,68,85\n"
"mm v,112,v,128,a,216\nrt a,238,201,14336\nmm v,112,v,132,a,220\nwl 8\nmm v,20,v,136,a,192\nrt a,240,200,8704\n"
"wl 7\nmm v,20,v,140,a,196\nrd v,152,196,47104\nmm v,52,v,136,a,200\nrt a,242,200,10752\nmm v,52,v,140,a,204\n"
"mm v,84,v,136,a,208\nrd v,156,197,47104\nmm v,84,v,140,a,212\nrt a,244,200,12800\nmm v,116,v,136,a,216\nmm v,116,v,140,a,220\n"
"rt a,246,200,14848\nwl 9\nmm v,24,v,144,a,192\nwl 7\nmm v,24,v,148,a,196\ns_mul_i32 s85, s36, 163840\n"
"buffer_load_dword v166, v161, s[76:79], s85 offen\nmm v,56,v,144,a,200\ns_waitcnt vmcnt(6) lgkmcnt(2)\ns_barrier\nrt a,248,201,8704\nmm v,56,v,148,a,204\n"
"rd v,128,196,49152\nmm v,88,v,144,a,208\nrt a,250,201,10752\nmm v,88,v,148,a,212\nrd v,132,197,49152\nmm v,120,v,144,a,216\n"
"mm v,120,v,148,a,220\nrd v,136,196,51200\nrt a,252,201,12800\nmm v,28,v,152,a,192\nmm v,28,v,156,a,196\nrd v,140,197,51200\n"
"rt a,254,201,14848\nmm v,60,v,152,a,200\nmm v,60,v,156,a,204\nmm v,92,v,152,a,208\nmm v,92,v,156,a,212\nmm v,124,v,152,a,216\n"
"mm v,124,v,156,a,220\nwl 6\nmm v,0,v,128,a,224\nwl 4\nmm v,0,v,132,a,228\ns_cmp_eq_u32 s52, 0\n"
"s_cbranch_scc0 NOLN_V\nv_mov_b32 v240, s46\nv_mov_b32 v241, s47\ns_mov_b64 exec, 1\nbuffer_store_dwordx2 v[240:241], v245, s[96:99], s35 offen sc1\ns_mov_b64 exec, -1\n"
"NOLN_V:\nmm v,32,v,128,a,232\nrd v,144,196,53248\nmm v,32,v,132,a,236\nmm v,64,v,128,a,240\nrd v,148,197,53248\n"
"mm v,64,v,132,a,244\nmm v,96,v,128,a,248\nwl 2\nmm v,96,v,132,a,252\nmm v,4,v,136,a,224\nmm v,4,v,140,a,228\n"
"rd v,152,196,55296\nmm v,36,v,136,a,232\nmm v,36,v,140,a,236\nsa m0,s94,131072\nsa s85,s84,1048832\ndl 198,68,85\n"
"mm v,68,v,136,a,240\nrd v,156,197,55296\nmm v,68,v,140,a,244\nmm v,100,v,136,a,248\nmm v,100,v,140,a,252\nwl 3\n"
"mm v,8,v,144,a,224\nwl 2\nmm v,8,v,148,a,228\nrd v,128,196,57344\nmm v,40,v,144,a,232\nmm v,40,v,148,a,236\n"
"mm v,72,v,144,a,240\nrd v,132,197,57344\nmm v,72,v,148,a,244\nsa m0,s94,132096\nsa s85,s84,1057024\ndl 199,68,85\n"
"mm v,104,v,144,a,248\nmm v,104,v,148,a,252\nwl 3\nmm v,12,v,152,a,224\nwl 2\nmm v,12,v,156,a,228\n"
"rd v,136,196,59392\nmm v,44,v,152,a,232\nmm v,44,v,156,a,236\nmm v,76,v,152,a,240\nrd v,140,197,59392\nmm v,76,v,156,a,244\n"
"mm v,108,v,152,a,248\nmm v,108,v,156,a,252\nsa m0,s94,133120\nsa s85,s84,1065216\ndl 198,68,85\nwl 3\n"
"mm v,16,v,128,a,224\nwl 2\nmm v,16,v,132,a,228\nrd v,144,196,61440\nmm v,48,v,128,a,232\nmm v,48,v,132,a,236\n"
"mm v,80,v,128,a,240\nrd v,148,197,61440\nmm v,80,v,132,a,244\nmm v,112,v,128,a,248\nmm v,112,v,132,a,252\nwl 3\n"
"mm v,20,v,136,a,224\nwl 2\nmm v,20,v,140,a,228\nsa m0,s94,134144\nsa s85,s84,1073408\ndl 199,68,85\n"
"mm v,52,v,136,a,232\nrd v,152,196,63488\nmm v,52,v,140,a,236\nmm v,84,v,136,a,240\nrd v,156,197,63488\nmm v,84,v,140,a,244\n"
"mm v,116,v,136,a,248\nmm v,116,v,140,a,252\nwl 3\nmm v,24,v,144,a,224\nwl 2\nmm v,24,v,148,a,228\n"
"mm v,56,v,144,a,232\ns_waitcnt vmcnt(9) lgkmcnt(0)\ns_barrier\nmm v,56,v,148,a,236\ns_lshl_b32 s34, s36, 3\nsa s34,s34,s91\n"
"sa s33,s34,131072\nmm v,88,v,144,a,240\nrd v,128,196,0\nmm v,88,v,148,a,244\nrd v,132,197,0\nmm v,120,v,144,a,248\n"
"mm v,120,v,148,a,252\nrd v,136,196,2048\nmm v,28,v,152,a,224\nmm v,28,v,156,a,228\nrd v,140,197,2048\nmm v,60,v,152,a,232\n"
"mm v,60,v,156,a,236\nmm v,92,v,152,a,240\nmm v,92,v,156,a,244\nmm v,124,v,152,a,248\nmm v,124,v,156,a,252\ns_nop 2\n"
"cv 160,0,1\ncv 161,2,3\ncv 162,4,5\ncv 163,6,7\ncv 176,32,33\ncv 177,34,35\n"
"cv 178,36,37\ncv 179,38,39\nwl 3\nmz v,0,v,128,a,0\ncv 192,64,65\ncv 193,66,67\n"
"wl 2\nmm v,0,v,132,a,4\ncv 194,68,69\ncv 195,70,71\nmz v,32,v,128,a,8\nrd v,144,196,4096\n"
"cv 218,96,97\nmm v,32,v,132,a,12\nsa m0,s94,147456\nsa s85,s84,1048960\ndl 198,68,85\nmz v,64,v,128,a,16\n"
"cv 219,98,99\ncv 220,100,101\ncv 221,102,103\nmm v,64,v,132,a,20\nrd v,148,197,4096\ncv 164,8,9\n"
"mz v,96,v,128,a,24\ncv 165,10,11\nwv 5\ncv 166,12,13\nmm v,96,v,132,a,28\ncv 167,14,15\n"
"cv 180,40,41\nwl 3\nmz v,4,v,136,a,0\ncv 181,42,43\ncv 182,44,45\nwl 2\n"
"mm v,4,v,140,a,4\ncv 183,46,47\ncv 206,72,73\nmz v,36,v,136,a,8\nrd v,152,196,6144\ncv 207,74,75\n"
"mm v,36,v,140,a,12\ncv 208,76,77\ncv 209,78,79\nmz v,68,v,136,a,16\ncv 222,104,105\ncv 223,106,107\n"
"mm v,68,v,140,a,20\nrd v,156,197,6144\ncv 224,108,109\nmz v,100,v,136,a,24\nsa m0,s94,148480\nsa s85,s84,1057152\n"
"dl 199,68,85\nmm v,100,v,140,a,28\ncv 225,110,111\ncv 168,16,17\nwl 3\nmz v,8,v,144,a,0\n"
"cv 169,18,19\ncv 170,20,21\nwl 2\nmm v,8,v,148,a,4\ncv 171,22,23\ncv 184,48,49\n"
"mz v,40,v,144,a,8\nrd v,128,196,8192\ncv 185,50,51\nmm v,40,v,148,a,12\ncv 186,52,53\ncv 187,54,55\n"
"mz v,72,v,144,a,16\ncv 210,80,81\ncv 211,82,83\nmm v,72,v,148,a,20\nrd v,132,197,8192\ncv 212,84,85\n"
"mz v,104,v,144,a,24\ncv 213,86,87\ncv 226,112,113\nmm v,104,v,148,a,28\nsa m0,s94,149504\nsa s85,s84,1065344\n"
"dl 198,68,85\nwl 3\nmz v,12,v,152,a,0\ncv 227,114,115\ncv 228,116,117\nwl 2\n"
"mm v,12,v,156,a,4\ncv 229,118,119\ncv 172,24,25\nmz v,44,v,152,a,8\nrd v,136,196,10240\ncv 173,26,27\n"
"mm v,44,v,156,a,12\ncv 174,28,29\ncv 175,30,31\nmz v,76,v,152,a,16\nsa m0,s94,150528\nsa s85,s84,1073536\n"
"dl 199,68,85\nmm v,76,v,156,a,20\nrd v,140,197,10240\ncv 188,56,57\nmz v,108,v,152,a,24\ncv 189,58,59\n"
"cv 190,60,61\nmm v,108,v,156,a,28\ncv 191,62,63\ncv 214,88,89\nwl 3\nmz v,16,v,128,a,0\n"
"cv 215,90,91\ncv 216,92,93\nwl 2\nmm v,16,v,132,a,4\nrd v,144,196,12288\ncv 217,94,95\n"
"mz v,48,v,128,a,8\nsa m0,s89,0\nsa s85,s93,0\ndl 203,76,85\nmm v,48,v,132,a,12\ncv 246,120,121\n"
"cv 247,122,123\nmz v,80,v,128,a,16\nrd v,148,197,12288\ncv 248,124,125\nmm v,80,v,132,a,20\ncv 249,126,127\n"
"mz v,112,v,128,a,24\nmm v,112,v,132,a,28\nsa m0,s89,1024\nsa s85,s93,65536\ndl 203,76,85\nwl 3\n"
"mz v,20,v,136,a,0\nbuffer_store_dwordx4 v[160:163], v230, s[40:43], s86 offen offset:0 sc1\nbuffer_store_dwordx4 v[164:167], v230, s[40:43], s86 offen offset:1024 sc1\nsa s86,s86,s92\nwl 2\nmm v,20,v,140,a,4\n"
"rd v,152,196,14336\nmz v,52,v,136,a,8\nmm v,52,v,140,a,12\nmz v,84,v,136,a,16\nrd v,156,197,14336\nmm v,84,v,140,a,20\n"
"sa m0,s89,2048\nsa s85,s93,131072\ndl 203,76,85\nmz v,116,v,136,a,24\nmm v,116,v,140,a,28\nwl 3\n"
"mz v,24,v,144,a,0\nwl 2\nmm v,24,v,148,a,4\nmz v,56,v,144,a,8\nwl 0\ns_barrier\n"
"sa m0,s89,3072\nsa s85,s93,196608\ndl 203,76,85\nmm v,56,v,148,a,12\nrd v,128,196,16384\nmz v,88,v,144,a,16\n"
"mm v,88,v,148,a,20\nrd v,132,197,16384\nmz v,120,v,144,a,24\nmm v,120,v,148,a,28\nrd v,136,196,18432\nmz v,28,v,152,a,0\n"
"sa m0,s89,16384\nsa s85,s93,524288\ndl 203,76,85\nmm v,28,v,156,a,4\nrd v,140,197,18432\nmz v,60,v,152,a,8\n"
"mm v,60,v,156,a,12\nmz v,92,v,152,a,16\nmm v,92,v,156,a,20\nmz v,124,v,152,a,24\nmm v,124,v,156,a,28\nwl 3\n"
"mm v,0,v,128,a,32\nwl 2\nmm v,0,v,132,a,36\nsa m0,s94,98304\nsa s85,s84,1049088\ndl 198,68,85\n"
"mm v,32,v,128,a,40\nrd v,144,196,20480\nmm v,32,v,132,a,44\nmm v,64,v,128,a,48\nrd v,148,197,20480\nmm v,64,v,132,a,52\n"
"mm v,96,v,128,a,56\nbuffer_store_dwordx4 v[168:171], v230, s[40:43], s86 offen offset:0 sc1\nmm v,96,v,132,a,60\nwl 3\nmm v,4,v,136,a,32\nwl 2\n"
"mm v,4,v,140,a,36\nrd v,152,196,22528\nmm v,36,v,136,a,40\nmm v,36,v,140,a,44\nmm v,68,v,136,a,48\nsa m0,s94,99328\n"
"sa s85,s84,1057280\ndl 199,68,85\nmm v,68,v,140,a,52\nrd v,156,197,22528\nmm v,100,v,136,a,56\nmm v,100,v,140,a,60\n"
"wl 3\nmm v,8,v,144,a,32\nwl 2\nmm v,8,v,148,a,36\nbuffer_store_dwordx4 v[172:175], v230, s[40:43], s86 offen offset:1024 sc1\nsa s86,s86,s92\n"
"mm v,40,v,144,a,40\nrd v,128,196,24576\nmm v,40,v,148,a,44\nmm v,72,v,144,a,48\nrd v,132,197,24576\nmm v,72,v,148,a,52\n"
"mm v,104,v,144,a,56\nmm v,104,v,148,a,60\nsa m0,s94,100352\nsa s85,s84,1065472\ndl 198,68,85\nwl 3\n"
"mm v,12,v,152,a,32\nwl 2\nmm v,12,v,156,a,36\nrd v,136,196,26624\nmm v,44,v,152,a,40\nmm v,44,v,156,a,44\n"
"mm v,76,v,152,a,48\nsa m0,s94,101376\nsa s85,s84,1073664\ndl 199,68,85\nmm v,76,v,156,a,52\nrd v,140,197,26624\n"
"mm v,108,v,152,a,56\nmm v,108,v,156,a,60\nwl 3\nmm v,16,v,128,a,32\nwl 2\nmm v,16,v,132,a,36\n"
"rd v,144,196,28672\nmm v,48,v,128,a,40\nsa m0,s89,17408\nsa s85,s93,589824\ndl 203,76,85\nmm v,48,v,132,a,44\n"
"mm v,80,v,128,a,48\nrd v,148,197,28672\nmm v,80,v,132,a,52\nmm v,112,v,128,a,56\nmm v,112,v,132,a,60\nsa m0,s89,18432\n"
"sa s85,s93,655360\ndl 203,76,85\nwl 3\nmm v,20,v,136,a,32\nwl 2\nmm v,20,v,140,a,36\n"
"rd v,152,196,30720\nmm v,52,v,136,a,40\nmm v,52,v,140,a,44\nmm v,84,v,136,a,48\nrd v,156,197,30720\nmm v,84,v,140,a,52\n"
"sa m0,s89,19456\nsa s85,s93,720896\ndl 203,76,85\nmm v,116,v,136,a,56\nmm v,116,v,140,a,60\nwl 3\n"
"mm v,24,v,144,a,32\nwl 2\nmm v,24,v,148,a,36\nmm v,56,v,144,a,40\ns_waitcnt vmcnt(20) lgkmcnt(0)\ns_barrier\n"
"sa m0,s89,32768\nsa s85,s93,1048576\ndl 203,76,85\nmm v,56,v,148,a,44\nrd v,128,196,32768\nmm v,88,v,144,a,48\n"
"mm v,88,v,148,a,52\nrd v,132,197,32768\nmm v,120,v,144,a,56\nmm v,120,v,148,a,60\nrd v,136,196,34816\nmm v,28,v,152,a,32\n"
"sa m0,s89,33792\nsa s85,s93,1114112\ndl 203,76,85\nmm v,28,v,156,a,36\nrd v,140,197,34816\nmm v,60,v,152,a,40\n"
"mm v,60,v,156,a,44\nmm v,92,v,152,a,48\nmm v,92,v,156,a,52\nmm v,124,v,152,a,56\nmm v,124,v,156,a,60\nwl 3\n"
"mm v,0,v,128,a,64\nwl 2\nmm v,0,v,132,a,68\nsa m0,s94,114688\nsa s85,s84,1049216\ndl 198,68,85\n"
"mm v,32,v,128,a,72\nrd v,144,196,36864\nmm v,32,v,132,a,76\nmm v,64,v,128,a,80\nrd v,148,197,36864\nmm v,64,v,132,a,84\n"
"mm v,96,v,128,a,88\nbuffer_store_dwordx4 v[176:179], v230, s[40:43], s86 offen offset:0 sc1\nmm v,96,v,132,a,92\nwl 3\nmm v,4,v,136,a,64\nwl 2\n"
"mm v,4,v,140,a,68\nrd v,152,196,38912\nmm v,36,v,136,a,72\nmm v,36,v,140,a,76\nmm v,68,v,136,a,80\nsa m0,s94,115712\n"
"sa s85,s84,1057408\ndl 199,68,85\nmm v,68,v,140,a,84\nrd v,156,197,38912\nmm v,100,v,136,a,88\nmm v,100,v,140,a,92\n"
"wl 3\nmm v,8,v,144,a,64\nwl 2\nmm v,8,v,148,a,68\nbuffer_store_dwordx4 v[180:183], v230, s[40:43], s86 offen offset:1024 sc1\nsa s86,s86,s92\n"
"mm v,40,v,144,a,72\nrd v,128,196,40960\nmm v,40,v,148,a,76\nmm v,72,v,144,a,80\nrd v,132,197,40960\nmm v,72,v,148,a,84\n"
"mm v,104,v,144,a,88\nmm v,104,v,148,a,92\nsa m0,s94,116736\nsa s85,s84,1065600\ndl 198,68,85\nwl 3\n"
"mm v,12,v,152,a,64\nwl 2\nmm v,12,v,156,a,68\nrd v,136,196,43008\nmm v,44,v,152,a,72\nmm v,44,v,156,a,76\n"
"mm v,76,v,152,a,80\nsa m0,s94,117760\nsa s85,s84,1073792\ndl 199,68,85\nmm v,76,v,156,a,84\nrd v,140,197,43008\n"
"mm v,108,v,152,a,88\nmm v,108,v,156,a,92\nwl 3\nmm v,16,v,128,a,64\nwl 2\nmm v,16,v,132,a,68\n"
"rd v,144,196,45056\nmm v,48,v,128,a,72\nsa m0,s89,34816\nsa s85,s93,1179648\ndl 203,76,85\nmm v,48,v,132,a,76\n"
"mm v,80,v,128,a,80\nrd v,148,197,45056\nmm v,80,v,132,a,84\nmm v,112,v,128,a,88\nmm v,112,v,132,a,92\nsa m0,s89,35840\n"
"sa s85,s93,1245184\ndl 203,76,85\nwl 3\nmm v,20,v,136,a,64\nwl 2\nmm v,20,v,140,a,68\n"
"rd v,152,196,47104\nmm v,52,v,136,a,72\nmm v,52,v,140,a,76\nmm v,84,v,136,a,80\nrd v,156,197,47104\nmm v,84,v,140,a,84\n"
"sa m0,s89,49152\nsa s85,s93,1572864\ndl 203,76,85\nmm v,116,v,136,a,88\nmm v,116,v,140,a,92\nwl 3\n"
"mm v,24,v,144,a,64\nwl 2\nmm v,24,v,148,a,68\nmm v,56,v,144,a,72\ns_waitcnt vmcnt(27) lgkmcnt(0)\ns_barrier\n"
"sa m0,s89,50176\nsa s85,s93,1638400\ndl 203,76,85\nmm v,56,v,148,a,76\nrd v,128,196,49152\nmm v,88,v,144,a,80\n"
"mm v,88,v,148,a,84\nrd v,132,197,49152\nmm v,120,v,144,a,88\nmm v,120,v,148,a,92\nrd v,136,196,51200\nmm v,28,v,152,a,64\n"
"sa m0,s89,51200\nsa s85,s93,1703936\ndl 203,76,85\nmm v,28,v,156,a,68\nrd v,140,197,51200\nmm v,60,v,152,a,72\n"
"mm v,60,v,156,a,76\nmm v,92,v,152,a,80\nmm v,92,v,156,a,84\nmm v,124,v,152,a,88\nmm v,124,v,156,a,92\nwl 3\n"
"mm v,0,v,128,a,96\nwl 2\nmm v,0,v,132,a,100\nsa m0,s94,131072\nsa s85,s84,1049344\ndl 198,68,85\n"
"mm v,32,v,128,a,104\nrd v,144,196,53248\nmm v,32,v,132,a,108\nmm v,64,v,128,a,112\nrd v,148,197,53248\nmm v,64,v,132,a,116\n"
"mm v,96,v,128,a,120\nbuffer_store_dwordx4 v[184:187], v230, s[40:43], s86 offen offset:0 sc1\nmm v,96,v,132,a,124\nwl 3\nmm v,4,v,136,a,96\nwl 2\n"
"mm v,4,v,140,a,100\nrd v,152,196,55296\nmm v,36,v,136,a,104\nmm v,36,v,140,a,108\nsa m0,s94,132096\nsa s85,s84,1057536\n"
"dl 199,68,85\nmm v,68,v,136,a,112\nrd v,156,197,55296\nmm v,68,v,140,a,116\nmm v,100,v,136,a,120\nmm v,100,v,140,a,124\n"
"wl 3\nmm v,8,v,144,a,96\nbuffer_store_dwordx4 v[188:191], v230, s[40:43], s86 offen offset:1024 sc1\nsa s86,s86,s92\nwl 2\nmm v,8,v,148,a,100\n"
"rd v,128,196,57344\nmm v,40,v,144,a,104\nmm v,40,v,148,a,108\nmm v,72,v,144,a,112\nrd v,132,197,57344\nmm v,72,v,148,a,116\n"
"sa m0,s94,133120\nsa s85,s84,1065728\ndl 198,68,85\nmm v,104,v,144,a,120\nmm v,104,v,148,a,124\nwl 3\n"
"mm v,12,v,152,a,96\nwl 2\nmm v,12,v,156,a,100\nrd v,136,196,59392\nmm v,44,v,152,a,104\nsa m0,s94,134144\n"
"sa s85,s84,1073920\ndl 199,68,85\nmm v,44,v,156,a,108\nmm v,76,v,152,a,112\nrd v,140,197,59392\nmm v,76,v,156,a,116\n"
"mm v,108,v,152,a,120\nmm v,108,v,156,a,124\nsa m0,s89,52224\nsa s85,s93,1769472\ndl 203,76,85\nwl 3\n"
"mm v,16,v,128,a,96\nwl 2\nmm v,16,v,132,a,100\nrd v,144,196,61440\nmm v,48,v,128,a,104\nmm v,48,v,132,a,108\n"
"mm v,80,v,128,a,112\nsa m0,s89,65536\nsa s85,s93,2097152\ndl 203,76,85\nmm v,80,v,132,a,116\nrd v,148,197,61440\n"
"mm v,112,v,128,a,120\nmm v,112,v,132,a,124\nwl 3\nmm v,20,v,136,a,96\nwl 2\nmm v,20,v,140,a,100\n"
"sa m0,s89,66560\nsa s85,s93,2162688\ndl 203,76,85\nmm v,52,v,136,a,104\nrd v,152,196,63488\nmm v,52,v,140,a,108\n"
"mm v,84,v,136,a,112\nrd v,156,197,63488\nmm v,84,v,140,a,116\nmm v,116,v,136,a,120\nsa m0,s89,67584\nsa s85,s93,2228224\n"
"dl 203,76,85\nmm v,116,v,140,a,124\nwl 3\nmm v,24,v,144,a,96\nwl 2\nmm v,24,v,148,a,100\n"
"mm v,56,v,144,a,104\ns_waitcnt vmcnt(26) lgkmcnt(0)\ns_barrier\nmm v,56,v,148,a,108\nrd v,128,196,0\nsa m0,s89,68608\n"
"sa s85,s93,2293760\ndl 203,76,85\nmm v,88,v,144,a,112\nmm v,88,v,148,a,116\nrd v,132,197,0\nmm v,120,v,144,a,120\n"
"mm v,120,v,148,a,124\nrd v,136,196,2048\nmm v,28,v,152,a,96\nbuffer_load_dwordx2 v[242:243], v244, s[96:99], s90 offen sc1\nmm v,28,v,156,a,100\nrd v,140,197,2048\n"
"mm v,60,v,152,a,104\nmm v,60,v,156,a,108\nmm v,92,v,152,a,112\nmm v,92,v,156,a,116\nmm v,124,v,152,a,120\nmm v,124,v,156,a,124\n"
"wl 3\nmm v,0,v,128,a,128\nwl 2\nmm v,0,v,132,a,132\nwv 0\nv_cmp_ne_u64 vcc, s[46:47], v[242:243]\n"
"s_nop 0\ns_and_b64 vcc, vcc, s[38:39]\ns_cbranch_scc1 SPIN_HV\nSPUN_HV:\nmm v,32,v,128,a,136\nrd v,144,196,4096\n"
"mm v,32,v,132,a,140\nmm v,64,v,128,a,144\nrd v,148,197,4096\nmm v,64,v,132,a,148\nmm v,96,v,128,a,152\nbuffer_store_dwordx4 v[192:195], v230, s[40:43], s86 offen offset:0 sc1\n"
"mm v,96,v,132,a,156\nwl 3\nmm v,4,v,136,a,128\nwl 2\nmm v,4,v,140,a,132\nrd v,152,196,6144\n"
"mm v,36,v,136,a,136\nmm v,36,v,140,a,140\nmm v,68,v,136,a,144\nsa m0,s94,147456\nsa s85,s84,1049472\ndl 198,68,85\n"
"mm v,68,v,140,a,148\nrd v,156,197,6144\nmm v,100,v,136,a,152\nmm v,100,v,140,a,156\nwl 3\nmm v,8,v,144,a,128\n"
"wl 2\nmm v,8,v,148,a,132\nbuffer_store_dwordx4 v[206:209], v230, s[40:43], s86 offen offset:1024 sc1\nsa s86,s86,s92\nmm v,40,v,144,a,136\nrd v,128,196,8192\n"
"mm v,40,v,148,a,140\nmm v,72,v,144,a,144\nrd v,132,197,8192\nmm v,72,v,148,a,148\nmm v,104,v,144,a,152\nmm v,104,v,148,a,156\n"
"sa m0,s94,148480\nsa s85,s84,1057664\ndl 199,68,85\nwl 3\nmm v,12,v,152,a,128\nwl 2\n"
"mm v,12,v,156,a,132\nrd v,136,196,10240\nmm v,44,v,152,a,136\nmm v,44,v,156,a,140\nmm v,76,v,152,a,144\nsa m0,s94,149504\n"
"sa s85,s84,1065856\ndl 198,68,85\nmm v,76,v,156,a,148\nrd v,140,197,10240\nmm v,108,v,152,a,152\nmm v,108,v,156,a,156\n"
"wl 3\nmm v,16,v,128,a,128\nwl 2\nmm v,16,v,132,a,132\nrd v,144,196,12288\nmm v,48,v,128,a,136\n"
"sa m0,s94,150528\nsa s85,s84,1074048\ndl 199,68,85\nmm v,48,v,132,a,140\nmm v,80,v,128,a,144\nrd v,148,197,12288\n"
"mm v,80,v,132,a,148\nmm v,112,v,128,a,152\nmm v,112,v,132,a,156\nsa m0,s89,81920\nsa s85,s93,2621440\ndl 203,76,85\n"
"wl 3\nmm v,20,v,136,a,128\nwl 2\nmm v,20,v,140,a,132\nrd v,152,196,14336\nmm v,52,v,136,a,136\n"
"mm v,52,v,140,a,140\nmm v,84,v,136,a,144\nrd v,156,197,14336\nmm v,84,v,140,a,148\nsa m0,s89,82944\nsa s85,s93,2686976\n"
"dl 203,76,85\nmm v,116,v,136,a,152\nmm v,116,v,140,a,156\nwl 3\nmm v,24,v,144,a,128\nwl 2\n"
"mm v,24,v,148,a,132\nmm v,56,v,144,a,136\nwl 0\ns_barrier\nsa m0,s89,83968\nsa s85,s93,2752512\n"
"dl 203,76,85\nmm v,56,v,148,a,140\nrd v,128,196,16384\nmm v,88,v,144,a,144\nmm v,88,v,148,a,148\nrd v,132,197,16384\n"
"mm v,120,v,144,a,152\nmm v,120,v,148,a,156\nrd v,136,196,18432\nmm v,28,v,152,a,128\nsa m0,s89,84992\nsa s85,s93,2818048\n"
"dl 203,76,85\nmm v,28,v,156,a,132\nrd v,140,197,18432\nmm v,60,v,152,a,136\nmm v,60,v,156,a,140\nmm v,92,v,152,a,144\n"
"mm v,92,v,156,a,148\nmm v,124,v,152,a,152\nmm v,124,v,156,a,156\nwl 3\nmm v,0,v,128,a,160\nwl 2\n"
"mm v,0,v,132,a,164\nsa s85,s87,0\nbuffer_load_dwordx4 a[0:3], v230, s[80:83], s85 offen sc1\nmm v,32,v,128,a,168\nrd v,144,196,20480\nmm v,32,v,132,a,172\n"
"mm v,64,v,128,a,176\nrd v,148,197,20480\nmm v,64,v,132,a,180\nmm v,96,v,128,a,184\nbuffer_store_dwordx4 v[210:213], v230, s[40:43], s86 offen offset:0 sc1\nmm v,96,v,132,a,188\n"
"wl 3\nmm v,4,v,136,a,160\nwl 2\nmm v,4,v,140,a,164\nrd v,152,196,22528\nmm v,36,v,136,a,168\n"
"mm v,36,v,140,a,172\nmm v,68,v,136,a,176\nsa s85,s87,1024\nbuffer_load_dwordx4 a[4:7], v230, s[80:83], s85 offen sc1\nmm v,68,v,140,a,180\nrd v,156,197,22528\n"
"mm v,100,v,136,a,184\nmm v,100,v,140,a,188\nwl 3\nmm v,8,v,144,a,160\nwl 2\nmm v,8,v,148,a,164\n"
"buffer_store_dwordx4 v[214:217], v230, s[40:43], s86 offen offset:1024 sc1\nsa s86,s86,s92\nmm v,40,v,144,a,168\nrd v,128,196,24576\nmm v,40,v,148,a,172\nmm v,72,v,144,a,176\n"
"rd v,132,197,24576\nmm v,72,v,148,a,180\nmm v,104,v,144,a,184\nmm v,104,v,148,a,188\nsa s85,s66,0\nbuffer_load_dwordx4 a[8:11], v230, s[80:83], s85 offen sc1\n"
"wl 3\nmm v,12,v,152,a,160\nwl 2\nmm v,12,v,156,a,164\nrd v,136,196,26624\nmm v,44,v,152,a,168\n"
"mm v,44,v,156,a,172\nmm v,76,v,152,a,176\nsa s85,s66,1024\nbuffer_load_dwordx4 a[12:15], v230, s[80:83], s85 offen sc1\nmm v,76,v,156,a,180\nrd v,140,197,26624\n"
"mm v,108,v,152,a,184\nmm v,108,v,156,a,188\nwl 3\nmm v,16,v,128,a,160\nwl 2\nmm v,16,v,132,a,164\n"
"rd v,144,196,28672\nmm v,48,v,128,a,168\nsa s85,s88,0\nbuffer_load_dwordx4 a[16:19], v230, s[80:83], s85 offen sc1\nmm v,48,v,132,a,172\nmm v,80,v,128,a,176\n"
"rd v,148,197,28672\nmm v,80,v,132,a,180\nmm v,112,v,128,a,184\nmm v,112,v,132,a,188\nsa s85,s88,1024\nbuffer_load_dwordx4 a[20:23], v230, s[80:83], s85 offen sc1\n"
"wl 3\nmm v,20,v,136,a,160\nwl 2\nmm v,20,v,140,a,164\nrd v,152,196,30720\nmm v,52,v,136,a,168\n"
"mm v,52,v,140,a,172\nmm v,84,v,136,a,176\nrd v,156,197,30720\nmm v,84,v,140,a,180\nsa s85,s59,0\nbuffer_load_dwordx4 a[24:27], v230, s[80:83], s85 offen sc1\n"
"mm v,116,v,136,a,184\nmm v,116,v,140,a,188\nwl 3\nmm v,24,v,144,a,160\nwl 2\nmm v,24,v,148,a,164\n"
"mm v,56,v,144,a,168\nwl 0\ns_barrier\nsa s85,s59,1024\nbuffer_load_dwordx4 a[28:31], v230, s[80:83], s85 offen sc1\nmm v,56,v,148,a,172\n"
"rd v,128,196,32768\nmm v,88,v,144,a,176\nmm v,88,v,148,a,180\nrd v,132,197,32768\nmm v,120,v,144,a,184\nmm v,120,v,148,a,188\n"
"rd v,136,196,34816\nmm v,28,v,152,a,160\nsa s85,s87,2048\nbuffer_load_dwordx4 a[32:35], v230, s[80:83], s85 offen sc1\nmm v,28,v,156,a,164\nrd v,140,197,34816\n"
"mm v,60,v,152,a,168\nmm v,60,v,156,a,172\nmm v,92,v,152,a,176\nmm v,92,v,156,a,180\nmm v,124,v,152,a,184\nmm v,124,v,156,a,188\n"
"wl 3\nmm v,0,v,128,a,192\nwl 2\nmm v,0,v,132,a,196\nsa s85,s87,3072\nbuffer_load_dwordx4 a[36:39], v230, s[80:83], s85 offen sc1\n"
"mm v,32,v,128,a,200\nrd v,144,196,36864\nmm v,32,v,132,a,204\nmm v,64,v,128,a,208\nrd v,148,197,36864\nmm v,64,v,132,a,212\n"
"mm v,96,v,128,a,216\nbuffer_store_dwordx4 v[218:221], v230, s[40:43], s86 offen offset:0 sc1\nmm v,96,v,132,a,220\nwl 3\nmm v,4,v,136,a,192\nwl 2\n"
"mm v,4,v,140,a,196\nrd v,152,196,38912\nmm v,36,v,136,a,200\nmm v,36,v,140,a,204\nmm v,68,v,136,a,208\nsa s85,s66,2048\n"
"buffer_load_dwordx4 a[40:43], v230, s[80:83], s85 offen sc1\nmm v,68,v,140,a,212\nrd v,156,197,38912\nmm v,100,v,136,a,216\nmm v,100,v,140,a,220\nwl 3\n"
"mm v,8,v,144,a,192\nwl 2\nmm v,8,v,148,a,196\nbuffer_store_dwordx4 v[222:225], v230, s[40:43], s86 offen offset:1024 sc1\nsa s86,s86,s92\nmm v,40,v,144,a,200\n"
"rd v,128,196,40960\nmm v,40,v,148,a,204\nmm v,72,v,144,a,208\nrd v,132,197,40960\nmm v,72,v,148,a,212\nmm v,104,v,144,a,216\n"
"mm v,104,v,148,a,220\nsa s85,s66,3072\nbuffer_load_dwordx4 a[44:47], v230, s[80:83], s85 offen sc1\nwl 3\nmm v,12,v,152,a,192\nwl 2\n"
"mm v,12,v,156,a,196\nrd v,136,196,43008\nmm v,44,v,152,a,200\nmm v,44,v,156,a,204\nmm v,76,v,152,a,208\nsa s85,s88,2048\n"
"buffer_load_dwordx4 a[48:51], v230, s[80:83], s85 offen sc1\nmm v,76,v,156,a,212\nrd v,140,197,43008\nmm v,108,v,152,a,216\nmm v,108,v,156,a,220\nwl 3\n"
"mm v,16,v,128,a,192\nwl 2\nmm v,16,v,132,a,196\nrd v,144,196,45056\nmm v,48,v,128,a,200\nsa s85,s88,3072\n"
"buffer_load_dwordx4 a[52:55], v230, s[80:83], s85 offen sc1\nmm v,48,v,132,a,204\nmm v,80,v,128,a,208\nrd v,148,197,45056\nmm v,80,v,132,a,212\nmm v,112,v,128,a,216\n"
"mm v,112,v,132,a,220\nsa s85,s59,2048\nbuffer_load_dwordx4 a[56:59], v230, s[80:83], s85 offen sc1\nwl 3\nmm v,20,v,136,a,192\nwl 2\n"
"mm v,20,v,140,a,196\nrd v,152,196,47104\nmm v,52,v,136,a,200\nmm v,52,v,140,a,204\nmm v,84,v,136,a,208\nrd v,156,197,47104\n"
"mm v,84,v,140,a,212\nsa s85,s59,3072\nbuffer_load_dwordx4 a[60:63], v230, s[80:83], s85 offen sc1\nmm v,116,v,136,a,216\nmm v,116,v,140,a,220\nwl 3\n"
"mm v,24,v,144,a,192\nwl 2\nmm v,24,v,148,a,196\nmm v,56,v,144,a,200\ns_waitcnt vmcnt(24) lgkmcnt(0)\ns_barrier\n"
"sa s85,s87,4096\nbuffer_load_dwordx4 a[64:67], v230, s[80:83], s85 offen sc1\nmm v,56,v,148,a,204\nrd v,128,196,49152\nmm v,88,v,144,a,208\nmm v,88,v,148,a,212\n"
"rd v,132,197,49152\nmm v,120,v,144,a,216\nmm v,120,v,148,a,220\nrd v,136,196,51200\nmm v,28,v,152,a,192\nsa s85,s87,5120\n"
"buffer_load_dwordx4 a[68:71], v230, s[80:83], s85 offen sc1\nmm v,28,v,156,a,196\nrd v,140,197,51200\nmm v,60,v,152,a,200\nmm v,60,v,156,a,204\nmm v,92,v,152,a,208\n"
"mm v,92,v,156,a,212\nmm v,124,v,152,a,216\nmm v,124,v,156,a,220\nwl 3\nmm v,0,v,128,a,224\nwl 2\n"
"mm v,0,v,132,a,228\nsa s85,s66,4096\nbuffer_load_dwordx4 a[72:75], v230, s[80:83], s85 offen sc1\nmm v,32,v,128,a,232\nrd v,144,196,53248\nmm v,32,v,132,a,236\n"
"mm v,64,v,128,a,240\nrd v,148,197,53248\nmm v,64,v,132,a,244\nmm v,96,v,128,a,248\nbuffer_store_dwordx4 v[226:229], v230, s[40:43], s86 offen offset:0 sc1\nmm v,96,v,132,a,252\n"
"wl 3\nmm v,4,v,136,a,224\nwl 2\nmm v,4,v,140,a,228\nrd v,152,196,55296\nmm v,36,v,136,a,232\n"
"mm v,36,v,140,a,236\nmm v,68,v,136,a,240\nsa s85,s66,5120\nbuffer_load_dwordx4 a[76:79], v230, s[80:83], s85 offen sc1\nmm v,68,v,140,a,244\nrd v,156,197,55296\n"
"mm v,100,v,136,a,248\nmm v,100,v,140,a,252\nwl 3\nmm v,8,v,144,a,224\nwl 2\nmm v,8,v,148,a,228\n"
"buffer_store_dwordx4 v[246:249], v230, s[40:43], s86 offen offset:1024 sc1\nsa s86,s86,s92\nmm v,40,v,144,a,232\nrd v,128,196,57344\nmm v,40,v,148,a,236\nmm v,72,v,144,a,240\n"
"rd v,132,197,57344\nmm v,72,v,148,a,244\nmm v,104,v,144,a,248\nmm v,104,v,148,a,252\nsa s85,s88,4096\nbuffer_load_dwordx4 a[80:83], v230, s[80:83], s85 offen sc1\n"
"wl 3\nmm v,12,v,152,a,224\nwl 2\nmm v,12,v,156,a,228\nrd v,136,196,59392\nmm v,44,v,152,a,232\n"
"mm v,44,v,156,a,236\nmm v,76,v,152,a,240\nsa s85,s88,5120\nbuffer_load_dwordx4 a[84:87], v230, s[80:83], s85 offen sc1\nmm v,76,v,156,a,244\nrd v,140,197,59392\n"
"mm v,108,v,152,a,248\nmm v,108,v,156,a,252\nwl 3\nmm v,16,v,128,a,224\nwl 2\nmm v,16,v,132,a,228\n"
"rd v,144,196,61440\nmm v,48,v,128,a,232\nsa s85,s59,4096\nbuffer_load_dwordx4 a[88:91], v230, s[80:83], s85 offen sc1\nmm v,48,v,132,a,236\nmm v,80,v,128,a,240\n"
"rd v,148,197,61440\nmm v,80,v,132,a,244\nmm v,112,v,128,a,248\nmm v,112,v,132,a,252\nsa s85,s59,5120\nbuffer_load_dwordx4 a[92:95], v230, s[80:83], s85 offen sc1\n"
"wl 3\nmm v,20,v,136,a,224\nwl 2\nmm v,20,v,140,a,228\nrd v,152,196,63488\nmm v,52,v,136,a,232\n"
"mm v,52,v,140,a,236\nmm v,84,v,136,a,240\nrd v,156,197,63488\nmm v,84,v,140,a,244\nsa s85,s87,6144\nbuffer_load_dwordx4 a[96:99], v230, s[80:83], s85 offen sc1\n"
"mm v,116,v,136,a,248\nmm v,116,v,140,a,252\nwl 3\nmm v,24,v,144,a,224\nwl 2\nmm v,24,v,148,a,228\n"
"mm v,56,v,144,a,232\nsa s85,s87,7168\nbuffer_load_dwordx4 a[100:103], v230, s[80:83], s85 offen sc1\nmm v,56,v,148,a,236\nmm v,88,v,144,a,240\nmm v,88,v,148,a,244\n"
"mm v,120,v,144,a,248\nmm v,120,v,148,a,252\nwl 1\nmm v,28,v,152,a,224\nsa s85,s66,6144\nbuffer_load_dwordx4 a[104:107], v230, s[80:83], s85 offen sc1\n"
"wl 0\nmm v,28,v,156,a,228\nmm v,60,v,152,a,232\nmm v,60,v,156,a,236\nmm v,92,v,152,a,240\nmm v,92,v,156,a,244\n"
"mm v,124,v,152,a,248\nmm v,124,v,156,a,252\nKV_END:\nv_add_u32 v253, 0x10000, v204\nv_add_u32 v254, 0x10000, v205\nv_add_u32 v200, 0x20000, v204\n"
"v_add_u32 v201, 0x20000, v205\ns_barrier\nrt v,128,204,0\nrt v,130,204,2048\nrt v,132,204,4096\nrt v,134,204,6144\n"
"rt v,136,205,0\nrt v,138,205,2048\nrt v,140,205,4096\nrt v,142,205,6144\nsa s86,s86,s64\ncv 160,0,1\n"
"cv 161,2,3\ncv 162,4,5\ncv 163,6,7\ncv 176,32,33\ncv 177,34,35\ncv 178,36,37\n"
"cv 179,38,39\ns_waitcnt vmcnt(32) lgkmcnt(6)\nmz v,0,v,128,a,0\ncv 192,64,65\ncv 193,66,67\ns_waitcnt vmcnt(30) lgkmcnt(4)\n"
"mm v,0,v,132,a,4\nrt v,144,204,512\ncv 194,68,69\nwv 28\nmz v,32,v,128,a,8\ncv 195,70,71\n"
"cv 218,96,97\nwv 27\nmm v,32,v,132,a,12\nrt v,146,204,2560\nsa s85,s66,7168\nbuffer_load_dwordx4 a[108:111], v230, s[80:83], s85 offen sc1\n"
"wv 27\nmz v,64,v,128,a,16\ncv 219,98,99\ncv 220,100,101\ncv 221,102,103\nwv 26\n"
"mm v,64,v,132,a,20\nrt v,148,204,4608\ncv 164,8,9\nwv 25\nmz v,96,v,128,a,24\ncv 165,10,11\n"
"cv 166,12,13\nwv 24\nmm v,96,v,132,a,28\nrt v,150,204,6656\nsa s85,s88,6144\nbuffer_load_dwordx4 a[112:115], v230, s[80:83], s85 offen sc1\n"
"wl 6\nmz v,4,v,136,a,0\ncv 167,14,15\ncv 180,40,41\nwl 4\nmm v,4,v,140,a,4\n"
"rt v,152,205,512\ncv 181,42,43\nmz v,36,v,136,a,8\ncv 182,44,45\ncv 183,46,47\nmm v,36,v,140,a,12\n"
"rt v,154,205,2560\nsa s85,s88,7168\nbuffer_load_dwordx4 a[116:119], v230, s[80:83], s85 offen sc1\nmz v,68,v,136,a,16\ncv 206,72,73\ncv 207,74,75\n"
"mm v,68,v,140,a,20\nrt v,156,205,4608\ncv 208,76,77\nmz v,100,v,136,a,24\ncv 209,78,79\ncv 222,104,105\n"
"mm v,100,v,140,a,28\nrt v,158,205,6656\nsa s85,s59,6144\nbuffer_load_dwordx4 a[120:123], v230, s[80:83], s85 offen sc1\nwl 6\nmz v,8,v,144,a,0\n"
"cv 223,106,107\ncv 224,108,109\nwl 4\nmm v,8,v,148,a,4\nrt v,128,204,8192\ncv 225,110,111\n"
"mz v,40,v,144,a,8\ncv 168,16,17\ncv 169,18,19\nmm v,40,v,148,a,12\nrt v,130,204,10240\nsa s85,s59,7168\n"
"buffer_load_dwordx4 a[124:127], v230, s[80:83], s85 offen sc1\nmz v,72,v,144,a,16\ncv 170,20,21\ncv 171,22,23\nmm v,72,v,148,a,20\nrt v,132,204,12288\n"
"cv 184,48,49\nmz v,104,v,144,a,24\ncv 185,50,51\ncv 186,52,53\nmm v,104,v,148,a,28\nrt v,134,204,14336\n"
"cv 187,54,55\nwl 6\nmz v,12,v,152,a,0\nsa s85,s87,8192\nbuffer_load_dwordx4 a[128:131], v230, s[80:83], s85 offen sc1\nwl 4\n"
"mm v,12,v,156,a,4\nrt v,136,205,8192\ncv 210,80,81\nmz v,44,v,152,a,8\ncv 211,82,83\ncv 212,84,85\n"
"mm v,44,v,156,a,12\nrt v,138,205,10240\ncv 213,86,87\nmz v,76,v,152,a,16\nsa s85,s87,9216\nbuffer_load_dwordx4 a[132:135], v230, s[80:83], s85 offen sc1\n"
"mm v,76,v,156,a,20\nrt v,140,205,12288\ncv 226,112,113\nmz v,108,v,152,a,24\ncv 227,114,115\ncv 228,116,117\n"
"mm v,108,v,156,a,28\nrt v,142,205,14336\ncv 229,118,119\nwl 6\nmz v,16,v,128,a,0\nsa s85,s66,8192\n"
"buffer_load_dwordx4 a[136:139], v230, s[80:83], s85 offen sc1\nwl 4\nmm v,16,v,132,a,4\nrt v,144,204,8704\ncv 172,24,25\nmz v,48,v,128,a,8\n"
"cv 173,26,27\ncv 174,28,29\nmm v,48,v,132,a,12\nrt v,146,204,10752\ncv 175,30,31\nmz v,80,v,128,a,16\n"
"sa s85,s66,9216\nbuffer_load_dwordx4 a[140:143], v230, s[80:83], s85 offen sc1\nmm v,80,v,132,a,20\nrt v,148,204,12800\ncv 188,56,57\nmz v,112,v,128,a,24\n"
"cv 189,58,59\ncv 190,60,61\nmm v,112,v,132,a,28\nrt v,150,204,14848\ncv 191,62,63\nwl 6\n"
"mz v,20,v,136,a,0\nsa s85,s88,8192\nbuffer_load_dwordx4 a[144:147], v230, s[80:83], s85 offen sc1\nwl 4\nmm v,20,v,140,a,4\nrt v,152,205,8704\n"
"cv 214,88,89\nmz v,52,v,136,a,8\nrt v,154,205,10752\ncv 215,90,91\nmm v,52,v,140,a,12\ncv 216,92,93\n"
"cv 217,94,95\nmz v,84,v,136,a,16\nrt v,156,205,12800\nsa s85,s88,9216\nbuffer_load_dwordx4 a[148:151], v230, s[80:83], s85 offen sc1\nmm v,84,v,140,a,20\n"
"cv 246,120,121\ncv 247,122,123\nmz v,116,v,136,a,24\nrt v,158,205,14848\ncv 248,124,125\nmm v,116,v,140,a,28\n"
"sa s85,s59,8192\nbuffer_load_dwordx4 a[152:155], v230, s[80:83], s85 offen sc1\nwl 6\nmz v,24,v,144,a,0\ncv 249,126,127\nwl 4\n"
"mm v,24,v,148,a,4\nmz v,56,v,144,a,8\nwl 0\ns_barrier\nmm v,56,v,148,a,12\nrt v,128,204,16384\n"
"mz v,88,v,144,a,16\nrt v,130,204,18432\nmm v,88,v,148,a,20\nrt v,132,204,20480\nmz v,120,v,144,a,24\nrt v,134,204,22528\n"
"mm v,120,v,148,a,28\nrt v,136,205,16384\nmz v,28,v,152,a,0\nbuffer_store_dwordx4 v[160:163], v230, s[40:43], s86 offen offset:0 sc1\nrt v,138,205,18432\nmm v,28,v,156,a,4\n"
"buffer_store_dwordx4 v[164:167], v230, s[40:43], s86 offen offset:1024 sc1\nsa s86,s86,s92\nmz v,60,v,152,a,8\nrt v,140,205,20480\nrt v,142,205,22528\nmm v,60,v,156,a,12\n"
"mz v,92,v,152,a,16\nmm v,92,v,156,a,20\nmz v,124,v,152,a,24\nmm v,124,v,156,a,28\ns_waitcnt vmcnt(36) lgkmcnt(6)\nmm v,0,v,128,a,32\n"
"rt v,144,204,16896\ns_waitcnt vmcnt(35) lgkmcnt(5)\nmm v,0,v,132,a,36\nsa s85,s59,9216\nbuffer_load_dwordx4 a[156:159], v230, s[80:83], s85 offen sc1\nwv 34\n"
"mm v,32,v,128,a,40\nrt v,146,204,18944\nwv 32\nmm v,32,v,132,a,44\nwv 31\nmm v,64,v,128,a,48\n"
"rt v,148,204,20992\nwv 30\nmm v,64,v,132,a,52\nsa s85,s87,10240\nbuffer_load_dwordx4 a[160:163], v230, s[80:83], s85 offen sc1\nwv 30\n"
"mm v,96,v,128,a,56\nrt v,150,204,23040\nwv 29\nmm v,96,v,132,a,60\nwl 6\nmm v,4,v,136,a,32\n"
"rt v,152,205,16896\nwl 5\nmm v,4,v,140,a,36\nsa s85,s87,11264\nbuffer_load_dwordx4 a[164:167], v230, s[80:83], s85 offen sc1\nmm v,36,v,136,a,40\n"
"rt v,154,205,18944\nmm v,36,v,140,a,44\nmm v,68,v,136,a,48\nrt v,156,205,20992\nmm v,68,v,140,a,52\nsa s85,s66,10240\n"
"buffer_load_dwordx4 a[168:171], v230, s[80:83], s85 offen sc1\nmm v,100,v,136,a,56\nrt v,158,205,23040\nmm v,100,v,140,a,60\nwl 6\nmm v,8,v,144,a,32\n"
"rt v,128,204,24576\nwl 5\nmm v,8,v,148,a,36\nsa s85,s66,11264\nbuffer_load_dwordx4 a[172:175], v230, s[80:83], s85 offen sc1\nmm v,40,v,144,a,40\n"
"rt v,130,204,26624\nmm v,40,v,148,a,44\nmm v,72,v,144,a,48\nrt v,132,204,28672\nmm v,72,v,148,a,52\nmm v,104,v,144,a,56\n"
"rt v,134,204,30720\nsa s85,s88,10240\nbuffer_load_dwordx4 a[176:179], v230, s[80:83], s85 offen sc1\nmm v,104,v,148,a,60\nwl 6\nmm v,12,v,152,a,32\n"
"rt v,136,205,24576\nwl 5\nmm v,12,v,156,a,36\nmm v,44,v,152,a,40\nrt v,138,205,26624\nsa s85,s88,11264\n"
"buffer_load_dwordx4 a[180:183], v230, s[80:83], s85 offen sc1\nmm v,44,v,156,a,44\nmm v,76,v,152,a,48\nrt v,140,205,28672\nmm v,76,v,156,a,52\nmm v,108,v,152,a,56\n"
"rt v,142,205,30720\nsa s85,s59,10240\nbuffer_load_dwordx4 a[184:187], v230, s[80:83], s85 offen sc1\nmm v,108,v,156,a,60\nwl 6\nmm v,16,v,128,a,32\n"
"rt v,144,204,25088\nwl 5\nmm v,16,v,132,a,36\nmm v,48,v,128,a,40\nrt v,146,204,27136\nsa s85,s59,11264\n"
"buffer_load_dwordx4 a[188:191], v230, s[80:83], s85 offen sc1\nmm v,48,v,132,a,44\nmm v,80,v,128,a,48\nrt v,148,204,29184\nmm v,80,v,132,a,52\nmm v,112,v,128,a,56\n"
"rt v,150,204,31232\nsa m0,s89,98304\nsa s85,s93,3145728\ndl 203,76,85\nmm v,112,v,132,a,60\nwl 6\n"
"mm v,20,v,136,a,32\nrt v,152,205,25088\nwl 5\nmm v,20,v,140,a,36\nmm v,52,v,136,a,40\nrt v,154,205,27136\n"
"mm v,52,v,140,a,44\nsa m0,s89,99328\nsa s85,s93,3211264\ndl 203,76,85\nmm v,84,v,136,a,48\nrt v,156,205,29184\n"
"mm v,84,v,140,a,52\nmm v,116,v,136,a,56\nrt v,158,205,31232\nmm v,116,v,140,a,60\nsa m0,s89,100352\nsa s85,s93,3276800\n"
"dl 203,76,85\nwl 6\nmm v,24,v,144,a,32\nwl 4\nmm v,24,v,148,a,36\nmm v,56,v,144,a,40\n"
"wl 0\ns_barrier\nmm v,56,v,148,a,44\nrt v,128,204,32768\nbuffer_store_dwordx4 v[168:171], v230, s[40:43], s86 offen offset:0 sc1\nmm v,88,v,144,a,48\n"
"rt v,130,204,34816\nmm v,88,v,148,a,52\nrt v,132,204,36864\nmm v,120,v,144,a,56\nrt v,134,204,38912\nmm v,120,v,148,a,60\n"
"rt v,136,205,32768\nbuffer_store_dwordx4 v[172:175], v230, s[40:43], s86 offen offset:1024 sc1\nsa s86,s86,s92\nmm v,28,v,152,a,32\nrt v,138,205,34816\nmm v,28,v,156,a,36\n"
"rt v,140,205,36864\nmm v,60,v,152,a,40\nrt v,142,205,38912\nmm v,60,v,156,a,44\nmm v,92,v,152,a,48\nmm v,92,v,156,a,52\n"
"mm v,124,v,152,a,56\nmm v,124,v,156,a,60\ns_waitcnt vmcnt(40) lgkmcnt(6)\nmm v,0,v,128,a,64\nrt v,144,204,33280\ns_waitcnt vmcnt(39) lgkmcnt(5)\n"
"mm v,0,v,132,a,68\nsa m0,s89,101376\nsa s85,s93,3342336\ndl 203,76,85\nwv 39\nmm v,32,v,128,a,72\n"
"rt v,146,204,35328\nwv 37\nmm v,32,v,132,a,76\nwv 35\nmm v,64,v,128,a,80\nsa s85,s87,12288\n"
"buffer_load_dwordx4 a[192:195], v230, s[80:83], s85 offen sc1\nwv 35\nmm v,64,v,132,a,84\nrt v,148,204,37376\nwv 34\nmm v,96,v,128,a,88\n"
"rt v,150,204,39424\nwv 33\nmm v,96,v,132,a,92\nwl 6\nmm v,4,v,136,a,64\nsa s85,s87,13312\n"
"buffer_load_dwordx4 a[196:199], v230, s[80:83], s85 offen sc1\nwl 4\nmm v,4,v,140,a,68\nrt v,152,205,33280\nmm v,36,v,136,a,72\nrt v,154,205,35328\n"
"mm v,36,v,140,a,76\nmm v,68,v,136,a,80\nsa s85,s66,12288\nbuffer_load_dwordx4 a[200:203], v230, s[80:83], s85 offen sc1\nmm v,68,v,140,a,84\nrt v,156,205,37376\n"
"mm v,100,v,136,a,88\nrt v,158,205,39424\nmm v,100,v,140,a,92\nwl 6\nmm v,8,v,144,a,64\nsa s85,s66,13312\n"
"buffer_load_dwordx4 a[204:207], v230, s[80:83], s85 offen sc1\nwl 4\nmm v,8,v,148,a,68\nrt v,128,204,40960\nmm v,40,v,144,a,72\nrt v,130,204,43008\n"
"mm v,40,v,148,a,76\nmm v,72,v,144,a,80\nsa s85,s88,12288\nbuffer_load_dwordx4 a[208:211], v230, s[80:83], s85 offen sc1\nmm v,72,v,148,a,84\nrt v,132,204,45056\n"
"mm v,104,v,144,a,88\nrt v,134,204,47104\nmm v,104,v,148,a,92\nwl 6\nmm v,12,v,152,a,64\nsa s85,s88,13312\n"
"buffer_load_dwordx4 a[212:215], v230, s[80:83], s85 offen sc1\nwl 4\nmm v,12,v,156,a,68\nrt v,136,205,40960\nmm v,44,v,152,a,72\nrt v,138,205,43008\n"
"mm v,44,v,156,a,76\nmm v,76,v,152,a,80\nsa s85,s59,12288\nbuffer_load_dwordx4 a[216:219], v230, s[80:83], s85 offen sc1\nrt v,140,205,45056\nmm v,76,v,156,a,84\n"
"mm v,108,v,152,a,88\nrt v,142,205,47104\nmm v,108,v,156,a,92\nwl 6\nmm v,16,v,128,a,64\nsa s85,s59,13312\n"
"buffer_load_dwordx4 a[220:223], v230, s[80:83], s85 offen sc1\nrt v,144,204,41472\nwl 5\nmm v,16,v,132,a,68\nmm v,48,v,128,a,72\nrt v,146,204,43520\n"
"mm v,48,v,132,a,76\nmm v,80,v,128,a,80\nsa m0,s89,114688\nsa s85,s93,3670016\ndl 203,76,85\nmm v,80,v,132,a,84\n"
"rt v,148,204,45568\nmm v,112,v,128,a,88\nrt v,150,204,47616\nmm v,112,v,132,a,92\nwl 6\nmm v,20,v,136,a,64\n"
"sa m0,s89,115712\nsa s85,s93,3735552\ndl 203,76,85\nwl 4\nmm v,20,v,140,a,68\nrt v,152,205,41472\n"
"mm v,52,v,136,a,72\nrt v,154,205,43520\nmm v,52,v,140,a,76\nmm v,84,v,136,a,80\nsa m0,s89,116736\nsa s85,s93,3801088\n"
"dl 203,76,85\nmm v,84,v,140,a,84\nrt v,156,205,45568\nmm v,116,v,136,a,88\nrt v,158,205,47616\nmm v,116,v,140,a,92\n"
"wl 6\nmm v,24,v,144,a,64\nbuffer_store_dwordx4 v[176:179], v230, s[40:43], s86 offen offset:0 sc1\nwl 4\nmm v,24,v,148,a,68\nmm v,56,v,144,a,72\n"
"wl 0\ns_barrier\nmm v,56,v,148,a,76\nrt v,128,204,49152\nmm v,88,v,144,a,80\nbuffer_store_dwordx4 v[180:183], v230, s[40:43], s86 offen offset:1024 sc1\n"
"sa s86,s86,s92\nrt v,130,204,51200\nmm v,88,v,148,a,84\nrt v,132,204,53248\nmm v,120,v,144,a,88\nrt v,134,204,55296\n"
"mm v,120,v,148,a,92\nrt v,136,205,49152\nmm v,28,v,152,a,64\nv_mov_b32 v240, s46\nv_mov_b32 v241, s47\ns_mov_b64 exec, 1\n"
"buffer_store_dwordx2 v[240:241], v245, s[96:99], s34 offen sc1\ns_mov_b64 exec, -1\nmm v,28,v,156,a,68\nrt v,138,205,51200\nrt v,140,205,53248\nmm v,60,v,152,a,72\n"
"rt v,142,205,55296\nmm v,60,v,156,a,76\nmm v,92,v,152,a,80\nmm v,92,v,156,a,84\nmm v,124,v,152,a,88\nmm v,124,v,156,a,92\n"
"s_waitcnt vmcnt(45) lgkmcnt(6)\nmm v,0,v,128,a,96\nrt v,144,204,49664\ns_waitcnt vmcnt(44) lgkmcnt(5)\nmm v,0,v,132,a,100\nsa m0,s89,117760\n"
"sa s85,s93,3866624\ndl 203,76,85\nwv 44\nmm v,32,v,128,a,104\nrt v,146,204,51712\nwv 43\n"
"mm v,32,v,132,a,108\nwv 42\nmm v,64,v,128,a,112\nrt v,148,204,53760\nwv 41\nmm v,64,v,132,a,116\n"
"sa s85,s87,14336\nbuffer_load_dwordx4 a[224:227], v230, s[80:83], s85 offen sc1\nwv 41\nmm v,96,v,128,a,120\nrt v,150,204,55808\nwv 40\n"
"mm v,96,v,132,a,124\nwl 6\nmm v,4,v,136,a,96\nrt v,152,205,49664\nwl 5\nmm v,4,v,140,a,100\n"
"sa s85,s87,15360\nbuffer_load_dwordx4 a[228:231], v230, s[80:83], s85 offen sc1\nmm v,36,v,136,a,104\nrt v,154,205,51712\nmm v,36,v,140,a,108\nmm v,68,v,136,a,112\n"
"rt v,156,205,53760\nmm v,68,v,140,a,116\nsa s85,s66,14336\nbuffer_load_dwordx4 a[232:235], v230, s[80:83], s85 offen sc1\nmm v,100,v,136,a,120\nrt v,158,205,55808\n"
"mm v,100,v,140,a,124\nwl 6\nmm v,8,v,144,a,96\nrt v,128,204,57344\nwl 5\nmm v,8,v,148,a,100\n"
"sa s85,s66,15360\nbuffer_load_dwordx4 a[236:239], v230, s[80:83], s85 offen sc1\nmm v,40,v,144,a,104\nrt v,130,204,59392\nmm v,40,v,148,a,108\nmm v,72,v,144,a,112\n"
"rt v,132,204,61440\nmm v,72,v,148,a,116\nmm v,104,v,144,a,120\nrt v,134,204,63488\nsa s85,s88,14336\nbuffer_load_dwordx4 a[240:243], v230, s[80:83], s85 offen sc1\n"
"mm v,104,v,148,a,124\nwl 6\nmm v,12,v,152,a,96\nrt v,136,205,57344\nwl 5\nmm v,12,v,156,a,100\n"
"mm v,44,v,152,a,104\nrt v,138,205,59392\nsa s85,s88,15360\nbuffer_load_dwordx4 a[244:247], v230, s[80:83], s85 offen sc1\nmm v,44,v,156,a,108\nmm v,76,v,152,a,112\n"
"rt v,140,205,61440\nmm v,76,v,156,a,116\nmm v,108,v,152,a,120\nrt v,142,205,63488\nsa s85,s59,14336\nbuffer_load_dwordx4 a[248:251], v230, s[80:83], s85 offen sc1\n"
"mm v,108,v,156,a,124\nwl 6\nmm v,16,v,128,a,96\nrt v,144,204,57856\nwl 5\nmm v,16,v,132,a,100\n"
"mm v,48,v,128,a,104\nrt v,146,204,59904\nsa s85,s59,15360\nbuffer_load_dwordx4 a[252:255], v230, s[80:83], s85 offen sc1\nmm v,48,v,132,a,108\nmm v,80,v,128,a,112\n"
"rt v,148,204,61952\nmm v,80,v,132,a,116\nmm v,112,v,128,a,120\nrt v,150,204,64000\nsa m0,s89,131072\nsa s85,s93,4194304\n"
"dl 203,76,85\nmm v,112,v,132,a,124\nwl 6\nmm v,20,v,136,a,96\nrt v,152,205,57856\nwl 5\n"
"mm v,20,v,140,a,100\nmm v,52,v,136,a,104\nrt v,154,205,59904\nmm v,52,v,140,a,108\nsa m0,s89,132096\nsa s85,s93,4259840\n"
"dl 203,76,85\nmm v,84,v,136,a,112\nrt v,156,205,61952\nmm v,84,v,140,a,116\nmm v,116,v,136,a,120\nrt v,158,205,64000\n"
"mm v,116,v,140,a,124\nsa m0,s89,133120\nsa s85,s93,4325376\ndl 203,76,85\nwl 6\nmm v,24,v,144,a,96\n"
"wl 4\nmm v,24,v,148,a,100\nmm v,56,v,144,a,104\nwl 0\ns_barrier\nmm v,56,v,148,a,108\n"
"rt v,128,253,0\nbuffer_store_dwordx4 v[184:187], v230, s[40:43], s86 offen offset:0 sc1\nmm v,88,v,144,a,112\nrt v,130,253,2048\nmm v,88,v,148,a,116\nrt v,132,253,4096\n"
"mm v,120,v,144,a,120\nrt v,134,253,6144\nmm v,120,v,148,a,124\nrt v,136,254,0\nbuffer_store_dwordx4 v[188:191], v230, s[40:43], s86 offen offset:1024 sc1\nsa s86,s86,s92\n"
"mm v,28,v,152,a,96\nrt v,138,254,2048\nmm v,28,v,156,a,100\nrt v,140,254,4096\nmm v,60,v,152,a,104\nrt v,142,254,6144\n"
"mm v,60,v,156,a,108\nmm v,92,v,152,a,112\nmm v,92,v,156,a,116\nmm v,124,v,152,a,120\nmm v,124,v,156,a,124\ns_waitcnt vmcnt(51) lgkmcnt(6)\n"
"mm v,0,v,128,a,128\nrt v,144,253,512\ns_waitcnt vmcnt(50) lgkmcnt(5)\nmm v,0,v,132,a,132\nsa m0,s89,134144\nsa s85,s93,4390912\n"
"dl 203,76,85\nwv 50\nmm v,32,v,128,a,136\nrt v,146,253,2560\nwv 49\nmm v,32,v,132,a,140\n"
"wv 48\nmm v,64,v,128,a,144\nrt v,148,253,4608\nwv 47\nmm v,64,v,132,a,148\nsa s85,s87,16384\n"
"buffer_load_dwordx4 a[0:3], v230, s[80:83], s85 offen sc1\nwv 47\nmm v,96,v,128,a,152\nrt v,150,253,6656\nwv 44\nmm v,96,v,132,a,156\n"
"wl 6\nmm v,4,v,136,a,128\nrt v,152,254,512\nwl 5\nmm v,4,v,140,a,132\nsa s85,s87,17408\n"
"buffer_load_dwordx4 a[4:7], v230, s[80:83], s85 offen sc1\nmm v,36,v,136,a,136\nrt v,154,254,2560\nmm v,36,v,140,a,140\nmm v,68,v,136,a,144\nrt v,156,254,4608\n"
"mm v,68,v,140,a,148\nsa s85,s66,16384\nbuffer_load_dwordx4 a[8:11], v230, s[80:83], s85 offen sc1\nmm v,100,v,136,a,152\nrt v,158,254,6656\nmm v,100,v,140,a,156\n"
"wl 6\nmm v,8,v,144,a,128\nrt v,128,253,8192\nwl 5\nmm v,8,v,148,a,132\nsa s85,s66,17408\n"
"buffer_load_dwordx4 a[12:15], v230, s[80:83], s85 offen sc1\nmm v,40,v,144,a,136\nrt v,130,253,10240\nmm v,40,v,148,a,140\nmm v,72,v,144,a,144\nrt v,132,253,12288\n"
"mm v,72,v,148,a,148\nmm v,104,v,144,a,152\nrt v,134,253,14336\nsa s85,s88,16384\nbuffer_load_dwordx4 a[16:19], v230, s[80:83], s85 offen sc1\nmm v,104,v,148,a,156\n"
"wl 6\nmm v,12,v,152,a,128\nrt v,136,254,8192\nwl 5\nmm v,12,v,156,a,132\nmm v,44,v,152,a,136\n"
"rt v,138,254,10240\nsa s85,s88,17408\nbuffer_load_dwordx4 a[20:23], v230, s[80:83], s85 offen sc1\nmm v,44,v,156,a,140\nmm v,76,v,152,a,144\nrt v,140,254,12288\n"
"mm v,76,v,156,a,148\nmm v,108,v,152,a,152\nrt v,142,254,14336\nsa s85,s59,16384\nbuffer_load_dwordx4 a[24:27], v230, s[80:83], s85 offen sc1\nmm v,108,v,156,a,156\n"
"wl 6\nmm v,16,v,128,a,128\nrt v,144,253,8704\nwl 5\nmm v,16,v,132,a,132\nmm v,48,v,128,a,136\n"
"rt v,146,253,10752\nsa s85,s59,17408\nbuffer_load_dwordx4 a[28:31], v230, s[80:83], s85 offen sc1\nmm v,48,v,132,a,140\nmm v,80,v,128,a,144\nrt v,148,253,12800\n"
"mm v,80,v,132,a,148\nmm v,112,v,128,a,152\nrt v,150,253,14848\nsa m0,s89,147456\nsa s85,s93,4718592\ndl 203,76,85\n"
"mm v,112,v,132,a,156\nwl 6\nmm v,20,v,136,a,128\nrt v,152,254,8704\nwl 5\nmm v,20,v,140,a,132\n"
"mm v,52,v,136,a,136\nrt v,154,254,10752\nmm v,52,v,140,a,140\nsa m0,s89,148480\nsa s85,s93,4784128\ndl 203,76,85\n"
"mm v,84,v,136,a,144\nrt v,156,254,12800\nmm v,84,v,140,a,148\nmm v,116,v,136,a,152\nrt v,158,254,14848\nmm v,116,v,140,a,156\n"
"sa m0,s89,149504\nsa s85,s93,4849664\ndl 203,76,85\nwl 6\nmm v,24,v,144,a,128\nwl 4\n"
"mm v,24,v,148,a,132\nmm v,56,v,144,a,136\nwl 0\ns_barrier\nmm v,56,v,148,a,140\nrt v,128,253,16384\n"
"buffer_store_dwordx4 v[192:195], v230, s[40:43], s86 offen offset:0 sc1\nmm v,88,v,144,a,144\nrt v,130,253,18432\nmm v,88,v,148,a,148\nrt v,132,253,20480\nmm v,120,v,144,a,152\n"
"rt v,134,253,22528\nmm v,120,v,148,a,156\nrt v,136,254,16384\nbuffer_store_dwordx4 v[206:209], v230, s[40:43], s86 offen offset:1024 sc1\nsa s86,s86,s92\nmm v,28,v,152,a,128\n"
"rt v,138,254,18432\nmm v,28,v,156,a,132\nrt v,140,254,20480\nmm v,60,v,152,a,136\nrt v,142,254,22528\nmm v,60,v,156,a,140\n"
"mm v,92,v,152,a,144\nmm v,92,v,156,a,148\nmm v,124,v,152,a,152\nmm v,124,v,156,a,156\ns_waitcnt vmcnt(55) lgkmcnt(6)\nmm v,0,v,128,a,160\n"
"rt v,144,253,16896\ns_waitcnt vmcnt(54) lgkmcnt(5)\nmm v,0,v,132,a,164\nsa m0,s89,150528\nsa s85,s93,4915200\ndl 203,76,85\n"
"wv 54\nmm v,32,v,128,a,168\nrt v,146,253,18944\nwv 53\nmm v,32,v,132,a,172\nwv 52\n"
"mm v,64,v,128,a,176\nrt v,148,253,20992\nwv 51\nmm v,64,v,132,a,180\nsa s85,s87,18432\nbuffer_load_dwordx4 a[32:35], v230, s[80:83], s85 offen sc1\n"
"wv 51\nmm v,96,v,128,a,184\nrt v,150,253,23040\nwv 50\nmm v,96,v,132,a,188\nwl 6\n"
"mm v,4,v,136,a,160\nrt v,152,254,16896\nwl 5\nmm v,4,v,140,a,164\nmm v,36,v,136,a,168\nrt v,154,254,18944\n"
"sa s85,s87,19456\nbuffer_load_dwordx4 a[36:39], v230, s[80:83], s85 offen sc1\nmm v,36,v,140,a,172\nmm v,68,v,136,a,176\nrt v,156,254,20992\nmm v,68,v,140,a,180\n"
"mm v,100,v,136,a,184\nrt v,158,254,23040\nmm v,100,v,140,a,188\nsa s85,s66,18432\nbuffer_load_dwordx4 a[40:43], v230, s[80:83], s85 offen sc1\nwl 6\n"
"mm v,8,v,144,a,160\nrt v,128,253,24576\nwl 5\nmm v,8,v,148,a,164\nmm v,40,v,144,a,168\nrt v,130,253,26624\n"
"mm v,40,v,148,a,172\nmm v,72,v,144,a,176\nrt v,132,253,28672\nsa s85,s66,19456\nbuffer_load_dwordx4 a[44:47], v230, s[80:83], s85 offen sc1\nmm v,72,v,148,a,180\n"
"mm v,104,v,144,a,184\nrt v,134,253,30720\nmm v,104,v,148,a,188\nwl 6\nmm v,12,v,152,a,160\nrt v,136,254,24576\n"
"wl 5\nmm v,12,v,156,a,164\nsa s85,s88,18432\nbuffer_load_dwordx4 a[48:51], v230, s[80:83], s85 offen sc1\nmm v,44,v,152,a,168\nrt v,138,254,26624\n"
"mm v,44,v,156,a,172\nmm v,76,v,152,a,176\nrt v,140,254,28672\nmm v,76,v,156,a,180\nmm v,108,v,152,a,184\nrt v,142,254,30720\n"
"sa s85,s88,19456\nbuffer_load_dwordx4 a[52:55], v230, s[80:83], s85 offen sc1\nmm v,108,v,156,a,188\nwl 6\nmm v,16,v,128,a,160\nrt v,144,253,25088\n"
"wl 5\nmm v,16,v,132,a,164\nmm v,48,v,128,a,168\nrt v,146,253,27136\nmm v,48,v,132,a,172\nsa s85,s59,18432\n"
"buffer_load_dwordx4 a[56:59], v230, s[80:83], s85 offen sc1\nmm v,80,v,128,a,176\nrt v,148,253,29184\nmm v,80,v,132,a,180\nmm v,112,v,128,a,184\nrt v,150,253,31232\n"
"mm v,112,v,132,a,188\nwl 6\nmm v,20,v,136,a,160\nrt v,152,254,25088\nsa s85,s59,19456\nbuffer_load_dwordx4 a[60:63], v230, s[80:83], s85 offen sc1\n"
"wl 5\nmm v,20,v,140,a,164\nmm v,52,v,136,a,168\nrt v,154,254,27136\nmm v,52,v,140,a,172\nmm v,84,v,136,a,176\n"
"rt v,156,254,29184\nmm v,84,v,140,a,180\nbuffer_store_dwordx4 v[210:213], v230, s[40:43], s86 offen offset:0 sc1\nmm v,116,v,136,a,184\nrt v,158,254,31232\nmm v,116,v,140,a,188\n"
"wl 6\nmm v,24,v,144,a,160\nwl 4\nmm v,24,v,148,a,164\nmm v,56,v,144,a,168\ns_waitcnt vmcnt(52) lgkmcnt(0)\n"
"s_barrier\nbuffer_store_dwordx4 v[214:217], v230, s[40:43], s86 offen offset:1024 sc1\nsa s86,s86,s92\nmm v,56,v,148,a,172\nrt v,128,253,32768\nmm v,88,v,144,a,176\n"
"rt v,130,253,34816\nmm v,88,v,148,a,180\nrt v,132,253,36864\nmm v,120,v,144,a,184\nrt v,134,253,38912\nmm v,120,v,148,a,188\n"
"rt v,136,254,32768\nbuffer_load_dwordx2 v[242:243], v244, s[96:99], s91 offen sc1\nmm v,28,v,152,a,160\nrt v,138,254,34816\nmm v,28,v,156,a,164\nrt v,140,254,36864\n"
"mm v,60,v,152,a,168\nrt v,142,254,38912\nmm v,60,v,156,a,172\nmm v,92,v,152,a,176\nmm v,92,v,156,a,180\nmm v,124,v,152,a,184\n"
"mm v,124,v,156,a,188\ns_waitcnt vmcnt(53) lgkmcnt(6)\nmm v,0,v,128,a,192\nrt v,144,253,33280\ns_waitcnt vmcnt(52) lgkmcnt(5)\nmm v,0,v,132,a,196\n"
"wv 0\nv_cmp_ne_u64 vcc, s[46:47], v[242:243]\ns_nop 0\ns_and_b64 vcc, vcc, s[38:39]\ns_cbranch_scc1 SPIN_AQ\nSPUN_AQ:\n"
"s_nop 0\nmm v,32,v,128,a,200\nrt v,146,253,35328\nmm v,32,v,132,a,204\nmm v,64,v,128,a,208\nrt v,148,253,37376\n"
"mm v,64,v,132,a,212\nmm v,96,v,128,a,216\nrt v,150,253,39424\nmm v,96,v,132,a,220\nwl 6\nmm v,4,v,136,a,192\n"
"rt v,152,254,33280\nwl 5\nmm v,4,v,140,a,196\nmm v,36,v,136,a,200\nrt v,154,254,35328\nmm v,36,v,140,a,204\n"
"mm v,68,v,136,a,208\nrt v,156,254,37376\nmm v,68,v,140,a,212\nmm v,100,v,136,a,216\nrt v,158,254,39424\nmm v,100,v,140,a,220\n"
"wl 6\nmm v,8,v,144,a,192\nrt v,128,253,40960\nwl 5\nmm v,8,v,148,a,196\nmm v,40,v,144,a,200\n"
"rt v,130,253,43008\nmm v,40,v,148,a,204\nmm v,72,v,144,a,208\nrt v,132,253,45056\nbuffer_store_dwordx4 v[218:221], v230, s[40:43], s86 offen offset:0 sc1\nmm v,72,v,148,a,212\n"
"mm v,104,v,144,a,216\nrt v,134,253,47104\nmm v,104,v,148,a,220\nwl 6\nmm v,12,v,152,a,192\nrt v,136,254,40960\n"
"wl 5\nmm v,12,v,156,a,196\nmm v,44,v,152,a,200\nrt v,138,254,43008\nmm v,44,v,156,a,204\nmm v,76,v,152,a,208\n"
"rt v,140,254,45056\nmm v,76,v,156,a,212\nmm v,108,v,152,a,216\nrt v,142,254,47104\nmm v,108,v,156,a,220\nwl 6\n"
"mm v,16,v,128,a,192\nrt v,144,253,41472\nwl 5\nmm v,16,v,132,a,196\nmm v,48,v,128,a,200\nrt v,146,253,43520\n"
"mm v,48,v,132,a,204\nmm v,80,v,128,a,208\nrt v,148,253,45568\nmm v,80,v,132,a,212\nmm v,112,v,128,a,216\nrt v,150,253,47616\n"
"mm v,112,v,132,a,220\nwl 6\nmm v,20,v,136,a,192\nbuffer_store_dwordx4 v[222:225], v230, s[40:43], s86 offen offset:1024 sc1\nsa s86,s86,s92\nwl 4\n"
"mm v,20,v,140,a,196\nrt v,152,254,41472\nmm v,52,v,136,a,200\nrt v,154,254,43520\nmm v,52,v,140,a,204\nmm v,84,v,136,a,208\n"
"rt v,156,254,45568\nmm v,84,v,140,a,212\nmm v,116,v,136,a,216\nrt v,158,254,47616\nmm v,116,v,140,a,220\nwl 6\n"
"mm v,24,v,144,a,192\nwl 4\nmm v,24,v,148,a,196\nmm v,56,v,144,a,200\nwl 0\ns_barrier\n"
"mm v,56,v,148,a,204\nrt v,128,253,49152\nmm v,88,v,144,a,208\nrt v,130,253,51200\nmm v,88,v,148,a,212\nrt v,132,253,53248\n"
"mm v,120,v,144,a,216\nrt v,134,253,55296\nmm v,120,v,148,a,220\nrt v,136,254,49152\nmm v,28,v,152,a,192\nrt v,138,254,51200\n"
"mm v,28,v,156,a,196\nrt v,140,254,53248\nmm v,60,v,152,a,200\nrt v,142,254,55296\nmm v,60,v,156,a,204\nmm v,92,v,152,a,208\n"
"mm v,92,v,156,a,212\nmm v,124,v,152,a,216\nmm v,124,v,156,a,220\nwl 6\nmm v,0,v,128,a,224\nrt v,144,253,49664\n"
"wl 5\nmm v,0,v,132,a,228\ns_mov_b32 m0, s57\ns_nop 0\ndc 230,40,53,0\nmm v,32,v,128,a,232\n"
"rt v,146,253,51712\nmm v,32,v,132,a,236\nmm v,64,v,128,a,240\nrt v,148,253,53760\nmm v,64,v,132,a,244\nmm v,96,v,128,a,248\n"
"rt v,150,253,55808\ndc 230,40,53,1024\nmm v,96,v,132,a,252\nwl 6\nmm v,4,v,136,a,224\nrt v,152,254,49664\n"
"wl 5\nmm v,4,v,140,a,228\nmm v,36,v,136,a,232\nrt v,154,254,51712\nmm v,36,v,140,a,236\nmm v,68,v,136,a,240\n"
"rt v,156,254,53760\ndc 230,40,53,2048\nmm v,68,v,140,a,244\nmm v,100,v,136,a,248\nrt v,158,254,55808\nmm v,100,v,140,a,252\n"
"wl 6\nmm v,8,v,144,a,224\nrt v,128,253,57344\nwl 5\nmm v,8,v,148,a,228\nmm v,40,v,144,a,232\n"
"rt v,130,253,59392\ndc 230,40,53,3072\nmm v,40,v,148,a,236\nmm v,72,v,144,a,240\nrt v,132,253,61440\nmm v,72,v,148,a,244\n"
"mm v,104,v,144,a,248\nrt v,134,253,63488\nmm v,104,v,148,a,252\nwl 6\nmm v,12,v,152,a,224\nrt v,136,254,57344\n"
"sa m0,m0,0x1000\ns_nop 0\ndc 231,40,53,0\nwl 5\nmm v,12,v,156,a,228\nmm v,44,v,152,a,232\n"
"rt v,138,254,59392\nmm v,44,v,156,a,236\nmm v,76,v,152,a,240\nrt v,140,254,61440\nmm v,76,v,156,a,244\nmm v,108,v,152,a,248\n"
"rt v,142,254,63488\ndc 231,40,53,1024\nmm v,108,v,156,a,252\nwl 6\nmm v,16,v,128,a,224\nrt v,144,253,57856\n"
"wl 5\nmm v,16,v,132,a,228\nmm v,48,v,128,a,232\nrt v,146,253,59904\nmm v,48,v,132,a,236\nmm v,80,v,128,a,240\n"
"rt v,148,253,61952\ndc 231,40,53,2048\nmm v,80,v,132,a,244\nmm v,112,v,128,a,248\nrt v,150,253,64000\nmm v,112,v,132,a,252\n"
"wl 6\nmm v,20,v,136,a,224\nrt v,152,254,57856\nwl 5\nmm v,20,v,140,a,228\nmm v,52,v,136,a,232\n"
"rt v,154,254,59904\ndc 231,40,53,3072\nmm v,52,v,140,a,236\nmm v,84,v,136,a,240\nrt v,156,254,61952\nmm v,84,v,140,a,244\n"
"mm v,116,v,136,a,248\nrt v,158,254,64000\nmm v,116,v,140,a,252\nwl 6\nmm v,24,v,144,a,224\nbuffer_store_dwordx4 v[226:229], v230, s[40:43], s86 offen offset:0 sc1\n"
"wl 4\nmm v,24,v,148,a,228\nmm v,56,v,144,a,232\nwl 0\ns_barrier\nmm v,56,v,148,a,236\n"
"rt v,128,200,0\nmm v,88,v,144,a,240\nrt v,130,200,2048\nmm v,88,v,148,a,244\nrt v,132,200,4096\nmm v,120,v,144,a,248\n"
"rt v,134,200,6144\nbuffer_store_dwordx4 v[246:249], v230, s[40:43], s86 offen offset:1024 sc1\nsa s86,s86,s92\nmm v,120,v,148,a,252\nrt v,136,201,0\nmm v,28,v,152,a,224\n"
"rt v,138,201,2048\nmm v,28,v,156,a,228\nrt v,140,201,4096\nmm v,60,v,152,a,232\nrt v,142,201,6144\nmm v,60,v,156,a,236\n"
"mm v,92,v,152,a,240\nmm v,92,v,156,a,244\nmm v,124,v,152,a,248\nmm v,124,v,156,a,252\nwl 6\nmm v,0,v,128,a,0\n"
"rt v,144,200,512\nv_mov_b32 v208, v230\nwl 5\nmm v,0,v,132,a,4\nv_add_u32 v209, 0x18000, v230\ns_mov_b32 s59, 0x4e800000\n"
"mm v,32,v,128,a,8\nrt v,146,200,2560\nv_lshrrev_b32 v234, 4, v230\nmm v,32,v,132,a,12\ns_xor_b32 m0, s57, 0x10000\ns_nop 0\n"
"dc 232,40,54,0\nmm v,64,v,128,a,16\nrt v,148,200,4608\nv_lshrrev_b32 v235, 4, v234\nmm v,64,v,132,a,20\nv_and_b32 v234, 15, v234\n"
"v_lshlrev_b32 v235, 2, v235\nmm v,96,v,128,a,24\nrt v,150,200,6656\nv_sub_u32 v234, v234, v235\nmm v,96,v,132,a,28\nv_mov_b32 v235, 0\n"
"v_mov_b32 v236, 0xff800000\nwl 6\nmm v,4,v,136,a,0\nrt v,152,201,512\nv_cmp_le_i32 vcc, 0, v234\ns_nop 1\n"
"v_cndmask_b32 v184, v236, v235, vcc\nwl 5\nmm v,4,v,140,a,4\nv_cmp_le_i32 vcc, 1, v234\ns_nop 1\nv_cndmask_b32 v185, v236, v235, vcc\n"
"mm v,36,v,136,a,8\ndc 232,40,54,1024\nrt v,154,201,2560\nmm v,36,v,140,a,12\nv_cmp_le_i32 vcc, 2, v234\ns_nop 1\n"
"v_cndmask_b32 v186, v236, v235, vcc\nmm v,68,v,136,a,16\nrt v,156,201,4608\nv_cmp_le_i32 vcc, 3, v234\ns_nop 1\nv_cndmask_b32 v187, v236, v235, vcc\n"
"mm v,68,v,140,a,20\nv_mov_b32 v176, 0\nv_mov_b32 v177, 0\nmm v,100,v,136,a,24\nrt v,158,201,6656\nv_mov_b32 v178, 0\n"
"mm v,100,v,140,a,28\ndc 232,40,54,2048\nv_mov_b32 v179, 0\nwl 6\nmm v,8,v,144,a,0\nrt v,128,200,8192\n"
"v_mov_b32 v180, 0\nwl 5\nmm v,8,v,148,a,4\nv_mov_b32 v181, 0\nv_mov_b32 v182, 0\nmm v,40,v,144,a,8\n"
"rt v,130,200,10240\nv_mov_b32 v183, 0\nmm v,40,v,148,a,12\nmm v,72,v,144,a,16\nrt v,132,200,12288\nmm v,72,v,148,a,20\n"
"mm v,104,v,144,a,24\nrt v,134,200,14336\nmm v,104,v,148,a,28\ndc 232,40,54,3072\nwl 6\nmm v,12,v,152,a,0\n"
"rt v,136,201,8192\nwl 5\nmm v,12,v,156,a,4\nmm v,44,v,152,a,8\nrt v,138,201,10240\nmm v,44,v,156,a,12\n"
"mm v,76,v,152,a,16\nrt v,140,201,12288\nmm v,76,v,156,a,20\nmm v,108,v,152,a,24\nrt v,142,201,14336\nsa m0,m0,0x1000\n"
"s_nop 0\ndc 233,40,54,0\nmm v,108,v,156,a,28\nwl 6\nmm v,16,v,128,a,0\nrt v,144,200,8704\n"
"wl 5\nmm v,16,v,132,a,4\nmm v,48,v,128,a,8\nrt v,146,200,10752\nmm v,48,v,132,a,12\nmm v,80,v,128,a,16\n"
"rt v,148,200,12800\nmm v,80,v,132,a,20\ndc 233,40,54,1024\nmm v,112,v,128,a,24\nrt v,150,200,14848\nmm v,112,v,132,a,28\n"
"wl 6\nmm v,20,v,136,a,0\nrt v,152,201,8704\nwl 5\nmm v,20,v,140,a,4\nmm v,52,v,136,a,8\n"
"rt v,154,201,10752\nmm v,52,v,140,a,12\nmm v,84,v,136,a,16\nrt v,156,201,12800\nmm v,84,v,140,a,20\ndc 233,40,54,2048\n"
"mm v,116,v,136,a,24\nrt v,158,201,14848\nmm v,116,v,140,a,28\nwl 6\nmm v,24,v,144,a,0\nwl 4\n"
"mm v,24,v,148,a,4\nmm v,56,v,144,a,8\nwl 0\ns_barrier\nmm v,56,v,148,a,12\nrt v,128,200,16384\n"
"mm v,88,v,144,a,16\nrt v,130,200,18432\ndc 233,40,54,3072\nmm v,88,v,148,a,20\nrt v,132,200,20480\nmm v,120,v,144,a,24\n"
"rt v,134,200,22528\nmm v,120,v,148,a,28\nrt v,136,201,16384\nmm v,28,v,152,a,0\nrt v,138,201,18432\nmm v,28,v,156,a,4\n"
"rt v,140,201,20480\nmm v,60,v,152,a,8\nrt v,142,201,22528\nmm v,60,v,156,a,12\nmm v,92,v,152,a,16\nmm v,92,v,156,a,20\n"
"mm v,124,v,152,a,24\nmm v,124,v,156,a,28\nwl 6\nmm v,0,v,128,a,32\nrt v,144,200,16896\nwl 5\n"
"mm v,0,v,132,a,36\nsa s53,s53,0x10000\ns_xor_b32 m0, s57, 0x8000\ns_nop 0\ndc 230,40,53,0\nmm v,32,v,128,a,40\n"
"rt v,146,200,18944\nmm v,32,v,132,a,44\nmm v,64,v,128,a,48\nrt v,148,200,20992\nmm v,64,v,132,a,52\nmm v,96,v,128,a,56\n"
"rt v,150,200,23040\nmm v,96,v,132,a,60\nwl 6\nmm v,4,v,136,a,32\nrt v,152,201,16896\ndc 230,40,53,1024\n"
"wl 5\nmm v,4,v,140,a,36\nmm v,36,v,136,a,40\nrt v,154,201,18944\nmm v,36,v,140,a,44\nmm v,68,v,136,a,48\n"
"rt v,156,201,20992\nmm v,68,v,140,a,52\nmm v,100,v,136,a,56\nrt v,158,201,23040\nmm v,100,v,140,a,60\ndc 230,40,53,2048\n"
"wl 6\nmm v,8,v,144,a,32\nrt v,128,200,24576\nwl 5\nmm v,8,v,148,a,36\nmm v,40,v,144,a,40\n"
"rt v,130,200,26624\nmm v,40,v,148,a,44\nmm v,72,v,144,a,48\nrt v,132,200,28672\nmm v,72,v,148,a,52\ncv 0,0,1\n"
"cv 1,2,3\ncv 2,4,5\ncv 3,6,7\nv_accvgpr_write_b32 a0, v0\nv_accvgpr_write_b32 a1, v1\nv_accvgpr_write_b32 a2, v2\n"
"v_accvgpr_write_b32 a3, v3\ncv 32,32,33\ncv 33,34,35\ncv 34,36,37\ncv 35,38,39\nv_accvgpr_write_b32 a16, v32\n"
"v_accvgpr_write_b32 a17, v33\nv_accvgpr_write_b32 a18, v34\nv_accvgpr_write_b32 a19, v35\ncv 64,64,65\ncv 65,66,67\ncv 66,68,69\n"
"cv 67,70,71\nv_accvgpr_write_b32 a160, v64\nv_accvgpr_write_b32 a161, v65\nv_accvgpr_write_b32 a162, v66\nv_accvgpr_write_b32 a163, v67\ncv 96,96,97\n"
"cv 97,98,99\ncv 98,100,101\ncv 99,102,103\nv_accvgpr_write_b32 a176, v96\nmm v,104,v,144,a,56\nv_accvgpr_write_b32 a177, v97\n"
"v_accvgpr_write_b32 a178, v98\nmm v,104,v,148,a,60\nrt v,134,200,30720\nv_accvgpr_write_b32 a179, v99\nwl 6\nmm v,12,v,152,a,32\n"
"rt v,136,201,24576\ndc 230,40,53,3072\nwl 5\nmm v,12,v,156,a,36\nmm v,44,v,152,a,40\nrt v,138,201,26624\n"
"mm v,44,v,156,a,44\nmm v,76,v,152,a,48\nrt v,140,201,28672\nmm v,76,v,156,a,52\nmm v,108,v,152,a,56\nrt v,142,201,30720\n"
"sa m0,m0,0x1000\ns_nop 0\ndc 231,40,53,0\nmm v,108,v,156,a,60\nwl 6\nmm v,16,v,128,a,32\n"
"rt v,144,200,25088\nwl 5\nmm v,16,v,132,a,36\nmm v,48,v,128,a,40\nrt v,146,200,27136\nmm v,48,v,132,a,44\n"
"mm v,80,v,128,a,48\nrt v,148,200,29184\nmm v,80,v,132,a,52\ncv 8,8,9\ncv 9,10,11\ncv 10,12,13\n"
"cv 11,14,15\nv_accvgpr_write_b32 a4, v8\nv_accvgpr_write_b32 a5, v9\nv_accvgpr_write_b32 a6, v10\nv_accvgpr_write_b32 a7, v11\ncv 40,40,41\n"
"cv 41,42,43\ncv 42,44,45\ncv 43,46,47\nv_accvgpr_write_b32 a20, v40\nv_accvgpr_write_b32 a21, v41\nv_accvgpr_write_b32 a22, v42\n"
"v_accvgpr_write_b32 a23, v43\ncv 72,72,73\ncv 73,74,75\nmm v,112,v,128,a,56\ncv 74,76,77\ncv 75,78,79\n"
"mm v,112,v,132,a,60\nrt v,150,200,31232\ndc 231,40,53,1024\nwl 6\nmm v,20,v,136,a,32\nv_accvgpr_write_b32 a164, v72\n"
"v_accvgpr_write_b32 a165, v73\nwl 4\nmm v,20,v,140,a,36\nrt v,152,201,25088\nv_accvgpr_write_b32 a166, v74\nmm v,52,v,136,a,40\n"
"v_accvgpr_write_b32 a167, v75\ncv 104,104,105\nmm v,52,v,140,a,44\nrt v,154,201,27136\ncv 105,106,107\nmm v,84,v,136,a,48\n"
"cv 106,108,109\ncv 107,110,111\nv_accvgpr_write_b32 a180, v104\nmm v,84,v,140,a,52\nrt v,156,201,29184\nv_accvgpr_write_b32 a181, v105\n"
"mm v,116,v,136,a,56\nv_accvgpr_write_b32 a182, v106\nv_accvgpr_write_b32 a183, v107\nmm v,116,v,140,a,60\nrt v,158,201,31232\ndc 231,40,53,2048\n"
"wl 6\nmm v,24,v,144,a,32\nwl 4\nmm v,24,v,148,a,36\nmm v,56,v,144,a,40\nmm v,56,v,148,a,44\n"
"mm v,88,v,144,a,48\ndc 231,40,53,3072\nmm v,88,v,148,a,52\ncv 16,16,17\ncv 17,18,19\ncv 18,20,21\n"
"cv 19,22,23\nv_accvgpr_write_b32 a8, v16\nv_accvgpr_write_b32 a9, v17\nv_accvgpr_write_b32 a10, v18\nv_accvgpr_write_b32 a11, v19\ncv 48,48,49\n"
"cv 49,50,51\ncv 50,52,53\nmm v,120,v,144,a,56\ncv 51,54,55\nv_accvgpr_write_b32 a24, v48\nmm v,120,v,148,a,60\n"
"v_accvgpr_write_b32 a25, v49\nv_accvgpr_write_b32 a26, v50\nwl 2\nmm v,28,v,152,a,32\nv_accvgpr_write_b32 a27, v51\ncv 80,80,81\n"
"wl 0\nmm v,28,v,156,a,36\ncv 81,82,83\ncv 82,84,85\ncv 83,86,87\nmm v,60,v,152,a,40\n"
"v_accvgpr_write_b32 a168, v80\nv_accvgpr_write_b32 a169, v81\nmm v,60,v,156,a,44\nv_accvgpr_write_b32 a170, v82\nv_accvgpr_write_b32 a171, v83\nmm v,92,v,152,a,48\n"
"cv 112,112,113\ncv 113,114,115\nmm v,92,v,156,a,52\ncv 114,116,117\ncv 115,118,119\nmm v,124,v,152,a,56\n"
"v_accvgpr_write_b32 a184, v112\nv_accvgpr_write_b32 a185, v113\nmm v,124,v,156,a,60\nv_accvgpr_write_b32 a186, v114\nv_accvgpr_write_b32 a187, v115\nv_readlane_b32 s68, v250, 0\n"
"v_readlane_b32 s69, v250, 1\nv_readlane_b32 s70, v250, 2\nv_readlane_b32 s71, v250, 3\nv_readlane_b32 s72, v250, 4\nv_readlane_b32 s73, v250, 5\nv_readlane_b32 s74, v250, 6\n"
"v_readlane_b32 s75, v250, 7\nv_readlane_b32 s76, v250, 8\nv_readlane_b32 s77, v250, 9\nv_readlane_b32 s78, v250, 10\nv_readlane_b32 s79, v250, 11\nv_readlane_b32 s80, v250, 12\n"
"v_readlane_b32 s81, v250, 13\nv_readlane_b32 s82, v250, 14\nv_readlane_b32 s83, v250, 15\ns_mov_b64 s[88:89], s[20:21]\ns_and_b32 s89, s89, 0xffff\ns_mov_b32 s90, 134217728\n"
"s_mov_b32 s91, 0x20000\nv_lshrrev_b32 v246, 13, v203\nv_and_b32 v247, 127, v203\ns_lshl_b32 s85, s52, 13\nv_add_u32 v248, s85, v200\nv_add_u32 v249, s85, v201\n"
"s_mov_b32 s56, 0x70000000\ns_nop 4\ncv 24,24,25\ncv 25,26,27\ncv 26,28,29\ncv 27,30,31\n"
"v_accvgpr_write_b32 a12, v24\nv_accvgpr_write_b32 a13, v25\nv_accvgpr_write_b32 a14, v26\nv_accvgpr_write_b32 a15, v27\ncv 56,56,57\ncv 57,58,59\n"
"cv 58,60,61\ncv 59,62,63\nv_accvgpr_write_b32 a28, v56\nv_accvgpr_write_b32 a29, v57\nv_accvgpr_write_b32 a30, v58\nv_accvgpr_write_b32 a31, v59\n"
"cv 88,88,89\ncv 89,90,91\ncv 90,92,93\ncv 91,94,95\nv_accvgpr_write_b32 a172, v88\nv_accvgpr_write_b32 a173, v89\n"
"v_accvgpr_write_b32 a174, v90\nv_accvgpr_write_b32 a175, v91\ncv 120,120,121\ncv 121,122,123\ncv 122,124,125\ncv 123,126,127\n"
"v_accvgpr_write_b32 a188, v120\nv_accvgpr_write_b32 a189, v121\nv_accvgpr_write_b32 a190, v122\nv_accvgpr_write_b32 a191, v123\nwv 18\ns_barrier\n"
"s_cmp_lt_u32 s63, 2\ns_cbranch_scc1 NORP\nNORP:\ns_mov_b32 s92, 5\nrd a,32,208,0\nrd a,36,208,1024\n"
"rd a,40,208,2048\nrd a,44,208,3072\nrd a,48,208,4096\nrd a,52,208,5120\nrd a,56,208,6144\nrd a,60,208,7168\n"
"wl 7\nmz v,80,a,32,a,160\nv_mov_b32 v204, 0\nv_mov_b32 v205, 0\nwl 6\nmm v,80,a,36,a,164\n"
"v_mov_b32 v206, 0\nv_mov_b32 v207, 0\nwl 5\nmm v,80,a,40,a,168\nwl 4\nmm v,80,a,44,a,172\n"
"mz v,64,a,32,a,0\nrd a,64,208,8192\nmm v,64,a,36,a,4\nrd a,68,208,9216\nmm v,64,a,40,a,8\nrd a,72,208,10240\n"
"mm v,64,a,44,a,12\nrd a,76,208,11264\nmz v,72,a,32,a,16\nrd a,80,208,12288\nmm v,72,a,36,a,20\nrd a,84,208,13312\n"
"mm v,72,a,40,a,24\nrd a,88,208,14336\nmm v,72,a,44,a,28\nrd a,92,208,15360\nmz v,88,a,32,a,176\nmm v,88,a,36,a,180\n"
"mm v,88,a,40,a,184\nmm v,88,a,44,a,188\ns_or_b32 s64, s63, s52\ns_cbranch_scc1 PRE_NOTRI\nad 80,80,184\nad 81,81,185\n"
"ad 82,82,186\nad 83,83,187\nPRE_NOTRI:\ns_nop 1\nv_max3_f32 v236, v80, v81, v82\nv_max_f32 v236, v236, v83\n"
"v_max3_f32 v234, v64, v65, v66\nv_max_f32 v234, v234, v67\nv_max3_f32 v235, v72, v73, v74\nv_max_f32 v235, v235, v75\nv_max3_f32 v237, v88, v89, v90\nv_max_f32 v237, v237, v91\n"
"v_mov_b32 v188, v234\nv_mov_b32 v189, v235\ns_nop 0\nv_permlane32_swap_b32 v234, v188\nv_permlane32_swap_b32 v235, v189\nv_max_f32 v234, v234, v188\n"
"v_max_f32 v235, v235, v189\nv_mov_b32 v188, v234\nv_mov_b32 v189, v235\ns_nop 0\nv_permlane16_swap_b32 v234, v188\nv_permlane16_swap_b32 v235, v189\n"
"v_max_f32 v234, v234, v188\nv_max_f32 v235, v235, v189\nv_mov_b32 v190, v236\nv_mov_b32 v191, v237\ns_nop 0\nv_permlane32_swap_b32 v236, v190\n"
"v_permlane32_swap_b32 v237, v191\nv_max_f32 v236, v236, v190\nv_max_f32 v237, v237, v191\nv_mov_b32 v190, v236\nv_mov_b32 v191, v237\ns_nop 0\n"
"v_permlane16_swap_b32 v236, v190\nv_permlane16_swap_b32 v237, v191\nv_max_f32 v236, v236, v190\nv_max_f32 v237, v237, v191\nv_sub_f32 v160, 0, v234\nv_sub_f32 v161, 0, v234\n"
"v_sub_f32 v162, 0, v234\nv_sub_f32 v163, 0, v234\nv_sub_f32 v164, 0, v235\nv_sub_f32 v165, 0, v235\nv_sub_f32 v166, 0, v235\nv_sub_f32 v167, 0, v235\n"
"v_sub_f32 v168, 0, v236\nv_sub_f32 v169, 0, v236\nv_sub_f32 v170, 0, v236\nv_sub_f32 v171, 0, v236\nv_sub_f32 v172, 0, v237\nv_sub_f32 v173, 0, v237\n"
"v_sub_f32 v174, 0, v237\nv_sub_f32 v175, 0, v237\ns_nop 0\nwl 11\nmc v,68,a,48,a,0,160\nsb 64,64,234\n"
"sb 65,65,234\nwl 10\nmm v,68,a,52,a,4\nsb 66,66,234\nsb 67,67,234\nwl 9\n"
"mm v,68,a,56,a,8\nsb 72,72,235\nsb 73,73,235\nwl 8\nmm v,68,a,60,a,12\nsb 74,74,235\n"
"sb 75,75,235\nmc v,76,a,48,a,16,164\nsb 80,80,236\nsb 81,81,236\nmm v,76,a,52,a,20\nsb 82,82,236\n"
"sb 83,83,236\nmm v,76,a,56,a,24\nsb 88,88,237\nsb 89,89,237\nmm v,76,a,60,a,28\nsb 90,90,237\n"
"sb 91,91,237\nmc v,84,a,48,a,160,168\nmm v,84,a,52,a,164\nmm v,84,a,56,a,168\nmm v,84,a,60,a,172\nmc v,92,a,48,a,176,172\n"
"mm v,92,a,52,a,180\nmm v,92,a,56,a,184\nmm v,92,a,60,a,188\ns_nop 2\nwl 0\ns_cmp_eq_u32 s63, 0\n"
"s_cbranch_scc1 ZERO_S\ns_sub_u32 s58, s63, 1\nmc v,96,a,64,a,0,160\nex 188,64\nex 189,65\nmm v,96,a,68,a,4\n"
"ex 190,66\nad 196,188,189\ncv 128,188,189\nmm v,96,a,72,a,8\nex 191,67\nad 196,196,190\n"
"mm v,96,a,76,a,12\nex 192,68\nad 196,196,191\ncv 129,190,191\nmc v,100,a,80,a,0,160\nex 193,69\n"
"ad 196,196,192\nex 194,70\nmm v,100,a,84,a,4\ns_mov_b32 s54, s53\nad 196,196,193\ncv 130,192,193\n"
"mm v,100,a,88,a,8\nex 195,71\nad 196,196,194\nex 188,72\nmm v,100,a,92,a,12\nad 196,196,195\n"
"cv 131,194,195\nex 189,73\nmc v,104,a,64,a,16,164\nex 190,74\nad 197,188,189\nmm v,104,a,68,a,20\n"
"cv 132,188,189\nex 191,75\nad 197,197,190\nmm v,104,a,72,a,24\nex 192,76\nad 197,197,191\n"
"cv 133,190,191\nmm v,104,a,76,a,28\nsa s53,s53,0x10000\nex 193,77\nad 197,197,192\nmc v,108,a,80,a,16,164\n"
"ex 194,78\nad 197,197,193\ncv 134,192,193\nmm v,108,a,84,a,20\nex 195,79\nad 197,197,194\n"
"mm v,108,a,88,a,24\nex 188,80\nad 197,197,195\ncv 135,194,195\nmm v,108,a,92,a,28\nex 189,81\n"
"ex 190,82\nmc v,112,a,64,a,160,168\nad 198,188,189\ncv 136,188,189\nex 191,83\nmm v,112,a,68,a,164\n"
"rd a,32,208,16384\ns_cmp_eq_u32 s53, s55\ns_cselect_b32 s53, s56, s53\nad 198,198,190\nmm v,112,a,72,a,168\nrd a,36,208,17408\n"
"ex 192,84\nad 198,198,191\nmm v,112,a,76,a,172\nrd a,40,208,18432\ncv 137,190,191\nex 193,85\n"
"mc v,116,a,80,a,160,168\nrd a,44,208,19456\nad 198,198,192\nex 194,86\nmm v,116,a,84,a,164\nrd a,48,208,20480\n"
"ad 198,198,193\ncv 138,192,193\nmm v,116,a,88,a,168\nrd a,52,208,21504\nex 195,87\nad 198,198,194\n"
"mm v,116,a,92,a,172\nrd a,56,208,22528\nex 188,88\nad 198,198,195\ncv 139,194,195\nmc v,120,a,64,a,176,172\n"
"rd a,60,208,23552\nex 189,89\nmm v,120,a,68,a,180\nex 190,90\nad 199,188,189\ncv 140,188,189\n"
"mm v,120,a,72,a,184\nex 191,91\nad 199,199,190\nex 192,92\nmm v,120,a,76,a,188\nad 199,199,191\n"
"cv 141,190,191\nex 193,93\nmc v,124,a,80,a,176,172\nad 199,199,192\nex 194,94\nmm v,124,a,84,a,180\n"
"ad 199,199,193\ncv 142,192,193\nex 195,95\nmm v,124,a,88,a,184\nad 199,199,194\ns_nop 0\n"
"ad 199,199,195\ncv 143,194,195\nad 238,196,197\nad 239,198,199\nad 238,238,239\nv_cmp_nge_f32 vcc, s59, v238\n"
"mm v,124,a,92,a,188\ns_waitcnt vmcnt(8) lgkmcnt(0)\ns_barrier\nv_xor_b32 v209, 0x8000, v209\ns_cbranch_vccnz RS_0\nRR_0:\n"
"mc v,64,a,32,a,0,160\nad 204,204,196\nad 205,205,197\nmm v,64,a,36,a,4\nrd a,96,209,0\nad 206,206,198\n"
"ad 207,207,199\nmm v,64,a,40,a,8\nex 188,96\nmm v,64,a,44,a,12\ns_xor_b32 m0, s57, 0x18000\ns_nop 0\n"
"dc 232,40,54,0\nmc v,68,a,48,a,0,160\nrd a,104,209,4096\nv_mov_b32 v240, s46\nv_mov_b32 v241, s47\ns_mov_b64 exec, 1\n"
"buffer_store_dwordx2 v[240:241], v245, s[96:99], s33 offen sc1\ns_mov_b64 exec, -1\nmm v,68,a,52,a,4\nex 189,97\nmm v,68,a,56,a,8\nex 190,98\n"
"mm v,68,a,60,a,12\nrd a,112,209,8192\nad 200,188,189\nmc v,72,a,32,a,16,164\ncv 144,188,189\nex 191,99\n"
"mm v,72,a,36,a,20\nad 200,200,190\nex 192,100\nmm v,72,a,40,a,24\nad 200,200,191\ncv 145,190,191\n"
"mm v,72,a,44,a,28\nrd a,120,209,12288\nex 193,101\nmc v,76,a,48,a,16,164\nad 200,200,192\nex 194,102\n"
"mm v,76,a,52,a,20\ndc 232,40,54,1024\nad 200,200,193\nmm v,76,a,56,a,24\nrd a,128,209,16384\ncv 146,192,193\n"
"mm v,76,a,60,a,28\nex 195,103\nmc v,80,a,32,a,160,168\nad 200,200,194\nex 188,104\nmm v,80,a,36,a,164\n"
"rd a,136,209,20480\nad 200,200,195\nmm v,80,a,40,a,168\ncv 147,194,195\nex 189,105\nmm v,80,a,44,a,172\n"
"ex 190,106\nmc v,84,a,48,a,160,168\nad 201,188,189\ncv 148,188,189\nmm v,84,a,52,a,164\nrd a,144,209,24576\n"
"ex 191,107\nmm v,84,a,56,a,168\nad 201,201,190\nex 192,108\nmm v,84,a,60,a,172\ndc 232,40,54,2048\n"
"ad 201,201,191\nmc v,88,a,32,a,176,172\nrd a,152,209,28672\ncv 149,190,191\nmm v,88,a,36,a,180\nex 193,109\n"
"mm v,88,a,40,a,184\nad 201,201,192\nex 194,110\nmm v,88,a,44,a,188\nad 201,201,193\ncv 150,192,193\n"
"mc v,92,a,48,a,176,172\nex 195,111\nmm v,92,a,52,a,180\nad 201,201,194\nex 188,112\nmm v,92,a,56,a,184\n"
"ad 201,201,195\ncv 151,194,195\nmm v,92,a,60,a,188\nwl 0\nex 189,113\nmz v,0,a,96,v,128\n"
"ex 190,114\nmz v,32,a,96,v,132\nrd a,64,208,24576\ndc 232,40,54,3072\nmz a,192,a,96,v,136\nad 202,188,189\n"
"cv 152,188,189\nmz a,224,a,96,v,140\nex 191,115\nmz v,4,a,104,v,128\nrd a,68,208,25600\nad 202,202,190\n"
"mz v,36,a,104,v,132\nex 192,116\nmz a,196,a,104,v,136\nad 202,202,191\ncv 153,190,191\nmz a,228,a,104,v,140\n"
"rd a,72,208,26624\nex 193,117\nmz v,8,a,112,v,128\nad 202,202,192\nex 194,118\nmz v,40,a,112,v,132\n"
"ad 202,202,193\ncv 154,192,193\nmz a,200,a,112,v,136\nrd a,76,208,27648\nex 195,119\nmz a,232,a,112,v,140\n"
"sa m0,m0,0x1000\ns_nop 0\ndc 233,40,54,0\nmz v,12,a,120,v,128\nad 202,202,194\nex 188,120\n"
"mz v,44,a,120,v,132\nrd a,80,208,28672\nad 202,202,195\nmz a,204,a,120,v,136\ncv 155,194,195\nex 189,121\n"
"mz a,236,a,120,v,140\nex 190,122\nmz v,16,a,128,v,128\nrd a,84,208,29696\nad 203,188,189\nmz v,48,a,128,v,132\n"
"cv 156,188,189\nex 191,123\nmz a,208,a,128,v,136\nad 203,203,190\nex 192,124\nmz a,240,a,128,v,140\n"
"rd a,88,208,30720\nad 203,203,191\nmz v,20,a,136,v,128\ncv 157,190,191\nex 193,125\nmz v,52,a,136,v,132\n"
"dc 233,40,54,1024\nad 203,203,192\nmz a,212,a,136,v,136\nrd a,92,208,31744\nex 194,126\nmz a,244,a,136,v,140\n"
"ad 203,203,193\ncv 158,192,193\nmz v,24,a,144,v,128\nex 195,127\nmz v,56,a,144,v,132\nad 203,203,194\n"
"s_nop 0\nmz a,216,a,144,v,136\nad 203,203,195\ncv 159,194,195\nmz a,248,a,144,v,140\nad 238,200,201\n"
"ad 239,202,203\nmz v,28,a,152,v,128\nad 238,238,239\nv_cmp_nge_f32 vcc, s59, v238\nmz v,60,a,152,v,132\nmz a,220,a,152,v,136\n"
"mz a,252,a,152,v,140\nwl 0\ns_cbranch_vccnz RS_1\nRR_1:\nmc v,96,a,64,a,0,160\nad 204,204,200\n"
"ad 205,205,201\nmm v,96,a,68,a,4\nrd a,100,209,1024\nad 206,206,202\nmm v,96,a,72,a,8\nad 207,207,203\n"
"ex 188,64\nmm v,96,a,76,a,12\ndc 233,40,54,2048\nex 189,65\nmc v,100,a,80,a,0,160\nrd a,108,209,5120\n"
"ex 190,66\nmm v,100,a,84,a,4\nad 196,188,189\ncv 128,188,189\nmm v,100,a,88,a,8\nex 191,67\n"
"mm v,100,a,92,a,12\nrd a,116,209,9216\nad 196,196,190\nmc v,104,a,64,a,16,164\nex 192,68\nmm v,104,a,68,a,20\n"
"ad 196,196,191\ncv 129,190,191\nmm v,104,a,72,a,24\nex 193,69\nmm v,104,a,76,a,28\nrd a,124,209,13312\n"
"ad 196,196,192\nmc v,108,a,80,a,16,164\nex 194,70\nmm v,108,a,84,a,20\nad 196,196,193\ncv 130,192,193\n"
"mm v,108,a,88,a,24\nrd a,132,209,17408\nex 195,71\nmm v,108,a,92,a,28\nad 196,196,194\nex 188,72\n"
"mc v,112,a,64,a,160,168\ndc 233,40,54,3072\nad 196,196,195\nmm v,112,a,68,a,164\nrd a,140,209,21504\ncv 131,194,195\n"
"mm v,112,a,72,a,168\nex 189,73\nmm v,112,a,76,a,172\nex 190,74\nmc v,116,a,80,a,160,168\nad 197,188,189\n"
"cv 132,188,189\nmm v,116,a,84,a,164\nrd a,148,209,25600\nex 191,75\nmm v,116,a,88,a,168\nad 197,197,190\n"
"ex 192,76\nmm v,116,a,92,a,172\nad 197,197,191\ncv 133,190,191\nmc v,120,a,64,a,176,172\nrd a,156,209,29696\n"
"ex 193,77\nmm v,120,a,68,a,180\nad 197,197,192\nex 194,78\nmm v,120,a,72,a,184\nad 197,197,193\n"
"cv 134,192,193\nmm v,120,a,76,a,188\nex 195,79\nmc v,124,a,80,a,176,172\nad 197,197,194\nex 188,80\n"
"mm v,124,a,84,a,180\nad 197,197,195\ncv 135,194,195\nmm v,124,a,88,a,184\nex 189,81\nmm v,124,a,92,a,188\n"
"s_waitcnt vmcnt(9) lgkmcnt(0)\ns_barrier\nv_xor_b32 v208, 0x8000, v208\nmm v,0,a,100,v,144\nex 190,82\nmm v,32,a,100,v,148\n"
"rd a,32,208,0\nad 198,188,189\nmm a,192,a,100,v,152\ncv 136,188,189\nex 191,83\nmm a,224,a,100,v,156\n"
"s_mov_b32 m0, s57\ns_nop 0\ndc 230,40,53,0\nmm v,4,a,108,v,144\nrd a,36,208,1024\nad 198,198,190\n"
"mm v,36,a,108,v,148\nex 192,84\nmm a,196,a,108,v,152\nad 198,198,191\ncv 137,190,191\nmm a,228,a,108,v,156\n"
"rd a,40,208,2048\nex 193,85\nmm v,8,a,116,v,144\nad 198,198,192\nex 194,86\nmm v,40,a,116,v,148\n"
"ad 198,198,193\ncv 138,192,193\nmm a,200,a,116,v,152\nrd a,44,208,3072\nex 195,87\nmm a,232,a,116,v,156\n"
"ad 198,198,194\nex 188,88\nmm v,12,a,124,v,144\ndc 230,40,53,1024\nad 198,198,195\nmm v,44,a,124,v,148\n"
"rd a,48,208,4096\ncv 139,194,195\nmm a,204,a,124,v,152\nex 189,89\nmm a,236,a,124,v,156\nex 190,90\n"
"mm v,16,a,132,v,144\nrd a,52,208,5120\nad 199,188,189\nmm v,48,a,132,v,148\ncv 140,188,189\nex 191,91\n"
"mm a,208,a,132,v,152\nad 199,199,190\nex 192,92\nmm a,240,a,132,v,156\nrd a,56,208,6144\nad 199,199,191\n"
"mm v,20,a,140,v,144\ncv 141,190,191\nex 193,93\nmm v,52,a,140,v,148\nad 199,199,192\nex 194,94\n"
"mm a,212,a,140,v,152\nrd a,60,208,7168\ndc 230,40,53,2048\nmm a,244,a,140,v,156\nad 199,199,193\ncv 142,192,193\n"
"mm v,24,a,148,v,144\nex 195,95\nmm v,56,a,148,v,148\nad 199,199,194\ns_nop 0\nmm a,216,a,148,v,152\n"
"ad 199,199,195\ncv 143,194,195\nmm a,248,a,148,v,156\nad 238,196,197\nad 239,198,199\nmm v,28,a,156,v,144\n"
"ad 238,238,239\nv_cmp_nge_f32 vcc, s59, v238\nmm v,60,a,156,v,148\nmm a,220,a,156,v,152\nmm a,252,a,156,v,156\nwl 0\n"
"s_cbranch_vccnz RS_2\nRR_2:\nmc v,64,a,32,a,0,160\nad 204,204,196\nad 205,205,197\nmm v,64,a,36,a,4\n"
"rd a,96,209,2048\nad 206,206,198\nmm v,64,a,40,a,8\nad 207,207,199\nex 188,96\nmm v,64,a,44,a,12\n"
"dc 230,40,53,3072\nex 189,97\nmc v,68,a,48,a,0,160\nrd a,104,209,6144\nex 190,98\nmm v,68,a,52,a,4\n"
"ad 200,188,189\ncv 144,188,189\nmm v,68,a,56,a,8\nex 191,99\nmm v,68,a,60,a,12\nrd a,112,209,10240\n"
"ad 200,200,190\nmc v,72,a,32,a,16,164\nex 192,100\nmm v,72,a,36,a,20\nad 200,200,191\ncv 145,190,191\n"
"mm v,72,a,40,a,24\nex 193,101\nmm v,72,a,44,a,28\nrd a,120,209,14336\nad 200,200,192\nmc v,76,a,48,a,16,164\n"
"ex 194,102\nmm v,76,a,52,a,20\nad 200,200,193\ncv 146,192,193\nmm v,76,a,56,a,24\nrd a,128,209,18432\n"
"sa m0,m0,0x1000\ns_nop 0\ndc 231,40,53,0\nmm v,76,a,60,a,28\nex 195,103\nmc v,80,a,32,a,160,168\n"
"ad 200,200,194\nex 188,104\nmm v,80,a,36,a,164\nrd a,136,209,22528\nad 200,200,195\nmm v,80,a,40,a,168\n"
"cv 147,194,195\nex 189,105\nmm v,80,a,44,a,172\nex 190,106\nmc v,84,a,48,a,160,168\nad 201,188,189\n"
"cv 148,188,189\nmm v,84,a,52,a,164\nrd a,144,209,26624\nex 191,107\nmm v,84,a,56,a,168\nad 201,201,190\n"
"ex 192,108\nmm v,84,a,60,a,172\nad 201,201,191\ncv 149,190,191\nmc v,88,a,32,a,176,172\nrd a,152,209,30720\n"
"ex 193,109\nmm v,88,a,36,a,180\ndc 231,40,53,1024\nad 201,201,192\nmm v,88,a,40,a,184\nex 194,110\n"
"mm v,88,a,44,a,188\nad 201,201,193\ncv 150,192,193\nmc v,92,a,48,a,176,172\nex 195,111\nmm v,92,a,52,a,180\n"
"ad 201,201,194\nex 188,112\nmm v,92,a,56,a,184\nad 201,201,195\ncv 151,194,195\nmm v,92,a,60,a,188\n"
"wl 0\nex 189,113\nmm v,0,a,96,v,128\nex 190,114\nmm v,32,a,96,v,132\nrd a,64,208,8192\n"
"ad 202,188,189\nmm a,192,a,96,v,136\ncv 152,188,189\nex 191,115\nmm a,224,a,96,v,140\nad 202,202,190\n"
"ex 192,116\nmm v,4,a,104,v,128\nrd a,68,208,9216\ndc 231,40,53,2048\nmm v,36,a,104,v,132\nad 202,202,191\n"
"cv 153,190,191\nmm a,196,a,104,v,136\nex 193,117\nmm a,228,a,104,v,140\nrd a,72,208,10240\nad 202,202,192\n"
"mm v,8,a,112,v,128\nex 194,118\nmm v,40,a,112,v,132\nad 202,202,193\ncv 154,192,193\nmm a,200,a,112,v,136\n"
"rd a,76,208,11264\nex 195,119\nmm a,232,a,112,v,140\nad 202,202,194\nex 188,120\nmm v,12,a,120,v,128\n"
"ad 202,202,195\ncv 155,194,195\nmm v,44,a,120,v,132\nrd a,80,208,12288\nex 189,121\nmm a,204,a,120,v,136\n"
"ex 190,122\nmm a,236,a,120,v,140\ndc 231,40,53,3072\nad 203,188,189\nmm v,16,a,128,v,128\nrd a,84,208,13312\n"
"cv 156,188,189\nmm v,48,a,128,v,132\nex 191,123\nmm a,208,a,128,v,136\nad 203,203,190\nex 192,124\n"
"mm a,240,a,128,v,140\nrd a,88,208,14336\nad 203,203,191\nmm v,20,a,136,v,128\ncv 157,190,191\nex 193,125\n"
"mm v,52,a,136,v,132\nad 203,203,192\nex 194,126\nmm a,212,a,136,v,136\nrd a,92,208,15360\nad 203,203,193\n"
"mm a,244,a,136,v,140\ncv 158,192,193\nex 195,127\nmm v,24,a,144,v,128\nad 203,203,194\ns_nop 0\n"
"mm v,56,a,144,v,132\nad 203,203,195\ncv 159,194,195\nmm a,216,a,144,v,136\nad 238,200,201\nad 239,202,203\n"
"mm a,248,a,144,v,140\nad 238,238,239\nv_cmp_nge_f32 vcc, s59, v238\nmm v,28,a,152,v,128\nmm v,60,a,152,v,132\ns_xor_b32 s57, s57, 0x8000\n"
"mm a,220,a,152,v,136\ns_sub_u32 s58, s58, 1\nmm a,252,a,152,v,140\nwl 0\ns_cbranch_vccnz RS_3\nRR_3:\n"
"s_cbranch_scc1 DIAG_S\n.p2align 6\nLOOP_S:\ns_sub_u32 s92, s92, 1\ns_cmp_lt_u32 s92, 2\ns_cbranch_scc1 TK_S\n"
"TKR_S:\ns_cmp_eq_u32 s58, 0\ns_cbranch_scc1 RP_ISSUE\nRP_BACK:\nmc v,96,a,64,a,0,160\nad 204,204,200\n"
"ad 205,205,201\nmm v,96,a,68,a,4\nrd a,100,209,3072\nad 206,206,202\nmm v,96,a,72,a,8\nad 207,207,203\n"
"ex 188,64\nmm v,96,a,76,a,12\nex 189,65\nmc v,100,a,80,a,0,160\nrd a,108,209,7168\nex 190,66\n"
"mm v,100,a,84,a,4\ns_mov_b32 s54, s53\nad 196,188,189\nmm v,100,a,88,a,8\ncv 128,188,189\nex 191,67\n"
"mm v,100,a,92,a,12\nrd a,116,209,11264\nad 196,196,190\nmc v,104,a,64,a,16,164\nex 192,68\nmm v,104,a,68,a,20\n"
"ad 196,196,191\ncv 129,190,191\nmm v,104,a,72,a,24\nex 193,69\nmm v,104,a,76,a,28\nrd a,124,209,15360\n"
"sa s53,s53,0x10000\nmc v,108,a,80,a,16,164\nad 196,196,192\nex 194,70\nmm v,108,a,84,a,20\nad 196,196,193\n"
"cv 130,192,193\nmm v,108,a,88,a,24\nrd a,132,209,19456\nex 195,71\nmm v,108,a,92,a,28\nad 196,196,194\n"
"ex 188,72\nmc v,112,a,64,a,160,168\nad 196,196,195\ncv 131,194,195\nmm v,112,a,68,a,164\nrd a,140,209,23552\n"
"s_cmp_eq_u32 s53, s55\ns_cselect_b32 s53, s56, s53\nmm v,112,a,72,a,168\nex 189,73\nmm v,112,a,76,a,172\nex 190,74\n"
"mc v,116,a,80,a,160,168\nad 197,188,189\ncv 132,188,189\nmm v,116,a,84,a,164\nrd a,148,209,27648\nex 191,75\n"
"mm v,116,a,88,a,168\nad 197,197,190\nex 192,76\nmm v,116,a,92,a,172\nad 197,197,191\ncv 133,190,191\n"
"mc v,120,a,64,a,176,172\nrd a,156,209,31744\nex 193,77\nmm v,120,a,68,a,180\nad 197,197,192\nex 194,78\n"
"mm v,120,a,72,a,184\nad 197,197,193\ncv 134,192,193\nmm v,120,a,76,a,188\nex 195,79\nmc v,124,a,80,a,176,172\n"
"ad 197,197,194\nex 188,80\nmm v,124,a,84,a,180\nad 197,197,195\ncv 135,194,195\nmm v,124,a,88,a,184\n"
"ex 189,81\nmm v,124,a,92,a,188\nwl 0\nex 190,82\nmm v,0,a,100,v,144\nad 198,188,189\n"
"cv 136,188,189\nmm v,32,a,100,v,148\nrd a,32,208,16384\nex 191,83\nmm a,192,a,100,v,152\nad 198,198,190\n"
"ex 192,84\nmm a,224,a,100,v,156\nad 198,198,191\ncv 137,190,191\nmm v,4,a,108,v,144\nrd a,36,208,17408\n"
"ex 193,85\nmm v,36,a,108,v,148\nad 198,198,192\nex 194,86\nmm a,196,a,108,v,152\nad 198,198,193\n"
"cv 138,192,193\nmm a,228,a,108,v,156\nrd a,40,208,18432\nex 195,87\nmm v,8,a,116,v,144\nad 198,198,194\n"
"ex 188,88\nmm v,40,a,116,v,148\nad 198,198,195\ncv 139,194,195\nmm a,200,a,116,v,152\nrd a,44,208,19456\n"
"ex 189,89\nmm a,232,a,116,v,156\nex 190,90\nmm v,12,a,124,v,144\nad 199,188,189\ncv 140,188,189\n"
"mm v,44,a,124,v,148\nrd a,48,208,20480\nex 191,91\nmm a,204,a,124,v,152\nad 199,199,190\nex 192,92\n"
"mm a,236,a,124,v,156\nad 199,199,191\ncv 141,190,191\nmm v,16,a,132,v,144\nrd a,52,208,21504\nex 193,93\n"
"mm v,48,a,132,v,148\nad 199,199,192\nex 194,94\nmm a,208,a,132,v,152\nad 199,199,193\ncv 142,192,193\n"
"mm a,240,a,132,v,156\nrd a,56,208,22528\nex 195,95\nmm v,20,a,140,v,144\nad 199,199,194\ns_nop 0\n"
"mm v,52,a,140,v,148\nad 199,199,195\nrd a,60,208,23552\nmm a,212,a,140,v,152\ncv 143,194,195\nad 238,196,197\n"
"mm a,244,a,140,v,156\nad 239,198,199\nad 238,238,239\nmm v,24,a,148,v,144\nv_cmp_nge_f32 vcc, s59, v238\nmm v,56,a,148,v,148\n"
"mm a,216,a,148,v,152\nmm a,248,a,148,v,156\nmm v,28,a,156,v,144\nmm v,60,a,156,v,148\nmm a,220,a,156,v,152\nmm a,252,a,156,v,156\n"
"s_waitcnt vmcnt(8) lgkmcnt(0)\ns_barrier\nv_xor_b32 v209, 0x8000, v209\ns_cbranch_vccnz RS_4\nRR_4:\nmc v,64,a,32,a,0,160\n"
"ad 204,204,196\nad 205,205,197\nmm v,64,a,36,a,4\nrd a,96,209,0\nad 206,206,198\nmm v,64,a,40,a,8\n"
"ad 207,207,199\nex 188,96\nmm v,64,a,44,a,12\ns_xor_b32 m0, s57, 0x18000\ns_nop 0\ndc 232,40,54,0\n"
"mc v,68,a,48,a,0,160\nrd a,104,209,4096\nex 189,97\nmm v,68,a,52,a,4\nex 190,98\nmm v,68,a,56,a,8\n"
"ad 200,188,189\ncv 144,188,189\nmm v,68,a,60,a,12\nrd a,112,209,8192\nex 191,99\nmc v,72,a,32,a,16,164\n"
"ad 200,200,190\nex 192,100\nmm v,72,a,36,a,20\nad 200,200,191\ncv 145,190,191\nmm v,72,a,40,a,24\n"
"ex 193,101\nmm v,72,a,44,a,28\nrd a,120,209,12288\nad 200,200,192\nmc v,76,a,48,a,16,164\nex 194,102\n"
"mm v,76,a,52,a,20\ndc 232,40,54,1024\nad 200,200,193\nmm v,76,a,56,a,24\nrd a,128,209,16384\ncv 146,192,193\n"
"mm v,76,a,60,a,28\nex 195,103\nmc v,80,a,32,a,160,168\nad 200,200,194\nex 188,104\nmm v,80,a,36,a,164\n"
"rd a,136,209,20480\nad 200,200,195\nmm v,80,a,40,a,168\ncv 147,194,195\nex 189,105\nmm v,80,a,44,a,172\n"
"ex 190,106\nmc v,84,a,48,a,160,168\nad 201,188,189\ncv 148,188,189\nmm v,84,a,52,a,164\nrd a,144,209,24576\n"
"ex 191,107\nmm v,84,a,56,a,168\nad 201,201,190\nex 192,108\nmm v,84,a,60,a,172\ndc 232,40,54,2048\n"
"ad 201,201,191\nmc v,88,a,32,a,176,172\nrd a,152,209,28672\ncv 149,190,191\nmm v,88,a,36,a,180\nex 193,109\n"
"mm v,88,a,40,a,184\nad 201,201,192\nex 194,110\nmm v,88,a,44,a,188\nad 201,201,193\ncv 150,192,193\n"
"mc v,92,a,48,a,176,172\nex 195,111\nmm v,92,a,52,a,180\nad 201,201,194\nex 188,112\nmm v,92,a,56,a,184\n"
"ad 201,201,195\ncv 151,194,195\nmm v,92,a,60,a,188\nwl 0\nex 189,113\nmm v,0,a,96,v,128\n"
"ex 190,114\nmm v,32,a,96,v,132\nrd a,64,208,24576\ndc 232,40,54,3072\nmm a,192,a,96,v,136\nad 202,188,189\n"
"cv 152,188,189\nmm a,224,a,96,v,140\nex 191,115\nmm v,4,a,104,v,128\nrd a,68,208,25600\nad 202,202,190\n"
"mm v,36,a,104,v,132\nex 192,116\nmm a,196,a,104,v,136\nad 202,202,191\ncv 153,190,191\nmm a,228,a,104,v,140\n"
"rd a,72,208,26624\nex 193,117\nmm v,8,a,112,v,128\nad 202,202,192\nex 194,118\nmm v,40,a,112,v,132\n"
"ad 202,202,193\ncv 154,192,193\nmm a,200,a,112,v,136\nrd a,76,208,27648\nex 195,119\nmm a,232,a,112,v,140\n"
"sa m0,m0,0x1000\ns_nop 0\ndc 233,40,54,0\nmm v,12,a,120,v,128\nad 202,202,194\nex 188,120\n"
"mm v,44,a,120,v,132\nrd a,80,208,28672\nad 202,202,195\nmm a,204,a,120,v,136\ncv 155,194,195\nex 189,121\n"
"mm a,236,a,120,v,140\nex 190,122\nmm v,16,a,128,v,128\nrd a,84,208,29696\nad 203,188,189\nmm v,48,a,128,v,132\n"
"cv 156,188,189\nex 191,123\nmm a,208,a,128,v,136\nad 203,203,190\nex 192,124\nmm a,240,a,128,v,140\n"
"rd a,88,208,30720\nad 203,203,191\nmm v,20,a,136,v,128\ncv 157,190,191\nex 193,125\nmm v,52,a,136,v,132\n"
"dc 233,40,54,1024\nad 203,203,192\nmm a,212,a,136,v,136\nrd a,92,208,31744\nex 194,126\nmm a,244,a,136,v,140\n"
"ad 203,203,193\ncv 158,192,193\nmm v,24,a,144,v,128\nex 195,127\nmm v,56,a,144,v,132\nad 203,203,194\n"
"s_nop 0\nmm a,216,a,144,v,136\nad 203,203,195\ncv 159,194,195\nmm a,248,a,144,v,140\nad 238,200,201\n"
"ad 239,202,203\nmm v,28,a,152,v,128\nad 238,238,239\nv_cmp_nge_f32 vcc, s59, v238\nmm v,60,a,152,v,132\nmm a,220,a,152,v,136\n"
"mm a,252,a,152,v,140\nwl 0\ns_cbranch_vccnz RS_5\nRR_5:\nmc v,96,a,64,a,0,160\nad 204,204,200\n"
"ad 205,205,201\nmm v,96,a,68,a,4\nrd a,100,209,1024\nad 206,206,202\nmm v,96,a,72,a,8\nad 207,207,203\n"
"ex 188,64\nmm v,96,a,76,a,12\ndc 233,40,54,2048\nex 189,65\nmc v,100,a,80,a,0,160\nrd a,108,209,5120\n"
"ex 190,66\nmm v,100,a,84,a,4\nad 196,188,189\ncv 128,188,189\nmm v,100,a,88,a,8\nex 191,67\n"
"mm v,100,a,92,a,12\nrd a,116,209,9216\nad 196,196,190\nmc v,104,a,64,a,16,164\nex 192,68\nmm v,104,a,68,a,20\n"
"ad 196,196,191\ncv 129,190,191\nmm v,104,a,72,a,24\nex 193,69\nmm v,104,a,76,a,28\nrd a,124,209,13312\n"
"ad 196,196,192\nmc v,108,a,80,a,16,164\nex 194,70\nmm v,108,a,84,a,20\nad 196,196,193\ncv 130,192,193\n"
"mm v,108,a,88,a,24\nrd a,132,209,17408\nex 195,71\nmm v,108,a,92,a,28\nad 196,196,194\nex 188,72\n"
"mc v,112,a,64,a,160,168\ndc 233,40,54,3072\nad 196,196,195\nmm v,112,a,68,a,164\nrd a,140,209,21504\ncv 131,194,195\n"
"mm v,112,a,72,a,168\nex 189,73\nmm v,112,a,76,a,172\nex 190,74\nmc v,116,a,80,a,160,168\nad 197,188,189\n"
"cv 132,188,189\nmm v,116,a,84,a,164\nrd a,148,209,25600\nex 191,75\nmm v,116,a,88,a,168\nad 197,197,190\n"
"ex 192,76\nmm v,116,a,92,a,172\nad 197,197,191\ncv 133,190,191\nmc v,120,a,64,a,176,172\nrd a,156,209,29696\n"
"ex 193,77\nmm v,120,a,68,a,180\nad 197,197,192\nex 194,78\nmm v,120,a,72,a,184\nad 197,197,193\n"
"cv 134,192,193\nmm v,120,a,76,a,188\nex 195,79\nmc v,124,a,80,a,176,172\nad 197,197,194\nex 188,80\n"
"mm v,124,a,84,a,180\nad 197,197,195\ncv 135,194,195\nmm v,124,a,88,a,184\nex 189,81\nmm v,124,a,92,a,188\n"
"s_waitcnt vmcnt(8) lgkmcnt(0)\ns_barrier\nv_xor_b32 v208, 0x8000, v208\nmm v,0,a,100,v,144\nex 190,82\nmm v,32,a,100,v,148\n"
"rd a,32,208,0\nad 198,188,189\nmm a,192,a,100,v,152\ncv 136,188,189\nex 191,83\nmm a,224,a,100,v,156\n"
"s_mov_b32 m0, s57\ns_nop 0\ndc 230,40,53,0\nmm v,4,a,108,v,144\nrd a,36,208,1024\nad 198,198,190\n"
"mm v,36,a,108,v,148\nex 192,84\nmm a,196,a,108,v,152\nad 198,198,191\ncv 137,190,191\nmm a,228,a,108,v,156\n"
"rd a,40,208,2048\nex 193,85\nmm v,8,a,116,v,144\nad 198,198,192\nex 194,86\nmm v,40,a,116,v,148\n"
"ad 198,198,193\ncv 138,192,193\nmm a,200,a,116,v,152\nrd a,44,208,3072\nex 195,87\nmm a,232,a,116,v,156\n"
"ad 198,198,194\nex 188,88\nmm v,12,a,124,v,144\ndc 230,40,53,1024\nad 198,198,195\nmm v,44,a,124,v,148\n"
"rd a,48,208,4096\ncv 139,194,195\nmm a,204,a,124,v,152\nex 189,89\nmm a,236,a,124,v,156\nex 190,90\n"
"mm v,16,a,132,v,144\nrd a,52,208,5120\nad 199,188,189\nmm v,48,a,132,v,148\ncv 140,188,189\nex 191,91\n"
"mm a,208,a,132,v,152\nad 199,199,190\nex 192,92\nmm a,240,a,132,v,156\nrd a,56,208,6144\nad 199,199,191\n"
"mm v,20,a,140,v,144\ncv 141,190,191\nex 193,93\nmm v,52,a,140,v,148\nad 199,199,192\nex 194,94\n"
"mm a,212,a,140,v,152\nrd a,60,208,7168\ndc 230,40,53,2048\nmm a,244,a,140,v,156\nad 199,199,193\ncv 142,192,193\n"
"mm v,24,a,148,v,144\nex 195,95\nmm v,56,a,148,v,148\nad 199,199,194\ns_nop 0\nmm a,216,a,148,v,152\n"
"ad 199,199,195\ncv 143,194,195\nmm a,248,a,148,v,156\nad 238,196,197\nad 239,198,199\nmm v,28,a,156,v,144\n"
"ad 238,238,239\nv_cmp_nge_f32 vcc, s59, v238\nmm v,60,a,156,v,148\nmm a,220,a,156,v,152\nmm a,252,a,156,v,156\nwl 0\n"
"s_cbranch_vccnz RS_6\nRR_6:\nmc v,64,a,32,a,0,160\nad 204,204,196\nad 205,205,197\nmm v,64,a,36,a,4\n"
"rd a,96,209,2048\nad 206,206,198\nmm v,64,a,40,a,8\nad 207,207,199\nex 188,96\nmm v,64,a,44,a,12\n"
"dc 230,40,53,3072\nex 189,97\nmc v,68,a,48,a,0,160\nrd a,104,209,6144\nex 190,98\nmm v,68,a,52,a,4\n"
"ad 200,188,189\ncv 144,188,189\nmm v,68,a,56,a,8\nex 191,99\nmm v,68,a,60,a,12\nrd a,112,209,10240\n"
"ad 200,200,190\nmc v,72,a,32,a,16,164\nex 192,100\nmm v,72,a,36,a,20\nad 200,200,191\ncv 145,190,191\n"
"mm v,72,a,40,a,24\nex 193,101\nmm v,72,a,44,a,28\nrd a,120,209,14336\nad 200,200,192\nmc v,76,a,48,a,16,164\n"
"ex 194,102\nmm v,76,a,52,a,20\nad 200,200,193\ncv 146,192,193\nmm v,76,a,56,a,24\nrd a,128,209,18432\n"
"sa m0,m0,0x1000\ns_nop 0\ndc 231,40,53,0\nmm v,76,a,60,a,28\nex 195,103\nmc v,80,a,32,a,160,168\n"
"ad 200,200,194\nex 188,104\nmm v,80,a,36,a,164\nrd a,136,209,22528\nad 200,200,195\nmm v,80,a,40,a,168\n"
"cv 147,194,195\nex 189,105\nmm v,80,a,44,a,172\nex 190,106\nmc v,84,a,48,a,160,168\nad 201,188,189\n"
"cv 148,188,189\nmm v,84,a,52,a,164\nrd a,144,209,26624\nex 191,107\nmm v,84,a,56,a,168\nad 201,201,190\n"
"ex 192,108\nmm v,84,a,60,a,172\nad 201,201,191\ncv 149,190,191\nmc v,88,a,32,a,176,172\nrd a,152,209,30720\n"
"ex 193,109\nmm v,88,a,36,a,180\ndc 231,40,53,1024\nad 201,201,192\nmm v,88,a,40,a,184\nex 194,110\n"
"mm v,88,a,44,a,188\nad 201,201,193\ncv 150,192,193\nmc v,92,a,48,a,176,172\nex 195,111\nmm v,92,a,52,a,180\n"
"ad 201,201,194\nex 188,112\nmm v,92,a,56,a,184\nad 201,201,195\ncv 151,194,195\nmm v,92,a,60,a,188\n"
"wl 0\nex 189,113\nmm v,0,a,96,v,128\nex 190,114\nmm v,32,a,96,v,132\nrd a,64,208,8192\n"
"ad 202,188,189\nmm a,192,a,96,v,136\ncv 152,188,189\nex 191,115\nmm a,224,a,96,v,140\nad 202,202,190\n"
"ex 192,116\nmm v,4,a,104,v,128\nrd a,68,208,9216\ndc 231,40,53,2048\nmm v,36,a,104,v,132\nad 202,202,191\n"
"cv 153,190,191\nmm a,196,a,104,v,136\nex 193,117\nmm a,228,a,104,v,140\nrd a,72,208,10240\nad 202,202,192\n"
"mm v,8,a,112,v,128\nex 194,118\nmm v,40,a,112,v,132\nad 202,202,193\ncv 154,192,193\nmm a,200,a,112,v,136\n"
"rd a,76,208,11264\nex 195,119\nmm a,232,a,112,v,140\nad 202,202,194\nex 188,120\nmm v,12,a,120,v,128\n"
"ad 202,202,195\ncv 155,194,195\nmm v,44,a,120,v,132\nrd a,80,208,12288\nex 189,121\nmm a,204,a,120,v,136\n"
"ex 190,122\nmm a,236,a,120,v,140\ndc 231,40,53,3072\nad 203,188,189\nmm v,16,a,128,v,128\nrd a,84,208,13312\n"
"cv 156,188,189\nmm v,48,a,128,v,132\nex 191,123\nmm a,208,a,128,v,136\nad 203,203,190\nex 192,124\n"
"mm a,240,a,128,v,140\nrd a,88,208,14336\nad 203,203,191\nmm v,20,a,136,v,128\ncv 157,190,191\nex 193,125\n"
"mm v,52,a,136,v,132\nad 203,203,192\nex 194,126\nmm a,212,a,136,v,136\nrd a,92,208,15360\nad 203,203,193\n"
"mm a,244,a,136,v,140\ncv 158,192,193\nex 195,127\nmm v,24,a,144,v,128\nad 203,203,194\ns_nop 0\n"
"mm v,56,a,144,v,132\nad 203,203,195\ncv 159,194,195\nmm a,216,a,144,v,136\nad 238,200,201\nad 239,202,203\n"
"mm a,248,a,144,v,140\nad 238,238,239\nv_cmp_nge_f32 vcc, s59, v238\nmm v,28,a,152,v,128\nmm v,60,a,152,v,132\ns_xor_b32 s57, s57, 0x8000\n"
"mm a,220,a,152,v,136\ns_sub_u32 s58, s58, 1\nmm a,252,a,152,v,140\nwl 0\ns_cbranch_vccnz RS_7\nRR_7:\n"
"s_cbranch_scc0 LOOP_S\nDIAG_S:\ns_sub_u32 s92, s92, 1\ns_cmp_lt_u32 s92, 2\ns_cbranch_scc1 TK_DS\nTKR_DS:\n"
"s_cmp_eq_u32 s52, 1\ns_cbranch_scc1 DIAG_S1\ns_cmp_eq_u32 s52, 2\ns_cbranch_scc1 DIAG_S2\ns_cmp_eq_u32 s52, 3\ns_cbranch_scc1 DIAG_S3\n"
"DIAG_S0:\nmc v,96,a,64,a,0,160\nad 80,80,184\nad 81,81,185\nmm v,96,a,68,a,4\nrd a,100,209,3072\n"
"ad 82,82,186\nad 83,83,187\nmm v,96,a,72,a,8\nv_mov_b32 v84, 0xff800000\nv_mov_b32 v85, 0xff800000\nmm v,96,a,76,a,12\n"
"rd a,108,209,7168\nv_mov_b32 v86, 0xff800000\nv_mov_b32 v87, 0xff800000\nmc v,100,a,80,a,0,160\nad 204,204,200\nad 205,205,201\n"
"mm v,100,a,84,a,4\nrd a,116,209,11264\ns_mov_b32 s54, s53\nmm v,100,a,88,a,8\nad 206,206,202\nad 207,207,203\n"
"v_mov_b32 v138, 0\nmm v,100,a,92,a,12\nv_mov_b32 v139, 0\nex 188,64\nmc v,104,a,64,a,16,164\nrd a,124,209,15360\n"
"ex 189,65\nmm v,104,a,68,a,20\nex 190,66\nmm v,104,a,72,a,24\nrd a,132,209,19456\nad 196,188,189\n"
"mm v,104,a,76,a,28\nsa s53,s53,0x10000\ncv 128,188,189\nmc v,108,a,80,a,16,164\nrd a,140,209,23552\nex 191,67\n"
"mm v,108,a,84,a,20\nad 196,196,190\nex 192,68\nmm v,108,a,88,a,24\nad 196,196,191\ncv 129,190,191\n"
"mm v,108,a,92,a,28\nrd a,148,209,27648\nex 193,69\nmc v,120,a,64,a,176,172\nad 196,196,192\nex 194,70\n"
"mm v,120,a,68,a,180\nrd a,156,209,31744\ns_cmp_eq_u32 s53, s55\ns_cselect_b32 s53, s56, s53\nmm v,120,a,72,a,184\nad 196,196,193\n"
"cv 130,192,193\nmm v,120,a,76,a,188\nex 195,71\nmc v,124,a,80,a,176,172\nad 196,196,194\nex 188,72\n"
"mm v,124,a,84,a,180\nad 196,196,195\ncv 131,194,195\nmm v,124,a,88,a,184\nex 189,73\nmm v,124,a,92,a,188\n"
"wl 0\nex 190,74\nmm v,0,a,100,v,144\nad 197,188,189\ncv 132,188,189\nmm v,32,a,100,v,148\n"
"rd a,32,208,16384\nex 191,75\nmm a,192,a,100,v,152\nad 197,197,190\nex 192,76\nmm a,224,a,100,v,156\n"
"ad 197,197,191\ncv 133,190,191\nmm v,4,a,108,v,144\nrd a,36,208,17408\nex 193,77\nmm v,36,a,108,v,148\n"
"ad 197,197,192\nex 194,78\nmm a,196,a,108,v,152\nad 197,197,193\ncv 134,192,193\nmm a,228,a,108,v,156\n"
"rd a,40,208,18432\nex 195,79\nmm v,8,a,116,v,144\nad 197,197,194\nex 188,80\nmm v,40,a,116,v,148\n"
"ad 197,197,195\ncv 135,194,195\nmm a,200,a,116,v,152\nrd a,44,208,19456\nex 189,81\nmm a,232,a,116,v,156\n"
"ex 190,82\nmm v,12,a,124,v,144\nad 198,188,189\ncv 136,188,189\nmm v,44,a,124,v,148\nrd a,48,208,20480\n"
"ex 191,83\nmm a,204,a,124,v,152\nad 198,198,190\nex 192,88\nmm a,236,a,124,v,156\nad 198,198,191\n"
"cv 137,190,191\nmm v,16,a,132,v,144\nrd a,52,208,21504\nex 193,89\nmm v,48,a,132,v,148\nex 194,90\n"
"mm a,208,a,132,v,152\nad 199,192,193\ncv 140,192,193\nmm a,240,a,132,v,156\nrd a,56,208,22528\nex 195,91\n"
"mm v,20,a,140,v,144\nad 199,199,194\nex 188,92\nmm v,52,a,140,v,148\nad 199,199,195\ncv 141,194,195\n"
"mm a,212,a,140,v,152\nrd a,60,208,23552\nex 189,93\nmm a,244,a,140,v,156\nad 199,199,188\nex 190,94\n"
"mm v,24,a,148,v,144\nad 199,199,189\ncv 142,188,189\nmm v,56,a,148,v,148\nex 191,95\nmm a,216,a,148,v,152\n"
"ad 199,199,190\ns_nop 0\nmm a,248,a,148,v,156\nad 199,199,191\ncv 143,190,191\nmm v,28,a,156,v,144\n"
"ad 238,196,197\nad 239,198,199\nmm v,60,a,156,v,148\nad 238,238,239\nv_cmp_nge_f32 vcc, s59, v238\nmm a,220,a,156,v,152\n"
"mm a,252,a,156,v,156\ns_waitcnt vmcnt(8) lgkmcnt(0)\ns_barrier\nv_xor_b32 v209, 0x8000, v209\ns_cbranch_vccnz RS_8\nRR_8:\n"
"mc v,64,a,32,a,0,160\nv_mov_b32 v112, 0xff800000\nv_mov_b32 v113, 0xff800000\nmm v,64,a,36,a,4\nrd a,96,209,0\nv_mov_b32 v114, 0xff800000\n"
"mm v,64,a,40,a,8\nv_mov_b32 v115, 0xff800000\nv_mov_b32 v116, 0xff800000\nmm v,64,a,44,a,12\nrd a,104,209,4096\nv_mov_b32 v117, 0xff800000\n"
"mc v,68,a,48,a,0,160\nv_mov_b32 v118, 0xff800000\nv_mov_b32 v119, 0xff800000\nmm v,68,a,52,a,4\nrd a,112,209,8192\nad 204,204,196\n"
"mm v,68,a,56,a,8\nad 205,205,197\nad 206,206,198\nmm v,68,a,60,a,12\nad 207,207,199\nex 188,96\n"
"mc v,72,a,32,a,16,164\nrd a,120,209,12288\nex 189,97\nmm v,72,a,36,a,20\nex 190,98\nmm v,72,a,40,a,24\n"
"rd a,128,209,16384\nad 200,188,189\nmm v,72,a,44,a,28\ncv 210,188,189\nex 191,99\nmc v,76,a,48,a,16,164\n"
"rd a,136,209,20480\nad 200,200,190\nmm v,76,a,52,a,20\nex 192,100\nmm v,76,a,56,a,24\nad 200,200,191\n"
"cv 211,190,191\nmm v,76,a,60,a,28\nrd a,144,209,24576\nex 193,101\nmc v,88,a,32,a,176,172\nad 200,200,192\n"
"ex 194,102\nmm v,88,a,36,a,180\nrd a,152,209,28672\nad 200,200,193\nmm v,88,a,40,a,184\ncv 212,192,193\n"
"ex 195,103\nmm v,88,a,44,a,188\nad 200,200,194\nex 188,104\nmc v,92,a,48,a,176,172\nad 200,200,195\n"
"cv 213,194,195\nmm v,92,a,52,a,180\nex 189,105\nmm v,92,a,56,a,184\nex 190,106\nmm v,92,a,60,a,188\n"
"wl 0\nad 201,188,189\nmm v,0,a,96,v,128\ncv 214,188,189\nex 191,107\nmm v,32,a,96,v,132\n"
"rd a,64,208,24576\nad 201,201,190\nmm a,192,a,96,v,136\nex 192,108\nmm a,224,a,96,v,140\nad 201,201,191\n"
"cv 215,190,191\nmm v,4,a,104,v,128\nrd a,68,208,25600\nex 193,109\nmm v,36,a,104,v,132\nad 201,201,192\n"
"ex 194,110\nmm a,196,a,104,v,136\nad 201,201,193\ncv 216,192,193\nmm a,228,a,104,v,140\nrd a,72,208,26624\n"
"ex 195,111\nmm v,8,a,112,v,128\nad 201,201,194\nex 188,120\nmm v,40,a,112,v,132\nad 201,201,195\n"
"cv 217,194,195\nmm a,200,a,112,v,136\nrd a,76,208,27648\nex 189,121\nmm a,232,a,112,v,140\nex 190,122\n"
"mm v,12,a,120,v,128\nad 203,188,189\ncv 222,188,189\nmm v,44,a,120,v,132\nrd a,80,208,28672\nex 191,123\n"
"mm a,204,a,120,v,136\nad 203,203,190\nex 192,124\nmm a,236,a,120,v,140\nad 203,203,191\ncv 223,190,191\n"
"mm v,16,a,128,v,128\nrd a,84,208,29696\nex 193,125\nmm v,48,a,128,v,132\nad 203,203,192\nex 194,126\n"
"mm a,208,a,128,v,136\nad 203,203,193\ncv 224,192,193\nmm a,240,a,128,v,140\nrd a,88,208,30720\nex 195,127\n"
"mm v,20,a,136,v,128\nad 203,203,194\ns_nop 0\nmm v,52,a,136,v,132\nrd a,92,208,31744\nad 203,203,195\n"
"mm a,212,a,136,v,136\ncv 225,194,195\nad 238,200,201\nmm a,244,a,136,v,140\nad 238,238,203\nv_cmp_nge_f32 vcc, s59, v238\n"
"mm v,24,a,144,v,128\nmm v,56,a,144,v,132\nmm a,216,a,144,v,136\nmm a,248,a,144,v,140\nmm v,28,a,152,v,128\nmm v,60,a,152,v,132\n"
"mm a,220,a,152,v,136\nmm a,252,a,152,v,140\nwl 0\ns_cbranch_vccnz RS_9\nRR_9:\nmc v,96,a,64,a,0,160\n"
"v_mov_b32 v80, 0xff800000\nv_mov_b32 v81, 0xff800000\nv_mov_b32 v82, 0xff800000\nmm v,96,a,68,a,4\nrd a,100,209,1024\nv_mov_b32 v83, 0xff800000\n"
"mm v,96,a,72,a,8\nv_mov_b32 v84, 0xff800000\nv_mov_b32 v85, 0xff800000\nv_mov_b32 v86, 0xff800000\nmm v,96,a,76,a,12\nrd a,108,209,5120\n"
"v_mov_b32 v87, 0xff800000\nad 204,204,200\nmc v,100,a,80,a,0,160\nad 205,205,201\nad 207,207,203\nmm v,100,a,84,a,4\n"
"rd a,116,209,9216\nex 188,64\nmm v,100,a,88,a,8\nex 189,65\nex 190,66\nmm v,100,a,92,a,12\n"
"ad 196,188,189\ncv 144,188,189\nmc v,104,a,64,a,16,164\nrd a,124,209,13312\nex 191,67\nmm v,104,a,68,a,20\n"
"ad 196,196,190\nex 192,68\nmm v,104,a,72,a,24\nrd a,132,209,17408\nad 196,196,191\nmm v,104,a,76,a,28\n"
"cv 145,190,191\nex 193,69\nmc v,108,a,80,a,16,164\nrd a,140,209,21504\nad 196,196,192\nmm v,108,a,84,a,20\n"
"ex 194,70\nad 196,196,193\nmm v,108,a,88,a,24\ncv 146,192,193\nex 195,71\nmm v,108,a,92,a,28\n"
"rd a,148,209,25600\nad 196,196,194\nex 188,72\nmc v,120,a,64,a,176,172\nad 196,196,195\ncv 147,194,195\n"
"mm v,120,a,68,a,180\nrd a,156,209,29696\nex 189,73\nmm v,120,a,72,a,184\nex 190,74\nmm v,120,a,76,a,188\n"
"ad 197,188,189\ncv 152,188,189\nex 191,75\nmc v,124,a,80,a,176,172\nad 197,197,190\nex 192,76\n"
"mm v,124,a,84,a,180\nad 197,197,191\ncv 153,190,191\nmm v,124,a,88,a,184\nex 193,77\nmm v,124,a,92,a,188\n"
"s_waitcnt vmcnt(0) lgkmcnt(0)\ns_barrier\nv_xor_b32 v208, 0x8000, v208\nad 197,197,192\nmm v,0,a,100,v,210\nex 194,78\n"
"ad 197,197,193\nmm v,32,a,100,v,214\nrd a,32,208,0\ncv 154,192,193\nmm a,224,a,100,v,222\nrd a,36,208,1024\n"
"ex 195,79\nmm v,4,a,108,v,210\nrd a,40,208,2048\ns_mov_b32 m0, s57\ns_nop 0\ndc 230,40,53,0\n"
"mm v,36,a,108,v,214\nrd a,44,208,3072\nad 197,197,194\nmm a,228,a,108,v,222\nrd a,48,208,4096\nex 188,88\n"
"mm v,8,a,116,v,210\nrd a,52,208,5120\ndc 230,40,53,1024\nmm v,40,a,116,v,214\nrd a,56,208,6144\nad 197,197,195\n"
"cv 155,194,195\nmm a,232,a,116,v,222\nrd a,60,208,7168\nex 189,89\nmm v,12,a,124,v,210\nrd a,64,208,8192\n"
"ex 190,90\nmm v,44,a,124,v,214\nrd a,68,208,9216\ndc 230,40,53,2048\nmm a,236,a,124,v,222\nrd a,72,208,10240\n"
"ad 199,188,189\ncv 132,188,189\nmm v,16,a,132,v,210\nrd a,76,208,11264\nex 191,91\nmm v,48,a,132,v,214\n"
"rd a,80,208,12288\nad 199,199,190\nmm a,240,a,132,v,222\nrd a,84,208,13312\ndc 230,40,53,3072\nex 192,92\n"
"mm v,20,a,140,v,210\nrd a,88,208,14336\nad 199,199,191\nmm v,52,a,140,v,214\nrd a,92,208,15360\ncv 133,190,191\n"
"mm a,244,a,140,v,222\nsa m0,m0,0x1000\ns_nop 0\ndc 231,40,53,0\nmm v,24,a,148,v,210\nex 193,93\n"
"ad 199,199,192\nmm v,56,a,148,v,214\nex 194,94\nad 199,199,193\nmm a,248,a,148,v,222\ncv 134,192,193\n"
"ex 195,95\nmm v,28,a,156,v,210\ndc 231,40,53,1024\nad 199,199,194\nmm v,60,a,156,v,214\ns_nop 0\n"
"ad 199,199,195\ncv 135,194,195\nad 238,196,197\nad 238,238,199\nv_cmp_nge_f32 vcc, s59, v238\nmm a,252,a,156,v,222\n"
"wl 8\ns_cbranch_vccnz RS_10\nRR_10:\nmc v,64,a,32,a,0,160\nv_mov_b32 v112, 0xff800000\nv_mov_b32 v113, 0xff800000\n"
"v_mov_b32 v114, 0xff800000\nmm v,64,a,36,a,4\nrd a,96,209,2048\nv_mov_b32 v115, 0xff800000\nv_mov_b32 v116, 0xff800000\nv_mov_b32 v117, 0xff800000\n"
"mm v,64,a,40,a,8\ndc 231,40,53,2048\nv_mov_b32 v118, 0xff800000\nv_mov_b32 v119, 0xff800000\nmm v,64,a,44,a,12\nad 124,124,184\n"
"ad 125,125,185\nad 126,126,186\nad 127,127,187\nmc v,80,a,32,a,16,164\nrd a,104,209,6144\nad 204,204,196\n"
"ad 205,205,197\nmm v,80,a,36,a,20\ndc 231,40,53,3072\nad 207,207,199\nex 188,96\nmm v,80,a,40,a,24\n"
"ex 189,97\nex 190,98\nmm v,80,a,44,a,28\nrd a,112,209,10240\nad 200,188,189\ncv 148,188,189\n"
"mc v,68,a,48,a,0,160\ns_xor_b32 m0, s57, 0x18000\ns_nop 0\ndc 232,40,54,0\nmm v,68,a,52,a,4\nex 191,99\n"
"ad 200,200,190\nex 192,100\nmm v,68,a,56,a,8\nad 200,200,191\ncv 149,190,191\nmm v,68,a,60,a,12\n"
"rd a,120,209,14336\nex 193,101\nad 200,200,192\nmc v,84,a,48,a,16,164\ndc 232,40,54,1024\nex 194,102\n"
"mm v,84,a,52,a,20\nad 200,200,193\ncv 150,192,193\nex 195,103\nmm v,84,a,56,a,24\nrd a,128,209,18432\n"
"ad 200,200,194\nex 188,104\nmm v,84,a,60,a,28\ndc 232,40,54,2048\nad 200,200,195\ncv 151,194,195\n"
"wl 12\nmc v,72,a,64,a,0,160\nex 189,105\nex 190,106\nwl 11\nmm v,72,a,68,a,4\n"
"rd a,136,209,22528\nad 201,188,189\ncv 156,188,189\nwl 11\nmm v,72,a,72,a,8\nex 191,107\n"
"ad 201,201,190\nwl 10\nmm v,72,a,76,a,12\ndc 232,40,54,3072\nex 192,108\nad 201,201,191\n"
"mc v,88,a,64,a,16,164\ncv 157,190,191\nex 193,109\nmm v,88,a,68,a,20\nrd a,144,209,26624\nad 201,201,192\n"
"ex 194,110\nmm v,88,a,72,a,24\nsa m0,m0,0x1000\ns_nop 0\ndc 233,40,54,0\nmm v,88,a,76,a,28\n"
"ad 201,201,193\ncv 158,192,193\nex 195,111\nwl 10\nmc v,76,a,80,a,0,160\nrd a,152,209,30720\n"
"ad 201,201,194\nex 188,120\nwl 10\nmm v,76,a,84,a,4\ndc 233,40,54,1024\nad 201,201,195\n"
"cv 159,194,195\nwl 9\nmm v,76,a,88,a,8\nex 189,121\nex 190,122\nwl 8\n"
"mm v,76,a,92,a,12\nad 203,188,189\ncv 140,188,189\nex 191,123\nmc v,92,a,80,a,16,164\nad 203,203,190\n"
"ex 192,124\nmm v,92,a,84,a,20\ndc 233,40,54,2048\nad 203,203,191\ncv 141,190,191\nmm v,92,a,88,a,24\n"
"ex 193,125\nad 203,203,192\nmm v,92,a,92,a,28\nwl 0\nex 194,126\nad 203,203,193\n"
"mm a,224,a,96,v,132\ndc 233,40,54,3072\nrd a,32,208,16384\nrd a,36,208,17408\nmm a,228,a,104,v,132\nrd a,40,208,18432\n"
"rd a,44,208,19456\nrd a,48,208,20480\nrd a,52,208,21504\nrd a,56,208,22528\nrd a,60,208,23552\nrd a,64,208,24576\n"
"rd a,68,208,25600\nrd a,72,208,26624\nrd a,76,208,27648\nrd a,80,208,28672\nrd a,84,208,29696\nrd a,88,208,30720\n"
"rd a,92,208,31744\nmm a,232,a,112,v,132\ncv 142,192,193\nex 195,127\nmm a,236,a,120,v,132\nad 203,203,194\n"
"s_nop 0\nmm a,240,a,128,v,132\nad 203,203,195\ncv 143,194,195\nmm a,244,a,136,v,132\nad 238,200,201\n"
"ad 238,238,203\nmm a,248,a,144,v,132\nv_cmp_nge_f32 vcc, s59, v238\ns_xor_b32 s57, s57, 0x8000\nmm a,252,a,152,v,132\ns_cbranch_vccnz RS_11\n"
"RR_11:\nrd a,100,209,3072\nrd a,108,209,7168\nrd a,116,209,11264\nrd a,124,209,15360\nrd a,132,209,19456\n"
"rd a,140,209,23552\nrd a,148,209,27648\nrd a,156,209,31744\nad 207,207,203\nwl 7\nmm a,224,a,100,v,140\n"
"wl 6\nmm a,228,a,108,v,140\nwl 5\nmm a,232,a,116,v,140\nwl 4\nmm a,236,a,124,v,140\n"
"wl 3\nmm a,240,a,132,v,140\nwl 2\nmm a,244,a,140,v,140\nwl 1\nmm a,248,a,148,v,140\n"
"wl 0\nmm a,252,a,156,v,140\ns_branch SHORT_END\nDIAG_S1:\nmc v,96,a,64,a,0,160\nad 84,84,184\n"
"ad 85,85,185\nad 86,86,186\nmm v,96,a,68,a,4\nrd a,100,209,3072\nad 87,87,187\nmm v,96,a,72,a,8\n"
"ad 204,204,200\nad 205,205,201\nad 206,206,202\nmm v,96,a,76,a,12\nrd a,108,209,7168\nad 207,207,203\n"
"mc v,100,a,80,a,0,160\nex 188,64\nmm v,100,a,84,a,4\nrd a,116,209,11264\ns_mov_b32 s54, s53\nex 189,65\n"
"mm v,100,a,88,a,8\nex 190,66\nmm v,100,a,92,a,12\nad 196,188,189\ncv 128,188,189\nmc v,104,a,64,a,16,164\n"
"rd a,124,209,15360\nex 191,67\nmm v,104,a,68,a,20\nad 196,196,190\nex 192,68\nmm v,104,a,72,a,24\n"
"rd a,132,209,19456\nad 196,196,191\nmm v,104,a,76,a,28\nsa s53,s53,0x10000\ncv 129,190,191\nmc v,108,a,80,a,16,164\n"
"rd a,140,209,23552\nex 193,69\nmm v,108,a,84,a,20\nad 196,196,192\nex 194,70\nmm v,108,a,88,a,24\n"
"ad 196,196,193\ncv 130,192,193\nmm v,108,a,92,a,28\nrd a,148,209,27648\nex 195,71\nmc v,120,a,64,a,176,172\n"
"ad 196,196,194\nex 188,72\nmm v,120,a,68,a,180\nrd a,156,209,31744\ns_cmp_eq_u32 s53, s55\ns_cselect_b32 s53, s56, s53\n"
"mm v,120,a,72,a,184\nad 196,196,195\ncv 131,194,195\nmm v,120,a,76,a,188\nex 189,73\nmc v,124,a,80,a,176,172\n"
"ex 190,74\nmm v,124,a,84,a,180\nad 197,188,189\ncv 132,188,189\nmm v,124,a,88,a,184\nex 191,75\n"
"ad 197,197,190\nmm v,124,a,92,a,188\nwl 0\nex 192,76\nmm v,0,a,100,v,144\nad 197,197,191\n"
"cv 133,190,191\nmm v,32,a,100,v,148\nrd a,32,208,16384\nex 193,77\nmm a,192,a,100,v,152\nad 197,197,192\n"
"ex 194,78\nmm a,224,a,100,v,156\nad 197,197,193\ncv 134,192,193\nmm v,4,a,108,v,144\nrd a,36,208,17408\n"
"ex 195,79\nmm v,36,a,108,v,148\nad 197,197,194\nex 188,80\nmm a,196,a,108,v,152\nad 197,197,195\n"
"cv 135,194,195\nmm a,228,a,108,v,156\nrd a,40,208,18432\nex 189,81\nmm v,8,a,116,v,144\nex 190,82\n"
"mm v,40,a,116,v,148\nad 198,188,189\ncv 136,188,189\nmm a,200,a,116,v,152\nrd a,44,208,19456\nex 191,83\n"
"mm a,232,a,116,v,156\nad 198,198,190\nex 192,84\nmm v,12,a,124,v,144\nad 198,198,191\ncv 137,190,191\n"
"mm v,44,a,124,v,148\nrd a,48,208,20480\nex 193,85\nmm a,204,a,124,v,152\nad 198,198,192\nex 194,86\n"
"mm a,236,a,124,v,156\nad 198,198,193\ncv 138,192,193\nmm v,16,a,132,v,144\nrd a,52,208,21504\nex 195,87\n"
"mm v,48,a,132,v,148\nad 198,198,194\nex 188,88\nmm a,208,a,132,v,152\nad 198,198,195\ncv 139,194,195\n"
"mm a,240,a,132,v,156\nrd a,56,208,22528\nex 189,89\nmm v,20,a,140,v,144\nex 190,90\nmm v,52,a,140,v,148\n"
"ad 199,188,189\ncv 140,188,189\nmm a,212,a,140,v,152\nrd a,60,208,23552\nex 191,91\nmm a,244,a,140,v,156\n"
"ad 199,199,190\nex 192,92\nmm v,24,a,148,v,144\nad 199,199,191\ncv 141,190,191\nmm v,56,a,148,v,148\n"
"ex 193,93\nmm a,216,a,148,v,152\nad 199,199,192\nex 194,94\nmm a,248,a,148,v,156\nad 199,199,193\n"
"cv 142,192,193\nmm v,28,a,156,v,144\nex 195,95\nmm v,60,a,156,v,148\nad 199,199,194\ns_nop 0\n"
"ad 199,199,195\nmm a,220,a,156,v,152\ncv 143,194,195\nad 238,196,197\nad 239,198,199\nad 238,238,239\n"
"v_cmp_nge_f32 vcc, s59, v238\nmm a,252,a,156,v,156\ns_waitcnt vmcnt(8) lgkmcnt(0)\ns_barrier\nv_xor_b32 v209, 0x8000, v209\ns_cbranch_vccnz RS_12\n"
"RR_12:\nmc v,64,a,32,a,0,160\nv_mov_b32 v112, 0xff800000\nv_mov_b32 v113, 0xff800000\nmm v,64,a,36,a,4\nrd a,96,209,0\n"
"v_mov_b32 v114, 0xff800000\nmm v,64,a,40,a,8\nv_mov_b32 v115, 0xff800000\nv_mov_b32 v116, 0xff800000\nmm v,64,a,44,a,12\nrd a,104,209,4096\n"
"v_mov_b32 v117, 0xff800000\nmc v,68,a,48,a,0,160\nv_mov_b32 v118, 0xff800000\nv_mov_b32 v119, 0xff800000\nmm v,68,a,52,a,4\nrd a,112,209,8192\n"
"ad 204,204,196\nmm v,68,a,56,a,8\nad 205,205,197\nad 206,206,198\nmm v,68,a,60,a,12\nad 207,207,199\n"
"ex 188,96\nmc v,72,a,32,a,16,164\nrd a,120,209,12288\nex 189,97\nmm v,72,a,36,a,20\nex 190,98\n"
"mm v,72,a,40,a,24\nrd a,128,209,16384\nad 200,188,189\nmm v,72,a,44,a,28\ncv 210,188,189\nex 191,99\n"
"mc v,76,a,48,a,16,164\nrd a,136,209,20480\nad 200,200,190\nmm v,76,a,52,a,20\nex 192,100\nmm v,76,a,56,a,24\n"
"ad 200,200,191\ncv 211,190,191\nmm v,76,a,60,a,28\nrd a,144,209,24576\nex 193,101\nmc v,88,a,32,a,176,172\n"
"ad 200,200,192\nex 194,102\nmm v,88,a,36,a,180\nrd a,152,209,28672\nad 200,200,193\nmm v,88,a,40,a,184\n"
"cv 212,192,193\nex 195,103\nmm v,88,a,44,a,188\nad 200,200,194\nex 188,104\nmc v,92,a,48,a,176,172\n"
"ad 200,200,195\ncv 213,194,195\nmm v,92,a,52,a,180\nex 189,105\nmm v,92,a,56,a,184\nex 190,106\n"
"mm v,92,a,60,a,188\nwl 0\nad 201,188,189\nmm v,0,a,96,v,128\ncv 214,188,189\nex 191,107\n"
"mm v,32,a,96,v,132\nrd a,64,208,24576\nad 201,201,190\nmm a,192,a,96,v,136\nex 192,108\nmm a,224,a,96,v,140\n"
"ad 201,201,191\ncv 215,190,191\nmm v,4,a,104,v,128\nrd a,68,208,25600\nex 193,109\nmm v,36,a,104,v,132\n"
"ad 201,201,192\nex 194,110\nmm a,196,a,104,v,136\nad 201,201,193\ncv 216,192,193\nmm a,228,a,104,v,140\n"
"rd a,72,208,26624\nex 195,111\nmm v,8,a,112,v,128\nad 201,201,194\nex 188,120\nmm v,40,a,112,v,132\n"
"ad 201,201,195\ncv 217,194,195\nmm a,200,a,112,v,136\nrd a,76,208,27648\nex 189,121\nmm a,232,a,112,v,140\n"
"ex 190,122\nmm v,12,a,120,v,128\nad 203,188,189\ncv 222,188,189\nmm v,44,a,120,v,132\nrd a,80,208,28672\n"
"ex 191,123\nmm a,204,a,120,v,136\nad 203,203,190\nex 192,124\nmm a,236,a,120,v,140\nad 203,203,191\n"
"cv 223,190,191\nmm v,16,a,128,v,128\nrd a,84,208,29696\nex 193,125\nmm v,48,a,128,v,132\nad 203,203,192\n"
"ex 194,126\nmm a,208,a,128,v,136\nad 203,203,193\ncv 224,192,193\nmm a,240,a,128,v,140\nrd a,88,208,30720\n"
"ex 195,127\nmm v,20,a,136,v,128\nad 203,203,194\ns_nop 0\nmm v,52,a,136,v,132\nrd a,92,208,31744\n"
"ad 203,203,195\nmm a,212,a,136,v,136\ncv 225,194,195\nad 238,200,201\nmm a,244,a,136,v,140\nad 238,238,203\n"
"v_cmp_nge_f32 vcc, s59, v238\nmm v,24,a,144,v,128\nmm v,56,a,144,v,132\nmm a,216,a,144,v,136\nmm a,248,a,144,v,140\nmm v,28,a,152,v,128\n"
"mm v,60,a,152,v,132\nmm a,220,a,152,v,136\nmm a,252,a,152,v,140\nwl 0\ns_cbranch_vccnz RS_13\nRR_13:\n"
"mc v,96,a,64,a,0,160\nv_mov_b32 v80, 0xff800000\nv_mov_b32 v81, 0xff800000\nv_mov_b32 v82, 0xff800000\nmm v,96,a,68,a,4\nrd a,100,209,1024\n"
"v_mov_b32 v83, 0xff800000\nv_mov_b32 v84, 0xff800000\nmm v,96,a,72,a,8\nrd a,108,209,5120\nv_mov_b32 v85, 0xff800000\nv_mov_b32 v86, 0xff800000\n"
"mm v,96,a,76,a,12\nv_mov_b32 v87, 0xff800000\nad 204,204,200\nad 205,205,201\nmc v,100,a,80,a,0,160\nrd a,116,209,9216\n"
"ad 207,207,203\nex 188,64\nmm v,100,a,84,a,4\nex 189,65\nmm v,100,a,88,a,8\nrd a,124,209,13312\n"
"ex 190,66\nmm v,100,a,92,a,12\nad 196,188,189\ncv 144,188,189\nex 191,67\nmc v,104,a,64,a,16,164\n"
"rd a,132,209,17408\nad 196,196,190\nmm v,104,a,68,a,20\nex 192,68\nad 196,196,191\nmm v,104,a,72,a,24\n"
"rd a,140,209,21504\ncv 145,190,191\nex 193,69\nmm v,104,a,76,a,28\nad 196,196,192\nex 194,70\n"
"mc v,108,a,80,a,16,164\nrd a,148,209,25600\nad 196,196,193\nmm v,108,a,84,a,20\ncv 146,192,193\nex 195,71\n"
"mm v,108,a,88,a,24\nrd a,156,209,29696\nad 196,196,194\nex 188,72\nmm v,108,a,92,a,28\nad 196,196,195\n"
"cv 147,194,195\nmc v,120,a,64,a,176,172\nex 189,73\nex 190,74\nmm v,120,a,68,a,180\nad 197,188,189\n"
"cv 152,188,189\nmm v,120,a,72,a,184\nex 191,75\nad 197,197,190\nmm v,120,a,76,a,188\ns_waitcnt vmcnt(0) lgkmcnt(0)\n"
"s_barrier\nv_xor_b32 v208, 0x8000, v208\nex 192,76\nmm v,0,a,100,v,210\nad 197,197,191\ncv 153,190,191\n"
"mm v,32,a,100,v,214\nrd a,32,208,0\nex 193,77\nmm a,224,a,100,v,222\nrd a,36,208,1024\nad 197,197,192\n"
"ex 194,78\nmm v,4,a,108,v,210\nrd a,40,208,2048\ns_mov_b32 m0, s57\ns_nop 0\ndc 230,40,53,0\n"
"mm v,36,a,108,v,214\nrd a,44,208,3072\nad 197,197,193\nmm a,228,a,108,v,222\nrd a,48,208,4096\ncv 154,192,193\n"
"mm v,8,a,116,v,210\nrd a,52,208,5120\ndc 230,40,53,1024\nex 195,79\nmm v,40,a,116,v,214\nrd a,56,208,6144\n"
"ad 197,197,194\nmm a,232,a,116,v,222\nrd a,60,208,7168\nex 188,88\nmm v,12,a,124,v,210\nrd a,64,208,8192\n"
"ad 197,197,195\ncv 155,194,195\nmm v,44,a,124,v,214\nrd a,68,208,9216\ndc 230,40,53,2048\nex 189,89\n"
"mm a,236,a,124,v,222\nrd a,72,208,10240\nex 190,90\nmm v,16,a,132,v,210\nrd a,76,208,11264\nad 199,188,189\n"
"mm v,48,a,132,v,214\nrd a,80,208,12288\ncv 132,188,189\nex 191,91\nmm a,240,a,132,v,222\nrd a,84,208,13312\n"
"dc 230,40,53,3072\nmm v,20,a,140,v,210\nrd a,88,208,14336\nad 199,199,190\nex 192,92\nmm v,52,a,140,v,214\n"
"rd a,92,208,15360\nad 199,199,191\nmm a,244,a,140,v,222\nsa m0,m0,0x1000\ns_nop 0\ndc 231,40,53,0\n"
"mm v,24,a,148,v,210\ncv 133,190,191\nex 193,93\nmm v,56,a,148,v,214\nad 199,199,192\nex 194,94\n"
"mm a,248,a,148,v,222\nad 199,199,193\ncv 134,192,193\nex 195,95\nmm v,28,a,156,v,210\ndc 231,40,53,1024\n"
"ad 199,199,194\nmm v,60,a,156,v,214\ns_nop 0\nad 199,199,195\ncv 135,194,195\nad 238,196,197\n"
"ad 238,238,199\nv_cmp_nge_f32 vcc, s59, v238\nmm a,252,a,156,v,222\nwl 8\ns_cbranch_vccnz RS_14\nRR_14:\n"
"mc v,64,a,32,a,0,160\nv_mov_b32 v112, 0xff800000\nv_mov_b32 v113, 0xff800000\nv_mov_b32 v114, 0xff800000\nmm v,64,a,36,a,4\nrd a,96,209,2048\n"
"v_mov_b32 v115, 0xff800000\nv_mov_b32 v116, 0xff800000\nv_mov_b32 v117, 0xff800000\nmm v,64,a,40,a,8\ndc 231,40,53,2048\nv_mov_b32 v118, 0xff800000\n"
"v_mov_b32 v119, 0xff800000\nmm v,64,a,44,a,12\nad 120,120,184\nad 121,121,185\nad 122,122,186\nmc v,80,a,32,a,16,164\n"
"rd a,104,209,6144\nad 123,123,187\nv_mov_b32 v124, 0xff800000\nmm v,80,a,36,a,20\ndc 231,40,53,3072\nv_mov_b32 v125, 0xff800000\n"
"v_mov_b32 v126, 0xff800000\nv_mov_b32 v127, 0xff800000\nmm v,80,a,40,a,24\nad 204,204,196\nad 205,205,197\nad 207,207,199\n"
"mm v,80,a,44,a,28\nrd a,112,209,10240\nv_mov_b32 v142, 0\nv_mov_b32 v143, 0\nmc v,68,a,48,a,0,160\ns_xor_b32 m0, s57, 0x18000\n"
"s_nop 0\ndc 232,40,54,0\nmm v,68,a,52,a,4\nex 188,96\nex 189,97\nmm v,68,a,56,a,8\n"
"ex 190,98\nad 200,188,189\nmm v,68,a,60,a,12\nrd a,120,209,14336\ncv 148,188,189\nex 191,99\n"
"mc v,84,a,48,a,16,164\ndc 232,40,54,1024\nad 200,200,190\nmm v,84,a,52,a,20\nex 192,100\nad 200,200,191\n"
"cv 149,190,191\nmm v,84,a,56,a,24\nrd a,128,209,18432\nex 193,101\nmm v,84,a,60,a,28\ndc 232,40,54,2048\n"
"ad 200,200,192\nex 194,102\nwl 12\nmc v,72,a,64,a,0,160\nad 200,200,193\ncv 150,192,193\n"
"ex 195,103\nwl 11\nmm v,72,a,68,a,4\nrd a,136,209,22528\nad 200,200,194\nwl 11\n"
"mm v,72,a,72,a,8\nex 188,104\nad 200,200,195\ncv 151,194,195\nwl 10\nmm v,72,a,76,a,12\n"
"dc 232,40,54,3072\nex 189,105\nmc v,88,a,64,a,16,164\nex 190,106\nad 201,188,189\nmm v,88,a,68,a,20\n"
"rd a,144,209,26624\ncv 156,188,189\nex 191,107\nmm v,88,a,72,a,24\nsa m0,m0,0x1000\ns_nop 0\n"
"dc 233,40,54,0\nmm v,88,a,76,a,28\nad 201,201,190\nex 192,108\nwl 10\nmc v,76,a,80,a,0,160\n"
"rd a,152,209,30720\nad 201,201,191\ncv 157,190,191\nwl 10\nmm v,76,a,84,a,4\ndc 233,40,54,1024\n"
"ex 193,109\nad 201,201,192\nwl 9\nmm v,76,a,88,a,8\nex 194,110\nad 201,201,193\n"
"wl 8\nmm v,76,a,92,a,12\ncv 158,192,193\nex 195,111\nmc v,92,a,80,a,16,164\nad 201,201,194\n"
"ex 188,120\nad 201,201,195\nmm v,92,a,84,a,20\ndc 233,40,54,2048\ncv 159,194,195\nex 189,121\n"
"mm v,92,a,88,a,24\nex 190,122\nmm v,92,a,92,a,28\nwl 0\nad 203,188,189\ncv 140,188,189\n"
"mm a,224,a,96,v,132\ndc 233,40,54,3072\nrd a,32,208,16384\nrd a,36,208,17408\nrd a,40,208,18432\nmm a,228,a,104,v,132\n"
"rd a,44,208,19456\nrd a,48,208,20480\nrd a,52,208,21504\nrd a,56,208,22528\nrd a,60,208,23552\nrd a,64,208,24576\n"
"rd a,68,208,25600\nrd a,72,208,26624\nrd a,76,208,27648\nrd a,80,208,28672\nrd a,84,208,29696\nrd a,88,208,30720\n"
"rd a,92,208,31744\nmm a,232,a,112,v,132\nex 191,123\nmm a,236,a,120,v,132\nad 203,203,190\ns_nop 0\n"
"mm a,240,a,128,v,132\nad 203,203,191\ncv 141,190,191\nmm a,244,a,136,v,132\nad 238,200,201\nad 238,238,203\n"
"mm a,248,a,144,v,132\nv_cmp_nge_f32 vcc, s59, v238\ns_xor_b32 s57, s57, 0x8000\nmm a,252,a,152,v,132\ns_cbranch_vccnz RS_15\nRR_15:\n"
"rd a,100,209,3072\nrd a,108,209,7168\nrd a,116,209,11264\nrd a,124,209,15360\nrd a,132,209,19456\nrd a,140,209,23552\n"
"rd a,148,209,27648\nrd a,156,209,31744\nad 207,207,203\nwl 7\nmm a,224,a,100,v,140\nwl 6\n"
"mm a,228,a,108,v,140\nwl 5\nmm a,232,a,116,v,140\nwl 4\nmm a,236,a,124,v,140\nwl 3\n"
"mm a,240,a,132,v,140\nwl 2\nmm a,244,a,140,v,140\nwl 1\nmm a,248,a,148,v,140\nwl 0\n"
"mm a,252,a,156,v,140\ns_branch SHORT_END\nDIAG_S2:\nmc v,96,a,64,a,0,160\nad 204,204,200\nad 205,205,201\n"
"mm v,96,a,68,a,4\nrd a,100,209,3072\nad 206,206,202\nad 207,207,203\nmm v,96,a,72,a,8\nex 188,64\n"
"mm v,96,a,76,a,12\nrd a,108,209,7168\nex 189,65\nmc v,100,a,80,a,0,160\nex 190,66\nmm v,100,a,84,a,4\n"
"s_mov_b32 s54, s53\nad 196,188,189\nmm v,100,a,88,a,8\nrd a,116,209,11264\ncv 128,188,189\nmm v,100,a,92,a,12\n"
"ex 191,67\nmc v,104,a,64,a,16,164\nad 196,196,190\nex 192,68\nmm v,104,a,68,a,20\nrd a,124,209,15360\n"
"ad 196,196,191\nmm v,104,a,72,a,24\ncv 129,190,191\nex 193,69\nmm v,104,a,76,a,28\nsa s53,s53,0x10000\n"
"ad 196,196,192\nmc v,108,a,80,a,16,164\nrd a,132,209,19456\nex 194,70\nmm v,108,a,84,a,20\nad 196,196,193\n"
"cv 130,192,193\nmm v,108,a,88,a,24\nex 195,71\nmm v,108,a,92,a,28\nrd a,140,209,23552\nad 196,196,194\n"
"mc v,112,a,64,a,160,168\nex 188,72\nmm v,112,a,68,a,164\ns_cmp_eq_u32 s53, s55\ns_cselect_b32 s53, s56, s53\nmm v,112,a,72,a,168\n"
"rd a,148,209,27648\nad 196,196,195\ncv 131,194,195\nmm v,112,a,76,a,172\nex 189,73\nmc v,120,a,64,a,176,172\n"
"ex 190,74\nmm v,120,a,68,a,180\nrd a,156,209,31744\nad 197,188,189\ncv 132,188,189\nmm v,120,a,72,a,184\n"
"ex 191,75\nmm v,120,a,76,a,188\nad 197,197,190\nex 192,76\nmc v,124,a,80,a,176,172\nad 197,197,191\n"
"cv 133,190,191\nmm v,124,a,84,a,180\nex 193,77\nmm v,124,a,88,a,184\nad 197,197,192\nex 194,78\n"
"mm v,124,a,92,a,188\nwl 0\nad 197,197,193\nmm v,0,a,100,v,144\ncv 134,192,193\nex 195,79\n"
"mm v,32,a,100,v,148\nrd a,32,208,16384\nad 197,197,194\nmm a,192,a,100,v,152\nex 188,80\nmm a,224,a,100,v,156\n"
"ad 197,197,195\ncv 135,194,195\nmm v,4,a,108,v,144\nrd a,36,208,17408\nex 189,81\nmm v,36,a,108,v,148\n"
"ex 190,82\nmm a,196,a,108,v,152\nad 198,188,189\ncv 136,188,189\nmm a,228,a,108,v,156\nrd a,40,208,18432\n"
"ex 191,83\nmm v,8,a,116,v,144\nad 198,198,190\nex 192,84\nmm v,40,a,116,v,148\nad 198,198,191\n"
"cv 137,190,191\nmm a,200,a,116,v,152\nrd a,44,208,19456\nex 193,85\nmm a,232,a,116,v,156\nad 198,198,192\n"
"ex 194,86\nmm v,12,a,124,v,144\nad 198,198,193\ncv 138,192,193\nmm v,44,a,124,v,148\nrd a,48,208,20480\n"
"ex 195,87\nmm a,204,a,124,v,152\nad 198,198,194\nex 188,88\nmm a,236,a,124,v,156\nad 198,198,195\n"
"cv 139,194,195\nmm v,16,a,132,v,144\nrd a,52,208,21504\nex 189,89\nmm v,48,a,132,v,148\nex 190,90\n"
"mm a,208,a,132,v,152\nad 199,188,189\ncv 140,188,189\nmm a,240,a,132,v,156\nrd a,56,208,22528\nex 191,91\n"
"mm v,20,a,140,v,144\nad 199,199,190\nex 192,92\nmm v,52,a,140,v,148\nad 199,199,191\ncv 141,190,191\n"
"mm a,212,a,140,v,152\nrd a,60,208,23552\nex 193,93\nmm a,244,a,140,v,156\nad 199,199,192\nex 194,94\n"
"mm v,24,a,148,v,144\nad 199,199,193\ncv 142,192,193\nmm v,56,a,148,v,148\nex 195,95\nmm a,216,a,148,v,152\n"
"ad 199,199,194\ns_nop 0\nmm a,248,a,148,v,156\nad 199,199,195\ncv 143,194,195\nmm v,28,a,156,v,144\n"
"ad 238,196,197\nad 239,198,199\nmm v,60,a,156,v,148\nad 238,238,239\nv_cmp_nge_f32 vcc, s59, v238\nmm a,220,a,156,v,152\n"
"mm a,252,a,156,v,156\ns_waitcnt vmcnt(8) lgkmcnt(0)\ns_barrier\nv_xor_b32 v209, 0x8000, v209\ns_cbranch_vccnz RS_16\nRR_16:\n"
"mc v,64,a,32,a,0,160\nad 112,112,184\nad 113,113,185\nmm v,64,a,36,a,4\nrd a,96,209,0\nad 114,114,186\n"
"ad 115,115,187\nmm v,64,a,40,a,8\nv_mov_b32 v116, 0xff800000\nv_mov_b32 v117, 0xff800000\nmm v,64,a,44,a,12\nrd a,104,209,4096\n"
"v_mov_b32 v118, 0xff800000\nmc v,68,a,48,a,0,160\nv_mov_b32 v119, 0xff800000\nad 204,204,196\nad 205,205,197\nmm v,68,a,52,a,4\n"
"rd a,112,209,8192\nad 206,206,198\nmm v,68,a,56,a,8\nad 207,207,199\nv_mov_b32 v220, 0\nmm v,68,a,60,a,12\n"
"v_mov_b32 v221, 0\nex 188,96\nmc v,72,a,32,a,16,164\nrd a,120,209,12288\nex 189,97\nmm v,72,a,36,a,20\n"
"ex 190,98\nmm v,72,a,40,a,24\nrd a,128,209,16384\nad 200,188,189\nmm v,72,a,44,a,28\ncv 210,188,189\n"
"ex 191,99\nmc v,76,a,48,a,16,164\nrd a,136,209,20480\nad 200,200,190\nmm v,76,a,52,a,20\nex 192,100\n"
"mm v,76,a,56,a,24\nad 200,200,191\ncv 211,190,191\nmm v,76,a,60,a,28\nrd a,144,209,24576\nex 193,101\n"
"mc v,88,a,32,a,176,172\nad 200,200,192\nex 194,102\nmm v,88,a,36,a,180\nrd a,152,209,28672\nad 200,200,193\n"
"mm v,88,a,40,a,184\ncv 212,192,193\nex 195,103\nmm v,88,a,44,a,188\nad 200,200,194\nex 188,104\n"
"mc v,92,a,48,a,176,172\nad 200,200,195\ncv 213,194,195\nmm v,92,a,52,a,180\nex 189,105\nmm v,92,a,56,a,184\n"
"ex 190,106\nmm v,92,a,60,a,188\nwl 0\nad 201,188,189\nmm v,0,a,96,v,128\ncv 214,188,189\n"
"ex 191,107\nmm v,32,a,96,v,132\nrd a,64,208,24576\nad 201,201,190\nmm a,192,a,96,v,136\nex 192,108\n"
"mm a,224,a,96,v,140\nad 201,201,191\ncv 215,190,191\nmm v,4,a,104,v,128\nrd a,68,208,25600\nex 193,109\n"
"mm v,36,a,104,v,132\nad 201,201,192\nex 194,110\nmm a,196,a,104,v,136\nad 201,201,193\ncv 216,192,193\n"
"mm a,228,a,104,v,140\nrd a,72,208,26624\nex 195,111\nmm v,8,a,112,v,128\nad 201,201,194\nex 188,112\n"
"mm v,40,a,112,v,132\nad 201,201,195\ncv 217,194,195\nmm a,200,a,112,v,136\nrd a,76,208,27648\nex 189,113\n"
"mm a,232,a,112,v,140\nex 190,114\nmm v,12,a,120,v,128\nad 202,188,189\ncv 218,188,189\nmm v,44,a,120,v,132\n"
"rd a,80,208,28672\nex 191,115\nmm a,204,a,120,v,136\nad 202,202,190\nex 192,120\nmm a,236,a,120,v,140\n"
"ad 202,202,191\ncv 219,190,191\nmm v,16,a,128,v,128\nrd a,84,208,29696\nex 193,121\nmm v,48,a,128,v,132\n"
"ex 194,122\nmm a,208,a,128,v,136\nad 203,192,193\ncv 222,192,193\nmm a,240,a,128,v,140\nrd a,88,208,30720\n"
"ex 195,123\nmm v,20,a,136,v,128\nad 203,203,194\nex 188,124\nmm v,52,a,136,v,132\nad 203,203,195\n"
"cv 223,194,195\nmm a,212,a,136,v,136\nrd a,92,208,31744\nex 189,125\nmm a,244,a,136,v,140\nad 203,203,188\n"
"ex 190,126\nmm v,24,a,144,v,128\nad 203,203,189\ncv 224,188,189\nmm v,56,a,144,v,132\nex 191,127\n"
"mm a,216,a,144,v,136\nad 203,203,190\ns_nop 0\nmm a,248,a,144,v,140\nad 203,203,191\ncv 225,190,191\n"
"mm v,28,a,152,v,128\nad 238,200,201\nad 239,202,203\nmm v,60,a,152,v,132\nad 238,238,239\nv_cmp_nge_f32 vcc, s59, v238\n"
"mm a,220,a,152,v,136\nmm a,252,a,152,v,140\nwl 0\ns_cbranch_vccnz RS_17\nRR_17:\nmc v,96,a,64,a,0,160\n"
"v_mov_b32 v80, 0xff800000\nv_mov_b32 v81, 0xff800000\nv_mov_b32 v82, 0xff800000\nmm v,96,a,68,a,4\nrd a,100,209,1024\nv_mov_b32 v83, 0xff800000\n"
"v_mov_b32 v84, 0xff800000\nmm v,96,a,72,a,8\nrd a,108,209,5120\nv_mov_b32 v85, 0xff800000\nv_mov_b32 v86, 0xff800000\nmm v,96,a,76,a,12\n"
"rd a,116,209,9216\nv_mov_b32 v87, 0xff800000\nmc v,100,a,80,a,0,160\nad 92,92,184\nad 93,93,185\nad 94,94,186\n"
"mm v,100,a,84,a,4\nrd a,124,209,13312\nad 95,95,187\nad 204,204,200\nmm v,100,a,88,a,8\nrd a,132,209,17408\n"
"ad 205,205,201\nad 206,206,202\nmm v,100,a,92,a,12\nrd a,140,209,21504\nad 207,207,203\nmc v,104,a,64,a,16,164\n"
"ex 188,64\nex 189,65\nmm v,104,a,68,a,20\nrd a,148,209,25600\nex 190,66\nmm v,104,a,72,a,24\n"
"rd a,156,209,29696\nad 196,188,189\nmm v,104,a,76,a,28\ncv 144,188,189\nex 191,67\nmc v,108,a,80,a,16,164\n"
"ad 196,196,190\nex 192,68\nmm v,108,a,84,a,20\nad 196,196,191\ncv 145,190,191\nmm v,108,a,88,a,24\n"
"ex 193,69\nad 196,196,192\nmm v,108,a,92,a,28\ns_waitcnt vmcnt(0) lgkmcnt(0)\ns_barrier\nv_xor_b32 v208, 0x8000, v208\n"
"ex 194,70\nmm v,0,a,100,v,210\nad 196,196,193\ncv 146,192,193\nmm v,32,a,100,v,214\nrd a,32,208,0\n"
"ex 195,71\nmm a,192,a,100,v,218\nrd a,36,208,1024\nad 196,196,194\nex 188,72\nmm a,224,a,100,v,222\n"
"s_mov_b32 m0, s57\ns_nop 0\ndc 230,40,53,0\nmm v,4,a,108,v,210\nrd a,40,208,2048\nad 196,196,195\n"
"mm v,36,a,108,v,214\nrd a,44,208,3072\ncv 147,194,195\nmm a,196,a,108,v,218\nex 189,73\nex 190,74\n"
"mm a,228,a,108,v,222\nrd a,48,208,4096\nad 197,188,189\nmm v,8,a,116,v,210\nrd a,52,208,5120\ndc 230,40,53,1024\n"
"cv 152,188,189\nmm v,40,a,116,v,214\nex 191,75\nad 197,197,190\nmm a,200,a,116,v,218\nrd a,56,208,6144\n"
"ex 192,76\nmm a,232,a,116,v,222\nrd a,60,208,7168\nad 197,197,191\ncv 153,190,191\nmm v,12,a,124,v,210\n"
"ex 193,77\nad 197,197,192\nmm v,44,a,124,v,214\nrd a,64,208,8192\ndc 230,40,53,2048\nmm a,204,a,124,v,218\n"
"ex 194,78\nad 197,197,193\nmm a,236,a,124,v,222\nrd a,68,208,9216\ncv 154,192,193\nex 195,79\n"
"mm v,16,a,132,v,210\nrd a,72,208,10240\nad 197,197,194\nmm v,48,a,132,v,214\nex 188,88\nad 197,197,195\n"
"mm a,208,a,132,v,218\nrd a,76,208,11264\ndc 230,40,53,3072\ncv 155,194,195\nmm a,240,a,132,v,222\nrd a,80,208,12288\n"
"ex 189,89\nmm v,20,a,140,v,210\nex 190,90\nad 199,188,189\nmm v,52,a,140,v,214\nrd a,84,208,13312\n"
"cv 132,188,189\nmm a,212,a,140,v,218\nrd a,88,208,14336\nex 191,91\nmm a,244,a,140,v,222\nsa m0,m0,0x1000\n"
"s_nop 0\ndc 231,40,53,0\nmm v,24,a,148,v,210\nrd a,92,208,15360\nad 199,199,190\nex 192,92\n"
"mm v,56,a,148,v,214\nad 199,199,191\ncv 133,190,191\nmm a,216,a,148,v,218\nex 193,93\nad 199,199,192\n"
"mm a,248,a,148,v,222\nex 194,94\nad 199,199,193\nmm v,28,a,156,v,210\ndc 231,40,53,1024\ncv 134,192,193\n"
"mm v,60,a,156,v,214\nex 195,95\nad 199,199,194\nmm a,220,a,156,v,218\ns_nop 0\nad 199,199,195\n"
"cv 135,194,195\nad 238,196,197\nad 238,238,199\nv_cmp_nge_f32 vcc, s59, v238\nmm a,252,a,156,v,222\nwl 8\n"
"s_cbranch_vccnz RS_18\nRR_18:\nmc v,64,a,32,a,0,160\nv_mov_b32 v112, 0xff800000\nv_mov_b32 v113, 0xff800000\nv_mov_b32 v114, 0xff800000\n"
"mm v,64,a,36,a,4\nrd a,96,209,2048\nv_mov_b32 v115, 0xff800000\nv_mov_b32 v116, 0xff800000\nmm v,64,a,40,a,8\ndc 231,40,53,2048\n"
"v_mov_b32 v117, 0xff800000\nv_mov_b32 v118, 0xff800000\nmm v,64,a,44,a,12\nv_mov_b32 v119, 0xff800000\nv_mov_b32 v120, 0xff800000\nv_mov_b32 v121, 0xff800000\n"
"mc v,80,a,32,a,16,164\nrd a,104,209,6144\nv_mov_b32 v122, 0xff800000\nmm v,80,a,36,a,20\ndc 231,40,53,3072\nv_mov_b32 v123, 0xff800000\n"
"v_mov_b32 v124, 0xff800000\nmm v,80,a,40,a,24\nv_mov_b32 v125, 0xff800000\nv_mov_b32 v126, 0xff800000\nv_mov_b32 v127, 0xff800000\nmm v,80,a,44,a,28\n"
"rd a,112,209,10240\nad 204,204,196\nad 205,205,197\nmc v,68,a,48,a,0,160\ns_xor_b32 m0, s57, 0x18000\ns_nop 0\n"
"dc 232,40,54,0\nmm v,68,a,52,a,4\nad 207,207,199\nex 188,96\nmm v,68,a,56,a,8\nex 189,97\n"
"mm v,68,a,60,a,12\nrd a,120,209,14336\nex 190,98\nad 200,188,189\nmc v,84,a,48,a,16,164\ndc 232,40,54,1024\n"
"cv 148,188,189\nmm v,84,a,52,a,20\nex 191,99\nad 200,200,190\nmm v,84,a,56,a,24\nrd a,128,209,18432\n"
"ex 192,100\nmm v,84,a,60,a,28\ndc 232,40,54,2048\nad 200,200,191\ncv 149,190,191\nwl 12\n"
"mc v,72,a,64,a,0,160\nex 193,101\nad 200,200,192\nwl 11\nmm v,72,a,68,a,4\nrd a,136,209,22528\n"
"ex 194,102\nwl 11\nmm v,72,a,72,a,8\nad 200,200,193\ncv 150,192,193\nex 195,103\n"
"wl 10\nmm v,72,a,76,a,12\ndc 232,40,54,3072\nad 200,200,194\nmc v,88,a,64,a,16,164\nex 188,104\n"
"ad 200,200,195\nmm v,88,a,68,a,20\nrd a,144,209,26624\ncv 151,194,195\nmm v,88,a,72,a,24\nsa m0,m0,0x1000\n"
"s_nop 0\ndc 233,40,54,0\nmm v,88,a,76,a,28\nex 189,105\nex 190,106\nwl 10\n"
"mc v,76,a,80,a,0,160\nrd a,152,209,30720\nad 201,188,189\nwl 10\nmm v,76,a,84,a,4\ndc 233,40,54,1024\n"
"cv 156,188,189\nex 191,107\nwl 9\nmm v,76,a,88,a,8\nad 201,201,190\nex 192,108\n"
"wl 8\nmm v,76,a,92,a,12\nad 201,201,191\ncv 157,190,191\nmc v,92,a,80,a,16,164\nex 193,109\n"
"ad 201,201,192\nmm v,92,a,84,a,20\ndc 233,40,54,2048\nex 194,110\nmm v,92,a,88,a,24\nad 201,201,193\n"
"cv 158,192,193\nmm v,92,a,92,a,28\nwl 0\nex 195,111\nad 201,201,194\nmm a,224,a,96,v,132\n"
"dc 233,40,54,3072\nrd a,32,208,16384\nrd a,36,208,17408\nmm a,228,a,104,v,132\nrd a,40,208,18432\nrd a,44,208,19456\n"
"rd a,48,208,20480\nrd a,52,208,21504\nrd a,56,208,22528\nrd a,60,208,23552\nrd a,64,208,24576\nrd a,68,208,25600\n"
"rd a,72,208,26624\nrd a,76,208,27648\nrd a,80,208,28672\nrd a,84,208,29696\nrd a,88,208,30720\nrd a,92,208,31744\n"
"mm a,232,a,112,v,132\ns_nop 0\nad 201,201,195\nmm a,236,a,120,v,132\ncv 159,194,195\nad 238,200,201\n"
"mm a,240,a,128,v,132\nv_cmp_nge_f32 vcc, s59, v238\nmm a,244,a,136,v,132\ns_xor_b32 s57, s57, 0x8000\nmm a,248,a,144,v,132\nmm a,252,a,152,v,132\n"
"s_cbranch_vccnz RS_19\nRR_19:\nwl 0\ns_branch SHORT_END\nDIAG_S3:\nmc v,96,a,64,a,0,160\n"
"ad 204,204,200\nad 205,205,201\nmm v,96,a,68,a,4\nrd a,100,209,3072\nad 206,206,202\nmm v,96,a,72,a,8\n"
"ad 207,207,203\nex 188,64\nmm v,96,a,76,a,12\nex 189,65\nmc v,100,a,80,a,0,160\nrd a,108,209,7168\n"
"ex 190,66\nmm v,100,a,84,a,4\ns_mov_b32 s54, s53\nad 196,188,189\nmm v,100,a,88,a,8\ncv 128,188,189\n"
"ex 191,67\nmm v,100,a,92,a,12\nrd a,116,209,11264\nad 196,196,190\nmc v,104,a,64,a,16,164\nex 192,68\n"
"mm v,104,a,68,a,20\nad 196,196,191\ncv 129,190,191\nmm v,104,a,72,a,24\nex 193,69\nmm v,104,a,76,a,28\n"
"rd a,124,209,15360\nsa s53,s53,0x10000\nmc v,108,a,80,a,16,164\nad 196,196,192\nex 194,70\nmm v,108,a,84,a,20\n"
"ad 196,196,193\ncv 130,192,193\nmm v,108,a,88,a,24\nrd a,132,209,19456\nex 195,71\nmm v,108,a,92,a,28\n"
"ad 196,196,194\nex 188,72\nmc v,112,a,64,a,160,168\nad 196,196,195\ncv 131,194,195\nmm v,112,a,68,a,164\n"
"rd a,140,209,23552\ns_cmp_eq_u32 s53, s55\ns_cselect_b32 s53, s56, s53\nmm v,112,a,72,a,168\nex 189,73\nmm v,112,a,76,a,172\n"
"ex 190,74\nmc v,116,a,80,a,160,168\nad 197,188,189\ncv 132,188,189\nmm v,116,a,84,a,164\nrd a,148,209,27648\n"
"ex 191,75\nmm v,116,a,88,a,168\nad 197,197,190\nex 192,76\nmm v,116,a,92,a,172\nad 197,197,191\n"
"cv 133,190,191\nmc v,120,a,64,a,176,172\nrd a,156,209,31744\nex 193,77\nmm v,120,a,68,a,180\nad 197,197,192\n"
"ex 194,78\nmm v,120,a,72,a,184\nad 197,197,193\ncv 134,192,193\nmm v,120,a,76,a,188\nex 195,79\n"
"mc v,124,a,80,a,176,172\nad 197,197,194\nex 188,80\nmm v,124,a,84,a,180\nad 197,197,195\ncv 135,194,195\n"
"mm v,124,a,88,a,184\nex 189,81\nmm v,124,a,92,a,188\nwl 0\nex 190,82\nmm v,0,a,100,v,144\n"
"ad 198,188,189\ncv 136,188,189\nmm v,32,a,100,v,148\nrd a,32,208,16384\nex 191,83\nmm a,192,a,100,v,152\n"
"ad 198,198,190\nex 192,84\nmm a,224,a,100,v,156\nad 198,198,191\ncv 137,190,191\nmm v,4,a,108,v,144\n"
"rd a,36,208,17408\nex 193,85\nmm v,36,a,108,v,148\nad 198,198,192\nex 194,86\nmm a,196,a,108,v,152\n"
"ad 198,198,193\ncv 138,192,193\nmm a,228,a,108,v,156\nrd a,40,208,18432\nex 195,87\nmm v,8,a,116,v,144\n"
"ad 198,198,194\nex 188,88\nmm v,40,a,116,v,148\nad 198,198,195\ncv 139,194,195\nmm a,200,a,116,v,152\n"
"rd a,44,208,19456\nex 189,89\nmm a,232,a,116,v,156\nex 190,90\nmm v,12,a,124,v,144\nad 199,188,189\n"
"cv 140,188,189\nmm v,44,a,124,v,148\nrd a,48,208,20480\nex 191,91\nmm a,204,a,124,v,152\nad 199,199,190\n"
"ex 192,92\nmm a,236,a,124,v,156\nad 199,199,191\ncv 141,190,191\nmm v,16,a,132,v,144\nrd a,52,208,21504\n"
"ex 193,93\nmm v,48,a,132,v,148\nad 199,199,192\nex 194,94\nmm a,208,a,132,v,152\nad 199,199,193\n"
"cv 142,192,193\nmm a,240,a,132,v,156\nrd a,56,208,22528\nex 195,95\nmm v,20,a,140,v,144\nad 199,199,194\n"
"s_nop 0\nmm v,52,a,140,v,148\nad 199,199,195\nrd a,60,208,23552\nmm a,212,a,140,v,152\ncv 143,194,195\n"
"ad 238,196,197\nmm a,244,a,140,v,156\nad 239,198,199\nad 238,238,239\nmm v,24,a,148,v,144\nv_cmp_nge_f32 vcc, s59, v238\n"
"mm v,56,a,148,v,148\nmm a,216,a,148,v,152\nmm a,248,a,148,v,156\nmm v,28,a,156,v,144\nmm v,60,a,156,v,148\nmm a,220,a,156,v,152\n"
"mm a,252,a,156,v,156\ns_waitcnt vmcnt(8) lgkmcnt(0)\ns_barrier\nv_xor_b32 v209, 0x8000, v209\ns_cbranch_vccnz RS_20\nRR_20:\n"
"mc v,64,a,32,a,0,160\nad 116,116,184\nad 117,117,185\nad 118,118,186\nmm v,64,a,36,a,4\nrd a,96,209,0\n"
"ad 119,119,187\nmm v,64,a,40,a,8\nrd a,104,209,4096\nad 204,204,196\nad 205,205,197\nmm v,64,a,44,a,12\n"
"ad 206,206,198\nad 207,207,199\nmc v,68,a,48,a,0,160\nrd a,112,209,8192\nex 188,96\nmm v,68,a,52,a,4\n"
"ex 189,97\nmm v,68,a,56,a,8\nrd a,120,209,12288\nex 190,98\nad 200,188,189\nmm v,68,a,60,a,12\n"
"cv 210,188,189\nex 191,99\nmc v,72,a,32,a,16,164\nrd a,128,209,16384\nad 200,200,190\nmm v,72,a,36,a,20\n"
"ex 192,100\nad 200,200,191\nmm v,72,a,40,a,24\nrd a,136,209,20480\ncv 211,190,191\nmm v,72,a,44,a,28\n"
"ex 193,101\nad 200,200,192\nmc v,76,a,48,a,16,164\nrd a,144,209,24576\nex 194,102\nmm v,76,a,52,a,20\n"
"ad 200,200,193\ncv 212,192,193\nmm v,76,a,56,a,24\nrd a,152,209,28672\nex 195,103\nmm v,76,a,60,a,28\n"
"ad 200,200,194\nex 188,104\nmc v,88,a,32,a,176,172\nad 200,200,195\ncv 213,194,195\nmm v,88,a,36,a,180\n"
"ex 189,105\nmm v,88,a,40,a,184\nex 190,106\nad 201,188,189\nmm v,88,a,44,a,188\nwl 0\n"
"cv 214,188,189\nex 191,107\nmm v,0,a,96,v,128\nad 201,201,190\nex 192,108\nmm v,32,a,96,v,132\n"
"rd a,64,208,24576\nad 201,201,191\nmm a,192,a,96,v,136\ncv 215,190,191\nex 193,109\nmm a,224,a,96,v,140\n"
"ad 201,201,192\nex 194,110\nmm v,4,a,104,v,128\nrd a,68,208,25600\nad 201,201,193\nmm v,36,a,104,v,132\n"
"cv 216,192,193\nex 195,111\nmm a,196,a,104,v,136\nad 201,201,194\nex 188,112\nmm a,228,a,104,v,140\n"
"rd a,72,208,26624\nad 201,201,195\nmm v,8,a,112,v,128\ncv 217,194,195\nex 189,113\nmm v,40,a,112,v,132\n"
"ex 190,114\nmm a,200,a,112,v,136\nrd a,76,208,27648\nad 202,188,189\nmm a,232,a,112,v,140\ncv 218,188,189\n"
"ex 191,115\nmm v,12,a,120,v,128\nad 202,202,190\nex 192,116\nmm v,44,a,120,v,132\nrd a,80,208,28672\n"
"ad 202,202,191\nmm a,204,a,120,v,136\ncv 219,190,191\nex 193,117\nmm a,236,a,120,v,140\nad 202,202,192\n"
"ex 194,118\nmm v,16,a,128,v,128\nrd a,84,208,29696\nad 202,202,193\nmm v,48,a,128,v,132\ncv 220,192,193\n"
"ex 195,119\nmm a,208,a,128,v,136\nad 202,202,194\nex 188,120\nmm a,240,a,128,v,140\nrd a,88,208,30720\n"
"ad 202,202,195\nmm v,20,a,136,v,128\ncv 221,194,195\nex 189,121\nmm v,52,a,136,v,132\nex 190,122\n"
"mm a,212,a,136,v,136\nrd a,92,208,31744\nad 203,188,189\ncv 222,188,189\nmm a,244,a,136,v,140\nex 191,123\n"
"ad 203,203,190\nmm v,24,a,144,v,128\nex 192,124\nmm v,56,a,144,v,132\nad 203,203,191\ncv 223,190,191\n"
"ex 193,125\nmm a,216,a,144,v,136\nad 203,203,192\nex 194,126\nmm a,248,a,144,v,140\nad 203,203,193\n"
"cv 224,192,193\nmm v,28,a,152,v,128\nex 195,127\nmm v,60,a,152,v,132\nad 203,203,194\ns_nop 0\n"
"ad 203,203,195\nmm a,220,a,152,v,136\ncv 225,194,195\nad 238,200,201\nad 239,202,203\nad 238,238,239\n"
"v_cmp_nge_f32 vcc, s59, v238\nmm a,252,a,152,v,140\nwl 0\ns_cbranch_vccnz RS_21\nRR_21:\nmc v,96,a,64,a,0,160\n"
"v_mov_b32 v80, 0xff800000\nv_mov_b32 v81, 0xff800000\nv_mov_b32 v82, 0xff800000\nmm v,96,a,68,a,4\nrd a,100,209,1024\nv_mov_b32 v83, 0xff800000\n"
"mm v,96,a,72,a,8\nrd a,108,209,5120\nv_mov_b32 v84, 0xff800000\nv_mov_b32 v85, 0xff800000\nmm v,96,a,76,a,12\nrd a,116,209,9216\n"
"v_mov_b32 v86, 0xff800000\nv_mov_b32 v87, 0xff800000\nmc v,100,a,80,a,0,160\nad 88,88,184\nad 89,89,185\nmm v,100,a,84,a,4\n"
"rd a,124,209,13312\nad 90,90,186\nad 91,91,187\nmm v,100,a,88,a,8\nrd a,132,209,17408\nv_mov_b32 v92, 0xff800000\n"
"v_mov_b32 v93, 0xff800000\nmm v,100,a,92,a,12\nrd a,140,209,21504\nv_mov_b32 v94, 0xff800000\nmc v,104,a,64,a,16,164\nv_mov_b32 v95, 0xff800000\n"
"ad 204,204,200\nad 205,205,201\nmm v,104,a,68,a,20\nrd a,148,209,25600\nad 206,206,202\nad 207,207,203\n"
"mm v,104,a,72,a,24\nrd a,156,209,29696\nv_mov_b32 v134, 0\nv_mov_b32 v135, 0\nmm v,104,a,76,a,28\nex 188,64\n"
"mc v,108,a,80,a,16,164\nex 189,65\nmm v,108,a,84,a,20\nex 190,66\nad 196,188,189\ncv 144,188,189\n"
"mm v,108,a,88,a,24\nex 191,67\nmm v,108,a,92,a,28\ns_waitcnt vmcnt(0) lgkmcnt(0)\ns_barrier\nv_xor_b32 v208, 0x8000, v208\n"
"ad 196,196,190\nmm v,0,a,100,v,210\nex 192,68\nad 196,196,191\nmm v,32,a,100,v,214\nrd a,32,208,0\n"
"cv 145,190,191\nmm a,192,a,100,v,218\nrd a,36,208,1024\nex 193,69\nmm a,224,a,100,v,222\ns_mov_b32 m0, s57\n"
"s_nop 0\ndc 230,40,53,0\nmm v,4,a,108,v,210\nrd a,40,208,2048\nad 196,196,192\nmm v,36,a,108,v,214\n"
"rd a,44,208,3072\nex 194,70\nmm a,196,a,108,v,218\nad 196,196,193\ncv 146,192,193\nmm a,228,a,108,v,222\n"
"rd a,48,208,4096\nex 195,71\nmm v,8,a,116,v,210\nrd a,52,208,5120\ndc 230,40,53,1024\nad 196,196,194\n"
"mm v,40,a,116,v,214\nex 188,72\nad 196,196,195\nmm a,200,a,116,v,218\nrd a,56,208,6144\ncv 147,194,195\n"
"mm a,232,a,116,v,222\nrd a,60,208,7168\nex 189,73\nmm v,12,a,124,v,210\nex 190,74\nad 197,188,189\n"
"mm v,44,a,124,v,214\nrd a,64,208,8192\ndc 230,40,53,2048\ncv 152,188,189\nmm a,204,a,124,v,218\nex 191,75\n"
"mm a,236,a,124,v,222\nrd a,68,208,9216\nad 197,197,190\nex 192,76\nmm v,16,a,132,v,210\nrd a,72,208,10240\n"
"ad 197,197,191\nmm v,48,a,132,v,214\ncv 153,190,191\nex 193,77\nmm a,208,a,132,v,218\nrd a,76,208,11264\n"
"dc 230,40,53,3072\nmm a,240,a,132,v,222\nrd a,80,208,12288\nad 197,197,192\nmm v,20,a,140,v,210\nex 194,78\n"
"ad 197,197,193\nmm v,52,a,140,v,214\nrd a,84,208,13312\ncv 154,192,193\nex 195,79\nmm a,212,a,140,v,218\n"
"rd a,88,208,14336\nad 197,197,194\nmm a,244,a,140,v,222\nsa m0,m0,0x1000\ns_nop 0\ndc 231,40,53,0\n"
"mm v,24,a,148,v,210\nrd a,92,208,15360\nex 188,88\nmm v,56,a,148,v,214\nad 197,197,195\ncv 155,194,195\n"
"mm a,216,a,148,v,218\nex 189,89\nmm a,248,a,148,v,222\nex 190,90\nad 199,188,189\nmm v,28,a,156,v,210\n"
"dc 231,40,53,1024\ncv 132,188,189\nmm v,60,a,156,v,214\nex 191,91\nad 199,199,190\ns_nop 0\n"
"mm a,220,a,156,v,218\nad 199,199,191\ncv 133,190,191\nad 238,196,197\nad 238,238,199\nv_cmp_nge_f32 vcc, s59, v238\n"
"mm a,252,a,156,v,222\nwl 8\ns_cbranch_vccnz RS_22\nRR_22:\nmc v,64,a,32,a,0,160\nv_mov_b32 v112, 0xff800000\n"
"v_mov_b32 v113, 0xff800000\nv_mov_b32 v114, 0xff800000\nmm v,64,a,36,a,4\nrd a,96,209,2048\nv_mov_b32 v115, 0xff800000\nv_mov_b32 v116, 0xff800000\n"
"mm v,64,a,40,a,8\ndc 231,40,53,2048\nv_mov_b32 v117, 0xff800000\nv_mov_b32 v118, 0xff800000\nmm v,64,a,44,a,12\nv_mov_b32 v119, 0xff800000\n"
"v_mov_b32 v120, 0xff800000\nv_mov_b32 v121, 0xff800000\nmc v,80,a,32,a,16,164\nrd a,104,209,6144\nv_mov_b32 v122, 0xff800000\nmm v,80,a,36,a,20\n"
"dc 231,40,53,3072\nv_mov_b32 v123, 0xff800000\nv_mov_b32 v124, 0xff800000\nmm v,80,a,40,a,24\nv_mov_b32 v125, 0xff800000\nv_mov_b32 v126, 0xff800000\n"
"v_mov_b32 v127, 0xff800000\nmm v,80,a,44,a,28\nrd a,112,209,10240\nad 204,204,196\nad 205,205,197\nmc v,68,a,48,a,0,160\n"
"s_xor_b32 m0, s57, 0x18000\ns_nop 0\ndc 232,40,54,0\nmm v,68,a,52,a,4\nad 207,207,199\nex 188,96\n"
"mm v,68,a,56,a,8\nex 189,97\nmm v,68,a,60,a,12\nrd a,120,209,14336\nex 190,98\nad 200,188,189\n"
"mc v,84,a,48,a,16,164\ndc 232,40,54,1024\ncv 148,188,189\nmm v,84,a,52,a,20\nex 191,99\nad 200,200,190\n"
"mm v,84,a,56,a,24\nrd a,128,209,18432\nex 192,100\nmm v,84,a,60,a,28\ndc 232,40,54,2048\nad 200,200,191\n"
"cv 149,190,191\nwl 12\nmc v,72,a,64,a,0,160\nex 193,101\nad 200,200,192\nwl 11\n"
"mm v,72,a,68,a,4\nrd a,136,209,22528\nex 194,102\nwl 11\nmm v,72,a,72,a,8\nad 200,200,193\n"
"cv 150,192,193\nex 195,103\nwl 10\nmm v,72,a,76,a,12\ndc 232,40,54,3072\nad 200,200,194\n"
"mc v,88,a,64,a,16,164\nex 188,104\nad 200,200,195\nmm v,88,a,68,a,20\nrd a,144,209,26624\ncv 151,194,195\n"
"mm v,88,a,72,a,24\nsa m0,m0,0x1000\ns_nop 0\ndc 233,40,54,0\nmm v,88,a,76,a,28\nex 189,105\n"
"ex 190,106\nwl 10\nmc v,76,a,80,a,0,160\nrd a,152,209,30720\nad 201,188,189\nwl 10\n"
"mm v,76,a,84,a,4\ndc 233,40,54,1024\ncv 156,188,189\nex 191,107\nwl 9\nmm v,76,a,88,a,8\n"
"ad 201,201,190\nex 192,108\nwl 8\nmm v,76,a,92,a,12\nad 201,201,191\ncv 157,190,191\n"
"mc v,92,a,80,a,16,164\nex 193,109\nad 201,201,192\nmm v,92,a,84,a,20\ndc 233,40,54,2048\nex 194,110\n"
"mm v,92,a,88,a,24\nad 201,201,193\ncv 158,192,193\nmm v,92,a,92,a,28\nwl 0\nex 195,111\n"
"ad 201,201,194\nmm a,224,a,96,v,132\ndc 233,40,54,3072\nrd a,32,208,16384\nrd a,36,208,17408\nmm a,228,a,104,v,132\n"
"rd a,40,208,18432\nrd a,44,208,19456\nrd a,48,208,20480\nrd a,52,208,21504\nrd a,56,208,22528\nrd a,60,208,23552\n"
"rd a,64,208,24576\nrd a,68,208,25600\nrd a,72,208,26624\nrd a,76,208,27648\nrd a,80,208,28672\nrd a,84,208,29696\n"
"rd a,88,208,30720\nrd a,92,208,31744\nmm a,232,a,112,v,132\ns_nop 0\nad 201,201,195\nmm a,236,a,120,v,132\n"
"cv 159,194,195\nad 238,200,201\nmm a,240,a,128,v,132\nv_cmp_nge_f32 vcc, s59, v238\nmm a,244,a,136,v,132\ns_xor_b32 s57, s57, 0x8000\n"
"mm a,248,a,144,v,132\nmm a,252,a,152,v,132\ns_cbranch_vccnz RS_23\nRR_23:\nwl 0\ns_branch SHORT_END\n"
"ZERO_S:\ns_cmp_eq_u32 s52, 1\ns_cbranch_scc1 FD_S1\ns_cmp_eq_u32 s52, 2\ns_cbranch_scc1 FD_S2\ns_cmp_eq_u32 s52, 3\n"
"s_cbranch_scc1 FD_S3\nFD_S0:\nmc v,96,a,64,a,0,160\nad 80,80,184\nad 81,81,185\nad 82,82,186\n"
"ad 83,83,187\nv_mov_b32 v84, 0xff800000\nmm v,96,a,68,a,4\nv_mov_b32 v85, 0xff800000\nv_mov_b32 v86, 0xff800000\nv_mov_b32 v87, 0xff800000\n"
"v_mov_b32 v138, 0\nv_mov_b32 v139, 0\nmm v,96,a,72,a,8\nex 188,64\nex 189,65\nex 190,66\n"
"mm v,96,a,76,a,12\nad 196,188,189\ncv 128,188,189\nex 191,67\nmc v,100,a,80,a,0,160\nad 196,196,190\n"
"ex 192,68\nad 196,196,191\ncv 129,190,191\nmm v,100,a,84,a,4\ns_mov_b32 s54, s53\nex 193,69\n"
"ad 196,196,192\nex 194,70\nmm v,100,a,88,a,8\nad 196,196,193\ncv 130,192,193\nex 195,71\n"
"mm v,100,a,92,a,12\nad 196,196,194\nex 188,72\nad 196,196,195\ncv 131,194,195\nmc v,104,a,64,a,16,164\n"
"ex 189,73\nex 190,74\nad 197,188,189\nmm v,104,a,68,a,20\ncv 132,188,189\nex 191,75\n"
"ad 197,197,190\nex 192,76\nmm v,104,a,72,a,24\nad 197,197,191\ncv 133,190,191\nex 193,77\n"
"mm v,104,a,76,a,28\nsa s53,s53,0x10000\nad 197,197,192\nex 194,78\nad 197,197,193\nmc v,108,a,80,a,16,164\n"
"cv 134,192,193\nex 195,79\nad 197,197,194\nex 188,80\nmm v,108,a,84,a,20\nrd a,32,208,16384\n"
"rd a,36,208,17408\nad 197,197,195\ncv 135,194,195\nmm v,108,a,88,a,24\nrd a,40,208,18432\nrd a,44,208,19456\n"
"ex 189,81\nex 190,82\nmm v,108,a,92,a,28\nrd a,48,208,20480\nad 198,188,189\ncv 136,188,189\n"
"ex 191,83\nmc v,120,a,64,a,176,172\nrd a,52,208,21504\nrd a,56,208,22528\nad 198,198,190\nex 192,88\n"
"mm v,120,a,68,a,180\nrd a,60,208,23552\ns_cmp_eq_u32 s53, s55\ns_cselect_b32 s53, s56, s53\nad 198,198,191\ncv 137,190,191\n"
"mm v,120,a,72,a,184\nex 193,89\nex 194,90\nad 199,192,193\nmm v,120,a,76,a,188\ncv 140,192,193\n"
"ex 195,91\nad 199,199,194\nmc v,124,a,80,a,176,172\nex 188,92\nad 199,199,195\ncv 141,194,195\n"
"ex 189,93\nmm v,124,a,84,a,180\nad 199,199,188\nex 190,94\nad 199,199,189\ncv 142,188,189\n"
"mm v,124,a,88,a,184\nex 191,95\nad 199,199,190\ns_nop 0\nad 199,199,191\ncv 143,190,191\n"
"ad 238,196,197\nad 239,198,199\nad 238,238,239\nv_cmp_nge_f32 vcc, s59, v238\nmm v,124,a,92,a,188\ns_waitcnt vmcnt(8) lgkmcnt(0)\n"
"s_barrier\nv_xor_b32 v209, 0x8000, v209\ns_cbranch_vccnz RS_24\nRR_24:\nmc v,64,a,32,a,0,160\nv_mov_b32 v112, 0xff800000\n"
"v_mov_b32 v113, 0xff800000\nmm v,64,a,36,a,4\nrd a,96,209,0\nv_mov_b32 v114, 0xff800000\nmm v,64,a,40,a,8\nv_mov_b32 v115, 0xff800000\n"
"v_mov_b32 v116, 0xff800000\nmm v,64,a,44,a,12\nrd a,104,209,4096\nv_mov_b32 v240, s46\nv_mov_b32 v241, s47\ns_mov_b64 exec, 1\n"
"buffer_store_dwordx2 v[240:241], v245, s[96:99], s33 offen sc1\ns_mov_b64 exec, -1\nmc v,68,a,48,a,0,160\nv_mov_b32 v117, 0xff800000\nv_mov_b32 v118, 0xff800000\nmm v,68,a,52,a,4\n"
"rd a,112,209,8192\nv_mov_b32 v119, 0xff800000\nmm v,68,a,56,a,8\nad 204,204,196\nad 205,205,197\nmm v,68,a,60,a,12\n"
"ad 206,206,198\nad 207,207,199\nmc v,72,a,32,a,16,164\nrd a,120,209,12288\nex 188,96\nmm v,72,a,36,a,20\n"
"ex 189,97\nmm v,72,a,40,a,24\nrd a,128,209,16384\nex 190,98\nmm v,72,a,44,a,28\nad 200,188,189\n"
"cv 210,188,189\nmc v,76,a,48,a,16,164\nrd a,136,209,20480\nex 191,99\nmm v,76,a,52,a,20\nad 200,200,190\n"
"ex 192,100\nmm v,76,a,56,a,24\nad 200,200,191\ncv 211,190,191\nmm v,76,a,60,a,28\nrd a,144,209,24576\n"
"ex 193,101\nmc v,88,a,32,a,176,172\nad 200,200,192\nex 194,102\nmm v,88,a,36,a,180\nrd a,152,209,28672\n"
"ad 200,200,193\nmm v,88,a,40,a,184\ncv 212,192,193\nex 195,103\nmm v,88,a,44,a,188\nad 200,200,194\n"
"ex 188,104\nmc v,92,a,48,a,176,172\nad 200,200,195\ncv 213,194,195\nmm v,92,a,52,a,180\nex 189,105\n"
"mm v,92,a,56,a,184\nex 190,106\nmm v,92,a,60,a,188\nwl 0\nad 201,188,189\nmz v,0,a,96,v,128\n"
"cv 214,188,189\nex 191,107\nmz v,32,a,96,v,132\nrd a,64,208,24576\nad 201,201,190\nmz a,192,a,96,v,136\n"
"ex 192,108\nmz a,224,a,96,v,140\nad 201,201,191\ncv 215,190,191\nmz v,4,a,104,v,128\nrd a,68,208,25600\n"
"ex 193,109\nmz v,36,a,104,v,132\nad 201,201,192\nex 194,110\nmz a,196,a,104,v,136\nad 201,201,193\n"
"cv 216,192,193\nmz a,228,a,104,v,140\nrd a,72,208,26624\nex 195,111\nmz v,8,a,112,v,128\nad 201,201,194\n"
"ex 188,120\nmz v,40,a,112,v,132\nad 201,201,195\ncv 217,194,195\nmz a,200,a,112,v,136\nrd a,76,208,27648\n"
"ex 189,121\nmz a,232,a,112,v,140\nex 190,122\nmz v,12,a,120,v,128\nad 203,188,189\ncv 222,188,189\n"
"mz v,44,a,120,v,132\nrd a,80,208,28672\nex 191,123\nmz a,204,a,120,v,136\nad 203,203,190\nex 192,124\n"
"mz a,236,a,120,v,140\nad 203,203,191\ncv 223,190,191\nmz v,16,a,128,v,128\nrd a,84,208,29696\nex 193,125\n"
"mz v,48,a,128,v,132\nad 203,203,192\nex 194,126\nmz a,208,a,128,v,136\nad 203,203,193\ncv 224,192,193\n"
"mz a,240,a,128,v,140\nrd a,88,208,30720\nex 195,127\nmz v,20,a,136,v,128\nad 203,203,194\ns_nop 0\n"
"mz v,52,a,136,v,132\nrd a,92,208,31744\nad 203,203,195\nmz a,212,a,136,v,136\ncv 225,194,195\nad 238,200,201\n"
"mz a,244,a,136,v,140\nad 238,238,203\nv_cmp_nge_f32 vcc, s59, v238\nmz v,24,a,144,v,128\nmz v,56,a,144,v,132\nmz a,216,a,144,v,136\n"
"mz a,248,a,144,v,140\nmz v,28,a,152,v,128\nmz v,60,a,152,v,132\nmz a,220,a,152,v,136\nmz a,252,a,152,v,140\nwl 0\n"
"s_cbranch_vccnz RS_25\nRR_25:\nmc v,96,a,64,a,0,160\nv_mov_b32 v80, 0xff800000\nv_mov_b32 v81, 0xff800000\nv_mov_b32 v82, 0xff800000\n"
"mm v,96,a,68,a,4\nrd a,100,209,1024\nv_mov_b32 v83, 0xff800000\nmm v,96,a,72,a,8\nv_mov_b32 v84, 0xff800000\nv_mov_b32 v85, 0xff800000\n"
"v_mov_b32 v86, 0xff800000\nmm v,96,a,76,a,12\nrd a,108,209,5120\nv_mov_b32 v87, 0xff800000\nad 204,204,200\nmc v,100,a,80,a,0,160\n"
"ad 205,205,201\nad 207,207,203\nmm v,100,a,84,a,4\nrd a,116,209,9216\nex 188,64\nmm v,100,a,88,a,8\n"
"ex 189,65\nex 190,66\nmm v,100,a,92,a,12\nad 196,188,189\ncv 144,188,189\nmc v,104,a,64,a,16,164\n"
"rd a,124,209,13312\nex 191,67\nmm v,104,a,68,a,20\nad 196,196,190\nex 192,68\nmm v,104,a,72,a,24\n"
"rd a,132,209,17408\nad 196,196,191\nmm v,104,a,76,a,28\ncv 145,190,191\nex 193,69\nmc v,108,a,80,a,16,164\n"
"rd a,140,209,21504\nad 196,196,192\nmm v,108,a,84,a,20\nex 194,70\nad 196,196,193\nmm v,108,a,88,a,24\n"
"cv 146,192,193\nex 195,71\nmm v,108,a,92,a,28\nrd a,148,209,25600\nad 196,196,194\nex 188,72\n"
"mc v,120,a,64,a,176,172\nad 196,196,195\ncv 147,194,195\nmm v,120,a,68,a,180\nrd a,156,209,29696\nex 189,73\n"
"mm v,120,a,72,a,184\nex 190,74\nmm v,120,a,76,a,188\nad 197,188,189\ncv 152,188,189\nex 191,75\n"
"mc v,124,a,80,a,176,172\nad 197,197,190\nex 192,76\nmm v,124,a,84,a,180\nad 197,197,191\ncv 153,190,191\n"
"mm v,124,a,88,a,184\nex 193,77\nmm v,124,a,92,a,188\ns_waitcnt vmcnt(1) lgkmcnt(0)\ns_barrier\nv_xor_b32 v208, 0x8000, v208\n"
"ad 197,197,192\nmm v,0,a,100,v,210\nex 194,78\nad 197,197,193\nmm v,32,a,100,v,214\nrd a,32,208,0\n"
"cv 154,192,193\nmm a,224,a,100,v,222\nrd a,36,208,1024\nex 195,79\nmm v,4,a,108,v,210\nrd a,40,208,2048\n"
"s_mov_b32 m0, s57\ns_nop 0\ndc 230,40,53,0\nmm v,36,a,108,v,214\nrd a,44,208,3072\nad 197,197,194\n"
"mm a,228,a,108,v,222\nrd a,48,208,4096\nex 188,88\nmm v,8,a,116,v,210\nrd a,52,208,5120\ndc 230,40,53,1024\n"
"mm v,40,a,116,v,214\nrd a,56,208,6144\nad 197,197,195\ncv 155,194,195\nmm a,232,a,116,v,222\nrd a,60,208,7168\n"
"ex 189,89\nmm v,12,a,124,v,210\nrd a,64,208,8192\nex 190,90\nmm v,44,a,124,v,214\nrd a,68,208,9216\n"
"dc 230,40,53,2048\nmm a,236,a,124,v,222\nrd a,72,208,10240\nad 199,188,189\ncv 132,188,189\nmm v,16,a,132,v,210\n"
"rd a,76,208,11264\nex 191,91\nmm v,48,a,132,v,214\nrd a,80,208,12288\nad 199,199,190\nmm a,240,a,132,v,222\n"
"rd a,84,208,13312\ndc 230,40,53,3072\nex 192,92\nmm v,20,a,140,v,210\nrd a,88,208,14336\nad 199,199,191\n"
"mm v,52,a,140,v,214\nrd a,92,208,15360\ncv 133,190,191\nmm a,244,a,140,v,222\nsa m0,m0,0x1000\ns_nop 0\n"
"dc 231,40,53,0\nmm v,24,a,148,v,210\nex 193,93\nad 199,199,192\nmm v,56,a,148,v,214\nex 194,94\n"
"ad 199,199,193\nmm a,248,a,148,v,222\ncv 134,192,193\nex 195,95\nmm v,28,a,156,v,210\ndc 231,40,53,1024\n"
"ad 199,199,194\nmm v,60,a,156,v,214\ns_nop 0\nad 199,199,195\ncv 135,194,195\nad 238,196,197\n"
"ad 238,238,199\nv_cmp_nge_f32 vcc, s59, v238\nmm a,252,a,156,v,222\nwl 8\ns_cbranch_vccnz RS_26\nRR_26:\n"
"mc v,64,a,32,a,0,160\nv_mov_b32 v112, 0xff800000\nv_mov_b32 v113, 0xff800000\nv_mov_b32 v114, 0xff800000\nmm v,64,a,36,a,4\nrd a,96,209,2048\n"
"v_mov_b32 v115, 0xff800000\nv_mov_b32 v116, 0xff800000\nv_mov_b32 v117, 0xff800000\nmm v,64,a,40,a,8\ndc 231,40,53,2048\nv_mov_b32 v118, 0xff800000\n"
"v_mov_b32 v119, 0xff800000\nmm v,64,a,44,a,12\nad 124,124,184\nad 125,125,185\nad 126,126,186\nad 127,127,187\n"
"mc v,80,a,32,a,16,164\nrd a,104,209,6144\nad 204,204,196\nad 205,205,197\nmm v,80,a,36,a,20\ndc 231,40,53,3072\n"
"ad 207,207,199\nex 188,96\nmm v,80,a,40,a,24\nex 189,97\nex 190,98\nmm v,80,a,44,a,28\n"
"rd a,112,209,10240\nad 200,188,189\ncv 148,188,189\nmc v,68,a,48,a,0,160\ns_xor_b32 m0, s57, 0x18000\ns_nop 0\n"
"dc 232,40,54,0\nmm v,68,a,52,a,4\nex 191,99\nad 200,200,190\nex 192,100\nmm v,68,a,56,a,8\n"
"ad 200,200,191\ncv 149,190,191\nmm v,68,a,60,a,12\nrd a,120,209,14336\nex 193,101\nad 200,200,192\n"
"mc v,84,a,48,a,16,164\ndc 232,40,54,1024\nex 194,102\nmm v,84,a,52,a,20\nad 200,200,193\ncv 150,192,193\n"
"ex 195,103\nmm v,84,a,56,a,24\nrd a,128,209,18432\nad 200,200,194\nex 188,104\nmm v,84,a,60,a,28\n"
"dc 232,40,54,2048\nad 200,200,195\ncv 151,194,195\nwl 12\nmc v,72,a,64,a,0,160\nex 189,105\n"
"ex 190,106\nwl 11\nmm v,72,a,68,a,4\nrd a,136,209,22528\nad 201,188,189\ncv 156,188,189\n"
"wl 11\nmm v,72,a,72,a,8\nex 191,107\nad 201,201,190\nwl 10\nmm v,72,a,76,a,12\n"
"dc 232,40,54,3072\nex 192,108\nad 201,201,191\nmc v,88,a,64,a,16,164\ncv 157,190,191\nex 193,109\n"
"mm v,88,a,68,a,20\nrd a,144,209,26624\nad 201,201,192\nex 194,110\nmm v,88,a,72,a,24\nsa m0,m0,0x1000\n"
"s_nop 0\ndc 233,40,54,0\nmm v,88,a,76,a,28\nad 201,201,193\ncv 158,192,193\nex 195,111\n"
"wl 10\nmc v,76,a,80,a,0,160\nrd a,152,209,30720\nad 201,201,194\nex 188,120\nwl 10\n"
"mm v,76,a,84,a,4\ndc 233,40,54,1024\nad 201,201,195\ncv 159,194,195\nwl 9\nmm v,76,a,88,a,8\n"
"ex 189,121\nex 190,122\nwl 8\nmm v,76,a,92,a,12\nad 203,188,189\ncv 140,188,189\n"
"ex 191,123\nmc v,92,a,80,a,16,164\nad 203,203,190\nex 192,124\nmm v,92,a,84,a,20\ndc 233,40,54,2048\n"
"ad 203,203,191\ncv 141,190,191\nmm v,92,a,88,a,24\nex 193,125\nad 203,203,192\nmm v,92,a,92,a,28\n"
"wl 0\nex 194,126\nad 203,203,193\nmm a,224,a,96,v,132\ndc 233,40,54,3072\nrd a,32,208,16384\n"
"rd a,36,208,17408\nmm a,228,a,104,v,132\nrd a,40,208,18432\nrd a,44,208,19456\nrd a,48,208,20480\nrd a,52,208,21504\n"
"rd a,56,208,22528\nrd a,60,208,23552\nrd a,64,208,24576\nrd a,68,208,25600\nrd a,72,208,26624\nrd a,76,208,27648\n"
"rd a,80,208,28672\nrd a,84,208,29696\nrd a,88,208,30720\nrd a,92,208,31744\nmm a,232,a,112,v,132\ncv 142,192,193\n"
"ex 195,127\nmm a,236,a,120,v,132\nad 203,203,194\ns_nop 0\nmm a,240,a,128,v,132\nad 203,203,195\n"
"cv 143,194,195\nmm a,244,a,136,v,132\nad 238,200,201\nad 238,238,203\nmm a,248,a,144,v,132\nv_cmp_nge_f32 vcc, s59, v238\n"
"s_xor_b32 s57, s57, 0x8000\nmm a,252,a,152,v,132\ns_cbranch_vccnz RS_27\nRR_27:\nrd a,100,209,3072\nrd a,108,209,7168\n"
"rd a,116,209,11264\nrd a,124,209,15360\nrd a,132,209,19456\nrd a,140,209,23552\nrd a,148,209,27648\nrd a,156,209,31744\n"
"ad 207,207,203\nwl 7\nmm a,224,a,100,v,140\nwl 6\nmm a,228,a,108,v,140\nwl 5\n"
"mm a,232,a,116,v,140\nwl 4\nmm a,236,a,124,v,140\nwl 3\nmm a,240,a,132,v,140\nwl 2\n"
"mm a,244,a,140,v,140\nwl 1\nmm a,248,a,148,v,140\nwl 0\nmm a,252,a,156,v,140\ns_branch SHORT_END\n"
"FD_S1:\nmc v,96,a,64,a,0,160\nad 84,84,184\nad 85,85,185\nad 86,86,186\nad 87,87,187\n"
"ex 188,64\nmm v,96,a,68,a,4\nex 189,65\nex 190,66\nad 196,188,189\nmm v,96,a,72,a,8\n"
"cv 128,188,189\nex 191,67\nad 196,196,190\nex 192,68\nmm v,96,a,76,a,12\nad 196,196,191\n"
"cv 129,190,191\nex 193,69\nmc v,100,a,80,a,0,160\nad 196,196,192\nex 194,70\nad 196,196,193\n"
"cv 130,192,193\nmm v,100,a,84,a,4\ns_mov_b32 s54, s53\nex 195,71\nad 196,196,194\nex 188,72\n"
"mm v,100,a,88,a,8\nad 196,196,195\ncv 131,194,195\nex 189,73\nex 190,74\nmm v,100,a,92,a,12\n"
"ad 197,188,189\ncv 132,188,189\nex 191,75\nad 197,197,190\nmc v,104,a,64,a,16,164\nex 192,76\n"
"ad 197,197,191\ncv 133,190,191\nex 193,77\nmm v,104,a,68,a,20\nad 197,197,192\nex 194,78\n"
"ad 197,197,193\ncv 134,192,193\nmm v,104,a,72,a,24\nex 195,79\nad 197,197,194\nex 188,80\n"
"mm v,104,a,76,a,28\nsa s53,s53,0x10000\nad 197,197,195\ncv 135,194,195\nex 189,81\nmc v,108,a,80,a,16,164\n"
"ex 190,82\nad 198,188,189\ncv 136,188,189\nex 191,83\nmm v,108,a,84,a,20\nrd a,32,208,16384\n"
"rd a,36,208,17408\nad 198,198,190\nex 192,84\nmm v,108,a,88,a,24\nrd a,40,208,18432\nrd a,44,208,19456\n"
"ad 198,198,191\ncv 137,190,191\nex 193,85\nmm v,108,a,92,a,28\nrd a,48,208,20480\nad 198,198,192\n"
"ex 194,86\nad 198,198,193\nmc v,120,a,64,a,176,172\nrd a,52,208,21504\nrd a,56,208,22528\ncv 138,192,193\n"
"ex 195,87\nmm v,120,a,68,a,180\nrd a,60,208,23552\ns_cmp_eq_u32 s53, s55\ns_cselect_b32 s53, s56, s53\nad 198,198,194\n"
"ex 188,88\nmm v,120,a,72,a,184\nad 198,198,195\ncv 139,194,195\nex 189,89\nex 190,90\n"
"mm v,120,a,76,a,188\nad 199,188,189\ncv 140,188,189\nex 191,91\nmc v,124,a,80,a,176,172\nad 199,199,190\n"
"ex 192,92\nad 199,199,191\ncv 141,190,191\nmm v,124,a,84,a,180\nex 193,93\nad 199,199,192\n"
"ex 194,94\nad 199,199,193\nmm v,124,a,88,a,184\ncv 142,192,193\nex 195,95\nad 199,199,194\n"
"s_nop 0\nad 199,199,195\ncv 143,194,195\nad 238,196,197\nad 239,198,199\nad 238,238,239\n"
"v_cmp_nge_f32 vcc, s59, v238\nmm v,124,a,92,a,188\ns_waitcnt vmcnt(8) lgkmcnt(0)\ns_barrier\nv_xor_b32 v209, 0x8000, v209\ns_cbranch_vccnz RS_28\n"
"RR_28:\nmc v,64,a,32,a,0,160\nv_mov_b32 v112, 0xff800000\nv_mov_b32 v113, 0xff800000\nmm v,64,a,36,a,4\nrd a,96,209,0\n"
"v_mov_b32 v114, 0xff800000\nmm v,64,a,40,a,8\nv_mov_b32 v115, 0xff800000\nv_mov_b32 v116, 0xff800000\nmm v,64,a,44,a,12\nrd a,104,209,4096\n"
"v_mov_b32 v240, s46\nv_mov_b32 v241, s47\ns_mov_b64 exec, 1\nbuffer_store_dwordx2 v[240:241], v245, s[96:99], s33 offen sc1\ns_mov_b64 exec, -1\nmc v,68,a,48,a,0,160\n"
"v_mov_b32 v117, 0xff800000\nv_mov_b32 v118, 0xff800000\nmm v,68,a,52,a,4\nrd a,112,209,8192\nv_mov_b32 v119, 0xff800000\nmm v,68,a,56,a,8\n"
"ad 204,204,196\nad 205,205,197\nmm v,68,a,60,a,12\nad 206,206,198\nad 207,207,199\nmc v,72,a,32,a,16,164\n"
"rd a,120,209,12288\nex 188,96\nmm v,72,a,36,a,20\nex 189,97\nmm v,72,a,40,a,24\nrd a,128,209,16384\n"
"ex 190,98\nmm v,72,a,44,a,28\nad 200,188,189\ncv 210,188,189\nmc v,76,a,48,a,16,164\nrd a,136,209,20480\n"
"ex 191,99\nmm v,76,a,52,a,20\nad 200,200,190\nex 192,100\nmm v,76,a,56,a,24\nad 200,200,191\n"
"cv 211,190,191\nmm v,76,a,60,a,28\nrd a,144,209,24576\nex 193,101\nmc v,88,a,32,a,176,172\nad 200,200,192\n"
"ex 194,102\nmm v,88,a,36,a,180\nrd a,152,209,28672\nad 200,200,193\nmm v,88,a,40,a,184\ncv 212,192,193\n"
"ex 195,103\nmm v,88,a,44,a,188\nad 200,200,194\nex 188,104\nmc v,92,a,48,a,176,172\nad 200,200,195\n"
"cv 213,194,195\nmm v,92,a,52,a,180\nex 189,105\nmm v,92,a,56,a,184\nex 190,106\nmm v,92,a,60,a,188\n"
"wl 0\nad 201,188,189\nmz v,0,a,96,v,128\ncv 214,188,189\nex 191,107\nmz v,32,a,96,v,132\n"
"rd a,64,208,24576\nad 201,201,190\nmz a,192,a,96,v,136\nex 192,108\nmz a,224,a,96,v,140\nad 201,201,191\n"
"cv 215,190,191\nmz v,4,a,104,v,128\nrd a,68,208,25600\nex 193,109\nmz v,36,a,104,v,132\nad 201,201,192\n"
"ex 194,110\nmz a,196,a,104,v,136\nad 201,201,193\ncv 216,192,193\nmz a,228,a,104,v,140\nrd a,72,208,26624\n"
"ex 195,111\nmz v,8,a,112,v,128\nad 201,201,194\nex 188,120\nmz v,40,a,112,v,132\nad 201,201,195\n"
"cv 217,194,195\nmz a,200,a,112,v,136\nrd a,76,208,27648\nex 189,121\nmz a,232,a,112,v,140\nex 190,122\n"
"mz v,12,a,120,v,128\nad 203,188,189\ncv 222,188,189\nmz v,44,a,120,v,132\nrd a,80,208,28672\nex 191,123\n"
"mz a,204,a,120,v,136\nad 203,203,190\nex 192,124\nmz a,236,a,120,v,140\nad 203,203,191\ncv 223,190,191\n"
"mz v,16,a,128,v,128\nrd a,84,208,29696\nex 193,125\nmz v,48,a,128,v,132\nad 203,203,192\nex 194,126\n"
"mz a,208,a,128,v,136\nad 203,203,193\ncv 224,192,193\nmz a,240,a,128,v,140\nrd a,88,208,30720\nex 195,127\n"
"mz v,20,a,136,v,128\nad 203,203,194\ns_nop 0\nmz v,52,a,136,v,132\nrd a,92,208,31744\nad 203,203,195\n"
"mz a,212,a,136,v,136\ncv 225,194,195\nad 238,200,201\nmz a,244,a,136,v,140\nad 238,238,203\nv_cmp_nge_f32 vcc, s59, v238\n"
"mz v,24,a,144,v,128\nmz v,56,a,144,v,132\nmz a,216,a,144,v,136\nmz a,248,a,144,v,140\nmz v,28,a,152,v,128\nmz v,60,a,152,v,132\n"
"mz a,220,a,152,v,136\nmz a,252,a,152,v,140\nwl 0\ns_cbranch_vccnz RS_29\nRR_29:\nmc v,96,a,64,a,0,160\n"
"v_mov_b32 v80, 0xff800000\nv_mov_b32 v81, 0xff800000\nv_mov_b32 v82, 0xff800000\nmm v,96,a,68,a,4\nrd a,100,209,1024\nv_mov_b32 v83, 0xff800000\n"
"v_mov_b32 v84, 0xff800000\nmm v,96,a,72,a,8\nrd a,108,209,5120\nv_mov_b32 v85, 0xff800000\nv_mov_b32 v86, 0xff800000\nmm v,96,a,76,a,12\n"
"v_mov_b32 v87, 0xff800000\nad 204,204,200\nad 205,205,201\nmc v,100,a,80,a,0,160\nrd a,116,209,9216\nad 207,207,203\n"
"ex 188,64\nmm v,100,a,84,a,4\nex 189,65\nmm v,100,a,88,a,8\nrd a,124,209,13312\nex 190,66\n"
"mm v,100,a,92,a,12\nad 196,188,189\ncv 144,188,189\nex 191,67\nmc v,104,a,64,a,16,164\nrd a,132,209,17408\n"
"ad 196,196,190\nmm v,104,a,68,a,20\nex 192,68\nad 196,196,191\nmm v,104,a,72,a,24\nrd a,140,209,21504\n"
"cv 145,190,191\nex 193,69\nmm v,104,a,76,a,28\nad 196,196,192\nex 194,70\nmc v,108,a,80,a,16,164\n"
"rd a,148,209,25600\nad 196,196,193\nmm v,108,a,84,a,20\ncv 146,192,193\nex 195,71\nmm v,108,a,88,a,24\n"
"rd a,156,209,29696\nad 196,196,194\nex 188,72\nmm v,108,a,92,a,28\nad 196,196,195\ncv 147,194,195\n"
"mc v,120,a,64,a,176,172\nex 189,73\nex 190,74\nmm v,120,a,68,a,180\nad 197,188,189\ncv 152,188,189\n"
"mm v,120,a,72,a,184\nex 191,75\nad 197,197,190\nmm v,120,a,76,a,188\ns_waitcnt vmcnt(1) lgkmcnt(0)\ns_barrier\n"
"v_xor_b32 v208, 0x8000, v208\nex 192,76\nmm v,0,a,100,v,210\nad 197,197,191\ncv 153,190,191\nmm v,32,a,100,v,214\n"
"rd a,32,208,0\nex 193,77\nmm a,224,a,100,v,222\nrd a,36,208,1024\nad 197,197,192\nex 194,78\n"
"mm v,4,a,108,v,210\nrd a,40,208,2048\ns_mov_b32 m0, s57\ns_nop 0\ndc 230,40,53,0\nmm v,36,a,108,v,214\n"
"rd a,44,208,3072\nad 197,197,193\nmm a,228,a,108,v,222\nrd a,48,208,4096\ncv 154,192,193\nmm v,8,a,116,v,210\n"
"rd a,52,208,5120\ndc 230,40,53,1024\nex 195,79\nmm v,40,a,116,v,214\nrd a,56,208,6144\nad 197,197,194\n"
"mm a,232,a,116,v,222\nrd a,60,208,7168\nex 188,88\nmm v,12,a,124,v,210\nrd a,64,208,8192\nad 197,197,195\n"
"cv 155,194,195\nmm v,44,a,124,v,214\nrd a,68,208,9216\ndc 230,40,53,2048\nex 189,89\nmm a,236,a,124,v,222\n"
"rd a,72,208,10240\nex 190,90\nmm v,16,a,132,v,210\nrd a,76,208,11264\nad 199,188,189\nmm v,48,a,132,v,214\n"
"rd a,80,208,12288\ncv 132,188,189\nex 191,91\nmm a,240,a,132,v,222\nrd a,84,208,13312\ndc 230,40,53,3072\n"
"mm v,20,a,140,v,210\nrd a,88,208,14336\nad 199,199,190\nex 192,92\nmm v,52,a,140,v,214\nrd a,92,208,15360\n"
"ad 199,199,191\nmm a,244,a,140,v,222\nsa m0,m0,0x1000\ns_nop 0\ndc 231,40,53,0\nmm v,24,a,148,v,210\n"
"cv 133,190,191\nex 193,93\nmm v,56,a,148,v,214\nad 199,199,192\nex 194,94\nmm a,248,a,148,v,222\n"
"ad 199,199,193\ncv 134,192,193\nex 195,95\nmm v,28,a,156,v,210\ndc 231,40,53,1024\nad 199,199,194\n"
"mm v,60,a,156,v,214\ns_nop 0\nad 199,199,195\ncv 135,194,195\nad 238,196,197\nad 238,238,199\n"
"v_cmp_nge_f32 vcc, s59, v238\nmm a,252,a,156,v,222\nwl 8\ns_cbranch_vccnz RS_30\nRR_30:\nmc v,64,a,32,a,0,160\n"
"v_mov_b32 v112, 0xff800000\nv_mov_b32 v113, 0xff800000\nv_mov_b32 v114, 0xff800000\nmm v,64,a,36,a,4\nrd a,96,209,2048\nv_mov_b32 v115, 0xff800000\n"
"v_mov_b32 v116, 0xff800000\nv_mov_b32 v117, 0xff800000\nmm v,64,a,40,a,8\ndc 231,40,53,2048\nv_mov_b32 v118, 0xff800000\nv_mov_b32 v119, 0xff800000\n"
"mm v,64,a,44,a,12\nad 120,120,184\nad 121,121,185\nad 122,122,186\nmc v,80,a,32,a,16,164\nrd a,104,209,6144\n"
"ad 123,123,187\nv_mov_b32 v124, 0xff800000\nmm v,80,a,36,a,20\ndc 231,40,53,3072\nv_mov_b32 v125, 0xff800000\nv_mov_b32 v126, 0xff800000\n"
"v_mov_b32 v127, 0xff800000\nmm v,80,a,40,a,24\nad 204,204,196\nad 205,205,197\nad 207,207,199\nmm v,80,a,44,a,28\n"
"rd a,112,209,10240\nv_mov_b32 v142, 0\nv_mov_b32 v143, 0\nmc v,68,a,48,a,0,160\ns_xor_b32 m0, s57, 0x18000\ns_nop 0\n"
"dc 232,40,54,0\nmm v,68,a,52,a,4\nex 188,96\nex 189,97\nmm v,68,a,56,a,8\nex 190,98\n"
"ad 200,188,189\nmm v,68,a,60,a,12\nrd a,120,209,14336\ncv 148,188,189\nex 191,99\nmc v,84,a,48,a,16,164\n"
"dc 232,40,54,1024\nad 200,200,190\nmm v,84,a,52,a,20\nex 192,100\nad 200,200,191\ncv 149,190,191\n"
"mm v,84,a,56,a,24\nrd a,128,209,18432\nex 193,101\nmm v,84,a,60,a,28\ndc 232,40,54,2048\nad 200,200,192\n"
"ex 194,102\nwl 12\nmc v,72,a,64,a,0,160\nad 200,200,193\ncv 150,192,193\nex 195,103\n"
"wl 11\nmm v,72,a,68,a,4\nrd a,136,209,22528\nad 200,200,194\nwl 11\nmm v,72,a,72,a,8\n"
"ex 188,104\nad 200,200,195\ncv 151,194,195\nwl 10\nmm v,72,a,76,a,12\ndc 232,40,54,3072\n"
"ex 189,105\nmc v,88,a,64,a,16,164\nex 190,106\nad 201,188,189\nmm v,88,a,68,a,20\nrd a,144,209,26624\n"
"cv 156,188,189\nex 191,107\nmm v,88,a,72,a,24\nsa m0,m0,0x1000\ns_nop 0\ndc 233,40,54,0\n"
"mm v,88,a,76,a,28\nad 201,201,190\nex 192,108\nwl 10\nmc v,76,a,80,a,0,160\nrd a,152,209,30720\n"
"ad 201,201,191\ncv 157,190,191\nwl 10\nmm v,76,a,84,a,4\ndc 233,40,54,1024\nex 193,109\n"
"ad 201,201,192\nwl 9\nmm v,76,a,88,a,8\nex 194,110\nad 201,201,193\nwl 8\n"
"mm v,76,a,92,a,12\ncv 158,192,193\nex 195,111\nmc v,92,a,80,a,16,164\nad 201,201,194\nex 188,120\n"
"ad 201,201,195\nmm v,92,a,84,a,20\ndc 233,40,54,2048\ncv 159,194,195\nex 189,121\nmm v,92,a,88,a,24\n"
"ex 190,122\nmm v,92,a,92,a,28\nwl 0\nad 203,188,189\ncv 140,188,189\nmm a,224,a,96,v,132\n"
"dc 233,40,54,3072\nrd a,32,208,16384\nrd a,36,208,17408\nrd a,40,208,18432\nmm a,228,a,104,v,132\nrd a,44,208,19456\n"
"rd a,48,208,20480\nrd a,52,208,21504\nrd a,56,208,22528\nrd a,60,208,23552\nrd a,64,208,24576\nrd a,68,208,25600\n"
"rd a,72,208,26624\nrd a,76,208,27648\nrd a,80,208,28672\nrd a,84,208,29696\nrd a,88,208,30720\nrd a,92,208,31744\n"
"mm a,232,a,112,v,132\nex 191,123\nmm a,236,a,120,v,132\nad 203,203,190\ns_nop 0\nmm a,240,a,128,v,132\n"
"ad 203,203,191\ncv 141,190,191\nmm a,244,a,136,v,132\nad 238,200,201\nad 238,238,203\nmm a,248,a,144,v,132\n"
"v_cmp_nge_f32 vcc, s59, v238\ns_xor_b32 s57, s57, 0x8000\nmm a,252,a,152,v,132\ns_cbranch_vccnz RS_31\nRR_31:\nrd a,100,209,3072\n"
"rd a,108,209,7168\nrd a,116,209,11264\nrd a,124,209,15360\nrd a,132,209,19456\nrd a,140,209,23552\nrd a,148,209,27648\n"
"rd a,156,209,31744\nad 207,207,203\nwl 7\nmm a,224,a,100,v,140\nwl 6\nmm a,228,a,108,v,140\n"
"wl 5\nmm a,232,a,116,v,140\nwl 4\nmm a,236,a,124,v,140\nwl 3\nmm a,240,a,132,v,140\n"
"wl 2\nmm a,244,a,140,v,140\nwl 1\nmm a,248,a,148,v,140\nwl 0\nmm a,252,a,156,v,140\n"
"s_branch SHORT_END\nFD_S2:\nmc v,96,a,64,a,0,160\nex 188,64\nex 189,65\nmm v,96,a,68,a,4\n"
"ex 190,66\nad 196,188,189\ncv 128,188,189\nmm v,96,a,72,a,8\nex 191,67\nad 196,196,190\n"
"ex 192,68\nmm v,96,a,76,a,12\nad 196,196,191\ncv 129,190,191\nex 193,69\nad 196,196,192\n"
"mc v,100,a,80,a,0,160\nex 194,70\nad 196,196,193\ncv 130,192,193\nmm v,100,a,84,a,4\ns_mov_b32 s54, s53\n"
"ex 195,71\nad 196,196,194\nmm v,100,a,88,a,8\nex 188,72\nad 196,196,195\ncv 131,194,195\n"
"ex 189,73\nmm v,100,a,92,a,12\nex 190,74\nad 197,188,189\ncv 132,188,189\nmc v,104,a,64,a,16,164\n"
"ex 191,75\nad 197,197,190\nex 192,76\nmm v,104,a,68,a,20\nad 197,197,191\ncv 133,190,191\n"
"ex 193,77\nmm v,104,a,72,a,24\nad 197,197,192\nex 194,78\nad 197,197,193\nmm v,104,a,76,a,28\n"
"sa s53,s53,0x10000\ncv 134,192,193\nex 195,79\nad 197,197,194\nmc v,108,a,80,a,16,164\nex 188,80\n"
"ad 197,197,195\ncv 135,194,195\nmm v,108,a,84,a,20\nex 189,81\nex 190,82\nmm v,108,a,88,a,24\n"
"ad 198,188,189\ncv 136,188,189\nex 191,83\nad 198,198,190\nmm v,108,a,92,a,28\nrd a,32,208,16384\n"
"rd a,36,208,17408\nex 192,84\nmc v,112,a,64,a,160,168\nrd a,40,208,18432\nad 198,198,191\ncv 137,190,191\n"
"ex 193,85\nmm v,112,a,68,a,164\nrd a,44,208,19456\ns_cmp_eq_u32 s53, s55\ns_cselect_b32 s53, s56, s53\nad 198,198,192\n"
"mm v,112,a,72,a,168\nrd a,48,208,20480\nex 194,86\nad 198,198,193\ncv 138,192,193\nmm v,112,a,76,a,172\n"
"rd a,52,208,21504\nex 195,87\nad 198,198,194\nmc v,120,a,64,a,176,172\nrd a,56,208,22528\nex 188,88\n"
"ad 198,198,195\ncv 139,194,195\nmm v,120,a,68,a,180\nrd a,60,208,23552\nex 189,89\nex 190,90\n"
"mm v,120,a,72,a,184\nad 199,188,189\ncv 140,188,189\nex 191,91\nmm v,120,a,76,a,188\nad 199,199,190\n"
"ex 192,92\nad 199,199,191\nmc v,124,a,80,a,176,172\ncv 141,190,191\nex 193,93\nad 199,199,192\n"
"mm v,124,a,84,a,180\nex 194,94\nad 199,199,193\ncv 142,192,193\nex 195,95\nmm v,124,a,88,a,184\n"
"ad 199,199,194\ns_nop 0\nad 199,199,195\ncv 143,194,195\nad 238,196,197\nad 239,198,199\n"
"ad 238,238,239\nv_cmp_nge_f32 vcc, s59, v238\nmm v,124,a,92,a,188\ns_waitcnt vmcnt(8) lgkmcnt(0)\ns_barrier\nv_xor_b32 v209, 0x8000, v209\n"
"s_cbranch_vccnz RS_32\nRR_32:\nmc v,64,a,32,a,0,160\nad 112,112,184\nad 113,113,185\nmm v,64,a,36,a,4\n"
"rd a,96,209,0\nad 114,114,186\nad 115,115,187\nmm v,64,a,40,a,8\nv_mov_b32 v116, 0xff800000\nv_mov_b32 v117, 0xff800000\n"
"mm v,64,a,44,a,12\nrd a,104,209,4096\nv_mov_b32 v240, s46\nv_mov_b32 v241, s47\ns_mov_b64 exec, 1\nbuffer_store_dwordx2 v[240:241], v245, s[96:99], s33 offen sc1\n"
"s_mov_b64 exec, -1\nmc v,68,a,48,a,0,160\nv_mov_b32 v118, 0xff800000\nv_mov_b32 v119, 0xff800000\nmm v,68,a,52,a,4\nrd a,112,209,8192\n"
"ad 204,204,196\nmm v,68,a,56,a,8\nad 205,205,197\nad 206,206,198\nmm v,68,a,60,a,12\nad 207,207,199\n"
"v_mov_b32 v220, 0\nmc v,72,a,32,a,16,164\nrd a,120,209,12288\nv_mov_b32 v221, 0\nmm v,72,a,36,a,20\nex 188,96\n"
"mm v,72,a,40,a,24\nrd a,128,209,16384\nex 189,97\nmm v,72,a,44,a,28\nex 190,98\nmc v,76,a,48,a,16,164\n"
"rd a,136,209,20480\nad 200,188,189\nmm v,76,a,52,a,20\ncv 210,188,189\nex 191,99\nmm v,76,a,56,a,24\n"
"ad 200,200,190\nex 192,100\nmm v,76,a,60,a,28\nrd a,144,209,24576\nad 200,200,191\nmc v,88,a,32,a,176,172\n"
"cv 211,190,191\nex 193,101\nmm v,88,a,36,a,180\nrd a,152,209,28672\nad 200,200,192\nmm v,88,a,40,a,184\n"
"ex 194,102\nmm v,88,a,44,a,188\nad 200,200,193\ncv 212,192,193\nmc v,92,a,48,a,176,172\nex 195,103\n"
"mm v,92,a,52,a,180\nad 200,200,194\nex 188,104\nmm v,92,a,56,a,184\nad 200,200,195\ncv 213,194,195\n"
"mm v,92,a,60,a,188\nwl 0\nex 189,105\nmz v,0,a,96,v,128\nex 190,106\nmz v,32,a,96,v,132\n"
"rd a,64,208,24576\nad 201,188,189\nmz a,192,a,96,v,136\ncv 214,188,189\nex 191,107\nmz a,224,a,96,v,140\n"
"ad 201,201,190\nex 192,108\nmz v,4,a,104,v,128\nrd a,68,208,25600\nad 201,201,191\nmz v,36,a,104,v,132\n"
"cv 215,190,191\nex 193,109\nmz a,196,a,104,v,136\nad 201,201,192\nex 194,110\nmz a,228,a,104,v,140\n"
"rd a,72,208,26624\nad 201,201,193\nmz v,8,a,112,v,128\ncv 216,192,193\nex 195,111\nmz v,40,a,112,v,132\n"
"ad 201,201,194\nex 188,112\nmz a,200,a,112,v,136\nrd a,76,208,27648\nad 201,201,195\nmz a,232,a,112,v,140\n"
"cv 217,194,195\nex 189,113\nmz v,12,a,120,v,128\nex 190,114\nmz v,44,a,120,v,132\nrd a,80,208,28672\n"
"ad 202,188,189\nmz a,204,a,120,v,136\ncv 218,188,189\nex 191,115\nmz a,236,a,120,v,140\nad 202,202,190\n"
"ex 192,120\nmz v,16,a,128,v,128\nrd a,84,208,29696\nad 202,202,191\nmz v,48,a,128,v,132\ncv 219,190,191\n"
"ex 193,121\nmz a,208,a,128,v,136\nex 194,122\nmz a,240,a,128,v,140\nrd a,88,208,30720\nad 203,192,193\n"
"mz v,20,a,136,v,128\ncv 222,192,193\nex 195,123\nmz v,52,a,136,v,132\nad 203,203,194\nex 188,124\n"
"mz a,212,a,136,v,136\nrd a,92,208,31744\nad 203,203,195\nmz a,244,a,136,v,140\ncv 223,194,195\nex 189,125\n"
"mz v,24,a,144,v,128\nad 203,203,188\nex 190,126\nmz v,56,a,144,v,132\nad 203,203,189\ncv 224,188,189\n"
"mz a,216,a,144,v,136\nex 191,127\nmz a,248,a,144,v,140\nad 203,203,190\ns_nop 0\nmz v,28,a,152,v,128\n"
"ad 203,203,191\ncv 225,190,191\nmz v,60,a,152,v,132\nad 238,200,201\nad 239,202,203\nmz a,220,a,152,v,136\n"
"ad 238,238,239\nv_cmp_nge_f32 vcc, s59, v238\nmz a,252,a,152,v,140\nwl 0\ns_cbranch_vccnz RS_33\nRR_33:\n"
"mc v,96,a,64,a,0,160\nv_mov_b32 v80, 0xff800000\nv_mov_b32 v81, 0xff800000\nv_mov_b32 v82, 0xff800000\nmm v,96,a,68,a,4\nrd a,100,209,1024\n"
"v_mov_b32 v83, 0xff800000\nv_mov_b32 v84, 0xff800000\nmm v,96,a,72,a,8\nrd a,108,209,5120\nv_mov_b32 v85, 0xff800000\nv_mov_b32 v86, 0xff800000\n"
"mm v,96,a,76,a,12\nrd a,116,209,9216\nv_mov_b32 v87, 0xff800000\nmc v,100,a,80,a,0,160\nad 92,92,184\nad 93,93,185\n"
"ad 94,94,186\nmm v,100,a,84,a,4\nrd a,124,209,13312\nad 95,95,187\nad 204,204,200\nmm v,100,a,88,a,8\n"
"rd a,132,209,17408\nad 205,205,201\nad 206,206,202\nmm v,100,a,92,a,12\nrd a,140,209,21504\nad 207,207,203\n"
"mc v,104,a,64,a,16,164\nex 188,64\nex 189,65\nmm v,104,a,68,a,20\nrd a,148,209,25600\nex 190,66\n"
"mm v,104,a,72,a,24\nrd a,156,209,29696\nad 196,188,189\nmm v,104,a,76,a,28\ncv 144,188,189\nex 191,67\n"
"mc v,108,a,80,a,16,164\nad 196,196,190\nex 192,68\nmm v,108,a,84,a,20\nad 196,196,191\ncv 145,190,191\n"
"mm v,108,a,88,a,24\nex 193,69\nad 196,196,192\nmm v,108,a,92,a,28\ns_waitcnt vmcnt(1) lgkmcnt(0)\ns_barrier\n"
"v_xor_b32 v208, 0x8000, v208\nex 194,70\nmm v,0,a,100,v,210\nad 196,196,193\ncv 146,192,193\nmm v,32,a,100,v,214\n"
"rd a,32,208,0\nex 195,71\nmm a,192,a,100,v,218\nrd a,36,208,1024\nad 196,196,194\nex 188,72\n"
"mm a,224,a,100,v,222\ns_mov_b32 m0, s57\ns_nop 0\ndc 230,40,53,0\nmm v,4,a,108,v,210\nrd a,40,208,2048\n"
"ad 196,196,195\nmm v,36,a,108,v,214\nrd a,44,208,3072\ncv 147,194,195\nmm a,196,a,108,v,218\nex 189,73\n"
"ex 190,74\nmm a,228,a,108,v,222\nrd a,48,208,4096\nad 197,188,189\nmm v,8,a,116,v,210\nrd a,52,208,5120\n"
"dc 230,40,53,1024\ncv 152,188,189\nmm v,40,a,116,v,214\nex 191,75\nad 197,197,190\nmm a,200,a,116,v,218\n"
"rd a,56,208,6144\nex 192,76\nmm a,232,a,116,v,222\nrd a,60,208,7168\nad 197,197,191\ncv 153,190,191\n"
"mm v,12,a,124,v,210\nex 193,77\nad 197,197,192\nmm v,44,a,124,v,214\nrd a,64,208,8192\ndc 230,40,53,2048\n"
"mm a,204,a,124,v,218\nex 194,78\nad 197,197,193\nmm a,236,a,124,v,222\nrd a,68,208,9216\ncv 154,192,193\n"
"ex 195,79\nmm v,16,a,132,v,210\nrd a,72,208,10240\nad 197,197,194\nmm v,48,a,132,v,214\nex 188,88\n"
"ad 197,197,195\nmm a,208,a,132,v,218\nrd a,76,208,11264\ndc 230,40,53,3072\ncv 155,194,195\nmm a,240,a,132,v,222\n"
"rd a,80,208,12288\nex 189,89\nmm v,20,a,140,v,210\nex 190,90\nad 199,188,189\nmm v,52,a,140,v,214\n"
"rd a,84,208,13312\ncv 132,188,189\nmm a,212,a,140,v,218\nrd a,88,208,14336\nex 191,91\nmm a,244,a,140,v,222\n"
"sa m0,m0,0x1000\ns_nop 0\ndc 231,40,53,0\nmm v,24,a,148,v,210\nrd a,92,208,15360\nad 199,199,190\n"
"ex 192,92\nmm v,56,a,148,v,214\nad 199,199,191\ncv 133,190,191\nmm a,216,a,148,v,218\nex 193,93\n"
"ad 199,199,192\nmm a,248,a,148,v,222\nex 194,94\nad 199,199,193\nmm v,28,a,156,v,210\ndc 231,40,53,1024\n"
"cv 134,192,193\nmm v,60,a,156,v,214\nex 195,95\nad 199,199,194\nmm a,220,a,156,v,218\ns_nop 0\n"
"ad 199,199,195\ncv 135,194,195\nad 238,196,197\nad 238,238,199\nv_cmp_nge_f32 vcc, s59, v238\nmm a,252,a,156,v,222\n"
"wl 8\ns_cbranch_vccnz RS_34\nRR_34:\nmc v,64,a,32,a,0,160\nv_mov_b32 v112, 0xff800000\nv_mov_b32 v113, 0xff800000\n"
"v_mov_b32 v114, 0xff800000\nmm v,64,a,36,a,4\nrd a,96,209,2048\nv_mov_b32 v115, 0xff800000\nv_mov_b32 v116, 0xff800000\nmm v,64,a,40,a,8\n"
"dc 231,40,53,2048\nv_mov_b32 v117, 0xff800000\nv_mov_b32 v118, 0xff800000\nmm v,64,a,44,a,12\nv_mov_b32 v119, 0xff800000\nv_mov_b32 v120, 0xff800000\n"
"v_mov_b32 v121, 0xff800000\nmc v,80,a,32,a,16,164\nrd a,104,209,6144\nv_mov_b32 v122, 0xff800000\nmm v,80,a,36,a,20\ndc 231,40,53,3072\n"
"v_mov_b32 v123, 0xff800000\nv_mov_b32 v124, 0xff800000\nmm v,80,a,40,a,24\nv_mov_b32 v125, 0xff800000\nv_mov_b32 v126, 0xff800000\nv_mov_b32 v127, 0xff800000\n"
"mm v,80,a,44,a,28\nrd a,112,209,10240\nad 204,204,196\nad 205,205,197\nmc v,68,a,48,a,0,160\ns_xor_b32 m0, s57, 0x18000\n"
"s_nop 0\ndc 232,40,54,0\nmm v,68,a,52,a,4\nad 207,207,199\nex 188,96\nmm v,68,a,56,a,8\n"
"ex 189,97\nmm v,68,a,60,a,12\nrd a,120,209,14336\nex 190,98\nad 200,188,189\nmc v,84,a,48,a,16,164\n"
"dc 232,40,54,1024\ncv 148,188,189\nmm v,84,a,52,a,20\nex 191,99\nad 200,200,190\nmm v,84,a,56,a,24\n"
"rd a,128,209,18432\nex 192,100\nmm v,84,a,60,a,28\ndc 232,40,54,2048\nad 200,200,191\ncv 149,190,191\n"
"wl 12\nmc v,72,a,64,a,0,160\nex 193,101\nad 200,200,192\nwl 11\nmm v,72,a,68,a,4\n"
"rd a,136,209,22528\nex 194,102\nwl 11\nmm v,72,a,72,a,8\nad 200,200,193\ncv 150,192,193\n"
"ex 195,103\nwl 10\nmm v,72,a,76,a,12\ndc 232,40,54,3072\nad 200,200,194\nmc v,88,a,64,a,16,164\n"
"ex 188,104\nad 200,200,195\nmm v,88,a,68,a,20\nrd a,144,209,26624\ncv 151,194,195\nmm v,88,a,72,a,24\n"
"sa m0,m0,0x1000\ns_nop 0\ndc 233,40,54,0\nmm v,88,a,76,a,28\nex 189,105\nex 190,106\n"
"wl 10\nmc v,76,a,80,a,0,160\nrd a,152,209,30720\nad 201,188,189\nwl 10\nmm v,76,a,84,a,4\n"
"dc 233,40,54,1024\ncv 156,188,189\nex 191,107\nwl 9\nmm v,76,a,88,a,8\nad 201,201,190\n"
"ex 192,108\nwl 8\nmm v,76,a,92,a,12\nad 201,201,191\ncv 157,190,191\nmc v,92,a,80,a,16,164\n"
"ex 193,109\nad 201,201,192\nmm v,92,a,84,a,20\ndc 233,40,54,2048\nex 194,110\nmm v,92,a,88,a,24\n"
"ad 201,201,193\ncv 158,192,193\nmm v,92,a,92,a,28\nwl 0\nex 195,111\nad 201,201,194\n"
"mm a,224,a,96,v,132\ndc 233,40,54,3072\nrd a,32,208,16384\nrd a,36,208,17408\nmm a,228,a,104,v,132\nrd a,40,208,18432\n"
"rd a,44,208,19456\nrd a,48,208,20480\nrd a,52,208,21504\nrd a,56,208,22528\nrd a,60,208,23552\nrd a,64,208,24576\n"
"rd a,68,208,25600\nrd a,72,208,26624\nrd a,76,208,27648\nrd a,80,208,28672\nrd a,84,208,29696\nrd a,88,208,30720\n"
"rd a,92,208,31744\nmm a,232,a,112,v,132\ns_nop 0\nad 201,201,195\nmm a,236,a,120,v,132\ncv 159,194,195\n"
"ad 238,200,201\nmm a,240,a,128,v,132\nv_cmp_nge_f32 vcc, s59, v238\nmm a,244,a,136,v,132\ns_xor_b32 s57, s57, 0x8000\nmm a,248,a,144,v,132\n"
"mm a,252,a,152,v,132\ns_cbranch_vccnz RS_35\nRR_35:\nwl 0\ns_branch SHORT_END\nFD_S3:\n"
"mc v,96,a,64,a,0,160\nex 188,64\nex 189,65\nmm v,96,a,68,a,4\nex 190,66\nad 196,188,189\n"
"cv 128,188,189\nmm v,96,a,72,a,8\nex 191,67\nad 196,196,190\nmm v,96,a,76,a,12\nex 192,68\n"
"ad 196,196,191\ncv 129,190,191\nmc v,100,a,80,a,0,160\nex 193,69\nad 196,196,192\nex 194,70\n"
"mm v,100,a,84,a,4\ns_mov_b32 s54, s53\nad 196,196,193\ncv 130,192,193\nmm v,100,a,88,a,8\nex 195,71\n"
"ad 196,196,194\nex 188,72\nmm v,100,a,92,a,12\nad 196,196,195\ncv 131,194,195\nex 189,73\n"
"mc v,104,a,64,a,16,164\nex 190,74\nad 197,188,189\nmm v,104,a,68,a,20\ncv 132,188,189\nex 191,75\n"
"ad 197,197,190\nmm v,104,a,72,a,24\nex 192,76\nad 197,197,191\ncv 133,190,191\nmm v,104,a,76,a,28\n"
"sa s53,s53,0x10000\nex 193,77\nad 197,197,192\nmc v,108,a,80,a,16,164\nex 194,78\nad 197,197,193\n"
"cv 134,192,193\nmm v,108,a,84,a,20\nex 195,79\nad 197,197,194\nmm v,108,a,88,a,24\nex 188,80\n"
"ad 197,197,195\ncv 135,194,195\nmm v,108,a,92,a,28\nex 189,81\nex 190,82\nmc v,112,a,64,a,160,168\n"
"ad 198,188,189\ncv 136,188,189\nex 191,83\nmm v,112,a,68,a,164\nrd a,32,208,16384\ns_cmp_eq_u32 s53, s55\n"
"s_cselect_b32 s53, s56, s53\nad 198,198,190\nmm v,112,a,72,a,168\nrd a,36,208,17408\nex 192,84\nad 198,198,191\n"
"mm v,112,a,76,a,172\nrd a,40,208,18432\ncv 137,190,191\nex 193,85\nmc v,116,a,80,a,160,168\nrd a,44,208,19456\n"
"ad 198,198,192\nex 194,86\nmm v,116,a,84,a,164\nrd a,48,208,20480\nad 198,198,193\ncv 138,192,193\n"
"mm v,116,a,88,a,168\nrd a,52,208,21504\nex 195,87\nad 198,198,194\nmm v,116,a,92,a,172\nrd a,56,208,22528\n"
"ex 188,88\nad 198,198,195\ncv 139,194,195\nmc v,120,a,64,a,176,172\nrd a,60,208,23552\nex 189,89\n"
"mm v,120,a,68,a,180\nex 190,90\nad 199,188,189\ncv 140,188,189\nmm v,120,a,72,a,184\nex 191,91\n"
"ad 199,199,190\nex 192,92\nmm v,120,a,76,a,188\nad 199,199,191\ncv 141,190,191\nex 193,93\n"
"mc v,124,a,80,a,176,172\nad 199,199,192\nex 194,94\nmm v,124,a,84,a,180\nad 199,199,193\ncv 142,192,193\n"
"ex 195,95\nmm v,124,a,88,a,184\nad 199,199,194\ns_nop 0\nad 199,199,195\ncv 143,194,195\n"
"ad 238,196,197\nad 239,198,199\nad 238,238,239\nv_cmp_nge_f32 vcc, s59, v238\nmm v,124,a,92,a,188\ns_waitcnt vmcnt(8) lgkmcnt(0)\n"
"s_barrier\nv_xor_b32 v209, 0x8000, v209\ns_cbranch_vccnz RS_36\nRR_36:\nmc v,64,a,32,a,0,160\nad 116,116,184\n"
"ad 117,117,185\nad 118,118,186\nmm v,64,a,36,a,4\nrd a,96,209,0\nad 119,119,187\nmm v,64,a,40,a,8\n"
"rd a,104,209,4096\nad 204,204,196\nad 205,205,197\nmm v,64,a,44,a,12\nv_mov_b32 v240, s46\nv_mov_b32 v241, s47\n"
"s_mov_b64 exec, 1\nbuffer_store_dwordx2 v[240:241], v245, s[96:99], s33 offen sc1\ns_mov_b64 exec, -1\nmc v,68,a,48,a,0,160\nrd a,112,209,8192\nad 206,206,198\n"
"mm v,68,a,52,a,4\nad 207,207,199\nex 188,96\nmm v,68,a,56,a,8\nrd a,120,209,12288\nex 189,97\n"
"mm v,68,a,60,a,12\nex 190,98\nmc v,72,a,32,a,16,164\nrd a,128,209,16384\nad 200,188,189\nmm v,72,a,36,a,20\n"
"cv 210,188,189\nex 191,99\nmm v,72,a,40,a,24\nrd a,136,209,20480\nad 200,200,190\nmm v,72,a,44,a,28\n"
"ex 192,100\nad 200,200,191\nmc v,76,a,48,a,16,164\nrd a,144,209,24576\ncv 211,190,191\nmm v,76,a,52,a,20\n"
"ex 193,101\nad 200,200,192\nmm v,76,a,56,a,24\nrd a,152,209,28672\nex 194,102\nmm v,76,a,60,a,28\n"
"ad 200,200,193\ncv 212,192,193\nex 195,103\nmc v,88,a,32,a,176,172\nad 200,200,194\nex 188,104\n"
"mm v,88,a,36,a,180\nad 200,200,195\ncv 213,194,195\nmm v,88,a,40,a,184\nex 189,105\nmm v,88,a,44,a,188\n"
"wl 0\nex 190,106\nmz v,0,a,96,v,128\nad 201,188,189\ncv 214,188,189\nmz v,32,a,96,v,132\n"
"rd a,64,208,24576\nex 191,107\nmz a,192,a,96,v,136\nad 201,201,190\nex 192,108\nmz a,224,a,96,v,140\n"
"ad 201,201,191\ncv 215,190,191\nex 193,109\nmz v,4,a,104,v,128\nrd a,68,208,25600\nad 201,201,192\n"
"mz v,36,a,104,v,132\nex 194,110\nmz a,196,a,104,v,136\nad 201,201,193\ncv 216,192,193\nex 195,111\n"
"mz a,228,a,104,v,140\nrd a,72,208,26624\nad 201,201,194\nmz v,8,a,112,v,128\nex 188,112\nad 201,201,195\n"
"mz v,40,a,112,v,132\ncv 217,194,195\nex 189,113\nmz a,200,a,112,v,136\nrd a,76,208,27648\nex 190,114\n"
"mz a,232,a,112,v,140\nad 202,188,189\ncv 218,188,189\nmz v,12,a,120,v,128\nex 191,115\nad 202,202,190\n"
"mz v,44,a,120,v,132\nrd a,80,208,28672\nex 192,116\nmz a,204,a,120,v,136\nad 202,202,191\ncv 219,190,191\n"
"mz a,236,a,120,v,140\nex 193,117\nad 202,202,192\nmz v,16,a,128,v,128\nrd a,84,208,29696\nex 194,118\n"
"mz v,48,a,128,v,132\nad 202,202,193\ncv 220,192,193\nmz a,208,a,128,v,136\nex 195,119\nad 202,202,194\n"
"mz a,240,a,128,v,140\nrd a,88,208,30720\nex 188,120\nmz v,20,a,136,v,128\nad 202,202,195\ncv 221,194,195\n"
"mz v,52,a,136,v,132\nex 189,121\nex 190,122\nmz a,212,a,136,v,136\nrd a,92,208,31744\nad 203,188,189\n"
"mz a,244,a,136,v,140\ncv 222,188,189\nex 191,123\nmz v,24,a,144,v,128\nad 203,203,190\nex 192,124\n"
"mz v,56,a,144,v,132\nad 203,203,191\ncv 223,190,191\nmz a,216,a,144,v,136\nex 193,125\nad 203,203,192\n"
"mz a,248,a,144,v,140\nex 194,126\nad 203,203,193\nmz v,28,a,152,v,128\ncv 224,192,193\nex 195,127\n"
"mz v,60,a,152,v,132\nad 203,203,194\ns_nop 0\nmz a,220,a,152,v,136\nad 203,203,195\ncv 225,194,195\n"
"ad 238,200,201\nad 239,202,203\nad 238,238,239\nv_cmp_nge_f32 vcc, s59, v238\nmz a,252,a,152,v,140\nwl 0\n"
"s_cbranch_vccnz RS_37\nRR_37:\nmc v,96,a,64,a,0,160\nv_mov_b32 v80, 0xff800000\nv_mov_b32 v81, 0xff800000\nv_mov_b32 v82, 0xff800000\n"
"mm v,96,a,68,a,4\nrd a,100,209,1024\nv_mov_b32 v83, 0xff800000\nmm v,96,a,72,a,8\nrd a,108,209,5120\nv_mov_b32 v84, 0xff800000\n"
"v_mov_b32 v85, 0xff800000\nmm v,96,a,76,a,12\nrd a,116,209,9216\nv_mov_b32 v86, 0xff800000\nv_mov_b32 v87, 0xff800000\nmc v,100,a,80,a,0,160\n"
"ad 88,88,184\nad 89,89,185\nmm v,100,a,84,a,4\nrd a,124,209,13312\nad 90,90,186\nad 91,91,187\n"
"mm v,100,a,88,a,8\nrd a,132,209,17408\nv_mov_b32 v92, 0xff800000\nv_mov_b32 v93, 0xff800000\nmm v,100,a,92,a,12\nrd a,140,209,21504\n"
"v_mov_b32 v94, 0xff800000\nmc v,104,a,64,a,16,164\nv_mov_b32 v95, 0xff800000\nad 204,204,200\nad 205,205,201\nmm v,104,a,68,a,20\n"
"rd a,148,209,25600\nad 206,206,202\nad 207,207,203\nmm v,104,a,72,a,24\nrd a,156,209,29696\nv_mov_b32 v134, 0\n"
"v_mov_b32 v135, 0\nmm v,104,a,76,a,28\nex 188,64\nmc v,108,a,80,a,16,164\nex 189,65\nmm v,108,a,84,a,20\n"
"ex 190,66\nad 196,188,189\ncv 144,188,189\nmm v,108,a,88,a,24\nex 191,67\nmm v,108,a,92,a,28\n"
"s_waitcnt vmcnt(1) lgkmcnt(0)\ns_barrier\nv_xor_b32 v208, 0x8000, v208\nad 196,196,190\nmm v,0,a,100,v,210\nex 192,68\n"
"ad 196,196,191\nmm v,32,a,100,v,214\nrd a,32,208,0\ncv 145,190,191\nmm a,192,a,100,v,218\nrd a,36,208,1024\n"
"ex 193,69\nmm a,224,a,100,v,222\ns_mov_b32 m0, s57\ns_nop 0\ndc 230,40,53,0\nmm v,4,a,108,v,210\n"
"rd a,40,208,2048\nad 196,196,192\nmm v,36,a,108,v,214\nrd a,44,208,3072\nex 194,70\nmm a,196,a,108,v,218\n"
"ad 196,196,193\ncv 146,192,193\nmm a,228,a,108,v,222\nrd a,48,208,4096\nex 195,71\nmm v,8,a,116,v,210\n"
"rd a,52,208,5120\ndc 230,40,53,1024\nad 196,196,194\nmm v,40,a,116,v,214\nex 188,72\nad 196,196,195\n"
"mm a,200,a,116,v,218\nrd a,56,208,6144\ncv 147,194,195\nmm a,232,a,116,v,222\nrd a,60,208,7168\nex 189,73\n"
"mm v,12,a,124,v,210\nex 190,74\nad 197,188,189\nmm v,44,a,124,v,214\nrd a,64,208,8192\ndc 230,40,53,2048\n"
"cv 152,188,189\nmm a,204,a,124,v,218\nex 191,75\nmm a,236,a,124,v,222\nrd a,68,208,9216\nad 197,197,190\n"
"ex 192,76\nmm v,16,a,132,v,210\nrd a,72,208,10240\nad 197,197,191\nmm v,48,a,132,v,214\ncv 153,190,191\n"
"ex 193,77\nmm a,208,a,132,v,218\nrd a,76,208,11264\ndc 230,40,53,3072\nmm a,240,a,132,v,222\nrd a,80,208,12288\n"
"ad 197,197,192\nmm v,20,a,140,v,210\nex 194,78\nad 197,197,193\nmm v,52,a,140,v,214\nrd a,84,208,13312\n"
"cv 154,192,193\nex 195,79\nmm a,212,a,140,v,218\nrd a,88,208,14336\nad 197,197,194\nmm a,244,a,140,v,222\n"
"sa m0,m0,0x1000\ns_nop 0\ndc 231,40,53,0\nmm v,24,a,148,v,210\nrd a,92,208,15360\nex 188,88\n"
"mm v,56,a,148,v,214\nad 197,197,195\ncv 155,194,195\nmm a,216,a,148,v,218\nex 189,89\nmm a,248,a,148,v,222\n"
"ex 190,90\nad 199,188,189\nmm v,28,a,156,v,210\ndc 231,40,53,1024\ncv 132,188,189\nmm v,60,a,156,v,214\n"
"ex 191,91\nad 199,199,190\ns_nop 0\nmm a,220,a,156,v,218\nad 199,199,191\ncv 133,190,191\n"
"ad 238,196,197\nad 238,238,199\nv_cmp_nge_f32 vcc, s59, v238\nmm a,252,a,156,v,222\nwl 8\ns_cbranch_vccnz RS_38\n"
"RR_38:\nmc v,64,a,32,a,0,160\nv_mov_b32 v112, 0xff800000\nv_mov_b32 v113, 0xff800000\nv_mov_b32 v114, 0xff800000\nmm v,64,a,36,a,4\n"
"rd a,96,209,2048\nv_mov_b32 v115, 0xff800000\nv_mov_b32 v116, 0xff800000\nmm v,64,a,40,a,8\ndc 231,40,53,2048\nv_mov_b32 v117, 0xff800000\n"
"v_mov_b32 v118, 0xff800000\nmm v,64,a,44,a,12\nv_mov_b32 v119, 0xff800000\nv_mov_b32 v120, 0xff800000\nv_mov_b32 v121, 0xff800000\nmc v,80,a,32,a,16,164\n"
"rd a,104,209,6144\nv_mov_b32 v122, 0xff800000\nmm v,80,a,36,a,20\ndc 231,40,53,3072\nv_mov_b32 v123, 0xff800000\nv_mov_b32 v124, 0xff800000\n"
"mm v,80,a,40,a,24\nv_mov_b32 v125, 0xff800000\nv_mov_b32 v126, 0xff800000\nv_mov_b32 v127, 0xff800000\nmm v,80,a,44,a,28\nrd a,112,209,10240\n"
"ad 204,204,196\nad 205,205,197\nmc v,68,a,48,a,0,160\ns_xor_b32 m0, s57, 0x18000\ns_nop 0\ndc 232,40,54,0\n"
"mm v,68,a,52,a,4\nad 207,207,199\nex 188,96\nmm v,68,a,56,a,8\nex 189,97\nmm v,68,a,60,a,12\n"
"rd a,120,209,14336\nex 190,98\nad 200,188,189\nmc v,84,a,48,a,16,164\ndc 232,40,54,1024\ncv 148,188,189\n"
"mm v,84,a,52,a,20\nex 191,99\nad 200,200,190\nmm v,84,a,56,a,24\nrd a,128,209,18432\nex 192,100\n"
"mm v,84,a,60,a,28\ndc 232,40,54,2048\nad 200,200,191\ncv 149,190,191\nwl 12\nmc v,72,a,64,a,0,160\n"
"ex 193,101\nad 200,200,192\nwl 11\nmm v,72,a,68,a,4\nrd a,136,209,22528\nex 194,102\n"
"wl 11\nmm v,72,a,72,a,8\nad 200,200,193\ncv 150,192,193\nex 195,103\nwl 10\n"
"mm v,72,a,76,a,12\ndc 232,40,54,3072\nad 200,200,194\nmc v,88,a,64,a,16,164\nex 188,104\nad 200,200,195\n"
"mm v,88,a,68,a,20\nrd a,144,209,26624\ncv 151,194,195\nmm v,88,a,72,a,24\nsa m0,m0,0x1000\ns_nop 0\n"
"dc 233,40,54,0\nmm v,88,a,76,a,28\nex 189,105\nex 190,106\nwl 10\nmc v,76,a,80,a,0,160\n"
"rd a,152,209,30720\nad 201,188,189\nwl 10\nmm v,76,a,84,a,4\ndc 233,40,54,1024\ncv 156,188,189\n"
"ex 191,107\nwl 9\nmm v,76,a,88,a,8\nad 201,201,190\nex 192,108\nwl 8\n"
"mm v,76,a,92,a,12\nad 201,201,191\ncv 157,190,191\nmc v,92,a,80,a,16,164\nex 193,109\nad 201,201,192\n"
"mm v,92,a,84,a,20\ndc 233,40,54,2048\nex 194,110\nmm v,92,a,88,a,24\nad 201,201,193\ncv 158,192,193\n"
"mm v,92,a,92,a,28\nwl 0\nex 195,111\nad 201,201,194\nmm a,224,a,96,v,132\ndc 233,40,54,3072\n"
"rd a,32,208,16384\nrd a,36,208,17408\nmm a,228,a,104,v,132\nrd a,40,208,18432\nrd a,44,208,19456\nrd a,48,208,20480\n"
"rd a,52,208,21504\nrd a,56,208,22528\nrd a,60,208,23552\nrd a,64,208,24576\nrd a,68,208,25600\nrd a,72,208,26624\n"
"rd a,76,208,27648\nrd a,80,208,28672\nrd a,84,208,29696\nrd a,88,208,30720\nrd a,92,208,31744\nmm a,232,a,112,v,132\n"
"s_nop 0\nad 201,201,195\nmm a,236,a,120,v,132\ncv 159,194,195\nad 238,200,201\nmm a,240,a,128,v,132\n"
"v_cmp_nge_f32 vcc, s59, v238\nmm a,244,a,136,v,132\ns_xor_b32 s57, s57, 0x8000\nmm a,248,a,144,v,132\nmm a,252,a,152,v,132\ns_cbranch_vccnz RS_39\n"
"RR_39:\nwl 0\nSHORT_END:\nv_mov_b32 v234, v206\nv_mov_b32 v236, v207\nv_mov_b32 v235, v234\n"
"v_mov_b32 v237, v236\ns_nop 0\nv_permlane32_swap_b32 v234, v235\nv_permlane32_swap_b32 v236, v237\nad 234,234,235\nad 236,236,237\n"
"v_mov_b32 v235, v234\nv_mov_b32 v237, v236\ns_nop 0\nv_permlane16_swap_b32 v234, v235\nv_permlane16_swap_b32 v236, v237\nad 234,234,235\n"
"ad 236,236,237\nv_rcp_f32 v234, v234\nv_rcp_f32 v236, v236\ns_nop 0\nv_accvgpr_read_b32 v96, a192\nv_accvgpr_read_b32 v97, a193\n"
"v_accvgpr_read_b32 v98, a194\nv_accvgpr_read_b32 v99, a195\nv_accvgpr_read_b32 v100, a196\nv_accvgpr_read_b32 v101, a197\nv_accvgpr_read_b32 v102, a198\nv_accvgpr_read_b32 v103, a199\n"
"pm 96,234\npm 98,234\npm 100,234\npm 102,234\ncv 96,96,97\ncv 97,98,99\n"
"cv 98,100,101\ncv 99,102,103\nbuffer_store_dwordx4 v[96:99], v230, s[48:51], s62 offen offset:0 sc1\nv_accvgpr_read_b32 v104, a200\nv_accvgpr_read_b32 v105, a201\nv_accvgpr_read_b32 v106, a202\n"
"v_accvgpr_read_b32 v107, a203\nv_accvgpr_read_b32 v108, a204\nv_accvgpr_read_b32 v109, a205\nv_accvgpr_read_b32 v110, a206\nv_accvgpr_read_b32 v111, a207\npm 104,234\n"
"pm 106,234\npm 108,234\npm 110,234\ncv 104,104,105\ncv 105,106,107\ncv 106,108,109\n"
"cv 107,110,111\nbuffer_store_dwordx4 v[104:107], v230, s[48:51], s62 offen offset:1024 sc1\nv_accvgpr_read_b32 v112, a208\nv_accvgpr_read_b32 v113, a209\nv_accvgpr_read_b32 v114, a210\nv_accvgpr_read_b32 v115, a211\n"
"v_accvgpr_read_b32 v116, a212\nv_accvgpr_read_b32 v117, a213\nv_accvgpr_read_b32 v118, a214\nv_accvgpr_read_b32 v119, a215\npm 112,234\npm 114,234\n"
"pm 116,234\npm 118,234\ncv 112,112,113\ncv 113,114,115\ncv 114,116,117\ncv 115,118,119\n"
"buffer_store_dwordx4 v[112:115], v230, s[48:51], s62 offen offset:2048 sc1\nv_accvgpr_read_b32 v120, a216\nv_accvgpr_read_b32 v121, a217\nv_accvgpr_read_b32 v122, a218\nv_accvgpr_read_b32 v123, a219\nv_accvgpr_read_b32 v124, a220\n"
"v_accvgpr_read_b32 v125, a221\nv_accvgpr_read_b32 v126, a222\nv_accvgpr_read_b32 v127, a223\npm 120,234\npm 122,234\npm 124,234\n"
"pm 126,234\ncv 120,120,121\ncv 121,122,123\ncv 122,124,125\ncv 123,126,127\nbuffer_store_dwordx4 v[120:123], v230, s[48:51], s62 offen offset:3072 sc1\n"
"sa s64,s62,s60\nv_accvgpr_read_b32 v96, a224\nv_accvgpr_read_b32 v97, a225\nv_accvgpr_read_b32 v98, a226\nv_accvgpr_read_b32 v99, a227\nv_accvgpr_read_b32 v100, a228\n"
"v_accvgpr_read_b32 v101, a229\nv_accvgpr_read_b32 v102, a230\nv_accvgpr_read_b32 v103, a231\npm 96,236\npm 98,236\npm 100,236\n"
"pm 102,236\ncv 96,96,97\ncv 97,98,99\ncv 98,100,101\ncv 99,102,103\nbuffer_store_dwordx4 v[96:99], v230, s[48:51], s64 offen offset:0 sc1\n"
"v_accvgpr_read_b32 v104, a232\nv_accvgpr_read_b32 v105, a233\nv_accvgpr_read_b32 v106, a234\nv_accvgpr_read_b32 v107, a235\nv_accvgpr_read_b32 v108, a236\nv_accvgpr_read_b32 v109, a237\n"
"v_accvgpr_read_b32 v110, a238\nv_accvgpr_read_b32 v111, a239\npm 104,236\npm 106,236\npm 108,236\npm 110,236\n"
"cv 104,104,105\ncv 105,106,107\ncv 106,108,109\ncv 107,110,111\nbuffer_store_dwordx4 v[104:107], v230, s[48:51], s64 offen offset:1024 sc1\nv_accvgpr_read_b32 v112, a240\n"
"v_accvgpr_read_b32 v113, a241\nv_accvgpr_read_b32 v114, a242\nv_accvgpr_read_b32 v115, a243\nv_accvgpr_read_b32 v116, a244\nv_accvgpr_read_b32 v117, a245\nv_accvgpr_read_b32 v118, a246\n"
"v_accvgpr_read_b32 v119, a247\npm 112,236\npm 114,236\npm 116,236\npm 118,236\ncv 112,112,113\n"
"cv 113,114,115\ncv 114,116,117\ncv 115,118,119\nbuffer_store_dwordx4 v[112:115], v230, s[48:51], s64 offen offset:2048 sc1\nv_accvgpr_read_b32 v120, a248\nv_accvgpr_read_b32 v121, a249\n"
"v_accvgpr_read_b32 v122, a250\nv_accvgpr_read_b32 v123, a251\nv_accvgpr_read_b32 v124, a252\nv_accvgpr_read_b32 v125, a253\nv_accvgpr_read_b32 v126, a254\nv_accvgpr_read_b32 v127, a255\n"
"pm 120,236\npm 122,236\npm 124,236\npm 126,236\ncv 120,120,121\ncv 121,122,123\n"
"cv 122,124,125\ncv 123,126,127\nbuffer_store_dwordx4 v[120:123], v230, s[48:51], s64 offen offset:3072 sc1\ns_cmp_lt_u32 s63, 2\ns_cbranch_scc1 SOLO\nv_readlane_b32 s84, v250, 40\n"
"v_readlane_b32 s85, v250, 41\nv_readlane_b32 s86, v250, 42\ns_mov_b32 s87, 0x20000\nv_lshlrev_b32 v240, 3, v230\ns_nop 2\nbuffer_load_dword v226, v240, s[84:87], 0 offen\n"
"wv 25\nrt v,96,248,0\nrt v,98,248,2048\nrt v,100,249,0\nrt v,102,249,2048\nrt v,104,248,512\n"
"rt v,106,248,2560\nrt v,108,249,512\nrt v,110,249,2560\nrt v,112,248,4096\nrt v,114,248,6144\nrt v,116,249,4096\n"
"rt v,118,249,6144\nrt v,120,248,4608\nrt v,122,248,6656\nrt v,124,249,4608\nrt v,126,249,6656\nwl 0\n"
"buffer_store_dwordx4 v[96:99], v230, s[88:91], s72 offen sc1\nbuffer_store_dwordx4 v[100:103], v230, s[88:91], s73 offen sc1\nbuffer_store_dwordx4 v[104:107], v230, s[88:91], s74 offen sc1\nbuffer_store_dwordx4 v[108:111], v230, s[88:91], s75 offen sc1\nbuffer_store_dwordx4 v[112:115], v230, s[88:91], s80 offen sc1\nbuffer_store_dwordx4 v[116:119], v230, s[88:91], s81 offen sc1\n"
"buffer_store_dwordx4 v[120:123], v230, s[88:91], s82 offen sc1\nbuffer_store_dwordx4 v[124:127], v230, s[88:91], s83 offen sc1\nSOLO:\ns_lshl_b32 s58, s63, 1\ns_sub_u32 s58, 13, s58\ns_cbranch_scc1 DIAG\n"
".p2align 6\nLOOP_N:\ns_sub_u32 s92, s92, 1\ns_cmp_lt_u32 s92, 2\ns_cbranch_scc1 TK_N\nTKR_N:\n"
"mc v,96,a,32,a,0,160\nad 204,204,200\nad 205,205,201\nmm v,96,a,36,a,4\nrd a,96,209,2048\nex 188,64\n"
"mm v,96,a,40,a,8\nrd a,100,209,3072\nex 189,65\nmm v,96,a,44,a,12\nex 190,66\nmc v,112,a,32,a,16,164\n"
"rd a,104,209,6144\nad 196,188,189\ncv 128,188,189\nmm v,112,a,36,a,20\ns_mov_b32 s54, s53\nex 191,67\n"
"mm v,112,a,40,a,24\nrd a,108,209,7168\nad 196,196,190\nmm v,112,a,44,a,28\nex 192,68\nmc v,100,a,48,a,0,160\n"
"rd a,112,209,10240\nad 196,196,191\ncv 129,190,191\nmm v,100,a,52,a,4\nex 193,69\nmm v,100,a,56,a,8\n"
"rd a,116,209,11264\nad 196,196,192\nmm v,100,a,60,a,12\nsa s53,s53,0x10000\nex 194,70\nmc v,116,a,48,a,16,164\n"
"rd a,120,209,14336\nad 196,196,193\ncv 130,192,193\nmm v,116,a,52,a,20\nex 195,71\nmm v,116,a,56,a,24\n"
"rd a,124,209,15360\nad 196,196,194\nmm v,116,a,60,a,28\nwl 8\nex 188,80\nmc v,104,a,64,a,0,160\n"
"rd a,128,209,18432\nad 196,196,195\ncv 131,194,195\nmm v,104,a,68,a,4\nrd a,132,209,19456\ns_cmp_eq_u32 s53, s55\n"
"s_cselect_b32 s53, s56, s53\nmm v,104,a,72,a,8\nex 189,81\nmm v,104,a,76,a,12\nrd a,136,209,22528\nex 190,82\n"
"mc v,120,a,64,a,16,164\nad 197,188,189\ncv 136,188,189\nmm v,120,a,68,a,20\nrd a,140,209,23552\nex 191,83\n"
"mm v,120,a,72,a,24\nad 197,197,190\nex 192,84\nmm v,120,a,76,a,28\nrd a,144,209,26624\nad 197,197,191\n"
"mc v,108,a,80,a,0,160\ncv 137,190,191\nex 193,85\nmm v,108,a,84,a,4\nrd a,148,209,27648\nad 197,197,192\n"
"mm v,108,a,88,a,8\nex 194,86\nmm v,108,a,92,a,12\nrd a,152,209,30720\nad 197,197,193\ncv 138,192,193\n"
"mc v,124,a,80,a,16,164\nex 195,87\nmm v,124,a,84,a,20\nrd a,156,209,31744\nad 197,197,194\nmm v,124,a,88,a,24\n"
"ex 188,72\nad 197,197,195\nmm v,124,a,92,a,28\ns_waitcnt vmcnt(8) lgkmcnt(8)\ns_barrier\nv_xor_b32 v208, 0x8000, v208\n"
"cv 139,194,195\nex 189,73\nmm v,0,a,96,v,144\ns_mov_b32 m0, s57\nad 196,196,188\nmm v,0,a,100,v,148\n"
"rd a,32,208,0\nex 190,74\nmm v,32,a,96,v,152\nrd a,36,208,1024\nad 196,196,189\nmm v,32,a,100,v,156\n"
"dc 230,40,53,0\ncv 132,188,189\nmm v,4,a,104,v,144\nrd a,40,208,2048\nex 191,75\nmm v,4,a,108,v,148\n"
"ad 196,196,190\nex 192,76\nmm v,36,a,104,v,152\nrd a,44,208,3072\nad 196,196,191\nmm v,36,a,108,v,156\n"
"cv 133,190,191\nex 193,77\nmm v,8,a,112,v,144\nrd a,48,208,4096\nad 196,196,192\nmm v,8,a,116,v,148\n"
"dc 230,40,53,1024\nex 194,78\nmm v,40,a,112,v,152\nrd a,52,208,5120\nad 196,196,193\nmm v,40,a,116,v,156\n"
"cv 134,192,193\nex 195,79\nmm v,12,a,120,v,144\nrd a,56,208,6144\nad 196,196,194\nmm v,12,a,124,v,148\n"
"ex 188,88\nmm v,44,a,120,v,152\nrd a,60,208,7168\nad 196,196,195\ncv 135,194,195\nmm v,44,a,124,v,156\n"
"dc 230,40,53,2048\nwl 8\nmm v,16,a,128,v,144\nrd a,64,208,8192\nex 189,89\nmm v,16,a,132,v,148\n"
"rd a,68,208,9216\nad 197,197,188\nmm v,48,a,128,v,152\nex 190,90\nad 197,197,189\nmm v,48,a,132,v,156\n"
"rd a,72,208,10240\ncv 140,188,189\nmm v,20,a,136,v,144\nex 191,91\nad 197,197,190\nmm v,20,a,140,v,148\n"
"rd a,76,208,11264\ndc 230,40,53,3072\nmm v,52,a,136,v,152\nex 192,92\nad 197,197,191\nmm v,52,a,140,v,156\n"
"rd a,80,208,12288\ncv 141,190,191\nmm v,24,a,144,v,144\nsa m0,m0,0x1000\nex 193,93\nmm v,24,a,148,v,148\n"
"rd a,84,208,13312\nad 197,197,192\nmm v,56,a,144,v,152\nex 194,94\nad 197,197,193\nmm v,56,a,148,v,156\n"
"rd a,88,208,14336\ndc 231,40,53,0\ncv 142,192,193\nmm v,28,a,152,v,144\nex 195,95\nmm v,28,a,156,v,148\n"
"rd a,92,208,15360\nad 197,197,194\ns_nop 0\nmm v,60,a,152,v,152\nad 197,197,195\ncv 143,194,195\n"
"ad 238,196,197\nv_cmp_nge_f32 vcc, s59, v238\nmm v,60,a,156,v,156\ndc 231,40,53,1024\ns_waitcnt vmcnt(6) lgkmcnt(8)\ns_barrier\n"
"v_xor_b32 v209, 0x8000, v209\ns_cbranch_vccnz RS_40\nRR_40:\nmc v,64,a,32,a,0,160\nad 204,204,196\nad 205,205,197\n"
"mm v,64,a,36,a,4\nrd a,96,209,0\nex 188,96\nmm v,64,a,40,a,8\nrd a,100,209,1024\nex 189,97\n"
"mm v,64,a,44,a,12\nex 190,98\nmc v,80,a,32,a,16,164\nrd a,104,209,4096\nad 200,188,189\ncv 144,188,189\n"
"mm v,80,a,36,a,20\nex 191,99\nmm v,80,a,40,a,24\nrd a,108,209,5120\nad 200,200,190\nmm v,80,a,44,a,28\n"
"dc 231,40,53,2048\nex 192,100\nmc v,68,a,48,a,0,160\nrd a,112,209,8192\nad 200,200,191\ncv 145,190,191\n"
"mm v,68,a,52,a,4\nex 193,101\nad 200,200,192\nmm v,68,a,56,a,8\nrd a,116,209,9216\nex 194,102\n"
"mm v,68,a,60,a,12\nad 200,200,193\ncv 146,192,193\nmc v,84,a,48,a,16,164\nrd a,120,209,12288\nex 195,103\n"
"mm v,84,a,52,a,20\nad 200,200,194\nex 188,112\nmm v,84,a,56,a,24\nrd a,124,209,13312\ndc 231,40,53,3072\n"
"mm v,84,a,60,a,28\ns_xor_b32 m0, s57, 0x18000\nwl 8\nmc v,72,a,64,a,0,160\nrd a,128,209,16384\nad 200,200,195\n"
"cv 147,194,195\nmm v,72,a,68,a,4\nrd a,132,209,17408\nex 189,113\nmm v,72,a,72,a,8\ndc 232,40,54,0\n"
"ex 190,114\nmm v,72,a,76,a,12\nrd a,136,209,20480\nad 201,188,189\nmc v,88,a,64,a,16,164\ncv 152,188,189\n"
"ex 191,115\nmm v,88,a,68,a,20\nrd a,140,209,21504\nad 201,201,190\nmm v,88,a,72,a,24\nex 192,116\n"
"mm v,88,a,76,a,28\nrd a,144,209,24576\nad 201,201,191\nmc v,76,a,80,a,0,160\ndc 232,40,54,1024\ncv 153,190,191\n"
"ex 193,117\nmm v,76,a,84,a,4\nrd a,148,209,25600\nad 201,201,192\nmm v,76,a,88,a,8\nex 194,118\n"
"mm v,76,a,92,a,12\nrd a,152,209,28672\nad 201,201,193\ncv 154,192,193\nmc v,92,a,80,a,16,164\nex 195,119\n"
"mm v,92,a,84,a,20\nrd a,156,209,29696\nad 201,201,194\nmm v,92,a,88,a,24\ndc 232,40,54,2048\nex 188,104\n"
"mm v,92,a,92,a,28\nwl 8\nad 201,201,195\ncv 155,194,195\nmm v,0,a,96,v,128\nex 189,105\n"
"ad 200,200,188\nmm v,0,a,100,v,132\nrd a,32,208,16384\nex 190,106\nmm v,32,a,96,v,136\nrd a,36,208,17408\n"
"ad 200,200,189\nmm v,32,a,100,v,140\ncv 148,188,189\nex 191,107\nmm v,4,a,104,v,128\nrd a,40,208,18432\n"
"dc 232,40,54,3072\nmm v,4,a,108,v,132\nad 200,200,190\nex 192,108\nmm v,36,a,104,v,136\nrd a,44,208,19456\n"
"ad 200,200,191\nmm v,36,a,108,v,140\ncv 149,190,191\nex 193,109\nmm v,8,a,112,v,128\nrd a,48,208,20480\n"
"sa m0,m0,0x1000\nmm v,8,a,116,v,132\nad 200,200,192\nex 194,110\nmm v,40,a,112,v,136\nrd a,52,208,21504\n"
"ad 200,200,193\nmm v,40,a,116,v,140\ndc 233,40,54,0\ncv 150,192,193\nmm v,12,a,120,v,128\nrd a,56,208,22528\n"
"ex 195,111\nmm v,12,a,124,v,132\nad 200,200,194\nex 188,120\nmm v,44,a,120,v,136\nrd a,60,208,23552\n"
"ad 200,200,195\nmm v,44,a,124,v,140\nwl 8\ncv 151,194,195\nex 189,121\nmm v,16,a,128,v,128\n"
"rd a,64,208,24576\nad 201,201,188\nmm v,16,a,132,v,132\nrd a,68,208,25600\ndc 233,40,54,1024\nmm v,48,a,128,v,136\n"
"ex 190,122\nad 201,201,189\nmm v,48,a,132,v,140\nrd a,72,208,26624\ncv 156,188,189\nmm v,20,a,136,v,128\n"
"ex 191,123\nad 201,201,190\nmm v,20,a,140,v,132\nrd a,76,208,27648\nex 192,124\nmm v,52,a,136,v,136\n"
"ad 201,201,191\ncv 157,190,191\nmm v,52,a,140,v,140\nrd a,80,208,28672\ndc 233,40,54,2048\nmm v,24,a,144,v,128\n"
"ex 193,125\nad 201,201,192\nmm v,24,a,148,v,132\nrd a,84,208,29696\nex 194,126\nmm v,56,a,144,v,136\n"
"ad 201,201,193\ncv 158,192,193\nmm v,56,a,148,v,140\nrd a,88,208,30720\nex 195,127\nmm v,28,a,152,v,128\n"
"ad 201,201,194\ns_nop 0\nmm v,28,a,156,v,132\nrd a,92,208,31744\nad 201,201,195\ncv 159,194,195\n"
"mm v,60,a,152,v,136\nad 238,200,201\nv_cmp_nge_f32 vcc, s59, v238\ndc 233,40,54,3072\nmm v,60,a,156,v,140\ns_xor_b32 s57, s57, 0x8000\n"
"s_sub_u32 s58, s58, 1\nwl 8\ns_cbranch_vccnz RS_41\nRR_41:\ns_cbranch_scc0 LOOP_N\nDIAG:\n"
"s_cmp_eq_u32 s52, 0\ns_cbranch_scc1 DIAG_0\ns_cmp_eq_u32 s52, 1\ns_cbranch_scc1 DIAG_1\ns_cmp_eq_u32 s52, 2\ns_cbranch_scc1 DIAG_2\n"
"ad 180,164,184\nad 181,165,185\nad 182,166,186\nad 183,167,187\nrd a,96,209,2048\nrd a,100,209,3072\n"
"rd a,104,209,6144\nrd a,108,209,7168\nmc v,112,a,32,a,16,180\nrd a,112,209,10240\nrd a,116,209,11264\nad 76,76,184\n"
"ad 77,77,185\nmm v,112,a,36,a,20\nrd a,120,209,14336\nrd a,124,209,15360\nad 78,78,186\nad 79,79,187\n"
"ad 204,204,200\nmm v,112,a,40,a,24\nrd a,128,209,18432\nrd a,132,209,19456\nad 205,205,201\nex 188,64\n"
"mm v,112,a,44,a,28\nrd a,136,209,22528\nrd a,140,209,23552\nex 189,65\nwl 11\nmm v,0,a,96,v,144\n"
"rd a,144,209,26624\nrd a,148,209,27648\nex 190,66\nwl 12\nmm v,0,a,100,v,148\nrd a,152,209,30720\n"
"rd a,156,209,31744\nad 196,188,189\ncv 128,188,189\nmm v,32,a,96,v,152\nex 191,67\nad 196,196,190\n"
"ex 192,68\nmm v,32,a,100,v,156\nad 196,196,191\ncv 129,190,191\nex 193,69\nwl 13\n"
"mm v,4,a,104,v,144\nad 196,196,192\nex 194,70\nad 196,196,193\ncv 130,192,193\nwl 12\n"
"mm v,4,a,108,v,148\nex 195,71\nad 196,196,194\nex 188,80\nmm v,36,a,104,v,152\nad 196,196,195\n"
"cv 131,194,195\nex 189,81\nmm v,36,a,108,v,156\nex 190,82\nad 197,188,189\ncv 136,188,189\n"
"wl 11\nmm v,8,a,112,v,144\nex 191,83\nad 197,197,190\nex 192,84\nwl 10\n"
"mm v,8,a,116,v,148\nad 197,197,191\ncv 137,190,191\nex 193,85\nmm v,40,a,112,v,152\nad 197,197,192\n"
"ex 194,86\nad 197,197,193\nmm v,40,a,116,v,156\ncv 138,192,193\nex 195,87\nad 197,197,194\n"
"wl 9\nmm v,12,a,120,v,144\nex 188,72\nad 197,197,195\ncv 139,194,195\nex 189,73\n"
"wl 8\nmm v,12,a,124,v,148\nad 196,196,188\nex 190,74\nmm v,44,a,120,v,152\nad 196,196,189\n"
"cv 132,188,189\nex 191,75\nad 196,196,190\nmm v,44,a,124,v,156\nex 192,76\nad 196,196,191\n"
"cv 133,190,191\nwl 7\nmm v,16,a,128,v,144\nex 193,77\nad 196,196,192\nex 194,78\n"
"wl 6\nmm v,16,a,132,v,148\nad 196,196,193\ncv 134,192,193\nex 195,79\nmm v,48,a,128,v,152\n"
"ad 196,196,194\nex 188,88\nad 196,196,195\ncv 135,194,195\nmm v,48,a,132,v,156\nex 189,89\n"
"ad 197,197,188\nex 190,90\nwl 5\nmm v,20,a,136,v,144\nad 197,197,189\ncv 140,188,189\n"
"ex 191,91\nwl 4\nmm v,20,a,140,v,148\nad 197,197,190\nex 192,92\nad 197,197,191\n"
"mm v,52,a,136,v,152\ncv 141,190,191\nex 193,93\nad 197,197,192\nmm v,52,a,140,v,156\nex 194,94\n"
"ad 197,197,193\ncv 142,192,193\nwl 3\nmm v,24,a,144,v,144\nex 195,95\nad 197,197,194\n"
"s_nop 0\nad 197,197,195\nwl 2\nmm v,24,a,148,v,148\ncv 143,194,195\nad 238,196,197\n"
"v_cmp_nge_f32 vcc, s59, v238\nv_mov_b32 v96, 0xff800000\nmm v,56,a,144,v,152\nv_mov_b32 v97, 0xff800000\nv_mov_b32 v98, 0xff800000\nv_mov_b32 v99, 0xff800000\n"
"v_mov_b32 v100, 0xff800000\nv_mov_b32 v101, 0xff800000\nmm v,56,a,148,v,156\nv_mov_b32 v102, 0xff800000\nv_mov_b32 v103, 0xff800000\nv_mov_b32 v104, 0xff800000\n"
"v_mov_b32 v105, 0xff800000\nwl 1\nmm v,28,a,152,v,144\nv_mov_b32 v106, 0xff800000\nv_mov_b32 v107, 0xff800000\nv_mov_b32 v108, 0xff800000\n"
"v_mov_b32 v109, 0xff800000\nv_mov_b32 v110, 0xff800000\nwl 0\nmm v,28,a,156,v,148\nv_mov_b32 v111, 0xff800000\nv_mov_b32 v116, 0xff800000\n"
"v_mov_b32 v117, 0xff800000\nv_mov_b32 v118, 0xff800000\nmm v,60,a,152,v,152\nv_mov_b32 v119, 0xff800000\nv_mov_b32 v120, 0xff800000\nv_mov_b32 v121, 0xff800000\n"
"v_mov_b32 v122, 0xff800000\nv_mov_b32 v123, 0xff800000\nmm v,60,a,156,v,156\nv_mov_b32 v124, 0xff800000\nv_mov_b32 v125, 0xff800000\nv_mov_b32 v126, 0xff800000\n"
"v_mov_b32 v127, 0xff800000\nwv 0\ns_barrier\nv_xor_b32 v209, 0x8000, v209\nv_cmp_nge_f32 vcc, s59, v238\ns_cbranch_vccnz RS_42\n"
"RR_42:\nrd a,96,209,0\nrd a,100,209,1024\nrd a,104,209,4096\nrd a,108,209,5120\nrd a,112,209,8192\n"
"rd a,116,209,9216\nad 204,204,196\nad 205,205,197\nv_mov_b32 v154, 0\nv_mov_b32 v155, 0\nv_mov_b32 v200, 0\n"
"ex 188,112\nex 189,113\nex 190,114\nad 201,188,189\ncv 152,188,189\nex 191,115\n"
"ad 201,201,190\nwl 5\nmm v,0,a,96,v,128\nrd a,120,209,12288\ns_nop 0\nwl 5\n"
"mm v,0,a,100,v,132\nrd a,124,209,13312\nad 201,201,191\nmm v,32,a,96,v,136\nrd a,128,209,16384\nrd a,96,209,2048\n"
"mm v,32,a,100,v,140\nrd a,132,209,17408\ncv 153,190,191\nwl 8\nmm v,4,a,104,v,128\nrd a,136,209,20480\n"
"ad 238,200,201\nwl 8\nmm v,4,a,108,v,132\nrd a,140,209,21504\nv_cmp_nge_f32 vcc, s59, v238\nmm v,36,a,104,v,136\n"
"rd a,144,209,24576\nrd a,104,209,6144\nmm v,36,a,108,v,140\nrd a,148,209,25600\nwl 11\nmm v,8,a,112,v,128\n"
"rd a,152,209,28672\nwl 11\nmm v,8,a,116,v,132\nrd a,156,209,29696\nmm v,40,a,112,v,136\nrd a,112,209,10240\n"
"mm v,40,a,116,v,140\nwl 12\nmm v,12,a,120,v,128\nwl 11\nmm v,12,a,124,v,132\nmm v,44,a,120,v,136\n"
"rd a,120,209,14336\nmm v,44,a,124,v,140\nwl 11\nmm v,16,a,128,v,128\nwl 9\nmm v,16,a,132,v,132\n"
"mm v,48,a,128,v,136\nrd a,128,209,18432\nmm v,48,a,132,v,140\nwl 9\nmm v,20,a,136,v,128\nwl 8\n"
"mm v,20,a,140,v,132\nmm v,52,a,136,v,136\nrd a,136,209,22528\nmm v,52,a,140,v,140\nwl 8\nmm v,24,a,144,v,128\n"
"wl 6\nmm v,24,a,148,v,132\nmm v,56,a,144,v,136\nrd a,144,209,26624\nmm v,56,a,148,v,140\nwl 6\n"
"mm v,28,a,152,v,128\nwl 5\nmm v,28,a,156,v,132\nmm v,60,a,152,v,136\nrd a,152,209,30720\nmm v,60,a,156,v,140\n"
"s_cbranch_vccnz RS_43\nRR_43:\nad 205,205,201\nmm v,32,a,96,v,152\nv_mov_b32 v234, v204\nv_mov_b32 v236, v205\n"
"v_mov_b32 v235, v234\nv_mov_b32 v237, v236\ns_nop 0\nv_permlane32_swap_b32 v234, v235\nv_permlane32_swap_b32 v236, v237\nmm v,36,a,104,v,152\n"
"ad 234,234,235\nad 236,236,237\nv_mov_b32 v235, v234\nv_mov_b32 v237, v236\ns_nop 0\nv_permlane16_swap_b32 v234, v235\n"
"v_permlane16_swap_b32 v236, v237\nwl 5\nmm v,40,a,112,v,152\nad 234,234,235\nad 236,236,237\nv_rcp_f32 v234, v234\n"
"v_rcp_f32 v236, v236\ns_nop 0\npm 0,234\nwl 4\nmm v,44,a,120,v,152\npm 2,234\n"
"pm 4,234\npm 6,234\ncv 0,0,1\ncv 1,2,3\ncv 2,4,5\ncv 3,6,7\n"
"wl 3\nmm v,48,a,128,v,152\nbuffer_store_dwordx4 v[0:3], v230, s[48:51], s61 offen offset:0 sc1\npm 8,234\npm 10,234\npm 12,234\n"
"pm 14,234\ncv 8,8,9\ncv 9,10,11\nwl 2\nmm v,52,a,136,v,152\ncv 10,12,13\n"
"cv 11,14,15\nbuffer_store_dwordx4 v[8:11], v230, s[48:51], s61 offen offset:1024 sc1\npm 16,234\npm 18,234\npm 20,234\npm 22,234\n"
"wl 1\nmm v,56,a,144,v,152\ncv 16,16,17\ncv 17,18,19\ncv 18,20,21\ncv 19,22,23\n"
"buffer_store_dwordx4 v[16:19], v230, s[48:51], s61 offen offset:2048 sc1\npm 24,234\npm 26,234\npm 28,234\nwl 0\nmm v,60,a,152,v,152\n"
"pm 30,234\ncv 24,24,25\ncv 25,26,27\ncv 26,28,29\ncv 27,30,31\nbuffer_store_dwordx4 v[24:27], v230, s[48:51], s61 offen offset:3072 sc1\n"
"sa s64,s61,s60\ns_nop 3\npm 32,236\npm 34,236\npm 36,236\npm 38,236\n"
"cv 32,32,33\ncv 33,34,35\ncv 34,36,37\ncv 35,38,39\nbuffer_store_dwordx4 v[32:35], v230, s[48:51], s64 offen offset:0 sc1\npm 40,236\n"
"pm 42,236\npm 44,236\npm 46,236\ncv 40,40,41\ncv 41,42,43\ncv 42,44,45\n"
"cv 43,46,47\nbuffer_store_dwordx4 v[40:43], v230, s[48:51], s64 offen offset:1024 sc1\npm 48,236\npm 50,236\npm 52,236\npm 54,236\n"
"cv 48,48,49\ncv 49,50,51\ncv 50,52,53\ncv 51,54,55\nbuffer_store_dwordx4 v[48:51], v230, s[48:51], s64 offen offset:2048 sc1\npm 56,236\n"
"pm 58,236\npm 60,236\npm 62,236\ncv 56,56,57\ncv 57,58,59\ncv 58,60,61\n"
"cv 59,62,63\nbuffer_store_dwordx4 v[56:59], v230, s[48:51], s64 offen offset:3072 sc1\ns_branch FINAL\nDIAG_0:\nad 180,164,184\nad 181,165,185\n"
"ad 182,166,186\nad 183,167,187\nrd a,96,209,2048\nrd a,100,209,3072\nrd a,104,209,6144\nrd a,108,209,7168\n"
"mc v,112,a,32,a,16,164\nrd a,112,209,10240\nrd a,116,209,11264\nmm v,112,a,36,a,20\nrd a,120,209,14336\nrd a,124,209,15360\n"
"ad 64,64,184\nmm v,112,a,40,a,24\nrd a,128,209,18432\nrd a,132,209,19456\nmm v,112,a,44,a,28\nrd a,136,209,22528\n"
"rd a,140,209,23552\nad 65,65,185\nmc v,116,a,48,a,16,164\nrd a,144,209,26624\nrd a,148,209,27648\nmm v,116,a,52,a,20\n"
"rd a,152,209,30720\nrd a,156,209,31744\nad 66,66,186\nmm v,116,a,56,a,24\nad 67,67,187\nv_mov_b32 v68, 0xff800000\n"
"mm v,116,a,60,a,28\nv_mov_b32 v69, 0xff800000\nv_mov_b32 v70, 0xff800000\nv_mov_b32 v71, 0xff800000\nwl 15\nmc v,120,a,64,a,16,164\n"
"v_mov_b32 v72, 0xff800000\nv_mov_b32 v73, 0xff800000\nmm v,120,a,68,a,20\nv_mov_b32 v74, 0xff800000\nv_mov_b32 v75, 0xff800000\nv_mov_b32 v76, 0xff800000\n"
"mm v,120,a,72,a,24\nv_mov_b32 v77, 0xff800000\nv_mov_b32 v78, 0xff800000\nmm v,120,a,76,a,28\nv_mov_b32 v79, 0xff800000\nad 204,204,200\n"
"ad 205,205,201\nmc v,124,a,80,a,16,180\nv_mov_b32 v130, 0\nv_mov_b32 v131, 0\nmm v,124,a,84,a,20\nex 188,64\n"
"mm v,124,a,88,a,24\nex 189,65\nex 190,66\nmm v,124,a,92,a,28\nad 196,188,189\ncv 128,188,189\n"
"mm v,0,a,96,v,144\nex 191,67\nwl 14\nmm v,0,a,100,v,148\nad 196,196,190\nex 192,80\n"
"mm v,32,a,96,v,152\nad 196,196,191\ncv 129,190,191\nmm v,32,a,100,v,156\nex 193,81\nwl 13\n"
"mm v,4,a,104,v,144\nex 194,82\nad 197,192,193\nwl 12\nmm v,4,a,108,v,148\ncv 136,192,193\n"
"ex 195,83\nmm v,36,a,104,v,152\nad 197,197,194\nex 188,84\nmm v,36,a,108,v,156\nad 197,197,195\n"
"cv 137,194,195\nwl 11\nmm v,8,a,112,v,144\nex 189,85\nwl 10\nmm v,8,a,116,v,148\n"
"ad 197,197,188\nex 190,86\nmm v,40,a,112,v,152\nad 197,197,189\ncv 138,188,189\nmm v,40,a,116,v,156\n"
"ex 191,87\nwl 9\nmm v,12,a,120,v,144\nad 197,197,190\nex 192,88\nwl 8\n"
"mm v,12,a,124,v,148\nad 197,197,191\ncv 139,190,191\nmm v,44,a,120,v,152\nex 193,89\nad 197,197,192\n"
"mm v,44,a,124,v,156\nex 194,90\nwl 7\nmm v,16,a,128,v,144\nad 197,197,193\ncv 140,192,193\n"
"wl 6\nmm v,16,a,132,v,148\nex 195,91\nad 197,197,194\nmm v,48,a,128,v,152\nex 188,92\n"
"ad 197,197,195\nmm v,48,a,132,v,156\ncv 141,194,195\nex 189,93\nwl 5\nmm v,20,a,136,v,144\n"
"ad 197,197,188\nex 190,94\nwl 4\nmm v,20,a,140,v,148\nad 197,197,189\ncv 142,188,189\n"
"mm v,52,a,136,v,152\nex 191,95\nmm v,52,a,140,v,156\nad 197,197,190\ns_nop 0\nwl 3\n"
"mm v,24,a,144,v,144\nad 197,197,191\ncv 143,190,191\nad 238,196,197\nwl 2\nmm v,24,a,148,v,148\n"
"v_cmp_nge_f32 vcc, s59, v238\nv_mov_b32 v96, 0xff800000\nmm v,56,a,144,v,152\nv_mov_b32 v97, 0xff800000\nv_mov_b32 v98, 0xff800000\nv_mov_b32 v99, 0xff800000\n"
"mm v,56,a,148,v,156\nv_mov_b32 v100, 0xff800000\nv_mov_b32 v101, 0xff800000\nwl 1\nmm v,28,a,152,v,144\nv_mov_b32 v102, 0xff800000\n"
"v_mov_b32 v103, 0xff800000\nv_mov_b32 v104, 0xff800000\nwl 0\nmm v,28,a,156,v,148\nv_mov_b32 v105, 0xff800000\nv_mov_b32 v106, 0xff800000\n"
"mm v,60,a,152,v,152\nv_mov_b32 v107, 0xff800000\nv_mov_b32 v108, 0xff800000\nv_mov_b32 v109, 0xff800000\nmm v,60,a,156,v,156\nv_mov_b32 v110, 0xff800000\n"
"v_mov_b32 v111, 0xff800000\nwv 0\ns_barrier\nv_xor_b32 v209, 0x8000, v209\nv_cmp_nge_f32 vcc, s59, v238\ns_cbranch_vccnz RS_44\n"
"RR_44:\nrd a,96,209,0\nrd a,100,209,1024\nrd a,104,209,4096\nrd a,108,209,5120\nrd a,112,209,8192\n"
"rd a,116,209,9216\nad 204,204,196\nad 205,205,197\nv_mov_b32 v200, 0\nex 188,112\nex 189,113\n"
"ex 190,114\nad 201,188,189\ncv 152,188,189\nex 191,115\nad 201,201,190\nex 192,116\n"
"ad 201,201,191\nwl 5\nmm v,0,a,96,v,128\nrd a,120,209,12288\ncv 153,190,191\nmm v,32,a,96,v,136\n"
"rd a,124,209,13312\nrd a,96,209,2048\nex 193,117\nwl 7\nmm v,32,a,100,v,140\nrd a,128,209,16384\n"
"rd a,100,209,3072\nad 201,201,192\nwl 8\nmm v,4,a,104,v,128\nrd a,132,209,17408\nex 194,118\n"
"mm v,36,a,104,v,136\nrd a,136,209,20480\nrd a,104,209,6144\nad 201,201,193\nwl 10\nmm v,36,a,108,v,140\n"
"rd a,140,209,21504\nrd a,108,209,7168\nwl 11\nmm v,8,a,112,v,128\nrd a,144,209,24576\ncv 154,192,193\n"
"ex 195,119\nmm v,40,a,112,v,136\nrd a,148,209,25600\nrd a,112,209,10240\nwl 13\nmm v,40,a,116,v,140\n"
"rd a,152,209,28672\nrd a,116,209,11264\nad 201,201,194\nwl 14\nmm v,12,a,120,v,128\nrd a,156,209,29696\n"
"ex 188,120\nmm v,44,a,120,v,136\nrd a,120,209,14336\nad 201,201,195\ncv 155,194,195\nwl 15\n"
"mm v,44,a,124,v,140\nrd a,124,209,15360\nex 189,121\nwl 14\nmm v,16,a,128,v,128\nad 201,201,188\n"
"ex 190,122\nmm v,48,a,128,v,136\nrd a,128,209,18432\nad 201,201,189\ncv 156,188,189\nwl 13\n"
"mm v,48,a,132,v,140\nrd a,132,209,19456\nex 191,123\nwl 13\nmm v,20,a,136,v,128\nad 201,201,190\n"
"ex 192,124\nmm v,52,a,136,v,136\nrd a,136,209,22528\nad 201,201,191\ncv 157,190,191\nwl 12\n"
"mm v,52,a,140,v,140\nrd a,140,209,23552\nex 193,125\nwl 11\nmm v,24,a,144,v,128\nad 201,201,192\n"
"ex 194,126\nmm v,56,a,144,v,136\nrd a,144,209,26624\nad 201,201,193\nwl 11\nmm v,56,a,148,v,140\n"
"rd a,148,209,27648\ncv 158,192,193\nex 195,127\nwl 10\nmm v,28,a,152,v,128\nad 201,201,194\n"
"s_nop 0\nmm v,60,a,152,v,136\nrd a,152,209,30720\nad 201,201,195\ncv 159,194,195\nwl 9\n"
"mm v,60,a,156,v,140\nrd a,156,209,31744\nad 238,200,201\nv_cmp_nge_f32 vcc, s59, v238\ns_cbranch_vccnz RS_45\nRR_45:\n"
"ad 205,205,201\nmm v,32,a,96,v,152\nv_mov_b32 v234, v204\nv_mov_b32 v236, v205\nv_mov_b32 v235, v234\nv_mov_b32 v237, v236\n"
"mm v,32,a,100,v,156\ns_nop 0\nv_permlane32_swap_b32 v234, v235\nv_permlane32_swap_b32 v236, v237\nmm v,36,a,104,v,152\nad 234,234,235\n"
"ad 236,236,237\nv_mov_b32 v235, v234\nv_mov_b32 v237, v236\nmm v,36,a,108,v,156\ns_nop 0\nv_permlane16_swap_b32 v234, v235\n"
"v_permlane16_swap_b32 v236, v237\nmm v,40,a,112,v,152\nad 234,234,235\nad 236,236,237\nv_rcp_f32 v234, v234\nmm v,40,a,116,v,156\n"
"v_rcp_f32 v236, v236\ns_nop 0\npm 0,234\nwl 9\nmm v,44,a,120,v,152\npm 2,234\n"
"pm 4,234\npm 6,234\nwl 8\nmm v,44,a,124,v,156\ncv 0,0,1\ncv 1,2,3\n"
"cv 2,4,5\ncv 3,6,7\nwl 7\nmm v,48,a,128,v,152\nbuffer_store_dwordx4 v[0:3], v230, s[48:51], s61 offen offset:0 sc1\npm 8,234\n"
"pm 10,234\npm 12,234\nwl 6\nmm v,48,a,132,v,156\npm 14,234\ncv 8,8,9\n"
"cv 9,10,11\nwl 5\nmm v,52,a,136,v,152\ncv 10,12,13\ncv 11,14,15\nbuffer_store_dwordx4 v[8:11], v230, s[48:51], s61 offen offset:1024 sc1\n"
"pm 16,234\nwl 4\nmm v,52,a,140,v,156\npm 18,234\npm 20,234\npm 22,234\n"
"wl 3\nmm v,56,a,144,v,152\ncv 16,16,17\ncv 17,18,19\ncv 18,20,21\ncv 19,22,23\n"
"wl 2\nmm v,56,a,148,v,156\nbuffer_store_dwordx4 v[16:19], v230, s[48:51], s61 offen offset:2048 sc1\npm 24,234\npm 26,234\npm 28,234\n"
"wl 1\nmm v,60,a,152,v,152\npm 30,234\ncv 24,24,25\ncv 25,26,27\nwl 0\n"
"mm v,60,a,156,v,156\ncv 26,28,29\ncv 27,30,31\nbuffer_store_dwordx4 v[24:27], v230, s[48:51], s61 offen offset:3072 sc1\nsa s64,s61,s60\ns_nop 3\n"
"pm 32,236\npm 34,236\npm 36,236\npm 38,236\ncv 32,32,33\ncv 33,34,35\n"
"cv 34,36,37\ncv 35,38,39\nbuffer_store_dwordx4 v[32:35], v230, s[48:51], s64 offen offset:0 sc1\npm 40,236\npm 42,236\npm 44,236\n"
"pm 46,236\ncv 40,40,41\ncv 41,42,43\ncv 42,44,45\ncv 43,46,47\nbuffer_store_dwordx4 v[40:43], v230, s[48:51], s64 offen offset:1024 sc1\n"
"pm 48,236\npm 50,236\npm 52,236\npm 54,236\ncv 48,48,49\ncv 49,50,51\n"
"cv 50,52,53\ncv 51,54,55\nbuffer_store_dwordx4 v[48:51], v230, s[48:51], s64 offen offset:2048 sc1\npm 56,236\npm 58,236\npm 60,236\n"
"pm 62,236\ncv 56,56,57\ncv 57,58,59\ncv 58,60,61\ncv 59,62,63\nbuffer_store_dwordx4 v[56:59], v230, s[48:51], s64 offen offset:3072 sc1\n"
"s_branch FINAL\nDIAG_1:\nad 180,164,184\nad 181,165,185\nad 182,166,186\nad 183,167,187\n"
"rd a,96,209,2048\nrd a,100,209,3072\nrd a,104,209,6144\nrd a,108,209,7168\nmc v,112,a,32,a,16,164\nrd a,112,209,10240\n"
"rd a,116,209,11264\nad 68,68,184\nmm v,112,a,36,a,20\nrd a,120,209,14336\nrd a,124,209,15360\nad 69,69,185\n"
"mm v,112,a,40,a,24\nrd a,128,209,18432\nrd a,132,209,19456\nad 70,70,186\nmm v,112,a,44,a,28\nrd a,136,209,22528\n"
"rd a,140,209,23552\nad 71,71,187\nmc v,116,a,48,a,16,164\nrd a,144,209,26624\nrd a,148,209,27648\nv_mov_b32 v72, 0xff800000\n"
"mm v,116,a,52,a,20\nrd a,152,209,30720\nrd a,156,209,31744\nv_mov_b32 v73, 0xff800000\nmm v,116,a,56,a,24\nv_mov_b32 v74, 0xff800000\n"
"v_mov_b32 v75, 0xff800000\nv_mov_b32 v76, 0xff800000\nmm v,116,a,60,a,28\nv_mov_b32 v77, 0xff800000\nv_mov_b32 v78, 0xff800000\nv_mov_b32 v79, 0xff800000\n"
"wl 15\nmc v,120,a,64,a,16,180\nad 204,204,200\nad 205,205,201\nmm v,120,a,68,a,20\nex 188,64\n"
"ex 189,65\nmm v,120,a,72,a,24\nex 190,66\nad 196,188,189\nmm v,120,a,76,a,28\ncv 128,188,189\n"
"ex 191,67\nmm v,0,a,96,v,144\nad 196,196,190\nex 192,68\nwl 14\nmm v,0,a,100,v,148\n"
"ad 196,196,191\ncv 129,190,191\nmm v,32,a,96,v,152\nex 193,69\nad 196,196,192\nmm v,32,a,100,v,156\n"
"ex 194,70\nad 196,196,193\ncv 130,192,193\nwl 13\nmm v,4,a,104,v,144\nex 195,71\n"
"ad 196,196,194\nwl 12\nmm v,4,a,108,v,148\nex 188,80\nad 196,196,195\nmm v,36,a,104,v,152\n"
"cv 131,194,195\nex 189,81\nmm v,36,a,108,v,156\nex 190,82\nad 197,188,189\nwl 11\n"
"mm v,8,a,112,v,144\ncv 136,188,189\nex 191,83\nwl 10\nmm v,8,a,116,v,148\nad 197,197,190\n"
"ex 192,84\nmm v,40,a,112,v,152\nad 197,197,191\ncv 137,190,191\nmm v,40,a,116,v,156\nex 193,85\n"
"ad 197,197,192\nwl 9\nmm v,12,a,120,v,144\nex 194,86\nad 197,197,193\nwl 8\n"
"mm v,12,a,124,v,148\ncv 138,192,193\nex 195,87\nmm v,44,a,120,v,152\nad 197,197,194\nex 188,88\n"
"mm v,44,a,124,v,156\nad 197,197,195\ncv 139,194,195\nex 189,89\nwl 7\nmm v,16,a,128,v,144\n"
"ad 197,197,188\nex 190,90\nwl 6\nmm v,16,a,132,v,148\nad 197,197,189\ncv 140,188,189\n"
"mm v,48,a,128,v,152\nex 191,91\nad 197,197,190\nmm v,48,a,132,v,156\nex 192,92\nad 197,197,191\n"
"wl 5\nmm v,20,a,136,v,144\ncv 141,190,191\nex 193,93\nwl 4\nmm v,20,a,140,v,148\n"
"ad 197,197,192\nex 194,94\nmm v,52,a,136,v,152\nad 197,197,193\ncv 142,192,193\nex 195,95\n"
"mm v,52,a,140,v,156\nad 197,197,194\ns_nop 0\nwl 3\nmm v,24,a,144,v,144\nad 197,197,195\n"
"cv 143,194,195\nad 238,196,197\nwl 2\nmm v,24,a,148,v,148\nv_cmp_nge_f32 vcc, s59, v238\nv_mov_b32 v96, 0xff800000\n"
"v_mov_b32 v97, 0xff800000\nmm v,56,a,144,v,152\nv_mov_b32 v98, 0xff800000\nv_mov_b32 v99, 0xff800000\nv_mov_b32 v100, 0xff800000\nmm v,56,a,148,v,156\n"
"v_mov_b32 v101, 0xff800000\nv_mov_b32 v102, 0xff800000\nv_mov_b32 v103, 0xff800000\nwl 1\nmm v,28,a,152,v,144\nv_mov_b32 v104, 0xff800000\n"
"v_mov_b32 v105, 0xff800000\nv_mov_b32 v106, 0xff800000\nwl 0\nmm v,28,a,156,v,148\nv_mov_b32 v107, 0xff800000\nv_mov_b32 v108, 0xff800000\n"
"v_mov_b32 v109, 0xff800000\nmm v,60,a,152,v,152\nv_mov_b32 v110, 0xff800000\nv_mov_b32 v111, 0xff800000\nv_mov_b32 v124, 0xff800000\nmm v,60,a,156,v,156\n"
"v_mov_b32 v125, 0xff800000\nv_mov_b32 v126, 0xff800000\nv_mov_b32 v127, 0xff800000\nwv 0\ns_barrier\nv_xor_b32 v209, 0x8000, v209\n"
"v_cmp_nge_f32 vcc, s59, v238\ns_cbranch_vccnz RS_46\nRR_46:\nrd a,96,209,0\nrd a,100,209,1024\nrd a,104,209,4096\n"
"rd a,108,209,5120\nrd a,112,209,8192\nrd a,116,209,9216\nad 204,204,196\nad 205,205,197\nv_mov_b32 v158, 0\n"
"v_mov_b32 v159, 0\nv_mov_b32 v200, 0\nex 188,112\nex 189,113\nex 190,114\nad 201,188,189\n"
"cv 152,188,189\nex 191,115\nad 201,201,190\nwl 5\nmm v,0,a,96,v,128\nrd a,120,209,12288\n"
"ex 192,116\nmm v,32,a,96,v,136\nrd a,124,209,13312\nrd a,96,209,2048\nwl 7\nmm v,32,a,100,v,140\n"
"rd a,128,209,16384\nrd a,100,209,3072\nwl 8\nmm v,4,a,104,v,128\nrd a,132,209,17408\nad 201,201,191\n"
"cv 153,190,191\nmm v,36,a,104,v,136\nrd a,136,209,20480\nrd a,104,209,6144\nwl 10\nmm v,36,a,108,v,140\n"
"rd a,140,209,21504\nrd a,108,209,7168\nwl 11\nmm v,8,a,112,v,128\nrd a,144,209,24576\nex 193,117\n"
"mm v,40,a,112,v,136\nrd a,148,209,25600\nrd a,112,209,10240\nad 201,201,192\nwl 13\nmm v,40,a,116,v,140\n"
"rd a,152,209,28672\nrd a,116,209,11264\nwl 14\nmm v,12,a,120,v,128\nrd a,156,209,29696\nex 194,118\n"
"mm v,44,a,120,v,136\nrd a,120,209,14336\nad 201,201,193\nwl 15\nmm v,44,a,124,v,140\nrd a,124,209,15360\n"
"cv 154,192,193\nwl 14\nmm v,16,a,128,v,128\nex 195,119\nad 201,201,194\nmm v,48,a,128,v,136\n"
"rd a,128,209,18432\nex 188,120\nwl 13\nmm v,48,a,132,v,140\nrd a,132,209,19456\nad 201,201,195\n"
"wl 13\nmm v,20,a,136,v,128\ncv 155,194,195\nex 189,121\nmm v,52,a,136,v,136\nrd a,136,209,22528\n"
"ad 201,201,188\nwl 12\nmm v,52,a,140,v,140\nrd a,140,209,23552\nex 190,122\nwl 11\n"
"mm v,24,a,144,v,128\nad 201,201,189\ncv 156,188,189\nmm v,56,a,144,v,136\nrd a,144,209,26624\nex 191,123\n"
"wl 11\nmm v,56,a,148,v,140\nrd a,148,209,27648\nad 201,201,190\nwl 10\nmm v,28,a,152,v,128\n"
"s_nop 0\nad 201,201,191\nmm v,60,a,152,v,136\nrd a,152,209,30720\ncv 157,190,191\nad 238,200,201\n"
"wl 9\nmm v,60,a,156,v,140\nrd a,156,209,31744\nv_cmp_nge_f32 vcc, s59, v238\ns_cbranch_vccnz RS_47\nRR_47:\n"
"ad 205,205,201\nmm v,32,a,96,v,152\nv_mov_b32 v234, v204\nv_mov_b32 v236, v205\nv_mov_b32 v235, v234\nv_mov_b32 v237, v236\n"
"mm v,32,a,100,v,156\ns_nop 0\nv_permlane32_swap_b32 v234, v235\nv_permlane32_swap_b32 v236, v237\nmm v,36,a,104,v,152\nad 234,234,235\n"
"ad 236,236,237\nv_mov_b32 v235, v234\nv_mov_b32 v237, v236\nmm v,36,a,108,v,156\ns_nop 0\nv_permlane16_swap_b32 v234, v235\n"
"v_permlane16_swap_b32 v236, v237\nmm v,40,a,112,v,152\nad 234,234,235\nad 236,236,237\nv_rcp_f32 v234, v234\nmm v,40,a,116,v,156\n"
"v_rcp_f32 v236, v236\ns_nop 0\npm 0,234\nwl 9\nmm v,44,a,120,v,152\npm 2,234\n"
"pm 4,234\npm 6,234\nwl 8\nmm v,44,a,124,v,156\ncv 0,0,1\ncv 1,2,3\n"
"cv 2,4,5\ncv 3,6,7\nwl 7\nmm v,48,a,128,v,152\nbuffer_store_dwordx4 v[0:3], v230, s[48:51], s61 offen offset:0 sc1\npm 8,234\n"
"pm 10,234\npm 12,234\nwl 6\nmm v,48,a,132,v,156\npm 14,234\ncv 8,8,9\n"
"cv 9,10,11\nwl 5\nmm v,52,a,136,v,152\ncv 10,12,13\ncv 11,14,15\nbuffer_store_dwordx4 v[8:11], v230, s[48:51], s61 offen offset:1024 sc1\n"
"pm 16,234\nwl 4\nmm v,52,a,140,v,156\npm 18,234\npm 20,234\npm 22,234\n"
"wl 3\nmm v,56,a,144,v,152\ncv 16,16,17\ncv 17,18,19\ncv 18,20,21\ncv 19,22,23\n"
"wl 2\nmm v,56,a,148,v,156\nbuffer_store_dwordx4 v[16:19], v230, s[48:51], s61 offen offset:2048 sc1\npm 24,234\npm 26,234\npm 28,234\n"
"wl 1\nmm v,60,a,152,v,152\npm 30,234\ncv 24,24,25\ncv 25,26,27\nwl 0\n"
"mm v,60,a,156,v,156\ncv 26,28,29\ncv 27,30,31\nbuffer_store_dwordx4 v[24:27], v230, s[48:51], s61 offen offset:3072 sc1\nsa s64,s61,s60\ns_nop 3\n"
"pm 32,236\npm 34,236\npm 36,236\npm 38,236\ncv 32,32,33\ncv 33,34,35\n"
"cv 34,36,37\ncv 35,38,39\nbuffer_store_dwordx4 v[32:35], v230, s[48:51], s64 offen offset:0 sc1\npm 40,236\npm 42,236\npm 44,236\n"
"pm 46,236\ncv 40,40,41\ncv 41,42,43\ncv 42,44,45\ncv 43,46,47\nbuffer_store_dwordx4 v[40:43], v230, s[48:51], s64 offen offset:1024 sc1\n"
"pm 48,236\npm 50,236\npm 52,236\npm 54,236\ncv 48,48,49\ncv 49,50,51\n"
"cv 50,52,53\ncv 51,54,55\nbuffer_store_dwordx4 v[48:51], v230, s[48:51], s64 offen offset:2048 sc1\npm 56,236\npm 58,236\npm 60,236\n"
"pm 62,236\ncv 56,56,57\ncv 57,58,59\ncv 58,60,61\ncv 59,62,63\nbuffer_store_dwordx4 v[56:59], v230, s[48:51], s64 offen offset:3072 sc1\n"
"s_branch FINAL\nDIAG_2:\nad 180,164,184\nad 181,165,185\nad 182,166,186\nad 183,167,187\n"
"rd a,96,209,2048\nrd a,100,209,3072\nrd a,104,209,6144\nrd a,108,209,7168\nmc v,112,a,32,a,16,164\nrd a,112,209,10240\n"
"rd a,116,209,11264\nad 72,72,184\nad 73,73,185\nmm v,112,a,36,a,20\nrd a,120,209,14336\nrd a,124,209,15360\n"
"ad 74,74,186\nmm v,112,a,40,a,24\nrd a,128,209,18432\nrd a,132,209,19456\nad 75,75,187\nv_mov_b32 v76, 0xff800000\n"
"mm v,112,a,44,a,28\nrd a,136,209,22528\nrd a,140,209,23552\nv_mov_b32 v77, 0xff800000\nv_mov_b32 v78, 0xff800000\nmc v,116,a,48,a,16,180\n"
"rd a,144,209,26624\nrd a,148,209,27648\nv_mov_b32 v79, 0xff800000\nmm v,116,a,52,a,20\nrd a,152,209,30720\nrd a,156,209,31744\n"
"ad 204,204,200\nad 205,205,201\nmm v,116,a,56,a,24\nv_mov_b32 v134, 0\nv_mov_b32 v135, 0\nex 188,64\n"
"mm v,116,a,60,a,28\nex 189,65\nex 190,66\nwl 15\nmm v,0,a,96,v,144\nad 196,188,189\n"
"cv 128,188,189\nex 191,67\nwl 14\nmm v,0,a,100,v,148\nad 196,196,190\nex 192,68\n"
"mm v,32,a,96,v,152\nad 196,196,191\ncv 129,190,191\nex 193,69\nmm v,32,a,100,v,156\nad 196,196,192\n"
"ex 194,70\nwl 13\nmm v,4,a,104,v,144\nad 196,196,193\ncv 130,192,193\nex 195,71\n"
"wl 12\nmm v,4,a,108,v,148\nad 196,196,194\nex 188,80\nmm v,36,a,104,v,152\nad 196,196,195\n"
"cv 131,194,195\nex 189,81\nmm v,36,a,108,v,156\nex 190,82\nad 197,188,189\ncv 136,188,189\n"
"wl 11\nmm v,8,a,112,v,144\nex 191,83\nad 197,197,190\nwl 10\nmm v,8,a,116,v,148\n"
"ex 192,84\nad 197,197,191\ncv 137,190,191\nmm v,40,a,112,v,152\nex 193,85\nad 197,197,192\n"
"mm v,40,a,116,v,156\nex 194,86\nad 197,197,193\ncv 138,192,193\nwl 9\nmm v,12,a,120,v,144\n"
"ex 195,87\nad 197,197,194\nex 188,72\nwl 8\nmm v,12,a,124,v,148\nad 197,197,195\n"
"cv 139,194,195\nmm v,44,a,120,v,152\nex 189,73\nad 196,196,188\nex 190,74\nmm v,44,a,124,v,156\n"
"ad 196,196,189\ncv 132,188,189\nex 191,75\nwl 7\nmm v,16,a,128,v,144\nad 196,196,190\n"
"ex 192,88\nwl 6\nmm v,16,a,132,v,148\nad 196,196,191\ncv 133,190,191\nex 193,89\n"
"mm v,48,a,128,v,152\nad 197,197,192\nex 194,90\nmm v,48,a,132,v,156\nad 197,197,193\ncv 140,192,193\n"
"ex 195,91\nwl 5\nmm v,20,a,136,v,144\nad 197,197,194\nex 188,92\nad 197,197,195\n"
"wl 4\nmm v,20,a,140,v,148\ncv 141,194,195\nex 189,93\nmm v,52,a,136,v,152\nad 197,197,188\n"
"ex 190,94\nad 197,197,189\nmm v,52,a,140,v,156\ncv 142,188,189\nex 191,95\nad 197,197,190\n"
"wl 3\nmm v,24,a,144,v,144\ns_nop 0\nad 197,197,191\ncv 143,190,191\nwl 2\n"
"mm v,24,a,148,v,148\nad 238,196,197\nv_cmp_nge_f32 vcc, s59, v238\nv_mov_b32 v96, 0xff800000\nv_mov_b32 v97, 0xff800000\nmm v,56,a,144,v,152\n"
"v_mov_b32 v98, 0xff800000\nv_mov_b32 v99, 0xff800000\nv_mov_b32 v100, 0xff800000\nv_mov_b32 v101, 0xff800000\nmm v,56,a,148,v,156\nv_mov_b32 v102, 0xff800000\n"
"v_mov_b32 v103, 0xff800000\nv_mov_b32 v104, 0xff800000\nwl 1\nmm v,28,a,152,v,144\nv_mov_b32 v105, 0xff800000\nv_mov_b32 v106, 0xff800000\n"
"v_mov_b32 v107, 0xff800000\nv_mov_b32 v108, 0xff800000\nwl 0\nmm v,28,a,156,v,148\nv_mov_b32 v109, 0xff800000\nv_mov_b32 v110, 0xff800000\n"
"v_mov_b32 v111, 0xff800000\nv_mov_b32 v120, 0xff800000\nmm v,60,a,152,v,152\nv_mov_b32 v121, 0xff800000\nv_mov_b32 v122, 0xff800000\nv_mov_b32 v123, 0xff800000\n"
"mm v,60,a,156,v,156\nv_mov_b32 v124, 0xff800000\nv_mov_b32 v125, 0xff800000\nv_mov_b32 v126, 0xff800000\nv_mov_b32 v127, 0xff800000\nwv 0\n"
"s_barrier\nv_xor_b32 v209, 0x8000, v209\nv_cmp_nge_f32 vcc, s59, v238\ns_cbranch_vccnz RS_48\nRR_48:\nrd a,96,209,0\n"
"rd a,100,209,1024\nrd a,104,209,4096\nrd a,108,209,5120\nrd a,112,209,8192\nrd a,116,209,9216\nad 204,204,196\n"
"ad 205,205,197\nv_mov_b32 v200, 0\nex 188,112\nex 189,113\nex 190,114\nad 201,188,189\n"
"cv 152,188,189\nex 191,115\nad 201,201,190\nex 192,116\nad 201,201,191\nwl 5\n"
"mm v,0,a,96,v,128\nrd a,120,209,12288\ncv 153,190,191\nwl 5\nmm v,0,a,100,v,132\nrd a,124,209,13312\n"
"ex 193,117\nmm v,32,a,96,v,136\nrd a,128,209,16384\nrd a,96,209,2048\nmm v,32,a,100,v,140\nrd a,132,209,17408\n"
"ad 201,201,192\nwl 8\nmm v,4,a,104,v,128\nrd a,136,209,20480\nex 194,118\nwl 8\n"
"mm v,4,a,108,v,132\nrd a,140,209,21504\nad 201,201,193\nmm v,36,a,104,v,136\nrd a,144,209,24576\nrd a,104,209,6144\n"
"mm v,36,a,108,v,140\nrd a,148,209,25600\ncv 154,192,193\nwl 11\nmm v,8,a,112,v,128\nrd a,152,209,28672\n"
"ex 195,119\nwl 11\nmm v,8,a,116,v,132\nrd a,156,209,29696\nad 201,201,194\nmm v,40,a,112,v,136\n"
"rd a,112,209,10240\ns_nop 0\nmm v,40,a,116,v,140\nad 201,201,195\ncv 155,194,195\nwl 12\n"
"mm v,12,a,120,v,128\nad 238,200,201\nv_cmp_nge_f32 vcc, s59, v238\nwl 11\nmm v,12,a,124,v,132\nmm v,44,a,120,v,136\n"
"rd a,120,209,14336\nmm v,44,a,124,v,140\nwl 11\nmm v,16,a,128,v,128\nwl 9\nmm v,16,a,132,v,132\n"
"mm v,48,a,128,v,136\nrd a,128,209,18432\nmm v,48,a,132,v,140\nwl 9\nmm v,20,a,136,v,128\nwl 8\n"
"mm v,20,a,140,v,132\nmm v,52,a,136,v,136\nrd a,136,209,22528\nmm v,52,a,140,v,140\nwl 8\nmm v,24,a,144,v,128\n"
"wl 6\nmm v,24,a,148,v,132\nmm v,56,a,144,v,136\nrd a,144,209,26624\nmm v,56,a,148,v,140\nwl 6\n"
"mm v,28,a,152,v,128\nwl 5\nmm v,28,a,156,v,132\nmm v,60,a,152,v,136\nrd a,152,209,30720\nmm v,60,a,156,v,140\n"
"s_cbranch_vccnz RS_49\nRR_49:\nad 205,205,201\nmm v,32,a,96,v,152\nv_mov_b32 v234, v204\nv_mov_b32 v236, v205\n"
"v_mov_b32 v235, v234\nv_mov_b32 v237, v236\ns_nop 0\nv_permlane32_swap_b32 v234, v235\nv_permlane32_swap_b32 v236, v237\nmm v,36,a,104,v,152\n"
"ad 234,234,235\nad 236,236,237\nv_mov_b32 v235, v234\nv_mov_b32 v237, v236\ns_nop 0\nv_permlane16_swap_b32 v234, v235\n"
"v_permlane16_swap_b32 v236, v237\nwl 5\nmm v,40,a,112,v,152\nad 234,234,235\nad 236,236,237\nv_rcp_f32 v234, v234\n"
"v_rcp_f32 v236, v236\ns_nop 0\npm 0,234\nwl 4\nmm v,44,a,120,v,152\npm 2,234\n"
"pm 4,234\npm 6,234\ncv 0,0,1\ncv 1,2,3\ncv 2,4,5\ncv 3,6,7\n"
"wl 3\nmm v,48,a,128,v,152\nbuffer_store_dwordx4 v[0:3], v230, s[48:51], s61 offen offset:0 sc1\npm 8,234\npm 10,234\npm 12,234\n"
"pm 14,234\ncv 8,8,9\ncv 9,10,11\nwl 2\nmm v,52,a,136,v,152\ncv 10,12,13\n"
"cv 11,14,15\nbuffer_store_dwordx4 v[8:11], v230, s[48:51], s61 offen offset:1024 sc1\npm 16,234\npm 18,234\npm 20,234\npm 22,234\n"
"wl 1\nmm v,56,a,144,v,152\ncv 16,16,17\ncv 17,18,19\ncv 18,20,21\ncv 19,22,23\n"
"buffer_store_dwordx4 v[16:19], v230, s[48:51], s61 offen offset:2048 sc1\npm 24,234\npm 26,234\npm 28,234\nwl 0\nmm v,60,a,152,v,152\n"
"pm 30,234\ncv 24,24,25\ncv 25,26,27\ncv 26,28,29\ncv 27,30,31\nbuffer_store_dwordx4 v[24:27], v230, s[48:51], s61 offen offset:3072 sc1\n"
"sa s64,s61,s60\ns_nop 3\npm 32,236\npm 34,236\npm 36,236\npm 38,236\n"
"cv 32,32,33\ncv 33,34,35\ncv 34,36,37\ncv 35,38,39\nbuffer_store_dwordx4 v[32:35], v230, s[48:51], s64 offen offset:0 sc1\npm 40,236\n"
"pm 42,236\npm 44,236\npm 46,236\ncv 40,40,41\ncv 41,42,43\ncv 42,44,45\n"
"cv 43,46,47\nbuffer_store_dwordx4 v[40:43], v230, s[48:51], s64 offen offset:1024 sc1\npm 48,236\npm 50,236\npm 52,236\npm 54,236\n"
"cv 48,48,49\ncv 49,50,51\ncv 50,52,53\ncv 51,54,55\nbuffer_store_dwordx4 v[48:51], v230, s[48:51], s64 offen offset:2048 sc1\npm 56,236\n"
"pm 58,236\npm 60,236\npm 62,236\ncv 56,56,57\ncv 57,58,59\ncv 58,60,61\n"
"cv 59,62,63\nbuffer_store_dwordx4 v[56:59], v230, s[48:51], s64 offen offset:3072 sc1\ns_branch FINAL\nRP_ISSUE:\ns_mul_i32 s64, s52, 8192\nsa m0,s64,131072\n"
"s_mov_b32 s87, 0x20000\ns_mov_b32 s84, s68\ns_and_b32 s85, s69, 0xffff\ns_mov_b32 s86, s70\nv_mad_u32_u24 v240, v246, s71, v247\ns_lshl_b32 s71, s71, 3\n"
"s_mov_b32 s64, 0\ndl 240,84,64\nsa m0,m0,1024\nsa s64,s64,s71\ndl 240,84,64\nsa m0,m0,1024\n"
"sa s64,s64,s71\ndl 240,84,64\nsa m0,m0,1024\nsa s64,s64,s71\ndl 240,84,64\nsa m0,m0,1024\n"
"sa s64,s64,s71\ns_mov_b32 s84, s76\ns_and_b32 s85, s77, 0xffff\ns_mov_b32 s86, s78\nv_mad_u32_u24 v240, v246, s79, v247\ns_lshl_b32 s79, s79, 3\n"
"s_mov_b32 s64, 0\ndl 240,84,64\nsa m0,m0,1024\nsa s64,s64,s79\ndl 240,84,64\nsa m0,m0,1024\n"
"sa s64,s64,s79\ndl 240,84,64\nsa m0,m0,1024\nsa s64,s64,s79\ndl 240,84,64\nsa m0,m0,1024\n"
"sa s64,s64,s79\ns_branch RP_BACK\nTK_S:\ns_cmp_eq_u32 s92, 0\ns_cbranch_scc1 TKC_S\nbuffer_load_dwordx2 v[242:243], v244, s[96:99], s95 offen sc1\n"
"s_branch TKR_S\nTKC_S:\nv_cmp_ne_u64 vcc, s[46:47], v[242:243]\ns_nop 0\ns_and_b64 vcc, vcc, s[38:39]\ns_cbranch_scc1 SPIN_BS\n"
"SPUN_BS:\ns_branch TKR_S\nTK_DS:\ns_cmp_eq_u32 s92, 0\ns_cbranch_scc1 TKC_DS\nbuffer_load_dwordx2 v[242:243], v244, s[96:99], s95 offen sc1\n"
"s_branch TKR_DS\nTKC_DS:\nv_cmp_ne_u64 vcc, s[46:47], v[242:243]\ns_nop 0\ns_and_b64 vcc, vcc, s[38:39]\ns_cbranch_scc1 SPIN_BDS\n"
"SPUN_BDS:\ns_branch TKR_DS\nTK_N:\ns_cmp_eq_u32 s92, 0\ns_cbranch_scc1 TKC_N\nbuffer_load_dwordx2 v[242:243], v244, s[96:99], s95 offen sc1\n"
"s_branch TKR_N\nTKC_N:\nv_cmp_ne_u64 vcc, s[46:47], v[242:243]\ns_nop 0\ns_and_b64 vcc, vcc, s[38:39]\ns_cbranch_scc1 SPIN_BN\n"
"SPUN_BN:\ns_branch TKR_N\nRS_40:\ns_mov_b32 s65, 40\ns_branch RARE_0_1\nRS_42:\n"
"s_mov_b32 s65, 42\ns_branch RARE_0_1\nRS_44:\ns_mov_b32 s65, 44\ns_branch RARE_0_1\nRS_46:\n"
"s_mov_b32 s65, 46\ns_branch RARE_0_1\nRS_48:\ns_mov_b32 s65, 48\ns_branch RARE_0_1\nRARE_0_1:\n"
"s_cselect_b32 s66, 1, 0\nv_max3_f32 v234, v64, v65, v66\nv_max3_f32 v234, v234, v67, v68\nv_max3_f32 v234, v234, v69, v70\nv_max3_f32 v234, v234, v71, v72\nv_max3_f32 v234, v234, v73, v74\n"
"v_max3_f32 v234, v234, v75, v76\nv_max3_f32 v234, v234, v77, v78\nv_max3_f32 v234, v234, v79, 0\nv_max3_f32 v236, v80, v81, v82\nv_max3_f32 v236, v236, v83, v84\nv_max3_f32 v236, v236, v85, v86\n"
"v_max3_f32 v236, v236, v87, v88\nv_max3_f32 v236, v236, v89, v90\nv_max3_f32 v236, v236, v91, v92\nv_max3_f32 v236, v236, v93, v94\nv_max3_f32 v236, v236, v95, 0\nv_mov_b32 v235, v234\n"
"v_mov_b32 v237, v236\ns_nop 0\nv_permlane32_swap_b32 v234, v235\nv_permlane32_swap_b32 v236, v237\nv_max_f32 v234, v234, v235\nv_max_f32 v236, v236, v237\n"
"v_mov_b32 v235, v234\nv_mov_b32 v237, v236\ns_nop 0\nv_permlane16_swap_b32 v234, v235\nv_permlane16_swap_b32 v236, v237\nv_max_f32 v234, v234, v235\n"
"v_max_f32 v236, v236, v237\nv_exp_f32 v238, -v234\nv_exp_f32 v239, -v236\nsb 64,64,234\nsb 65,65,234\nsb 66,66,234\n"
"sb 67,67,234\nsb 68,68,234\nsb 69,69,234\nsb 70,70,234\nsb 71,71,234\nsb 72,72,234\n"
"sb 73,73,234\nsb 74,74,234\nsb 75,75,234\nsb 76,76,234\nsb 77,77,234\nsb 78,78,234\n"
"sb 79,79,234\nsb 96,96,234\nsb 97,97,234\nsb 98,98,234\nsb 99,99,234\nsb 100,100,234\n"
"sb 101,101,234\nsb 102,102,234\nsb 103,103,234\nsb 104,104,234\nsb 105,105,234\nsb 106,106,234\n"
"sb 107,107,234\nsb 108,108,234\nsb 109,109,234\nsb 110,110,234\nsb 111,111,234\nsb 160,160,234\n"
"sb 161,161,234\nsb 162,162,234\nsb 163,163,234\nsb 176,176,234\nsb 177,177,234\nsb 178,178,234\n"
"sb 179,179,234\nml 204,204,238\nml 0,0,238\nml 1,1,238\nml 2,2,238\nml 3,3,238\n"
"ml 4,4,238\nml 5,5,238\nml 6,6,238\nml 7,7,238\nml 8,8,238\nml 9,9,238\n"
"ml 10,10,238\nml 11,11,238\nml 12,12,238\nml 13,13,238\nml 14,14,238\nml 15,15,238\n"
"ml 16,16,238\nml 17,17,238\nml 18,18,238\nml 19,19,238\nml 20,20,238\nml 21,21,238\n"
"ml 22,22,238\nml 23,23,238\nml 24,24,238\nml 25,25,238\nml 26,26,238\nml 27,27,238\n"
"ml 28,28,238\nml 29,29,238\nml 30,30,238\nml 31,31,238\nsb 80,80,236\nsb 81,81,236\n"
"sb 82,82,236\nsb 83,83,236\nsb 84,84,236\nsb 85,85,236\nsb 86,86,236\nsb 87,87,236\n"
"sb 88,88,236\nsb 89,89,236\nsb 90,90,236\nsb 91,91,236\nsb 92,92,236\nsb 93,93,236\n"
"sb 94,94,236\nsb 95,95,236\nsb 112,112,236\nsb 113,113,236\nsb 114,114,236\nsb 115,115,236\n"
"sb 116,116,236\nsb 117,117,236\nsb 118,118,236\nsb 119,119,236\nsb 120,120,236\nsb 121,121,236\n"
"sb 122,122,236\nsb 123,123,236\nsb 124,124,236\nsb 125,125,236\nsb 126,126,236\nsb 127,127,236\n"
"sb 164,164,236\nsb 165,165,236\nsb 166,166,236\nsb 167,167,236\nsb 180,180,236\nsb 181,181,236\n"
"sb 182,182,236\nsb 183,183,236\nml 205,205,239\nml 32,32,239\nml 33,33,239\nml 34,34,239\n"
"ml 35,35,239\nml 36,36,239\nml 37,37,239\nml 38,38,239\nml 39,39,239\nml 40,40,239\n"
"ml 41,41,239\nml 42,42,239\nml 43,43,239\nml 44,44,239\nml 45,45,239\nml 46,46,239\n"
"ml 47,47,239\nml 48,48,239\nml 49,49,239\nml 50,50,239\nml 51,51,239\nml 52,52,239\n"
"ml 53,53,239\nml 54,54,239\nml 55,55,239\nml 56,56,239\nml 57,57,239\nml 58,58,239\n"
"ml 59,59,239\nml 60,60,239\nml 61,61,239\nml 62,62,239\nml 63,63,239\nex 188,64\n"
"ex 189,65\nex 190,66\nad 196,188,189\ncv 128,188,189\nex 191,67\nad 196,196,190\n"
"ex 192,68\nad 196,196,191\ncv 129,190,191\nex 193,69\nad 196,196,192\nex 194,70\n"
"ad 196,196,193\ncv 130,192,193\nex 195,71\nad 196,196,194\nex 188,80\nad 196,196,195\n"
"cv 131,194,195\nex 189,81\nex 190,82\nad 197,188,189\ncv 136,188,189\nex 191,83\n"
"ad 197,197,190\nex 192,84\nad 197,197,191\ncv 137,190,191\nex 193,85\nad 197,197,192\n"
"ex 194,86\nad 197,197,193\ncv 138,192,193\nex 195,87\nad 197,197,194\nex 188,72\n"
"ad 197,197,195\ncv 139,194,195\nex 189,73\nad 196,196,188\nex 190,74\nad 196,196,189\n"
"cv 132,188,189\nex 191,75\nad 196,196,190\nex 192,76\nad 196,196,191\ncv 133,190,191\n"
"ex 193,77\nad 196,196,192\nex 194,78\nad 196,196,193\ncv 134,192,193\nex 195,79\n"
"ad 196,196,194\nex 188,88\nad 196,196,195\ncv 135,194,195\nex 189,89\nad 197,197,188\n"
"ex 190,90\nad 197,197,189\ncv 140,188,189\nex 191,91\nad 197,197,190\nex 192,92\n"
"ad 197,197,191\ncv 141,190,191\nex 193,93\nad 197,197,192\nex 194,94\nad 197,197,193\n"
"cv 142,192,193\nex 195,95\nad 197,197,194\ns_nop 0\nad 197,197,195\ncv 143,194,195\n"
"s_nop 1\ns_cmp_eq_u32 s65, 40\ns_cbranch_scc1 RX_40\ns_cmp_eq_u32 s65, 42\ns_cbranch_scc1 RX_42\ns_cmp_eq_u32 s65, 44\n"
"s_cbranch_scc1 RX_44\ns_cmp_eq_u32 s65, 46\ns_cbranch_scc1 RX_46\ns_cmp_eq_u32 s65, 48\ns_cbranch_scc1 RX_48\nRX_40:\n"
"s_cmp_eq_u32 s66, 1\ns_branch RR_40\nRX_42:\ns_cmp_eq_u32 s66, 1\ns_branch RR_42\nRX_44:\n"
"s_cmp_eq_u32 s66, 1\ns_branch RR_44\nRX_46:\ns_cmp_eq_u32 s66, 1\ns_branch RR_46\nRX_48:\n"
"s_cmp_eq_u32 s66, 1\ns_branch RR_48\nRS_41:\ns_mov_b32 s65, 41\ns_branch RARE_1_1\nRS_43:\n"
"s_mov_b32 s65, 43\ns_branch RARE_1_1\nRS_45:\ns_mov_b32 s65, 45\ns_branch RARE_1_1\nRS_47:\n"
"s_mov_b32 s65, 47\ns_branch RARE_1_1\nRS_49:\ns_mov_b32 s65, 49\ns_branch RARE_1_1\nRARE_1_1:\n"
"s_cselect_b32 s66, 1, 0\nv_max3_f32 v234, v96, v97, v98\nv_max3_f32 v234, v234, v99, v100\nv_max3_f32 v234, v234, v101, v102\nv_max3_f32 v234, v234, v103, v104\nv_max3_f32 v234, v234, v105, v106\n"
"v_max3_f32 v234, v234, v107, v108\nv_max3_f32 v234, v234, v109, v110\nv_max3_f32 v234, v234, v111, 0\nv_max3_f32 v236, v112, v113, v114\nv_max3_f32 v236, v236, v115, v116\nv_max3_f32 v236, v236, v117, v118\n"
"v_max3_f32 v236, v236, v119, v120\nv_max3_f32 v236, v236, v121, v122\nv_max3_f32 v236, v236, v123, v124\nv_max3_f32 v236, v236, v125, v126\nv_max3_f32 v236, v236, v127, 0\nv_mov_b32 v235, v234\n"
"v_mov_b32 v237, v236\ns_nop 0\nv_permlane32_swap_b32 v234, v235\nv_permlane32_swap_b32 v236, v237\nv_max_f32 v234, v234, v235\nv_max_f32 v236, v236, v237\n"
"v_mov_b32 v235, v234\nv_mov_b32 v237, v236\ns_nop 0\nv_permlane16_swap_b32 v234, v235\nv_permlane16_swap_b32 v236, v237\nv_max_f32 v234, v234, v235\n"
"v_max_f32 v236, v236, v237\nv_exp_f32 v238, -v234\nv_exp_f32 v239, -v236\nsb 96,96,234\nsb 97,97,234\nsb 98,98,234\n"
"sb 99,99,234\nsb 100,100,234\nsb 101,101,234\nsb 102,102,234\nsb 103,103,234\nsb 104,104,234\n"
"sb 105,105,234\nsb 106,106,234\nsb 107,107,234\nsb 108,108,234\nsb 109,109,234\nsb 110,110,234\n"
"sb 111,111,234\nsb 64,64,234\nsb 65,65,234\nsb 66,66,234\nsb 67,67,234\nsb 68,68,234\n"
"sb 69,69,234\nsb 70,70,234\nsb 71,71,234\nsb 72,72,234\nsb 73,73,234\nsb 74,74,234\n"
"sb 75,75,234\nsb 76,76,234\nsb 77,77,234\nsb 78,78,234\nsb 79,79,234\nsb 160,160,234\n"
"sb 161,161,234\nsb 162,162,234\nsb 163,163,234\nsb 176,176,234\nsb 177,177,234\nsb 178,178,234\n"
"sb 179,179,234\nml 204,204,238\nml 0,0,238\nml 1,1,238\nml 2,2,238\nml 3,3,238\n"
"ml 4,4,238\nml 5,5,238\nml 6,6,238\nml 7,7,238\nml 8,8,238\nml 9,9,238\n"
"ml 10,10,238\nml 11,11,238\nml 12,12,238\nml 13,13,238\nml 14,14,238\nml 15,15,238\n"
"ml 16,16,238\nml 17,17,238\nml 18,18,238\nml 19,19,238\nml 20,20,238\nml 21,21,238\n"
"ml 22,22,238\nml 23,23,238\nml 24,24,238\nml 25,25,238\nml 26,26,238\nml 27,27,238\n"
"ml 28,28,238\nml 29,29,238\nml 30,30,238\nml 31,31,238\nsb 112,112,236\nsb 113,113,236\n"
"sb 114,114,236\nsb 115,115,236\nsb 116,116,236\nsb 117,117,236\nsb 118,118,236\nsb 119,119,236\n"
"sb 120,120,236\nsb 121,121,236\nsb 122,122,236\nsb 123,123,236\nsb 124,124,236\nsb 125,125,236\n"
"sb 126,126,236\nsb 127,127,236\nsb 80,80,236\nsb 81,81,236\nsb 82,82,236\nsb 83,83,236\n"
"sb 84,84,236\nsb 85,85,236\nsb 86,86,236\nsb 87,87,236\nsb 88,88,236\nsb 89,89,236\n"
"sb 90,90,236\nsb 91,91,236\nsb 92,92,236\nsb 93,93,236\nsb 94,94,236\nsb 95,95,236\n"
"sb 164,164,236\nsb 165,165,236\nsb 166,166,236\nsb 167,167,236\nsb 180,180,236\nsb 181,181,236\n"
"sb 182,182,236\nsb 183,183,236\nml 205,205,239\nml 32,32,239\nml 33,33,239\nml 34,34,239\n"
"ml 35,35,239\nml 36,36,239\nml 37,37,239\nml 38,38,239\nml 39,39,239\nml 40,40,239\n"
"ml 41,41,239\nml 42,42,239\nml 43,43,239\nml 44,44,239\nml 45,45,239\nml 46,46,239\n"
"ml 47,47,239\nml 48,48,239\nml 49,49,239\nml 50,50,239\nml 51,51,239\nml 52,52,239\n"
"ml 53,53,239\nml 54,54,239\nml 55,55,239\nml 56,56,239\nml 57,57,239\nml 58,58,239\n"
"ml 59,59,239\nml 60,60,239\nml 61,61,239\nml 62,62,239\nml 63,63,239\nex 188,96\n"
"ex 189,97\nex 190,98\nad 200,188,189\ncv 144,188,189\nex 191,99\nad 200,200,190\n"
"ex 192,100\nad 200,200,191\ncv 145,190,191\nex 193,101\nad 200,200,192\nex 194,102\n"
"ad 200,200,193\ncv 146,192,193\nex 195,103\nad 200,200,194\nex 188,112\nad 200,200,195\n"
"cv 147,194,195\nex 189,113\nex 190,114\nad 201,188,189\ncv 152,188,189\nex 191,115\n"
"ad 201,201,190\nex 192,116\nad 201,201,191\ncv 153,190,191\nex 193,117\nad 201,201,192\n"
"ex 194,118\nad 201,201,193\ncv 154,192,193\nex 195,119\nad 201,201,194\nex 188,104\n"
"ad 201,201,195\ncv 155,194,195\nex 189,105\nad 200,200,188\nex 190,106\nad 200,200,189\n"
"cv 148,188,189\nex 191,107\nad 200,200,190\nex 192,108\nad 200,200,191\ncv 149,190,191\n"
"ex 193,109\nad 200,200,192\nex 194,110\nad 200,200,193\ncv 150,192,193\nex 195,111\n"
"ad 200,200,194\nex 188,120\nad 200,200,195\ncv 151,194,195\nex 189,121\nad 201,201,188\n"
"ex 190,122\nad 201,201,189\ncv 156,188,189\nex 191,123\nad 201,201,190\nex 192,124\n"
"ad 201,201,191\ncv 157,190,191\nex 193,125\nad 201,201,192\nex 194,126\nad 201,201,193\n"
"cv 158,192,193\nex 195,127\nad 201,201,194\ns_nop 0\nad 201,201,195\ncv 159,194,195\n"
"s_nop 1\ns_cmp_eq_u32 s65, 41\ns_cbranch_scc1 RX_41\ns_cmp_eq_u32 s65, 43\ns_cbranch_scc1 RX_43\ns_cmp_eq_u32 s65, 45\n"
"s_cbranch_scc1 RX_45\ns_cmp_eq_u32 s65, 47\ns_cbranch_scc1 RX_47\ns_cmp_eq_u32 s65, 49\ns_cbranch_scc1 RX_49\nRX_41:\n"
"s_cmp_eq_u32 s66, 1\ns_branch RR_41\nRX_43:\ns_cmp_eq_u32 s66, 1\ns_branch RR_43\nRX_45:\n"
"s_cmp_eq_u32 s66, 1\ns_branch RR_45\nRX_47:\ns_cmp_eq_u32 s66, 1\ns_branch RR_47\nRX_49:\n"
"s_cmp_eq_u32 s66, 1\ns_branch RR_49\nRS_0:\ns_mov_b32 s65, 0\ns_branch RARES_0_0_0\nRS_24:\n"
"s_mov_b32 s65, 24\ns_branch RARES_0_0_0\nRS_28:\ns_mov_b32 s65, 28\ns_branch RARES_0_0_0\nRS_32:\n"
"s_mov_b32 s65, 32\ns_branch RARES_0_0_0\nRS_36:\ns_mov_b32 s65, 36\ns_branch RARES_0_0_0\nRARES_0_0_0:\n"
"s_cselect_b32 s66, 1, 0\nv_max3_f32 v234, v64, v65, v66\nv_max3_f32 v234, v234, v67, v68\nv_max3_f32 v234, v234, v69, v70\nv_max3_f32 v234, v234, v71, 0\nv_max3_f32 v235, v72, v73, v74\n"
"v_max3_f32 v235, v235, v75, v76\nv_max3_f32 v235, v235, v77, v78\nv_max3_f32 v235, v235, v79, 0\nv_max3_f32 v236, v80, v81, v82\nv_max3_f32 v236, v236, v83, v84\nv_max3_f32 v236, v236, v85, v86\n"
"v_max3_f32 v236, v236, v87, 0\nv_max3_f32 v237, v88, v89, v90\nv_max3_f32 v237, v237, v91, v92\nv_max3_f32 v237, v237, v93, v94\nv_max3_f32 v237, v237, v95, 0\nv_mov_b32 v188, v234\n"
"v_mov_b32 v189, v235\ns_nop 0\nv_permlane32_swap_b32 v234, v188\nv_permlane32_swap_b32 v235, v189\nv_max_f32 v234, v234, v188\nv_max_f32 v235, v235, v189\n"
"v_mov_b32 v188, v234\nv_mov_b32 v189, v235\ns_nop 0\nv_permlane16_swap_b32 v234, v188\nv_permlane16_swap_b32 v235, v189\nv_max_f32 v234, v234, v188\n"
"v_max_f32 v235, v235, v189\nv_mov_b32 v190, v236\nv_mov_b32 v191, v237\ns_nop 0\nv_permlane32_swap_b32 v236, v190\nv_permlane32_swap_b32 v237, v191\n"
"v_max_f32 v236, v236, v190\nv_max_f32 v237, v237, v191\nv_mov_b32 v190, v236\nv_mov_b32 v191, v237\ns_nop 0\nv_permlane16_swap_b32 v236, v190\n"
"v_permlane16_swap_b32 v237, v191\nv_max_f32 v236, v236, v190\nv_max_f32 v237, v237, v191\nv_ceil_f32 v234, v234\nv_ceil_f32 v235, v235\nv_ceil_f32 v236, v236\n"
"v_ceil_f32 v237, v237\nv_exp_f32 v192, -v234\nv_exp_f32 v193, -v235\nv_exp_f32 v194, -v236\nv_exp_f32 v195, -v237\nsb 64,64,234\n"
"sb 65,65,234\nsb 66,66,234\nsb 67,67,234\nsb 68,68,234\nsb 69,69,234\nsb 70,70,234\n"
"sb 71,71,234\nsb 160,160,234\nsb 161,161,234\nsb 162,162,234\nsb 163,163,234\nsb 96,96,234\n"
"sb 97,97,234\nsb 98,98,234\nsb 99,99,234\nsb 100,100,234\nsb 101,101,234\nsb 102,102,234\n"
"sb 103,103,234\nsb 72,72,235\nsb 73,73,235\nsb 74,74,235\nsb 75,75,235\nsb 76,76,235\n"
"sb 77,77,235\nsb 78,78,235\nsb 79,79,235\nsb 164,164,235\nsb 165,165,235\nsb 166,166,235\n"
"sb 167,167,235\nsb 104,104,235\nsb 105,105,235\nsb 106,106,235\nsb 107,107,235\nsb 108,108,235\n"
"sb 109,109,235\nsb 110,110,235\nsb 111,111,235\nsb 80,80,236\nsb 81,81,236\nsb 82,82,236\n"
"sb 83,83,236\nsb 84,84,236\nsb 85,85,236\nsb 86,86,236\nsb 87,87,236\nsb 168,168,236\n"
"sb 169,169,236\nsb 170,170,236\nsb 171,171,236\nsb 112,112,236\nsb 113,113,236\nsb 114,114,236\n"
"sb 115,115,236\nsb 116,116,236\nsb 117,117,236\nsb 118,118,236\nsb 119,119,236\nsb 88,88,237\n"
"sb 89,89,237\nsb 90,90,237\nsb 91,91,237\nsb 92,92,237\nsb 93,93,237\nsb 94,94,237\n"
"sb 95,95,237\nsb 172,172,237\nsb 173,173,237\nsb 174,174,237\nsb 175,175,237\nsb 120,120,237\n"
"sb 121,121,237\nsb 122,122,237\nsb 123,123,237\nsb 124,124,237\nsb 125,125,237\nsb 126,126,237\n"
"sb 127,127,237\ns_cmp_eq_u32 s65, 0\ns_cbranch_scc1 RARES_0_0_0_r0\ns_cmp_eq_u32 s65, 28\ns_cbranch_scc1 RARES_0_0_0_r0\ns_cmp_eq_u32 s65, 32\n"
"s_cbranch_scc1 RARES_0_0_0_r0\ns_cmp_eq_u32 s65, 36\ns_cbranch_scc1 RARES_0_0_0_r0\ns_cmp_eq_u32 s65, 24\ns_cbranch_scc1 RARES_0_0_0_r1\nRARES_0_0_0_r0:\n"
"ex 188,64\nex 189,65\nex 190,66\nad 196,188,189\ncv 128,188,189\nex 191,67\n"
"ad 196,196,190\nex 192,68\nad 196,196,191\ncv 129,190,191\nex 193,69\nad 196,196,192\n"
"ex 194,70\nad 196,196,193\ncv 130,192,193\nex 195,71\nad 196,196,194\nex 188,72\n"
"ad 196,196,195\ncv 131,194,195\nex 189,73\nex 190,74\nad 197,188,189\ncv 132,188,189\n"
"ex 191,75\nad 197,197,190\nex 192,76\nad 197,197,191\ncv 133,190,191\nex 193,77\n"
"ad 197,197,192\nex 194,78\nad 197,197,193\ncv 134,192,193\nex 195,79\nad 197,197,194\n"
"ex 188,80\nad 197,197,195\ncv 135,194,195\nex 189,81\nex 190,82\nad 198,188,189\n"
"cv 136,188,189\nex 191,83\nad 198,198,190\nex 192,84\nad 198,198,191\ncv 137,190,191\n"
"ex 193,85\nad 198,198,192\nex 194,86\nad 198,198,193\ncv 138,192,193\nex 195,87\n"
"ad 198,198,194\nex 188,88\nad 198,198,195\ncv 139,194,195\nex 189,89\nex 190,90\n"
"ad 199,188,189\ncv 140,188,189\nex 191,91\nad 199,199,190\nex 192,92\nad 199,199,191\n"
"cv 141,190,191\nex 193,93\nad 199,199,192\nex 194,94\nad 199,199,193\ncv 142,192,193\n"
"ex 195,95\nad 199,199,194\ns_nop 0\nad 199,199,195\ncv 143,194,195\ns_nop 1\n"
"s_cmp_eq_u32 s65, 0\ns_cbranch_scc1 RX_0\ns_cmp_eq_u32 s65, 28\ns_cbranch_scc1 RX_28\ns_cmp_eq_u32 s65, 32\ns_cbranch_scc1 RX_32\n"
"s_cmp_eq_u32 s65, 36\ns_cbranch_scc1 RX_36\nRARES_0_0_0_r1:\nv_mov_b32 v138, 0\nv_mov_b32 v139, 0\nex 188,64\n"
"ex 189,65\nex 190,66\nad 196,188,189\ncv 128,188,189\nex 191,67\nad 196,196,190\n"
"ex 192,68\nad 196,196,191\ncv 129,190,191\nex 193,69\nad 196,196,192\nex 194,70\n"
"ad 196,196,193\ncv 130,192,193\nex 195,71\nad 196,196,194\nex 188,72\nad 196,196,195\n"
"cv 131,194,195\nex 189,73\nex 190,74\nad 197,188,189\ncv 132,188,189\nex 191,75\n"
"ad 197,197,190\nex 192,76\nad 197,197,191\ncv 133,190,191\nex 193,77\nad 197,197,192\n"
"ex 194,78\nad 197,197,193\ncv 134,192,193\nex 195,79\nad 197,197,194\nex 188,80\n"
"ad 197,197,195\ncv 135,194,195\nex 189,81\nex 190,82\nad 198,188,189\ncv 136,188,189\n"
"ex 191,83\nad 198,198,190\nex 192,88\nad 198,198,191\ncv 137,190,191\nex 193,89\n"
"ex 194,90\nad 199,192,193\ncv 140,192,193\nex 195,91\nad 199,199,194\nex 188,92\n"
"ad 199,199,195\ncv 141,194,195\nex 189,93\nad 199,199,188\nex 190,94\nad 199,199,189\n"
"cv 142,188,189\nex 191,95\nad 199,199,190\ns_nop 0\nad 199,199,191\ncv 143,190,191\n"
"s_nop 1\ns_cmp_eq_u32 s65, 24\ns_cbranch_scc1 RX_24\nRX_0:\ns_cmp_eq_u32 s66, 1\ns_branch RR_0\n"
"RX_24:\ns_cmp_eq_u32 s66, 1\ns_branch RR_24\nRX_28:\ns_cmp_eq_u32 s66, 1\ns_branch RR_28\n"
"RX_32:\ns_cmp_eq_u32 s66, 1\ns_branch RR_32\nRX_36:\ns_cmp_eq_u32 s66, 1\ns_branch RR_36\n"
"RS_1:\ns_mov_b32 s65, 1\ns_branch RARES_1_1_0\nRS_3:\ns_mov_b32 s65, 3\ns_branch RARES_1_1_0\n"
"RS_5:\ns_mov_b32 s65, 5\ns_branch RARES_1_1_0\nRS_7:\ns_mov_b32 s65, 7\ns_branch RARES_1_1_0\n"
"RS_9:\ns_mov_b32 s65, 9\ns_branch RARES_1_1_0\nRS_13:\ns_mov_b32 s65, 13\ns_branch RARES_1_1_0\n"
"RS_17:\ns_mov_b32 s65, 17\ns_branch RARES_1_1_0\nRS_21:\ns_mov_b32 s65, 21\ns_branch RARES_1_1_0\n"
"RS_25:\ns_mov_b32 s65, 25\ns_branch RARES_1_1_0\nRS_29:\ns_mov_b32 s65, 29\ns_branch RARES_1_1_0\n"
"RS_33:\ns_mov_b32 s65, 33\ns_branch RARES_1_1_0\nRS_37:\ns_mov_b32 s65, 37\ns_branch RARES_1_1_0\n"
"RARES_1_1_0:\ns_cselect_b32 s66, 1, 0\nv_max3_f32 v234, v96, v97, v98\nv_max3_f32 v234, v234, v99, v100\nv_max3_f32 v234, v234, v101, v102\nv_max3_f32 v234, v234, v103, 0\n"
"v_max3_f32 v235, v104, v105, v106\nv_max3_f32 v235, v235, v107, v108\nv_max3_f32 v235, v235, v109, v110\nv_max3_f32 v235, v235, v111, 0\nv_max3_f32 v236, v112, v113, v114\nv_max3_f32 v236, v236, v115, v116\n"
"v_max3_f32 v236, v236, v117, v118\nv_max3_f32 v236, v236, v119, 0\nv_max3_f32 v237, v120, v121, v122\nv_max3_f32 v237, v237, v123, v124\nv_max3_f32 v237, v237, v125, v126\nv_max3_f32 v237, v237, v127, 0\n"
"v_mov_b32 v188, v234\nv_mov_b32 v189, v235\ns_nop 0\nv_permlane32_swap_b32 v234, v188\nv_permlane32_swap_b32 v235, v189\nv_max_f32 v234, v234, v188\n"
"v_max_f32 v235, v235, v189\nv_mov_b32 v188, v234\nv_mov_b32 v189, v235\ns_nop 0\nv_permlane16_swap_b32 v234, v188\nv_permlane16_swap_b32 v235, v189\n"
"v_max_f32 v234, v234, v188\nv_max_f32 v235, v235, v189\nv_mov_b32 v190, v236\nv_mov_b32 v191, v237\ns_nop 0\nv_permlane32_swap_b32 v236, v190\n"
"v_permlane32_swap_b32 v237, v191\nv_max_f32 v236, v236, v190\nv_max_f32 v237, v237, v191\nv_mov_b32 v190, v236\nv_mov_b32 v191, v237\ns_nop 0\n"
"v_permlane16_swap_b32 v236, v190\nv_permlane16_swap_b32 v237, v191\nv_max_f32 v236, v236, v190\nv_max_f32 v237, v237, v191\nv_ceil_f32 v234, v234\nv_ceil_f32 v235, v235\n"
"v_ceil_f32 v236, v236\nv_ceil_f32 v237, v237\nv_exp_f32 v192, -v234\nv_exp_f32 v193, -v235\nv_exp_f32 v194, -v236\nv_exp_f32 v195, -v237\n"
"sb 96,96,234\nsb 97,97,234\nsb 98,98,234\nsb 99,99,234\nsb 100,100,234\nsb 101,101,234\n"
"sb 102,102,234\nsb 103,103,234\nsb 160,160,234\nsb 161,161,234\nsb 162,162,234\nsb 163,163,234\n"
"sb 64,64,234\nsb 65,65,234\nsb 66,66,234\nsb 67,67,234\nsb 68,68,234\nsb 69,69,234\n"
"sb 70,70,234\nsb 71,71,234\nsb 104,104,235\nsb 105,105,235\nsb 106,106,235\nsb 107,107,235\n"
"sb 108,108,235\nsb 109,109,235\nsb 110,110,235\nsb 111,111,235\nsb 164,164,235\nsb 165,165,235\n"
"sb 166,166,235\nsb 167,167,235\nsb 72,72,235\nsb 73,73,235\nsb 74,74,235\nsb 75,75,235\n"
"sb 76,76,235\nsb 77,77,235\nsb 78,78,235\nsb 79,79,235\nsb 112,112,236\nsb 113,113,236\n"
"sb 114,114,236\nsb 115,115,236\nsb 116,116,236\nsb 117,117,236\nsb 118,118,236\nsb 119,119,236\n"
"sb 168,168,236\nsb 169,169,236\nsb 170,170,236\nsb 171,171,236\nsb 80,80,236\nsb 81,81,236\n"
"sb 82,82,236\nsb 83,83,236\nsb 84,84,236\nsb 85,85,236\nsb 86,86,236\nsb 87,87,236\n"
"sb 120,120,237\nsb 121,121,237\nsb 122,122,237\nsb 123,123,237\nsb 124,124,237\nsb 125,125,237\n"
"sb 126,126,237\nsb 127,127,237\nsb 172,172,237\nsb 173,173,237\nsb 174,174,237\nsb 175,175,237\n"
"sb 88,88,237\nsb 89,89,237\nsb 90,90,237\nsb 91,91,237\nsb 92,92,237\nsb 93,93,237\n"
"sb 94,94,237\nsb 95,95,237\nml 204,204,192\nml 0,0,192\nml 1,1,192\nml 2,2,192\n"
"ml 3,3,192\nml 4,4,192\nml 5,5,192\nml 6,6,192\nml 7,7,192\nml 8,8,192\n"
"ml 9,9,192\nml 10,10,192\nml 11,11,192\nml 12,12,192\nml 13,13,192\nml 14,14,192\n"
"ml 15,15,192\nml 16,16,192\nml 17,17,192\nml 18,18,192\nml 19,19,192\nml 20,20,192\n"
"ml 21,21,192\nml 22,22,192\nml 23,23,192\nml 24,24,192\nml 25,25,192\nml 26,26,192\n"
"ml 27,27,192\nml 28,28,192\nml 29,29,192\nml 30,30,192\nml 31,31,192\nml 205,205,193\n"
"ml 32,32,193\nml 33,33,193\nml 34,34,193\nml 35,35,193\nml 36,36,193\nml 37,37,193\n"
"ml 38,38,193\nml 39,39,193\nml 40,40,193\nml 41,41,193\nml 42,42,193\nml 43,43,193\n"
"ml 44,44,193\nml 45,45,193\nml 46,46,193\nml 47,47,193\nml 48,48,193\nml 49,49,193\n"
"ml 50,50,193\nml 51,51,193\nml 52,52,193\nml 53,53,193\nml 54,54,193\nml 55,55,193\n"
"ml 56,56,193\nml 57,57,193\nml 58,58,193\nml 59,59,193\nml 60,60,193\nml 61,61,193\n"
"ml 62,62,193\nml 63,63,193\nml 206,206,194\nv_accvgpr_read_b32 v188, a192\nml 188,188,194\nv_accvgpr_write_b32 a192, v188\n"
"v_accvgpr_read_b32 v189, a193\nml 189,189,194\nv_accvgpr_write_b32 a193, v189\nv_accvgpr_read_b32 v190, a194\nml 190,190,194\nv_accvgpr_write_b32 a194, v190\n"
"v_accvgpr_read_b32 v191, a195\nml 191,191,194\nv_accvgpr_write_b32 a195, v191\nv_accvgpr_read_b32 v188, a196\nml 188,188,194\nv_accvgpr_write_b32 a196, v188\n"
"v_accvgpr_read_b32 v189, a197\nml 189,189,194\nv_accvgpr_write_b32 a197, v189\nv_accvgpr_read_b32 v190, a198\nml 190,190,194\nv_accvgpr_write_b32 a198, v190\n"
"v_accvgpr_read_b32 v191, a199\nml 191,191,194\nv_accvgpr_write_b32 a199, v191\nv_accvgpr_read_b32 v188, a200\nml 188,188,194\nv_accvgpr_write_b32 a200, v188\n"
"v_accvgpr_read_b32 v189, a201\nml 189,189,194\nv_accvgpr_write_b32 a201, v189\nv_accvgpr_read_b32 v190, a202\nml 190,190,194\nv_accvgpr_write_b32 a202, v190\n"
"v_accvgpr_read_b32 v191, a203\nml 191,191,194\nv_accvgpr_write_b32 a203, v191\nv_accvgpr_read_b32 v188, a204\nml 188,188,194\nv_accvgpr_write_b32 a204, v188\n"
"v_accvgpr_read_b32 v189, a205\nml 189,189,194\nv_accvgpr_write_b32 a205, v189\nv_accvgpr_read_b32 v190, a206\nml 190,190,194\nv_accvgpr_write_b32 a206, v190\n"
"v_accvgpr_read_b32 v191, a207\nml 191,191,194\nv_accvgpr_write_b32 a207, v191\nv_accvgpr_read_b32 v188, a208\nml 188,188,194\nv_accvgpr_write_b32 a208, v188\n"
"v_accvgpr_read_b32 v189, a209\nml 189,189,194\nv_accvgpr_write_b32 a209, v189\nv_accvgpr_read_b32 v190, a210\nml 190,190,194\nv_accvgpr_write_b32 a210, v190\n"
"v_accvgpr_read_b32 v191, a211\nml 191,191,194\nv_accvgpr_write_b32 a211, v191\nv_accvgpr_read_b32 v188, a212\nml 188,188,194\nv_accvgpr_write_b32 a212, v188\n"
"v_accvgpr_read_b32 v189, a213\nml 189,189,194\nv_accvgpr_write_b32 a213, v189\nv_accvgpr_read_b32 v190, a214\nml 190,190,194\nv_accvgpr_write_b32 a214, v190\n"
"v_accvgpr_read_b32 v191, a215\nml 191,191,194\nv_accvgpr_write_b32 a215, v191\nv_accvgpr_read_b32 v188, a216\nml 188,188,194\nv_accvgpr_write_b32 a216, v188\n"
"v_accvgpr_read_b32 v189, a217\nml 189,189,194\nv_accvgpr_write_b32 a217, v189\nv_accvgpr_read_b32 v190, a218\nml 190,190,194\nv_accvgpr_write_b32 a218, v190\n"
"v_accvgpr_read_b32 v191, a219\nml 191,191,194\nv_accvgpr_write_b32 a219, v191\nv_accvgpr_read_b32 v188, a220\nml 188,188,194\nv_accvgpr_write_b32 a220, v188\n"
"v_accvgpr_read_b32 v189, a221\nml 189,189,194\nv_accvgpr_write_b32 a221, v189\nv_accvgpr_read_b32 v190, a222\nml 190,190,194\nv_accvgpr_write_b32 a222, v190\n"
"v_accvgpr_read_b32 v191, a223\nml 191,191,194\nv_accvgpr_write_b32 a223, v191\nml 207,207,195\nv_accvgpr_read_b32 v188, a224\nml 188,188,195\n"
"v_accvgpr_write_b32 a224, v188\nv_accvgpr_read_b32 v189, a225\nml 189,189,195\nv_accvgpr_write_b32 a225, v189\nv_accvgpr_read_b32 v190, a226\nml 190,190,195\n"
"v_accvgpr_write_b32 a226, v190\nv_accvgpr_read_b32 v191, a227\nml 191,191,195\nv_accvgpr_write_b32 a227, v191\nv_accvgpr_read_b32 v188, a228\nml 188,188,195\n"
"v_accvgpr_write_b32 a228, v188\nv_accvgpr_read_b32 v189, a229\nml 189,189,195\nv_accvgpr_write_b32 a229, v189\nv_accvgpr_read_b32 v190, a230\nml 190,190,195\n"
"v_accvgpr_write_b32 a230, v190\nv_accvgpr_read_b32 v191, a231\nml 191,191,195\nv_accvgpr_write_b32 a231, v191\nv_accvgpr_read_b32 v188, a232\nml 188,188,195\n"
"v_accvgpr_write_b32 a232, v188\nv_accvgpr_read_b32 v189, a233\nml 189,189,195\nv_accvgpr_write_b32 a233, v189\nv_accvgpr_read_b32 v190, a234\nml 190,190,195\n"
"v_accvgpr_write_b32 a234, v190\nv_accvgpr_read_b32 v191, a235\nml 191,191,195\nv_accvgpr_write_b32 a235, v191\nv_accvgpr_read_b32 v188, a236\nml 188,188,195\n"
"v_accvgpr_write_b32 a236, v188\nv_accvgpr_read_b32 v189, a237\nml 189,189,195\nv_accvgpr_write_b32 a237, v189\nv_accvgpr_read_b32 v190, a238\nml 190,190,195\n"
"v_accvgpr_write_b32 a238, v190\nv_accvgpr_read_b32 v191, a239\nml 191,191,195\nv_accvgpr_write_b32 a239, v191\nv_accvgpr_read_b32 v188, a240\nml 188,188,195\n"
"v_accvgpr_write_b32 a240, v188\nv_accvgpr_read_b32 v189, a241\nml 189,189,195\nv_accvgpr_write_b32 a241, v189\nv_accvgpr_read_b32 v190, a242\nml 190,190,195\n"
"v_accvgpr_write_b32 a242, v190\nv_accvgpr_read_b32 v191, a243\nml 191,191,195\nv_accvgpr_write_b32 a243, v191\nv_accvgpr_read_b32 v188, a244\nml 188,188,195\n"
"v_accvgpr_write_b32 a244, v188\nv_accvgpr_read_b32 v189, a245\nml 189,189,195\nv_accvgpr_write_b32 a245, v189\nv_accvgpr_read_b32 v190, a246\nml 190,190,195\n"
"v_accvgpr_write_b32 a246, v190\nv_accvgpr_read_b32 v191, a247\nml 191,191,195\nv_accvgpr_write_b32 a247, v191\nv_accvgpr_read_b32 v188, a248\nml 188,188,195\n"
"v_accvgpr_write_b32 a248, v188\nv_accvgpr_read_b32 v189, a249\nml 189,189,195\nv_accvgpr_write_b32 a249, v189\nv_accvgpr_read_b32 v190, a250\nml 190,190,195\n"
"v_accvgpr_write_b32 a250, v190\nv_accvgpr_read_b32 v191, a251\nml 191,191,195\nv_accvgpr_write_b32 a251, v191\nv_accvgpr_read_b32 v188, a252\nml 188,188,195\n"
"v_accvgpr_write_b32 a252, v188\nv_accvgpr_read_b32 v189, a253\nml 189,189,195\nv_accvgpr_write_b32 a253, v189\nv_accvgpr_read_b32 v190, a254\nml 190,190,195\n"
"v_accvgpr_write_b32 a254, v190\nv_accvgpr_read_b32 v191, a255\nml 191,191,195\nv_accvgpr_write_b32 a255, v191\ns_cmp_eq_u32 s65, 1\ns_cbranch_scc1 RARES_1_1_0_r0\n"
"s_cmp_eq_u32 s65, 3\ns_cbranch_scc1 RARES_1_1_0_r0\ns_cmp_eq_u32 s65, 5\ns_cbranch_scc1 RARES_1_1_0_r0\ns_cmp_eq_u32 s65, 7\ns_cbranch_scc1 RARES_1_1_0_r0\n"
"s_cmp_eq_u32 s65, 9\ns_cbranch_scc1 RARES_1_1_0_r1\ns_cmp_eq_u32 s65, 13\ns_cbranch_scc1 RARES_1_1_0_r1\ns_cmp_eq_u32 s65, 25\ns_cbranch_scc1 RARES_1_1_0_r1\n"
"s_cmp_eq_u32 s65, 29\ns_cbranch_scc1 RARES_1_1_0_r1\ns_cmp_eq_u32 s65, 17\ns_cbranch_scc1 RARES_1_1_0_r2\ns_cmp_eq_u32 s65, 33\ns_cbranch_scc1 RARES_1_1_0_r2\n"
"s_cmp_eq_u32 s65, 21\ns_cbranch_scc1 RARES_1_1_0_r3\ns_cmp_eq_u32 s65, 37\ns_cbranch_scc1 RARES_1_1_0_r3\nRARES_1_1_0_r0:\nex 188,96\n"
"ex 189,97\nex 190,98\nad 200,188,189\ncv 144,188,189\nex 191,99\nad 200,200,190\n"
"ex 192,100\nad 200,200,191\ncv 145,190,191\nex 193,101\nad 200,200,192\nex 194,102\n"
"ad 200,200,193\ncv 146,192,193\nex 195,103\nad 200,200,194\nex 188,104\nad 200,200,195\n"
"cv 147,194,195\nex 189,105\nex 190,106\nad 201,188,189\ncv 148,188,189\nex 191,107\n"
"ad 201,201,190\nex 192,108\nad 201,201,191\ncv 149,190,191\nex 193,109\nad 201,201,192\n"
"ex 194,110\nad 201,201,193\ncv 150,192,193\nex 195,111\nad 201,201,194\nex 188,112\n"
"ad 201,201,195\ncv 151,194,195\nex 189,113\nex 190,114\nad 202,188,189\ncv 152,188,189\n"
"ex 191,115\nad 202,202,190\nex 192,116\nad 202,202,191\ncv 153,190,191\nex 193,117\n"
"ad 202,202,192\nex 194,118\nad 202,202,193\ncv 154,192,193\nex 195,119\nad 202,202,194\n"
"ex 188,120\nad 202,202,195\ncv 155,194,195\nex 189,121\nex 190,122\nad 203,188,189\n"
"cv 156,188,189\nex 191,123\nad 203,203,190\nex 192,124\nad 203,203,191\ncv 157,190,191\n"
"ex 193,125\nad 203,203,192\nex 194,126\nad 203,203,193\ncv 158,192,193\nex 195,127\n"
"ad 203,203,194\ns_nop 0\nad 203,203,195\ncv 159,194,195\ns_nop 1\ns_cmp_eq_u32 s65, 1\n"
"s_cbranch_scc1 RX_1\ns_cmp_eq_u32 s65, 3\ns_cbranch_scc1 RX_3\ns_cmp_eq_u32 s65, 5\ns_cbranch_scc1 RX_5\ns_cmp_eq_u32 s65, 7\n"
"s_cbranch_scc1 RX_7\nRARES_1_1_0_r1:\nex 188,96\nex 189,97\nex 190,98\nad 200,188,189\n"
"cv 210,188,189\nex 191,99\nad 200,200,190\nex 192,100\nad 200,200,191\ncv 211,190,191\n"
"ex 193,101\nad 200,200,192\nex 194,102\nad 200,200,193\ncv 212,192,193\nex 195,103\n"
"ad 200,200,194\nex 188,104\nad 200,200,195\ncv 213,194,195\nex 189,105\nex 190,106\n"
"ad 201,188,189\ncv 214,188,189\nex 191,107\nad 201,201,190\nex 192,108\nad 201,201,191\n"
"cv 215,190,191\nex 193,109\nad 201,201,192\nex 194,110\nad 201,201,193\ncv 216,192,193\n"
"ex 195,111\nad 201,201,194\nex 188,120\nad 201,201,195\ncv 217,194,195\nex 189,121\n"
"ex 190,122\nad 203,188,189\ncv 222,188,189\nex 191,123\nad 203,203,190\nex 192,124\n"
"ad 203,203,191\ncv 223,190,191\nex 193,125\nad 203,203,192\nex 194,126\nad 203,203,193\n"
"cv 224,192,193\nex 195,127\nad 203,203,194\ns_nop 0\nad 203,203,195\ncv 225,194,195\n"
"s_nop 1\ns_cmp_eq_u32 s65, 9\ns_cbranch_scc1 RX_9\ns_cmp_eq_u32 s65, 13\ns_cbranch_scc1 RX_13\ns_cmp_eq_u32 s65, 25\n"
"s_cbranch_scc1 RX_25\ns_cmp_eq_u32 s65, 29\ns_cbranch_scc1 RX_29\nRARES_1_1_0_r2:\nv_mov_b32 v220, 0\nv_mov_b32 v221, 0\n"
"ex 188,96\nex 189,97\nex 190,98\nad 200,188,189\ncv 210,188,189\nex 191,99\n"
"ad 200,200,190\nex 192,100\nad 200,200,191\ncv 211,190,191\nex 193,101\nad 200,200,192\n"
"ex 194,102\nad 200,200,193\ncv 212,192,193\nex 195,103\nad 200,200,194\nex 188,104\n"
"ad 200,200,195\ncv 213,194,195\nex 189,105\nex 190,106\nad 201,188,189\ncv 214,188,189\n"
"ex 191,107\nad 201,201,190\nex 192,108\nad 201,201,191\ncv 215,190,191\nex 193,109\n"
"ad 201,201,192\nex 194,110\nad 201,201,193\ncv 216,192,193\nex 195,111\nad 201,201,194\n"
"ex 188,112\nad 201,201,195\ncv 217,194,195\nex 189,113\nex 190,114\nad 202,188,189\n"
"cv 218,188,189\nex 191,115\nad 202,202,190\nex 192,120\nad 202,202,191\ncv 219,190,191\n"
"ex 193,121\nex 194,122\nad 203,192,193\ncv 222,192,193\nex 195,123\nad 203,203,194\n"
"ex 188,124\nad 203,203,195\ncv 223,194,195\nex 189,125\nad 203,203,188\nex 190,126\n"
"ad 203,203,189\ncv 224,188,189\nex 191,127\nad 203,203,190\ns_nop 0\nad 203,203,191\n"
"cv 225,190,191\ns_nop 1\ns_cmp_eq_u32 s65, 17\ns_cbranch_scc1 RX_17\ns_cmp_eq_u32 s65, 33\ns_cbranch_scc1 RX_33\n"
"RARES_1_1_0_r3:\nex 188,96\nex 189,97\nex 190,98\nad 200,188,189\ncv 210,188,189\n"
"ex 191,99\nad 200,200,190\nex 192,100\nad 200,200,191\ncv 211,190,191\nex 193,101\n"
"ad 200,200,192\nex 194,102\nad 200,200,193\ncv 212,192,193\nex 195,103\nad 200,200,194\n"
"ex 188,104\nad 200,200,195\ncv 213,194,195\nex 189,105\nex 190,106\nad 201,188,189\n"
"cv 214,188,189\nex 191,107\nad 201,201,190\nex 192,108\nad 201,201,191\ncv 215,190,191\n"
"ex 193,109\nad 201,201,192\nex 194,110\nad 201,201,193\ncv 216,192,193\nex 195,111\n"
"ad 201,201,194\nex 188,112\nad 201,201,195\ncv 217,194,195\nex 189,113\nex 190,114\n"
"ad 202,188,189\ncv 218,188,189\nex 191,115\nad 202,202,190\nex 192,116\nad 202,202,191\n"
"cv 219,190,191\nex 193,117\nad 202,202,192\nex 194,118\nad 202,202,193\ncv 220,192,193\n"
"ex 195,119\nad 202,202,194\nex 188,120\nad 202,202,195\ncv 221,194,195\nex 189,121\n"
"ex 190,122\nad 203,188,189\ncv 222,188,189\nex 191,123\nad 203,203,190\nex 192,124\n"
"ad 203,203,191\ncv 223,190,191\nex 193,125\nad 203,203,192\nex 194,126\nad 203,203,193\n"
"cv 224,192,193\nex 195,127\nad 203,203,194\ns_nop 0\nad 203,203,195\ncv 225,194,195\n"
"s_nop 1\ns_cmp_eq_u32 s65, 21\ns_cbranch_scc1 RX_21\ns_cmp_eq_u32 s65, 37\ns_cbranch_scc1 RX_37\nRX_1:\n"
"s_cmp_eq_u32 s66, 1\ns_branch RR_1\nRX_3:\ns_cmp_eq_u32 s66, 1\ns_branch RR_3\nRX_5:\n"
"s_cmp_eq_u32 s66, 1\ns_branch RR_5\nRX_7:\ns_cmp_eq_u32 s66, 1\ns_branch RR_7\nRX_9:\n"
"s_cmp_eq_u32 s66, 1\ns_branch RR_9\nRX_13:\ns_cmp_eq_u32 s66, 1\ns_branch RR_13\nRX_17:\n"
"s_cmp_eq_u32 s66, 1\ns_branch RR_17\nRX_21:\ns_cmp_eq_u32 s66, 1\ns_branch RR_21\nRX_25:\n"
"s_cmp_eq_u32 s66, 1\ns_branch RR_25\nRX_29:\ns_cmp_eq_u32 s66, 1\ns_branch RR_29\nRX_33:\n"
"s_cmp_eq_u32 s66, 1\ns_branch RR_33\nRX_37:\ns_cmp_eq_u32 s66, 1\ns_branch RR_37\nRS_2:\n"
"s_mov_b32 s65, 2\ns_branch RARES_0_1_0\nRS_4:\ns_mov_b32 s65, 4\ns_branch RARES_0_1_0\nRS_6:\n"
"s_mov_b32 s65, 6\ns_branch RARES_0_1_0\nRS_8:\ns_mov_b32 s65, 8\ns_branch RARES_0_1_0\nRS_10:\n"
"s_mov_b32 s65, 10\ns_branch RARES_0_1_0\nRS_12:\ns_mov_b32 s65, 12\ns_branch RARES_0_1_0\nRS_14:\n"
"s_mov_b32 s65, 14\ns_branch RARES_0_1_0\nRS_16:\ns_mov_b32 s65, 16\ns_branch RARES_0_1_0\nRS_18:\n"
"s_mov_b32 s65, 18\ns_branch RARES_0_1_0\nRS_20:\ns_mov_b32 s65, 20\ns_branch RARES_0_1_0\nRS_22:\n"
"s_mov_b32 s65, 22\ns_branch RARES_0_1_0\nRS_26:\ns_mov_b32 s65, 26\ns_branch RARES_0_1_0\nRS_30:\n"
"s_mov_b32 s65, 30\ns_branch RARES_0_1_0\nRS_34:\ns_mov_b32 s65, 34\ns_branch RARES_0_1_0\nRS_38:\n"
"s_mov_b32 s65, 38\ns_branch RARES_0_1_0\nRARES_0_1_0:\ns_cselect_b32 s66, 1, 0\nv_max3_f32 v234, v64, v65, v66\nv_max3_f32 v234, v234, v67, v68\n"
"v_max3_f32 v234, v234, v69, v70\nv_max3_f32 v234, v234, v71, 0\nv_max3_f32 v235, v72, v73, v74\nv_max3_f32 v235, v235, v75, v76\nv_max3_f32 v235, v235, v77, v78\nv_max3_f32 v235, v235, v79, 0\n"
"v_max3_f32 v236, v80, v81, v82\nv_max3_f32 v236, v236, v83, v84\nv_max3_f32 v236, v236, v85, v86\nv_max3_f32 v236, v236, v87, 0\nv_max3_f32 v237, v88, v89, v90\nv_max3_f32 v237, v237, v91, v92\n"
"v_max3_f32 v237, v237, v93, v94\nv_max3_f32 v237, v237, v95, 0\nv_mov_b32 v188, v234\nv_mov_b32 v189, v235\ns_nop 0\nv_permlane32_swap_b32 v234, v188\n"
"v_permlane32_swap_b32 v235, v189\nv_max_f32 v234, v234, v188\nv_max_f32 v235, v235, v189\nv_mov_b32 v188, v234\nv_mov_b32 v189, v235\ns_nop 0\n"
"v_permlane16_swap_b32 v234, v188\nv_permlane16_swap_b32 v235, v189\nv_max_f32 v234, v234, v188\nv_max_f32 v235, v235, v189\nv_mov_b32 v190, v236\nv_mov_b32 v191, v237\n"
"s_nop 0\nv_permlane32_swap_b32 v236, v190\nv_permlane32_swap_b32 v237, v191\nv_max_f32 v236, v236, v190\nv_max_f32 v237, v237, v191\nv_mov_b32 v190, v236\n"
"v_mov_b32 v191, v237\ns_nop 0\nv_permlane16_swap_b32 v236, v190\nv_permlane16_swap_b32 v237, v191\nv_max_f32 v236, v236, v190\nv_max_f32 v237, v237, v191\n"
"v_ceil_f32 v234, v234\nv_ceil_f32 v235, v235\nv_ceil_f32 v236, v236\nv_ceil_f32 v237, v237\nv_exp_f32 v192, -v234\nv_exp_f32 v193, -v235\n"
"v_exp_f32 v194, -v236\nv_exp_f32 v195, -v237\nsb 64,64,234\nsb 65,65,234\nsb 66,66,234\nsb 67,67,234\n"
"sb 68,68,234\nsb 69,69,234\nsb 70,70,234\nsb 71,71,234\nsb 160,160,234\nsb 161,161,234\n"
"sb 162,162,234\nsb 163,163,234\nsb 96,96,234\nsb 97,97,234\nsb 98,98,234\nsb 99,99,234\n"
"sb 100,100,234\nsb 101,101,234\nsb 102,102,234\nsb 103,103,234\nsb 72,72,235\nsb 73,73,235\n"
"sb 74,74,235\nsb 75,75,235\nsb 76,76,235\nsb 77,77,235\nsb 78,78,235\nsb 79,79,235\n"
"sb 164,164,235\nsb 165,165,235\nsb 166,166,235\nsb 167,167,235\nsb 104,104,235\nsb 105,105,235\n"
"sb 106,106,235\nsb 107,107,235\nsb 108,108,235\nsb 109,109,235\nsb 110,110,235\nsb 111,111,235\n"
"sb 80,80,236\nsb 81,81,236\nsb 82,82,236\nsb 83,83,236\nsb 84,84,236\nsb 85,85,236\n"
"sb 86,86,236\nsb 87,87,236\nsb 168,168,236\nsb 169,169,236\nsb 170,170,236\nsb 171,171,236\n"
"sb 112,112,236\nsb 113,113,236\nsb 114,114,236\nsb 115,115,236\nsb 116,116,236\nsb 117,117,236\n"
"sb 118,118,236\nsb 119,119,236\nsb 88,88,237\nsb 89,89,237\nsb 90,90,237\nsb 91,91,237\n"
"sb 92,92,237\nsb 93,93,237\nsb 94,94,237\nsb 95,95,237\nsb 172,172,237\nsb 173,173,237\n"
"sb 174,174,237\nsb 175,175,237\nsb 120,120,237\nsb 121,121,237\nsb 122,122,237\nsb 123,123,237\n"
"sb 124,124,237\nsb 125,125,237\nsb 126,126,237\nsb 127,127,237\nml 204,204,192\nml 0,0,192\n"
"ml 1,1,192\nml 2,2,192\nml 3,3,192\nml 4,4,192\nml 5,5,192\nml 6,6,192\n"
"ml 7,7,192\nml 8,8,192\nml 9,9,192\nml 10,10,192\nml 11,11,192\nml 12,12,192\n"
"ml 13,13,192\nml 14,14,192\nml 15,15,192\nml 16,16,192\nml 17,17,192\nml 18,18,192\n"
"ml 19,19,192\nml 20,20,192\nml 21,21,192\nml 22,22,192\nml 23,23,192\nml 24,24,192\n"
"ml 25,25,192\nml 26,26,192\nml 27,27,192\nml 28,28,192\nml 29,29,192\nml 30,30,192\n"
"ml 31,31,192\nml 205,205,193\nml 32,32,193\nml 33,33,193\nml 34,34,193\nml 35,35,193\n"
"ml 36,36,193\nml 37,37,193\nml 38,38,193\nml 39,39,193\nml 40,40,193\nml 41,41,193\n"
"ml 42,42,193\nml 43,43,193\nml 44,44,193\nml 45,45,193\nml 46,46,193\nml 47,47,193\n"
"ml 48,48,193\nml 49,49,193\nml 50,50,193\nml 51,51,193\nml 52,52,193\nml 53,53,193\n"
"ml 54,54,193\nml 55,55,193\nml 56,56,193\nml 57,57,193\nml 58,58,193\nml 59,59,193\n"
"ml 60,60,193\nml 61,61,193\nml 62,62,193\nml 63,63,193\nml 206,206,194\nv_accvgpr_read_b32 v188, a192\n"
"ml 188,188,194\nv_accvgpr_write_b32 a192, v188\nv_accvgpr_read_b32 v189, a193\nml 189,189,194\nv_accvgpr_write_b32 a193, v189\nv_accvgpr_read_b32 v190, a194\n"
"ml 190,190,194\nv_accvgpr_write_b32 a194, v190\nv_accvgpr_read_b32 v191, a195\nml 191,191,194\nv_accvgpr_write_b32 a195, v191\nv_accvgpr_read_b32 v188, a196\n"
"ml 188,188,194\nv_accvgpr_write_b32 a196, v188\nv_accvgpr_read_b32 v189, a197\nml 189,189,194\nv_accvgpr_write_b32 a197, v189\nv_accvgpr_read_b32 v190, a198\n"
"ml 190,190,194\nv_accvgpr_write_b32 a198, v190\nv_accvgpr_read_b32 v191, a199\nml 191,191,194\nv_accvgpr_write_b32 a199, v191\nv_accvgpr_read_b32 v188, a200\n"
"ml 188,188,194\nv_accvgpr_write_b32 a200, v188\nv_accvgpr_read_b32 v189, a201\nml 189,189,194\nv_accvgpr_write_b32 a201, v189\nv_accvgpr_read_b32 v190, a202\n"
"ml 190,190,194\nv_accvgpr_write_b32 a202, v190\nv_accvgpr_read_b32 v191, a203\nml 191,191,194\nv_accvgpr_write_b32 a203, v191\nv_accvgpr_read_b32 v188, a204\n"
"ml 188,188,194\nv_accvgpr_write_b32 a204, v188\nv_accvgpr_read_b32 v189, a205\nml 189,189,194\nv_accvgpr_write_b32 a205, v189\nv_accvgpr_read_b32 v190, a206\n"
"ml 190,190,194\nv_accvgpr_write_b32 a206, v190\nv_accvgpr_read_b32 v191, a207\nml 191,191,194\nv_accvgpr_write_b32 a207, v191\nv_accvgpr_read_b32 v188, a208\n"
"ml 188,188,194\nv_accvgpr_write_b32 a208, v188\nv_accvgpr_read_b32 v189, a209\nml 189,189,194\nv_accvgpr_write_b32 a209, v189\nv_accvgpr_read_b32 v190, a210\n"
"ml 190,190,194\nv_accvgpr_write_b32 a210, v190\nv_accvgpr_read_b32 v191, a211\nml 191,191,194\nv_accvgpr_write_b32 a211, v191\nv_accvgpr_read_b32 v188, a212\n"
"ml 188,188,194\nv_accvgpr_write_b32 a212, v188\nv_accvgpr_read_b32 v189, a213\nml 189,189,194\nv_accvgpr_write_b32 a213, v189\nv_accvgpr_read_b32 v190, a214\n"
"ml 190,190,194\nv_accvgpr_write_b32 a214, v190\nv_accvgpr_read_b32 v191, a215\nml 191,191,194\nv_accvgpr_write_b32 a215, v191\nv_accvgpr_read_b32 v188, a216\n"
"ml 188,188,194\nv_accvgpr_write_b32 a216, v188\nv_accvgpr_read_b32 v189, a217\nml 189,189,194\nv_accvgpr_write_b32 a217, v189\nv_accvgpr_read_b32 v190, a218\n"
"ml 190,190,194\nv_accvgpr_write_b32 a218, v190\nv_accvgpr_read_b32 v191, a219\nml 191,191,194\nv_accvgpr_write_b32 a219, v191\nv_accvgpr_read_b32 v188, a220\n"
"ml 188,188,194\nv_accvgpr_write_b32 a220, v188\nv_accvgpr_read_b32 v189, a221\nml 189,189,194\nv_accvgpr_write_b32 a221, v189\nv_accvgpr_read_b32 v190, a222\n"
"ml 190,190,194\nv_accvgpr_write_b32 a222, v190\nv_accvgpr_read_b32 v191, a223\nml 191,191,194\nv_accvgpr_write_b32 a223, v191\nml 207,207,195\n"
"v_accvgpr_read_b32 v188, a224\nml 188,188,195\nv_accvgpr_write_b32 a224, v188\nv_accvgpr_read_b32 v189, a225\nml 189,189,195\nv_accvgpr_write_b32 a225, v189\n"
"v_accvgpr_read_b32 v190, a226\nml 190,190,195\nv_accvgpr_write_b32 a226, v190\nv_accvgpr_read_b32 v191, a227\nml 191,191,195\nv_accvgpr_write_b32 a227, v191\n"
"v_accvgpr_read_b32 v188, a228\nml 188,188,195\nv_accvgpr_write_b32 a228, v188\nv_accvgpr_read_b32 v189, a229\nml 189,189,195\nv_accvgpr_write_b32 a229, v189\n"
"v_accvgpr_read_b32 v190, a230\nml 190,190,195\nv_accvgpr_write_b32 a230, v190\nv_accvgpr_read_b32 v191, a231\nml 191,191,195\nv_accvgpr_write_b32 a231, v191\n"
"v_accvgpr_read_b32 v188, a232\nml 188,188,195\nv_accvgpr_write_b32 a232, v188\nv_accvgpr_read_b32 v189, a233\nml 189,189,195\nv_accvgpr_write_b32 a233, v189\n"
"v_accvgpr_read_b32 v190, a234\nml 190,190,195\nv_accvgpr_write_b32 a234, v190\nv_accvgpr_read_b32 v191, a235\nml 191,191,195\nv_accvgpr_write_b32 a235, v191\n"
"v_accvgpr_read_b32 v188, a236\nml 188,188,195\nv_accvgpr_write_b32 a236, v188\nv_accvgpr_read_b32 v189, a237\nml 189,189,195\nv_accvgpr_write_b32 a237, v189\n"
"v_accvgpr_read_b32 v190, a238\nml 190,190,195\nv_accvgpr_write_b32 a238, v190\nv_accvgpr_read_b32 v191, a239\nml 191,191,195\nv_accvgpr_write_b32 a239, v191\n"
"v_accvgpr_read_b32 v188, a240\nml 188,188,195\nv_accvgpr_write_b32 a240, v188\nv_accvgpr_read_b32 v189, a241\nml 189,189,195\nv_accvgpr_write_b32 a241, v189\n"
"v_accvgpr_read_b32 v190, a242\nml 190,190,195\nv_accvgpr_write_b32 a242, v190\nv_accvgpr_read_b32 v191, a243\nml 191,191,195\nv_accvgpr_write_b32 a243, v191\n"
"v_accvgpr_read_b32 v188, a244\nml 188,188,195\nv_accvgpr_write_b32 a244, v188\nv_accvgpr_read_b32 v189, a245\nml 189,189,195\nv_accvgpr_write_b32 a245, v189\n"
"v_accvgpr_read_b32 v190, a246\nml 190,190,195\nv_accvgpr_write_b32 a246, v190\nv_accvgpr_read_b32 v191, a247\nml 191,191,195\nv_accvgpr_write_b32 a247, v191\n"
"v_accvgpr_read_b32 v188, a248\nml 188,188,195\nv_accvgpr_write_b32 a248, v188\nv_accvgpr_read_b32 v189, a249\nml 189,189,195\nv_accvgpr_write_b32 a249, v189\n"
"v_accvgpr_read_b32 v190, a250\nml 190,190,195\nv_accvgpr_write_b32 a250, v190\nv_accvgpr_read_b32 v191, a251\nml 191,191,195\nv_accvgpr_write_b32 a251, v191\n"
"v_accvgpr_read_b32 v188, a252\nml 188,188,195\nv_accvgpr_write_b32 a252, v188\nv_accvgpr_read_b32 v189, a253\nml 189,189,195\nv_accvgpr_write_b32 a253, v189\n"
"v_accvgpr_read_b32 v190, a254\nml 190,190,195\nv_accvgpr_write_b32 a254, v190\nv_accvgpr_read_b32 v191, a255\nml 191,191,195\nv_accvgpr_write_b32 a255, v191\n"
"s_cmp_eq_u32 s65, 2\ns_cbranch_scc1 RARES_0_1_0_r0\ns_cmp_eq_u32 s65, 4\ns_cbranch_scc1 RARES_0_1_0_r0\ns_cmp_eq_u32 s65, 6\ns_cbranch_scc1 RARES_0_1_0_r0\n"
"s_cmp_eq_u32 s65, 12\ns_cbranch_scc1 RARES_0_1_0_r0\ns_cmp_eq_u32 s65, 16\ns_cbranch_scc1 RARES_0_1_0_r0\ns_cmp_eq_u32 s65, 20\ns_cbranch_scc1 RARES_0_1_0_r0\n"
"s_cmp_eq_u32 s65, 8\ns_cbranch_scc1 RARES_0_1_0_r1\ns_cmp_eq_u32 s65, 10\ns_cbranch_scc1 RARES_0_1_0_r2\ns_cmp_eq_u32 s65, 14\ns_cbranch_scc1 RARES_0_1_0_r2\n"
"s_cmp_eq_u32 s65, 18\ns_cbranch_scc1 RARES_0_1_0_r2\ns_cmp_eq_u32 s65, 26\ns_cbranch_scc1 RARES_0_1_0_r2\ns_cmp_eq_u32 s65, 30\ns_cbranch_scc1 RARES_0_1_0_r2\n"
"s_cmp_eq_u32 s65, 34\ns_cbranch_scc1 RARES_0_1_0_r2\ns_cmp_eq_u32 s65, 22\ns_cbranch_scc1 RARES_0_1_0_r3\ns_cmp_eq_u32 s65, 38\ns_cbranch_scc1 RARES_0_1_0_r3\n"
"RARES_0_1_0_r0:\nex 188,64\nex 189,65\nex 190,66\nad 196,188,189\ncv 128,188,189\n"
"ex 191,67\nad 196,196,190\nex 192,68\nad 196,196,191\ncv 129,190,191\nex 193,69\n"
"ad 196,196,192\nex 194,70\nad 196,196,193\ncv 130,192,193\nex 195,71\nad 196,196,194\n"
"ex 188,72\nad 196,196,195\ncv 131,194,195\nex 189,73\nex 190,74\nad 197,188,189\n"
"cv 132,188,189\nex 191,75\nad 197,197,190\nex 192,76\nad 197,197,191\ncv 133,190,191\n"
"ex 193,77\nad 197,197,192\nex 194,78\nad 197,197,193\ncv 134,192,193\nex 195,79\n"
"ad 197,197,194\nex 188,80\nad 197,197,195\ncv 135,194,195\nex 189,81\nex 190,82\n"
"ad 198,188,189\ncv 136,188,189\nex 191,83\nad 198,198,190\nex 192,84\nad 198,198,191\n"
"cv 137,190,191\nex 193,85\nad 198,198,192\nex 194,86\nad 198,198,193\ncv 138,192,193\n"
"ex 195,87\nad 198,198,194\nex 188,88\nad 198,198,195\ncv 139,194,195\nex 189,89\n"
"ex 190,90\nad 199,188,189\ncv 140,188,189\nex 191,91\nad 199,199,190\nex 192,92\n"
"ad 199,199,191\ncv 141,190,191\nex 193,93\nad 199,199,192\nex 194,94\nad 199,199,193\n"
"cv 142,192,193\nex 195,95\nad 199,199,194\ns_nop 0\nad 199,199,195\ncv 143,194,195\n"
"s_nop 1\ns_cmp_eq_u32 s65, 2\ns_cbranch_scc1 RX_2\ns_cmp_eq_u32 s65, 4\ns_cbranch_scc1 RX_4\ns_cmp_eq_u32 s65, 6\n"
"s_cbranch_scc1 RX_6\ns_cmp_eq_u32 s65, 12\ns_cbranch_scc1 RX_12\ns_cmp_eq_u32 s65, 16\ns_cbranch_scc1 RX_16\ns_cmp_eq_u32 s65, 20\n"
"s_cbranch_scc1 RX_20\nRARES_0_1_0_r1:\nv_mov_b32 v138, 0\nv_mov_b32 v139, 0\nex 188,64\nex 189,65\n"
"ex 190,66\nad 196,188,189\ncv 128,188,189\nex 191,67\nad 196,196,190\nex 192,68\n"
"ad 196,196,191\ncv 129,190,191\nex 193,69\nad 196,196,192\nex 194,70\nad 196,196,193\n"
"cv 130,192,193\nex 195,71\nad 196,196,194\nex 188,72\nad 196,196,195\ncv 131,194,195\n"
"ex 189,73\nex 190,74\nad 197,188,189\ncv 132,188,189\nex 191,75\nad 197,197,190\n"
"ex 192,76\nad 197,197,191\ncv 133,190,191\nex 193,77\nad 197,197,192\nex 194,78\n"
"ad 197,197,193\ncv 134,192,193\nex 195,79\nad 197,197,194\nex 188,80\nad 197,197,195\n"
"cv 135,194,195\nex 189,81\nex 190,82\nad 198,188,189\ncv 136,188,189\nex 191,83\n"
"ad 198,198,190\nex 192,88\nad 198,198,191\ncv 137,190,191\nex 193,89\nex 194,90\n"
"ad 199,192,193\ncv 140,192,193\nex 195,91\nad 199,199,194\nex 188,92\nad 199,199,195\n"
"cv 141,194,195\nex 189,93\nad 199,199,188\nex 190,94\nad 199,199,189\ncv 142,188,189\n"
"ex 191,95\nad 199,199,190\ns_nop 0\nad 199,199,191\ncv 143,190,191\ns_nop 1\n"
"s_cmp_eq_u32 s65, 8\ns_cbranch_scc1 RX_8\nRARES_0_1_0_r2:\nex 188,64\nex 189,65\nex 190,66\n"
"ad 196,188,189\ncv 144,188,189\nex 191,67\nad 196,196,190\nex 192,68\nad 196,196,191\n"
"cv 145,190,191\nex 193,69\nad 196,196,192\nex 194,70\nad 196,196,193\ncv 146,192,193\n"
"ex 195,71\nad 196,196,194\nex 188,72\nad 196,196,195\ncv 147,194,195\nex 189,73\n"
"ex 190,74\nad 197,188,189\ncv 152,188,189\nex 191,75\nad 197,197,190\nex 192,76\n"
"ad 197,197,191\ncv 153,190,191\nex 193,77\nad 197,197,192\nex 194,78\nad 197,197,193\n"
"cv 154,192,193\nex 195,79\nad 197,197,194\nex 188,88\nad 197,197,195\ncv 155,194,195\n"
"ex 189,89\nex 190,90\nad 199,188,189\ncv 132,188,189\nex 191,91\nad 199,199,190\n"
"ex 192,92\nad 199,199,191\ncv 133,190,191\nex 193,93\nad 199,199,192\nex 194,94\n"
"ad 199,199,193\ncv 134,192,193\nex 195,95\nad 199,199,194\ns_nop 0\nad 199,199,195\n"
"cv 135,194,195\ns_nop 1\ns_cmp_eq_u32 s65, 10\ns_cbranch_scc1 RX_10\ns_cmp_eq_u32 s65, 14\ns_cbranch_scc1 RX_14\n"
"s_cmp_eq_u32 s65, 18\ns_cbranch_scc1 RX_18\ns_cmp_eq_u32 s65, 26\ns_cbranch_scc1 RX_26\ns_cmp_eq_u32 s65, 30\ns_cbranch_scc1 RX_30\n"
"s_cmp_eq_u32 s65, 34\ns_cbranch_scc1 RX_34\nRARES_0_1_0_r3:\nv_mov_b32 v134, 0\nv_mov_b32 v135, 0\nex 188,64\n"
"ex 189,65\nex 190,66\nad 196,188,189\ncv 144,188,189\nex 191,67\nad 196,196,190\n"
"ex 192,68\nad 196,196,191\ncv 145,190,191\nex 193,69\nad 196,196,192\nex 194,70\n"
"ad 196,196,193\ncv 146,192,193\nex 195,71\nad 196,196,194\nex 188,72\nad 196,196,195\n"
"cv 147,194,195\nex 189,73\nex 190,74\nad 197,188,189\ncv 152,188,189\nex 191,75\n"
"ad 197,197,190\nex 192,76\nad 197,197,191\ncv 153,190,191\nex 193,77\nad 197,197,192\n"
"ex 194,78\nad 197,197,193\ncv 154,192,193\nex 195,79\nad 197,197,194\nex 188,88\n"
"ad 197,197,195\ncv 155,194,195\nex 189,89\nex 190,90\nad 199,188,189\ncv 132,188,189\n"
"ex 191,91\nad 199,199,190\ns_nop 0\nad 199,199,191\ncv 133,190,191\ns_nop 1\n"
"s_cmp_eq_u32 s65, 22\ns_cbranch_scc1 RX_22\ns_cmp_eq_u32 s65, 38\ns_cbranch_scc1 RX_38\nRX_2:\ns_cmp_eq_u32 s66, 1\n"
"s_branch RR_2\nRX_4:\ns_cmp_eq_u32 s66, 1\ns_branch RR_4\nRX_6:\ns_cmp_eq_u32 s66, 1\n"
"s_branch RR_6\nRX_8:\ns_cmp_eq_u32 s66, 1\ns_branch RR_8\nRX_10:\ns_cmp_eq_u32 s66, 1\n"
"s_branch RR_10\nRX_12:\ns_cmp_eq_u32 s66, 1\ns_branch RR_12\nRX_14:\ns_cmp_eq_u32 s66, 1\n"
"s_branch RR_14\nRX_16:\ns_cmp_eq_u32 s66, 1\ns_branch RR_16\nRX_18:\ns_cmp_eq_u32 s66, 1\n"
"s_branch RR_18\nRX_20:\ns_cmp_eq_u32 s66, 1\ns_branch RR_20\nRX_22:\ns_cmp_eq_u32 s66, 1\n"
"s_branch RR_22\nRX_26:\ns_cmp_eq_u32 s66, 1\ns_branch RR_26\nRX_30:\ns_cmp_eq_u32 s66, 1\n"
"s_branch RR_30\nRX_34:\ns_cmp_eq_u32 s66, 1\ns_branch RR_34\nRX_38:\ns_cmp_eq_u32 s66, 1\n"
"s_branch RR_38\nRS_11:\ns_mov_b32 s65, 11\ns_branch RARES_1_1_1\nRS_15:\ns_mov_b32 s65, 15\n"
"s_branch RARES_1_1_1\nRS_19:\ns_mov_b32 s65, 19\ns_branch RARES_1_1_1\nRS_23:\ns_mov_b32 s65, 23\n"
"s_branch RARES_1_1_1\nRS_27:\ns_mov_b32 s65, 27\ns_branch RARES_1_1_1\nRS_31:\ns_mov_b32 s65, 31\n"
"s_branch RARES_1_1_1\nRS_35:\ns_mov_b32 s65, 35\ns_branch RARES_1_1_1\nRS_39:\ns_mov_b32 s65, 39\n"
"s_branch RARES_1_1_1\nRARES_1_1_1:\ns_cselect_b32 s66, 1, 0\nv_max3_f32 v234, v96, v97, v98\nv_max3_f32 v234, v234, v99, v100\nv_max3_f32 v234, v234, v101, v102\n"
"v_max3_f32 v234, v234, v103, 0\nv_max3_f32 v235, v104, v105, v106\nv_max3_f32 v235, v235, v107, v108\nv_max3_f32 v235, v235, v109, v110\nv_max3_f32 v235, v235, v111, 0\nv_max3_f32 v236, v112, v113, v114\n"
"v_max3_f32 v236, v236, v115, v116\nv_max3_f32 v236, v236, v117, v118\nv_max3_f32 v236, v236, v119, 0\nv_max3_f32 v237, v120, v121, v122\nv_max3_f32 v237, v237, v123, v124\nv_max3_f32 v237, v237, v125, v126\n"
"v_max3_f32 v237, v237, v127, 0\nv_mov_b32 v188, v234\nv_mov_b32 v189, v235\ns_nop 0\nv_permlane32_swap_b32 v234, v188\nv_permlane32_swap_b32 v235, v189\n"
"v_max_f32 v234, v234, v188\nv_max_f32 v235, v235, v189\nv_mov_b32 v188, v234\nv_mov_b32 v189, v235\ns_nop 0\nv_permlane16_swap_b32 v234, v188\n"
"v_permlane16_swap_b32 v235, v189\nv_max_f32 v234, v234, v188\nv_max_f32 v235, v235, v189\nv_mov_b32 v190, v236\nv_mov_b32 v191, v237\ns_nop 0\n"
"v_permlane32_swap_b32 v236, v190\nv_permlane32_swap_b32 v237, v191\nv_max_f32 v236, v236, v190\nv_max_f32 v237, v237, v191\nv_mov_b32 v190, v236\nv_mov_b32 v191, v237\n"
"s_nop 0\nv_permlane16_swap_b32 v236, v190\nv_permlane16_swap_b32 v237, v191\nv_max_f32 v236, v236, v190\nv_max_f32 v237, v237, v191\nv_ceil_f32 v234, v234\n"
"v_ceil_f32 v235, v235\nv_ceil_f32 v236, v236\nv_ceil_f32 v237, v237\nv_exp_f32 v192, -v234\nv_exp_f32 v193, -v235\nv_exp_f32 v194, -v236\n"
"v_exp_f32 v195, -v237\nsb 96,96,234\nsb 97,97,234\nsb 98,98,234\nsb 99,99,234\nsb 100,100,234\n"
"sb 101,101,234\nsb 102,102,234\nsb 103,103,234\nsb 160,160,234\nsb 161,161,234\nsb 162,162,234\n"
"sb 163,163,234\nsb 64,64,234\nsb 65,65,234\nsb 66,66,234\nsb 67,67,234\nsb 68,68,234\n"
"sb 69,69,234\nsb 70,70,234\nsb 71,71,234\nsb 72,72,234\nsb 73,73,234\nsb 74,74,234\n"
"sb 75,75,234\nsb 76,76,234\nsb 77,77,234\nsb 78,78,234\nsb 79,79,234\nsb 104,104,235\n"
"sb 105,105,235\nsb 106,106,235\nsb 107,107,235\nsb 108,108,235\nsb 109,109,235\nsb 110,110,235\n"
"sb 111,111,235\nsb 164,164,235\nsb 165,165,235\nsb 166,166,235\nsb 167,167,235\nsb 80,80,235\n"
"sb 81,81,235\nsb 82,82,235\nsb 83,83,235\nsb 84,84,235\nsb 85,85,235\nsb 86,86,235\n"
"sb 87,87,235\nsb 88,88,235\nsb 89,89,235\nsb 90,90,235\nsb 91,91,235\nsb 92,92,235\n"
"sb 93,93,235\nsb 94,94,235\nsb 95,95,235\nsb 112,112,236\nsb 113,113,236\nsb 114,114,236\n"
"sb 115,115,236\nsb 116,116,236\nsb 117,117,236\nsb 118,118,236\nsb 119,119,236\nsb 168,168,236\n"
"sb 169,169,236\nsb 170,170,236\nsb 171,171,236\nsb 120,120,237\nsb 121,121,237\nsb 122,122,237\n"
"sb 123,123,237\nsb 124,124,237\nsb 125,125,237\nsb 126,126,237\nsb 127,127,237\nsb 172,172,237\n"
"sb 173,173,237\nsb 174,174,237\nsb 175,175,237\nml 204,204,192\nml 0,0,192\nml 1,1,192\n"
"ml 2,2,192\nml 3,3,192\nml 4,4,192\nml 5,5,192\nml 6,6,192\nml 7,7,192\n"
"ml 8,8,192\nml 9,9,192\nml 10,10,192\nml 11,11,192\nml 12,12,192\nml 13,13,192\n"
"ml 14,14,192\nml 15,15,192\nml 16,16,192\nml 17,17,192\nml 18,18,192\nml 19,19,192\n"
"ml 20,20,192\nml 21,21,192\nml 22,22,192\nml 23,23,192\nml 24,24,192\nml 25,25,192\n"
"ml 26,26,192\nml 27,27,192\nml 28,28,192\nml 29,29,192\nml 30,30,192\nml 31,31,192\n"
"ml 205,205,193\nml 32,32,193\nml 33,33,193\nml 34,34,193\nml 35,35,193\nml 36,36,193\n"
"ml 37,37,193\nml 38,38,193\nml 39,39,193\nml 40,40,193\nml 41,41,193\nml 42,42,193\n"
"ml 43,43,193\nml 44,44,193\nml 45,45,193\nml 46,46,193\nml 47,47,193\nml 48,48,193\n"
"ml 49,49,193\nml 50,50,193\nml 51,51,193\nml 52,52,193\nml 53,53,193\nml 54,54,193\n"
"ml 55,55,193\nml 56,56,193\nml 57,57,193\nml 58,58,193\nml 59,59,193\nml 60,60,193\n"
"ml 61,61,193\nml 62,62,193\nml 63,63,193\nml 206,206,194\nv_accvgpr_read_b32 v188, a192\nml 188,188,194\n"
"v_accvgpr_write_b32 a192, v188\nv_accvgpr_read_b32 v189, a193\nml 189,189,194\nv_accvgpr_write_b32 a193, v189\nv_accvgpr_read_b32 v190, a194\nml 190,190,194\n"
"v_accvgpr_write_b32 a194, v190\nv_accvgpr_read_b32 v191, a195\nml 191,191,194\nv_accvgpr_write_b32 a195, v191\nv_accvgpr_read_b32 v188, a196\nml 188,188,194\n"
"v_accvgpr_write_b32 a196, v188\nv_accvgpr_read_b32 v189, a197\nml 189,189,194\nv_accvgpr_write_b32 a197, v189\nv_accvgpr_read_b32 v190, a198\nml 190,190,194\n"
"v_accvgpr_write_b32 a198, v190\nv_accvgpr_read_b32 v191, a199\nml 191,191,194\nv_accvgpr_write_b32 a199, v191\nv_accvgpr_read_b32 v188, a200\nml 188,188,194\n"
"v_accvgpr_write_b32 a200, v188\nv_accvgpr_read_b32 v189, a201\nml 189,189,194\nv_accvgpr_write_b32 a201, v189\nv_accvgpr_read_b32 v190, a202\nml 190,190,194\n"
"v_accvgpr_write_b32 a202, v190\nv_accvgpr_read_b32 v191, a203\nml 191,191,194\nv_accvgpr_write_b32 a203, v191\nv_accvgpr_read_b32 v188, a204\nml 188,188,194\n"
"v_accvgpr_write_b32 a204, v188\nv_accvgpr_read_b32 v189, a205\nml 189,189,194\nv_accvgpr_write_b32 a205, v189\nv_accvgpr_read_b32 v190, a206\nml 190,190,194\n"
"v_accvgpr_write_b32 a206, v190\nv_accvgpr_read_b32 v191, a207\nml 191,191,194\nv_accvgpr_write_b32 a207, v191\nv_accvgpr_read_b32 v188, a208\nml 188,188,194\n"
"v_accvgpr_write_b32 a208, v188\nv_accvgpr_read_b32 v189, a209\nml 189,189,194\nv_accvgpr_write_b32 a209, v189\nv_accvgpr_read_b32 v190, a210\nml 190,190,194\n"
"v_accvgpr_write_b32 a210, v190\nv_accvgpr_read_b32 v191, a211\nml 191,191,194\nv_accvgpr_write_b32 a211, v191\nv_accvgpr_read_b32 v188, a212\nml 188,188,194\n"
"v_accvgpr_write_b32 a212, v188\nv_accvgpr_read_b32 v189, a213\nml 189,189,194\nv_accvgpr_write_b32 a213, v189\nv_accvgpr_read_b32 v190, a214\nml 190,190,194\n"
"v_accvgpr_write_b32 a214, v190\nv_accvgpr_read_b32 v191, a215\nml 191,191,194\nv_accvgpr_write_b32 a215, v191\nv_accvgpr_read_b32 v188, a216\nml 188,188,194\n"
"v_accvgpr_write_b32 a216, v188\nv_accvgpr_read_b32 v189, a217\nml 189,189,194\nv_accvgpr_write_b32 a217, v189\nv_accvgpr_read_b32 v190, a218\nml 190,190,194\n"
"v_accvgpr_write_b32 a218, v190\nv_accvgpr_read_b32 v191, a219\nml 191,191,194\nv_accvgpr_write_b32 a219, v191\nv_accvgpr_read_b32 v188, a220\nml 188,188,194\n"
"v_accvgpr_write_b32 a220, v188\nv_accvgpr_read_b32 v189, a221\nml 189,189,194\nv_accvgpr_write_b32 a221, v189\nv_accvgpr_read_b32 v190, a222\nml 190,190,194\n"
"v_accvgpr_write_b32 a222, v190\nv_accvgpr_read_b32 v191, a223\nml 191,191,194\nv_accvgpr_write_b32 a223, v191\nml 207,207,195\nv_accvgpr_read_b32 v188, a224\n"
"ml 188,188,195\nv_accvgpr_write_b32 a224, v188\nv_accvgpr_read_b32 v189, a225\nml 189,189,195\nv_accvgpr_write_b32 a225, v189\nv_accvgpr_read_b32 v190, a226\n"
"ml 190,190,195\nv_accvgpr_write_b32 a226, v190\nv_accvgpr_read_b32 v191, a227\nml 191,191,195\nv_accvgpr_write_b32 a227, v191\nv_accvgpr_read_b32 v188, a228\n"
"ml 188,188,195\nv_accvgpr_write_b32 a228, v188\nv_accvgpr_read_b32 v189, a229\nml 189,189,195\nv_accvgpr_write_b32 a229, v189\nv_accvgpr_read_b32 v190, a230\n"
"ml 190,190,195\nv_accvgpr_write_b32 a230, v190\nv_accvgpr_read_b32 v191, a231\nml 191,191,195\nv_accvgpr_write_b32 a231, v191\nv_accvgpr_read_b32 v188, a232\n"
"ml 188,188,195\nv_accvgpr_write_b32 a232, v188\nv_accvgpr_read_b32 v189, a233\nml 189,189,195\nv_accvgpr_write_b32 a233, v189\nv_accvgpr_read_b32 v190, a234\n"
"ml 190,190,195\nv_accvgpr_write_b32 a234, v190\nv_accvgpr_read_b32 v191, a235\nml 191,191,195\nv_accvgpr_write_b32 a235, v191\nv_accvgpr_read_b32 v188, a236\n"
"ml 188,188,195\nv_accvgpr_write_b32 a236, v188\nv_accvgpr_read_b32 v189, a237\nml 189,189,195\nv_accvgpr_write_b32 a237, v189\nv_accvgpr_read_b32 v190, a238\n"
"ml 190,190,195\nv_accvgpr_write_b32 a238, v190\nv_accvgpr_read_b32 v191, a239\nml 191,191,195\nv_accvgpr_write_b32 a239, v191\nv_accvgpr_read_b32 v188, a240\n"
"ml 188,188,195\nv_accvgpr_write_b32 a240, v188\nv_accvgpr_read_b32 v189, a241\nml 189,189,195\nv_accvgpr_write_b32 a241, v189\nv_accvgpr_read_b32 v190, a242\n"
"ml 190,190,195\nv_accvgpr_write_b32 a242, v190\nv_accvgpr_read_b32 v191, a243\nml 191,191,195\nv_accvgpr_write_b32 a243, v191\nv_accvgpr_read_b32 v188, a244\n"
"ml 188,188,195\nv_accvgpr_write_b32 a244, v188\nv_accvgpr_read_b32 v189, a245\nml 189,189,195\nv_accvgpr_write_b32 a245, v189\nv_accvgpr_read_b32 v190, a246\n"
"ml 190,190,195\nv_accvgpr_write_b32 a246, v190\nv_accvgpr_read_b32 v191, a247\nml 191,191,195\nv_accvgpr_write_b32 a247, v191\nv_accvgpr_read_b32 v188, a248\n"
"ml 188,188,195\nv_accvgpr_write_b32 a248, v188\nv_accvgpr_read_b32 v189, a249\nml 189,189,195\nv_accvgpr_write_b32 a249, v189\nv_accvgpr_read_b32 v190, a250\n"
"ml 190,190,195\nv_accvgpr_write_b32 a250, v190\nv_accvgpr_read_b32 v191, a251\nml 191,191,195\nv_accvgpr_write_b32 a251, v191\nv_accvgpr_read_b32 v188, a252\n"
"ml 188,188,195\nv_accvgpr_write_b32 a252, v188\nv_accvgpr_read_b32 v189, a253\nml 189,189,195\nv_accvgpr_write_b32 a253, v189\nv_accvgpr_read_b32 v190, a254\n"
"ml 190,190,195\nv_accvgpr_write_b32 a254, v190\nv_accvgpr_read_b32 v191, a255\nml 191,191,195\nv_accvgpr_write_b32 a255, v191\nv_lshlrev_b32 v188, 16, v144\n"
"v_and_b32 v189, 0xffff0000, v144\nml 188,188,192\nml 189,189,192\ncv 144,188,189\nv_lshlrev_b32 v188, 16, v145\nv_and_b32 v189, 0xffff0000, v145\n"
"ml 188,188,192\nml 189,189,192\ncv 145,188,189\nv_lshlrev_b32 v188, 16, v146\nv_and_b32 v189, 0xffff0000, v146\nml 188,188,192\n"
"ml 189,189,192\ncv 146,188,189\nv_lshlrev_b32 v188, 16, v147\nv_and_b32 v189, 0xffff0000, v147\nml 188,188,192\nml 189,189,192\n"
"cv 147,188,189\nv_lshlrev_b32 v188, 16, v152\nv_and_b32 v189, 0xffff0000, v152\nml 188,188,193\nml 189,189,193\ncv 152,188,189\n"
"v_lshlrev_b32 v188, 16, v153\nv_and_b32 v189, 0xffff0000, v153\nml 188,188,193\nml 189,189,193\ncv 153,188,189\nv_lshlrev_b32 v188, 16, v154\n"
"v_and_b32 v189, 0xffff0000, v154\nml 188,188,193\nml 189,189,193\ncv 154,188,189\nv_lshlrev_b32 v188, 16, v155\nv_and_b32 v189, 0xffff0000, v155\n"
"ml 188,188,193\nml 189,189,193\ncv 155,188,189\ns_cmp_eq_u32 s65, 11\ns_cbranch_scc1 RARES_1_1_1_r0\ns_cmp_eq_u32 s65, 27\n"
"s_cbranch_scc1 RARES_1_1_1_r0\ns_cmp_eq_u32 s65, 15\ns_cbranch_scc1 RARES_1_1_1_r1\ns_cmp_eq_u32 s65, 31\ns_cbranch_scc1 RARES_1_1_1_r1\ns_cmp_eq_u32 s65, 19\n"
"s_cbranch_scc1 RARES_1_1_1_r2\ns_cmp_eq_u32 s65, 23\ns_cbranch_scc1 RARES_1_1_1_r2\ns_cmp_eq_u32 s65, 35\ns_cbranch_scc1 RARES_1_1_1_r2\ns_cmp_eq_u32 s65, 39\n"
"s_cbranch_scc1 RARES_1_1_1_r2\nRARES_1_1_1_r0:\nex 188,96\nex 189,97\nex 190,98\nad 200,188,189\n"
"cv 148,188,189\nex 191,99\nad 200,200,190\nex 192,100\nad 200,200,191\ncv 149,190,191\n"
"ex 193,101\nad 200,200,192\nex 194,102\nad 200,200,193\ncv 150,192,193\nex 195,103\n"
"ad 200,200,194\nex 188,104\nad 200,200,195\ncv 151,194,195\nex 189,105\nex 190,106\n"
"ad 201,188,189\ncv 156,188,189\nex 191,107\nad 201,201,190\nex 192,108\nad 201,201,191\n"
"cv 157,190,191\nex 193,109\nad 201,201,192\nex 194,110\nad 201,201,193\ncv 158,192,193\n"
"ex 195,111\nad 201,201,194\nex 188,120\nad 201,201,195\ncv 159,194,195\nex 189,121\n"
"ex 190,122\nad 203,188,189\ncv 140,188,189\nex 191,123\nad 203,203,190\nex 192,124\n"
"ad 203,203,191\ncv 141,190,191\nex 193,125\nad 203,203,192\nex 194,126\nad 203,203,193\n"
"cv 142,192,193\nex 195,127\nad 203,203,194\ns_nop 0\nad 203,203,195\ncv 143,194,195\n"
"s_nop 1\ns_cmp_eq_u32 s65, 11\ns_cbranch_scc1 RX_11\ns_cmp_eq_u32 s65, 27\ns_cbranch_scc1 RX_27\nRARES_1_1_1_r1:\n"
"v_mov_b32 v142, 0\nv_mov_b32 v143, 0\nex 188,96\nex 189,97\nex 190,98\nad 200,188,189\n"
"cv 148,188,189\nex 191,99\nad 200,200,190\nex 192,100\nad 200,200,191\ncv 149,190,191\n"
"ex 193,101\nad 200,200,192\nex 194,102\nad 200,200,193\ncv 150,192,193\nex 195,103\n"
"ad 200,200,194\nex 188,104\nad 200,200,195\ncv 151,194,195\nex 189,105\nex 190,106\n"
"ad 201,188,189\ncv 156,188,189\nex 191,107\nad 201,201,190\nex 192,108\nad 201,201,191\n"
"cv 157,190,191\nex 193,109\nad 201,201,192\nex 194,110\nad 201,201,193\ncv 158,192,193\n"
"ex 195,111\nad 201,201,194\nex 188,120\nad 201,201,195\ncv 159,194,195\nex 189,121\n"
"ex 190,122\nad 203,188,189\ncv 140,188,189\nex 191,123\nad 203,203,190\ns_nop 0\n"
"ad 203,203,191\ncv 141,190,191\ns_nop 1\ns_cmp_eq_u32 s65, 15\ns_cbranch_scc1 RX_15\ns_cmp_eq_u32 s65, 31\n"
"s_cbranch_scc1 RX_31\nRARES_1_1_1_r2:\nex 188,96\nex 189,97\nex 190,98\nad 200,188,189\n"
"cv 148,188,189\nex 191,99\nad 200,200,190\nex 192,100\nad 200,200,191\ncv 149,190,191\n"
"ex 193,101\nad 200,200,192\nex 194,102\nad 200,200,193\ncv 150,192,193\nex 195,103\n"
"ad 200,200,194\nex 188,104\nad 200,200,195\ncv 151,194,195\nex 189,105\nex 190,106\n"
"ad 201,188,189\ncv 156,188,189\nex 191,107\nad 201,201,190\nex 192,108\nad 201,201,191\n"
"cv 157,190,191\nex 193,109\nad 201,201,192\nex 194,110\nad 201,201,193\ncv 158,192,193\n"
"ex 195,111\nad 201,201,194\ns_nop 0\nad 201,201,195\ncv 159,194,195\ns_nop 1\n"
"s_cmp_eq_u32 s65, 19\ns_cbranch_scc1 RX_19\ns_cmp_eq_u32 s65, 23\ns_cbranch_scc1 RX_23\ns_cmp_eq_u32 s65, 35\ns_cbranch_scc1 RX_35\n"
"s_cmp_eq_u32 s65, 39\ns_cbranch_scc1 RX_39\nRX_11:\ns_cmp_eq_u32 s66, 1\ns_branch RR_11\nRX_15:\n"
"s_cmp_eq_u32 s66, 1\ns_branch RR_15\nRX_19:\ns_cmp_eq_u32 s66, 1\ns_branch RR_19\nRX_23:\n"
"s_cmp_eq_u32 s66, 1\ns_branch RR_23\nRX_27:\ns_cmp_eq_u32 s66, 1\ns_branch RR_27\nRX_31:\n"
"s_cmp_eq_u32 s66, 1\ns_branch RR_31\nRX_35:\ns_cmp_eq_u32 s66, 1\ns_branch RR_35\nRX_39:\n"
"s_cmp_eq_u32 s66, 1\ns_branch RR_39\nFINAL:\ns_barrier\ns_branch FRONT_END\nSPIN_HK:\n"
"s_mov_b32 s67, 0x10000\nSPIN_HK_l:\nbuffer_load_dwordx2 v[242:243], v244, s[96:99], s90 offen sc1\ns_sub_u32 s67, s67, 1\nwv 0\ns_cbranch_scc1 SPIN_HK_x\n"
"v_cmp_ne_u64 vcc, s[46:47], v[242:243]\ns_cbranch_vccnz SPIN_HK_l\nSPIN_HK_x:\ns_branch SPUN_HK\nSPIN_HV:\ns_mov_b32 s67, 0x10000\n"
"SPIN_HV_l:\nbuffer_load_dwordx2 v[242:243], v244, s[96:99], s90 offen sc1\ns_sub_u32 s67, s67, 1\nwv 0\ns_cbranch_scc1 SPIN_HV_x\nv_cmp_ne_u64 vcc, s[46:47], v[242:243]\n"
"s_cbranch_vccnz SPIN_HV_l\nSPIN_HV_x:\ns_branch SPUN_HV\nSPIN_AQ:\ns_mov_b32 s67, 0x10000\nSPIN_AQ_l:\n"
"buffer_load_dwordx2 v[242:243], v244, s[96:99], s91 offen sc1\ns_sub_u32 s67, s67, 1\nwv 0\ns_cbranch_scc1 SPIN_AQ_x\nv_cmp_ne_u64 vcc, s[46:47], v[242:243]\ns_cbranch_vccnz SPIN_AQ_l\n"
"SPIN_AQ_x:\ns_branch SPUN_AQ\nSPIN_BS:\ns_mov_b32 s67, 0x10000\nSPIN_BS_l:\nbuffer_load_dwordx2 v[242:243], v244, s[96:99], s95 offen sc1\n"
"s_sub_u32 s67, s67, 1\nwv 0\ns_cbranch_scc1 SPIN_BS_x\nv_cmp_ne_u64 vcc, s[46:47], v[242:243]\ns_cbranch_vccnz SPIN_BS_l\nSPIN_BS_x:\n"
"s_branch SPUN_BS\nSPIN_BDS:\ns_mov_b32 s67, 0x10000\nSPIN_BDS_l:\nbuffer_load_dwordx2 v[242:243], v244, s[96:99], s95 offen sc1\ns_sub_u32 s67, s67, 1\n"
"wv 0\ns_cbranch_scc1 SPIN_BDS_x\nv_cmp_ne_u64 vcc, s[46:47], v[242:243]\ns_cbranch_vccnz SPIN_BDS_l\nSPIN_BDS_x:\ns_branch SPUN_BDS\n"
"SPIN_BN:\ns_mov_b32 s67, 0x10000\nSPIN_BN_l:\nbuffer_load_dwordx2 v[242:243], v244, s[96:99], s95 offen sc1\ns_sub_u32 s67, s67, 1\nwv 0\n"
"s_cbranch_scc1 SPIN_BN_x\nv_cmp_ne_u64 vcc, s[46:47], v[242:243]\ns_cbranch_vccnz SPIN_BN_l\nSPIN_BN_x:\ns_branch SPUN_BN\nSPIN_MK:\n"
"s_mov_b32 s67, 0x10000\nSPIN_MK_l:\nbuffer_load_dwordx2 v[242:243], v180, s[96:99], 0 offen offset:0 sc1\ns_sub_u32 s67, s67, 1\nwv 0\ns_cbranch_scc1 SPIN_MK_x\n"
"v_cmp_ne_u64 vcc, s[46:47], v[242:243]\ns_cbranch_vccnz SPIN_MK_l\nSPIN_MK_x:\ns_branch SPUN_MK\nSPIN_MV:\ns_mov_b32 s67, 0x10000\n"
"SPIN_MV_l:\nbuffer_load_dwordx2 v[242:243], v180, s[96:99], 0 offen offset:0 sc1\ns_sub_u32 s67, s67, 1\nwv 0\ns_cbranch_scc1 SPIN_MV_x\nv_cmp_ne_u64 vcc, s[46:47], v[242:243]\n"
"s_cbranch_vccnz SPIN_MV_l\nSPIN_MV_x:\ns_branch SPUN_MV\nFRONT_END:\n"
  :: "v"(tid), "s"(kp), "s"(b), "s"(lnoff), "s"(um), "s"(tag), "{v[168:171]}"(xa), "{v[172:175]}"(xb), "{v[176:177]}"(xd), "{v178}"(hoff), "{v250}"(desc) : "v0", "v1", "v2", "v3", "v4", "v5", "v6", "v7", "v8", "v9", "v10", "v11", "v12", "v13", "v14", "v15", "v16", "v17", "v18", "v19", "v20", "v21", "v22", "v23", "v24", "v25", "v26", "v27", "v28", "v29", "v30", "v31", "v32", "v33", "v34", "v35", "v36", "v37", "v38", "v39", "v40", "v41", "v42", "v43", "v44", "v45", "v46", "v47", "v48", "v49", "v50", "v51", "v52", "v53", "v54", "v55", "v56", "v57", "v58", "v59", "v60", "v61", "v62", "v63", "v64", "v65", "v66", "v67", "v68", "v69", "v70", "v71", "v72", "v73", "v74", "v75", "v76", "v77", "v78", "v79", "v80", "v81", "v82", "v83", "v84", "v85", "v86", "v87", "v88", "v89", "v90", "v91", "v92", "v93", "v94", "v95", "v96", "v97", "v98", "v99", "v100", "v101", "v102", "v103", "v104", "v105", "v106", "v107", "v108", "v109", "v110", "v111", "v112", "v113", "v114", "v115", "v116", "v117", "v118", "v119", "v120", "v121", "v122", "v123", "v124", "v125", "v126", "v127", "v128", "v129", "v130", "v131", "v132", "v133", "v134", "v135", "v136", "v137", "v138", "v139", "v140", "v141", "v142", "v143", "v144", "v145", "v146", "v147", "v148", "v149", "v150", "v151", "v152", "v153", "v154", "v155", "v156", "v157", "v158", "v159", "v160", "v161", "v162", "v163", "v164", "v165", "v166", "v167", "v179", "v180", "v181", "v182", "v183", "v184", "v185", "v186", "v187", "v188", "v189", "v190", "v191", "v192", "v193", "v194", "v195", "v196", "v197", "v198", "v199", "v200", "v201", "v202", "v203", "v204", "v205", "v206", "v207", "v208", "v209", "v210", "v211", "v212", "v213", "v214", "v215", "v216", "v217", "v218", "v219", "v220", "v221", "v222", "v223", "v224", "v225", "v226", "v227", "v228", "v229", "v230", "v231", "v232", "v233", "v234", "v235", "v236", "v237", "v238", "v239", "v240", "v241", "v242", "v243", "v244", "v245", "v246", "v247", "v248", "v249", "v253", "v254", "a0", "a1", "a2", "a3", "a4", "a5", "a6", "a7", "a8", "a9", "a10", "a11", "a12", "a13", "a14", "a15", "a16", "a17", "a18", "a19", "a20", "a21", "a22", "a23", "a24", "a25", "a26", "a27", "a28", "a29", "a30", "a31", "a32", "a33", "a34", "a35", "a36", "a37", "a38", "a39", "a40", "a41", "a42", "a43", "a44", "a45", "a46", "a47", "a48", "a49", "a50", "a51", "a52", "a53", "a54", "a55", "a56", "a57", "a58", "a59", "a60", "a61", "a62", "a63", "a64", "a65", "a66", "a67", "a68", "a69", "a70", "a71", "a72", "a73", "a74", "a75", "a76", "a77", "a78", "a79", "a80", "a81", "a82", "a83", "a84", "a85", "a86", "a87", "a88", "a89", "a90", "a91", "a92", "a93", "a94", "a95", "a96", "a97", "a98", "a99", "a100", "a101", "a102", "a103", "a104", "a105", "a106", "a107", "a108", "a109", "a110", "a111", "a112", "a113", "a114", "a115", "a116", "a117", "a118", "a119", "a120", "a121", "a122", "a123", "a124", "a125", "a126", "a127", "a128", "a129", "a130", "a131", "a132", "a133", "a134", "a135", "a136", "a137", "a138", "a139", "a140", "a141", "a142", "a143", "a144", "a145", "a146", "a147", "a148", "a149", "a150", "a151", "a152", "a153", "a154", "a155", "a156", "a157", "a158", "a159", "a160", "a161", "a162", "a163", "a164", "a165", "a166", "a167", "a168", "a169", "a170", "a171", "a172", "a173", "a174", "a175", "a176", "a177", "a178", "a179", "a180", "a181", "a182", "a183", "a184", "a185", "a186", "a187", "a188", "a189", "a190", "a191", "a192", "a193", "a194", "a195", "a196", "a197", "a198", "a199", "a200", "a201", "a202", "a203", "a204", "a205", "a206", "a207", "a208", "a209", "a210", "a211", "a212", "a213", "a214", "a215", "a216", "a217", "a218", "a219", "a220", "a221", "a222", "a223", "a224", "a225", "a226", "a227", "a228", "a229", "a230", "a231", "a232", "a233", "a234", "a235", "a236", "a237", "a238", "a239", "a240", "a241", "a242", "a243", "a244", "a245", "a246", "a247", "a248", "a249", "a250", "a251", "a252", "a253", "a254", "a255", "s20", "s21", "s22", "s23", "s24", "s25", "s26", "s27", "s28", "s29", "s30", "s31", "s33", "s34", "s35", "s36", "s37", "s38", "s39", "s40", "s41", "s42", "s43", "s44", "s45", "s46", "s47", "s48", "s49", "s50", "s51", "s52", "s53", "s54", "s55", "s56", "s57", "s58", "s59", "s60", "s61", "s62", "s63", "s64", "s65", "s66", "s67", "s68", "s69", "s70", "s71", "s72", "s73", "s74", "s75", "s76", "s77", "s78", "s79", "s80", "s81", "s82", "s83", "s84", "s85", "s86", "s87", "s88", "s89", "s90", "s91", "s92", "s93", "s94", "s95", "s96", "s97", "s98", "s99", "vcc", "scc", "m0", "memory");
  tail_part(b, tag);
}
__device__ __forceinline__ void tail_part(int b, u64 tag) {
  kargs ap = (kargs)__builtin_amdgcn_kernarg_segment_ptr(); asm volatile("" : "+s"(ap));
  unsigned tid = threadIdx.x; asm volatile("" : "+v"(tid));
  char* const ws = ap->ws; const u16 *x = ap->x; u16* out = ap->out;
  const int xc = b & 7, i = b >> 3, w = tid >> 6, lane = tid & 63, g = lane >> 4;
  const __amdgpu_buffer_rsrc_t rs = __builtin_amdgcn_make_buffer_rsrc(ws, 0, 128 << 20, 0x00020000);
  const int sp = xc >> 1, rbk = (xc & 1) * 8 + (i >> 2), c4 = i & 3;
  const char* wop = (const char*)ap->wo + sp * 1310720 + c4 * 320; const unsigned wosz = 5242880 - sp * 1310720 - c4 * 320;
  const __amdgpu_buffer_rsrc_t rwo = __builtin_amdgcn_make_buffer_rsrc((void*)wop, 0, wosz, 0x00020000);
  const unsigned vso = (w * 16 + (lane >> 2)) * 1280 + ((lane & 3) ^ ((lane >> 3) & 2)) * 16 + ((c4 & 1) ? 0 : 256);
  const unsigned vsw = (w * 8 + ((lane >> 2) & 7)) * 1280 + (lane >> 5) * 64 + ((lane & 3) ^ ((lane >> 3) & 2)) * 16 + (c4 & 1) * 64;
#define WREQO(lds, vv, off) asm volatile("s_add_u32 m0, %0, " #lds "\n s_mov_b32 s49, " #off "\n buffer_load_dwordx4 %1, %2, s49 offen lds" :: "s"(w1k), "v"(vv), "s"(rwo) : "s49", "scc", "memory")
  const unsigned w1k = __builtin_amdgcn_readfirstlane(w) * 1024, vlw = lane * 16 + w * 1024;
#define WREQ(lds, base, off) asm volatile("s_add_u32 m0, %0, " #lds "\n s_add_u32 s49, %3, " #off "\n buffer_load_dwordx4 %1, %2, s49 offen sc1 lds" :: "s"(w1k), "v"(vlw), "s"(rs), "s"(base) : "s49", "scc", "memory")
  const unsigned vtl = (i * 4 + w) * 8192 + lane * 128;
#define TOUCH(off, lim) asm volatile("s_mov_b32 m0, 0x27800\n s_mov_b32 s49, %2\n buffer_load_dword %0, %1, s49 offen lds" :: "v"(vtl < (lim) ? vtl : 0x10000000u), "s"(rs), "i"(off) : "s49", "memory")
  u32x4 wv[10]; unsigned cv[10]; const unsigned ul = (7 - (i & 7)) * 32 + xc * 4 + (i >> 3); const bool late = ul < 214, lm = ul < 160;
  WREQO(0, vsw, 0); WREQO(4096, vsw, 128); WREQO(8192, vsw, 40960); WREQO(12288, vsw, 41088); WREQO(16384, vso, 0); WREQO(20480, vsw, 81920); WREQO(24576, vsw, 82048); WREQO(28672, vsw, 122880); WREQO(32768, vsw, 123008); WREQO(36864, vso, 81920); WREQO(40960, vsw, 163840); WREQO(45056, vsw, 163968); WREQO(49152, vsw, 204800); WREQO(53248, vsw, 204928); WREQO(57344, vso, 163840); WREQO(61440, vsw, 245760);
  asm volatile("s_waitcnt vmcnt(16)\n s_barrier" ::: "memory");
  if (tid == 0) __hip_atomic_store((u64*)(ws + F_D + b * 128), tag, __ATOMIC_RELAXED, __HIP_MEMORY_SCOPE_AGENT);
  { const int hk = lm ? (ul & 1) * 320 : 0; const unsigned ld = lm ? 7680 : 3456;
    const u16* wp = lm ? ap->wmod + hk * 3840 + 1280 + (ul >> 1) * 32 : ap->w3 + (ul - 160) * 32;
    unsigned vw = (tid >> 2) * ld + (tid & 3) * 16; const unsigned vc = (tid >> 2) * 2;
    asm volatile("s_cmp_eq_u32 %[go], 0\n s_cbranch_scc1 1f\n" "global_load_dwordx4 %[w0], %[vw], %[wp]\n global_load_ushort %[c0], %[vc], %[cp] offset:0\n v_add_u32 %[vw], %[st], %[vw]\n" "global_load_dwordx4 %[w1], %[vw], %[wp]\n global_load_ushort %[c1], %[vc], %[cp] offset:128\n v_add_u32 %[vw], %[st], %[vw]\n" "global_load_dwordx4 %[w2], %[vw], %[wp]\n global_load_ushort %[c2], %[vc], %[cp] offset:256\n v_add_u32 %[vw], %[st], %[vw]\n" "global_load_dwordx4 %[w3], %[vw], %[wp]\n global_load_ushort %[c3], %[vc], %[cp] offset:384\n v_add_u32 %[vw], %[st], %[vw]\n" "global_load_dwordx4 %[w4], %[vw], %[wp]\n global_load_ushort %[c4], %[vc], %[cp] offset:512\n v_add_u32 %[vw], %[st], %[vw]\n" "s_cmp_eq_u32 %[go], 1\n s_cbranch_scc1 1f\n" "global_load_dwordx4 %[w5], %[vw], %[wp]\n global_load_ushort %[c5], %[vc], %[cp] offset:640\n v_add_u32 %[vw], %[st], %[vw]\n" "global_load_dwordx4 %[w6], %[vw], %[wp]\n global_load_ushort %[c6], %[vc], %[cp] offset:768\n v_add_u32 %[vw], %[st], %[vw]\n" "global_load_dwordx4 %[w7], %[vw], %[wp]\n global_load_ushort %[c7], %[vc], %[cp] offset:896\n v_add_u32 %[vw], %[st], %[vw]\n" "global_load_dwordx4 %[w8], %[vw], %[wp]\n global_load_ushort %[c8], %[vc], %[cp] offset:1024\n v_add_u32 %[vw], %[st], %[vw]\n" "global_load_dwordx4 %[w9], %[vw], %[wp]\n global_load_ushort %[c9], %[vc], %[cp] offset:1152\n v_add_u32 %[vw], %[st], %[vw]\n" "1:" : [w0] "=&v"(wv[0]), [c0] "=&v"(cv[0]), [w1] "=&v"(wv[1]), [c1] "=&v"(cv[1]), [w2] "=&v"(wv[2]), [c2] "=&v"(cv[2]), [w3] "=&v"(wv[3]), [c3] "=&v"(cv[3]), [w4] "=&v"(wv[4]), [c4] "=&v"(cv[4]), [w5] "=&v"(wv[5]), [c5] "=&v"(cv[5]), [w6] "=&v"(wv[6]), [c6] "=&v"(cv[6]), [w7] "=&v"(wv[7]), [c7] "=&v"(cv[7]), [w8] "=&v"(wv[8]), [c8] "=&v"(cv[8]), [w9] "=&v"(wv[9]), [c9] "=&v"(cv[9]), [vw] "+v"(vw)
                 : [vc] "v"(vc), [wp] "s"(wp), [cp] "s"(ap->cvec + hk), [st] "s"(ld * 64), [go] "s"(__builtin_amdgcn_readfirstlane(late ? (lm ? 1u : 2u) : 0u)) : "scc", "memory"); }
  WREQO(65536, vsw, 245888); WREQO(69632, vsw, 286720); WREQO(73728, vsw, 286848); WREQO(77824, vso, 245760); WREQO(81920, vsw, 327680); WREQO(86016, vsw, 327808); WREQO(90112, vsw, 368640); WREQO(94208, vsw, 368768);
  const int hd = sp * 8 + (lane & 7); const Polls k0 = wait_tag(ws, F_D + ((hd >> 2) + 8 * ((hd & 3) * 8 + (rbk < 8 ? rbk : 15 - rbk))) * 128, tag, w1k, 4);
  asm volatile("s_waitcnt vmcnt(0)\n s_barrier" : "+v"(wv[0]), "+v"(cv[0]), "+v"(wv[1]), "+v"(cv[1]), "+v"(wv[2]), "+v"(cv[2]), "+v"(wv[3]), "+v"(cv[3]), "+v"(wv[4]), "+v"(cv[4]), "+v"(wv[5]), "+v"(cv[5]), "+v"(wv[6]), "+v"(cv[6]), "+v"(wv[7]), "+v"(cv[7]), "+v"(wv[8]), "+v"(cv[8]), "+v"(wv[9]), "+v"(cv[9]) :: "memory");
  if (late) { const float gv = lm ? gemv_sum<5>(tid, wv, cv, false, (ldsf)(156 * 1024)) : gemv_sum<10>(tid, wv, cv, true, (ldsf)(156 * 1024));
    if (tid < 32) { if (lm) __hip_atomic_fetch_add((float*)(ws + WS_mod) + (ul >> 1) * 32 + tid, gv, __ATOMIC_RELAXED, __HIP_MEMORY_SCOPE_AGENT);
                    else __builtin_amdgcn_raw_buffer_store_b32(__float_as_uint(gv), rs, WS_cs + (ul - 160) * 128 + tid * 4, 0, 16); } }
  const int rb64 = xc * 4 + (i >> 3), cg = i & 7, rb = rb64 * 4 + w;
  const unsigned o1 = WS_w1 + cg * 102400, o2 = WS_w2 + cg * 102400, o3 = WS_w3 + (i & 15) * 143360, o4 = WS_w4 + cg * 276480;
  {
    const unsigned vo = lane * 16 + w * 262144, vp = (unsigned)WS_part + ((sp * 128 + rbk * 8 + w * 2) * 20 + c4 * 5) * 1024 + lane * 16;
    asm volatile(
"s_mov_b64 s[52:53], %8\nv_and_b32 v58, 63, %0\nv_lshrrev_b32 v59, 6, %0\nv_lshrrev_b32 v112, 2, v58\nv_readfirstlane_b32 s48, v59\nv_lshrrev_b32 v113, 4, v58\n"
"v_and_b32 v113, 1, v113\nv_and_b32 v114, 3, v58\nv_lshlrev_b32 v112, 6, v112\nv_lshl_add_u32 v112, v113, 5, v112\nv_lshl_add_u32 v112, v114, 3, v112\nv_xor_b32 v113, 32, v112\n"
"v_lshrrev_b32 v121, 5, v58\nv_lshl_add_u32 v120, v121, 9, v112\nv_xor_b32 v121, 32, v120\nv_add_u32 v122, 65536, v120\nv_add_u32 v123, 65536, v121\nv_add_u32 v114, 65536, v112\n"
"v_add_u32 v115, 65536, v113\nv_add_u32 v124, 131072, v120\nv_add_u32 v125, 131072, v121\nv_add_u32 v116, 131072, v112\nv_add_u32 v117, 131072, v113\nv_lshlrev_b32 v58, 4, v58\n"
"s_mov_b64 s[40:41], %1\ns_and_b32 s41, s41, 0xffff\ns_mov_b32 s42, 134217728\ns_mov_b32 s43, 0x20000\ns_mov_b64 s[44:45], %6\ns_and_b32 s45, s45, 0xffff\n"
"s_mov_b32 s46, %7\ns_mov_b32 s47, 0x20000\ns_mov_b64 s[56:57], %2\ns_and_b32 s57, s57, 0xffff\ns_mov_b32 s58, %3\ns_mov_b32 s59, 0x20000\n"
"s_lshl_b32 s50, s48, 10\nv_add_u32 v61, s50, v58\nv_mov_b32 v118, %11\ns_mov_b32 s49, 0\nbuffer_load_dwordx4 a[0:3], %10, s[44:47], s49 offen sc1\ns_mov_b32 s49, 131072\n"
"buffer_load_dwordx4 a[4:7], %10, s[44:47], s49 offen sc1\ns_mov_b32 s49, 1024\nbuffer_load_dwordx4 a[8:11], %10, s[44:47], s49 offen sc1\ns_mov_b32 s49, 132096\nbuffer_load_dwordx4 a[12:15], %10, s[44:47], s49 offen sc1\ns_mov_b32 s49, 2048\n"
"buffer_load_dwordx4 a[16:19], %10, s[44:47], s49 offen sc1\ns_mov_b32 s49, 133120\nbuffer_load_dwordx4 a[20:23], %10, s[44:47], s49 offen sc1\ns_mov_b32 s49, 3072\nbuffer_load_dwordx4 a[24:27], %10, s[44:47], s49 offen sc1\ns_mov_b32 s49, 134144\n"
"buffer_load_dwordx4 a[28:31], %10, s[44:47], s49 offen sc1\ns_mov_b32 s49, 4096\nbuffer_load_dwordx4 a[32:35], %10, s[44:47], s49 offen sc1\ns_mov_b32 s49, 135168\nbuffer_load_dwordx4 a[36:39], %10, s[44:47], s49 offen sc1\ns_mov_b32 s49, 5120\n"
"buffer_load_dwordx4 a[40:43], %10, s[44:47], s49 offen sc1\ns_mov_b32 s49, 136192\nbuffer_load_dwordx4 a[44:47], %10, s[44:47], s49 offen sc1\nwv 12\ns_barrier\ns_cmp_eq_u32 s48, 0\n"
"s_cbranch_scc0 NOPUB_wo\nv_mov_b32 v62, s52\nv_mov_b32 v63, s53\nv_mov_b32 v57, 0\ns_mov_b64 exec, 1\nbuffer_store_dwordx2 v[62:63], v57, s[40:43], %9 offen sc1\n"
"s_mov_b64 exec, -1\nNOPUB_wo:\nrt v,64,120,0\nrt v,66,120,2048\nrt v,68,120,8192\nrt v,70,120,10240\n"
"rt v,72,121,0\nrt v,74,121,2048\nrt v,76,121,8192\nrt v,78,121,10240\nrt v,80,120,512\nrt v,82,120,2560\n"
"rt v,84,120,8704\nrt v,86,120,10752\nrt v,88,121,512\nrt v,90,121,2560\nrt v,92,121,8704\nrt v,94,121,10752\n"
"s_waitcnt vmcnt(11) lgkmcnt(14)\nmz v,128,v,64,a,0\nrt v,96,120,4096\ns_waitcnt vmcnt(9) lgkmcnt(13)\nmm v,128,v,68,a,8\nrt v,98,120,6144\n"
"mz v,136,v,64,a,4\nrt v,100,120,12288\nwv 8\nmm v,136,v,68,a,12\nrt v,102,120,14336\ns_mov_b32 s49, 6144\n"
"buffer_load_dwordx4 a[48:51], %10, s[44:47], s49 offen sc1\nwl 14\nmz v,132,v,72,a,0\nrt v,104,121,4096\nsa m0,s50,98304\ns_mov_b32 s49, 327680\n"
"dl 118,56,49\nwl 13\nmm v,132,v,76,a,8\nrt v,106,121,6144\ns_mov_b32 s49, 137216\nbuffer_load_dwordx4 a[52:55], %10, s[44:47], s49 offen sc1\n"
"mz v,140,v,72,a,4\nrt v,108,121,12288\nmm v,140,v,76,a,12\nrt v,110,121,14336\nwl 14\nmz v,144,v,80,a,0\n"
"rt v,64,120,4608\nwl 13\nmm v,144,v,84,a,8\nrt v,66,120,6656\nmz v,152,v,80,a,4\nrt v,68,120,12800\n"
"sa m0,s50,102400\ns_mov_b32 s49, 409600\nbuffer_load_dwordx4 %13, s[56:59], s49 offen lds\nmm v,152,v,84,a,12\nrt v,70,120,14848\nwl 14\n"
"mz v,148,v,88,a,0\nrt v,72,121,4608\nwl 13\nmm v,148,v,92,a,8\nrt v,74,121,6656\nmz v,156,v,88,a,4\n"
"rt v,76,121,12800\nmm v,156,v,92,a,12\nrt v,78,121,14848\nwl 14\nmz v,160,v,96,a,0\nrt v,80,112,16384\n"
"wl 13\nmm v,160,v,100,a,8\nrt v,82,112,17408\nsa m0,s50,106496\ns_mov_b32 s49, 409728\nbuffer_load_dwordx4 %13, s[56:59], s49 offen lds\n"
"mz v,168,v,96,a,4\nrt v,84,112,18432\nmm v,168,v,100,a,12\nrt v,86,112,19456\nwl 14\nmz v,164,v,104,a,0\n"
"rt v,88,113,16384\nwl 13\nmm v,164,v,108,a,8\nrt v,90,113,17408\nmz v,172,v,104,a,4\nrt v,92,113,18432\n"
"mm v,172,v,108,a,12\nrt v,94,113,19456\ns_mov_b32 s49, 7168\nbuffer_load_dwordx4 a[56:59], %10, s[44:47], s49 offen sc1\nwl 14\nmz v,176,v,64,a,0\n"
"rt v,96,120,20480\nsa m0,s50,110592\ns_mov_b32 s49, 450560\nbuffer_load_dwordx4 %13, s[56:59], s49 offen lds\nwl 13\nmm v,176,v,68,a,8\n"
"rt v,98,120,22528\ns_mov_b32 s49, 138240\nbuffer_load_dwordx4 a[60:63], %10, s[44:47], s49 offen sc1\nmz v,184,v,64,a,4\nrt v,100,120,28672\nmm v,184,v,68,a,12\n"
"rt v,102,120,30720\nwl 14\nmz v,180,v,72,a,0\nrt v,104,121,20480\nwl 13\nmm v,180,v,76,a,8\n"
"rt v,106,121,22528\nmz v,188,v,72,a,4\nrt v,108,121,28672\nsa m0,s50,114688\ns_mov_b32 s49, 450688\nbuffer_load_dwordx4 %13, s[56:59], s49 offen lds\n"
"mm v,188,v,76,a,12\nrt v,110,121,30720\nwl 14\nmz v,192,v,80,a,0\nrt v,64,120,20992\nwl 13\n"
"mm v,192,v,84,a,8\nrt v,66,120,23040\nmz v,200,v,80,a,4\nrt v,68,120,29184\nmm v,200,v,84,a,12\nrt v,70,120,31232\n"
"wl 14\nmz v,196,v,88,a,0\nrt v,72,121,20992\nwl 13\nmm v,196,v,92,a,8\nrt v,74,121,23040\n"
"sa m0,s50,118784\ns_mov_b32 s49, 409600\ndl 118,56,49\nmz v,204,v,88,a,4\nrt v,76,121,29184\nmm v,204,v,92,a,12\n"
"rt v,78,121,31232\ns_waitcnt vmcnt(17) lgkmcnt(14)\nmm v,128,v,96,a,16\nrt v,80,120,24576\ns_waitcnt vmcnt(15) lgkmcnt(13)\nmm v,128,v,100,a,24\n"
"rt v,82,120,26624\nmm v,136,v,96,a,20\nrt v,84,120,32768\nwv 14\nmm v,136,v,100,a,28\nrt v,86,120,34816\n"
"s_mov_b32 s49, 8192\nbuffer_load_dwordx4 a[64:67], %10, s[44:47], s49 offen sc1\nwl 14\nmm v,132,v,104,a,16\nrt v,88,121,24576\nsa m0,s50,122880\n"
"s_mov_b32 s49, 491520\nbuffer_load_dwordx4 %13, s[56:59], s49 offen lds\nwl 13\nmm v,132,v,108,a,24\nrt v,90,121,26624\ns_mov_b32 s49, 139264\n"
"buffer_load_dwordx4 a[68:71], %10, s[44:47], s49 offen sc1\nmm v,140,v,104,a,20\nrt v,92,121,32768\nmm v,140,v,108,a,28\nrt v,94,121,34816\nwl 14\n"
"mm v,144,v,64,a,16\nrt v,96,120,25088\nwl 13\nmm v,144,v,68,a,24\nrt v,98,120,27136\nmm v,152,v,64,a,20\n"
"rt v,100,120,33280\nsa m0,s50,126976\ns_mov_b32 s49, 491648\nbuffer_load_dwordx4 %13, s[56:59], s49 offen lds\nmm v,152,v,68,a,28\nrt v,102,120,35328\n"
"wl 14\nmm v,148,v,72,a,16\nrt v,104,121,25088\nwl 13\nmm v,148,v,76,a,24\nrt v,106,121,27136\n"
"mm v,156,v,72,a,20\nrt v,108,121,33280\nmm v,156,v,76,a,28\nrt v,110,121,35328\nwl 14\nmm v,160,v,80,a,16\n"
"rt v,64,112,36864\nwl 13\nmm v,160,v,84,a,24\nrt v,66,112,37888\nsa m0,s50,131072\ns_mov_b32 s49, 532480\n"
"buffer_load_dwordx4 %13, s[56:59], s49 offen lds\nmm v,168,v,80,a,20\nrt v,68,112,38912\nmm v,168,v,84,a,28\nrt v,70,112,39936\nwl 14\n"
"mm v,164,v,88,a,16\nrt v,72,113,36864\nwl 13\nmm v,164,v,92,a,24\nrt v,74,113,37888\nmm v,172,v,88,a,20\n"
"rt v,76,113,38912\nmm v,172,v,92,a,28\nrt v,78,113,39936\ns_mov_b32 s49, 9216\nbuffer_load_dwordx4 a[72:75], %10, s[44:47], s49 offen sc1\nwl 14\n"
"mm v,176,v,96,a,16\nrt v,80,120,40960\nsa m0,s50,135168\ns_mov_b32 s49, 532608\nbuffer_load_dwordx4 %13, s[56:59], s49 offen lds\nwl 13\n"
"mm v,176,v,100,a,24\nrt v,82,120,43008\ns_mov_b32 s49, 140288\nbuffer_load_dwordx4 a[76:79], %10, s[44:47], s49 offen sc1\nmm v,184,v,96,a,20\nrt v,84,120,49152\n"
"mm v,184,v,100,a,28\nrt v,86,120,51200\nwl 14\nmm v,180,v,104,a,16\nrt v,88,121,40960\nwl 13\n"
"mm v,180,v,108,a,24\nrt v,90,121,43008\nmm v,188,v,104,a,20\nrt v,92,121,49152\nsa m0,s50,139264\ns_mov_b32 s49, 491520\n"
"dl 118,56,49\nmm v,188,v,108,a,28\nrt v,94,121,51200\nwl 14\nmm v,192,v,64,a,16\nrt v,96,120,41472\n"
"wl 13\nmm v,192,v,68,a,24\nrt v,98,120,43520\nmm v,200,v,64,a,20\nrt v,100,120,49664\nmm v,200,v,68,a,28\n"
"rt v,102,120,51712\nwl 14\nmm v,196,v,72,a,16\nrt v,104,121,41472\nwl 13\nmm v,196,v,76,a,24\n"
"rt v,106,121,43520\nsa m0,s50,143360\ns_mov_b32 s49, 573440\nbuffer_load_dwordx4 %13, s[56:59], s49 offen lds\nmm v,204,v,72,a,20\nrt v,108,121,49664\n"
"mm v,204,v,76,a,28\nrt v,110,121,51712\ns_waitcnt vmcnt(23) lgkmcnt(14)\nmm v,128,v,80,a,32\nwv 18\ns_barrier\n"
"rt v,64,120,45056\nwl 13\nmm v,128,v,84,a,40\nrt v,66,120,47104\nmm v,136,v,80,a,36\nrt v,68,120,53248\n"
"mm v,136,v,84,a,44\nrt v,70,120,55296\ns_mov_b32 s49, 10240\nbuffer_load_dwordx4 a[80:83], %10, s[44:47], s49 offen sc1\nwl 14\nmm v,132,v,88,a,32\n"
"rt v,72,121,45056\nsa m0,s50,147456\ns_mov_b32 s49, 573568\nbuffer_load_dwordx4 %13, s[56:59], s49 offen lds\nwl 13\nmm v,132,v,92,a,40\n"
"rt v,74,121,47104\ns_mov_b32 s49, 141312\nbuffer_load_dwordx4 a[84:87], %10, s[44:47], s49 offen sc1\nmm v,140,v,88,a,36\nrt v,76,121,53248\nmm v,140,v,92,a,44\n"
"rt v,78,121,55296\nwl 14\nmm v,144,v,96,a,32\nrt v,80,120,45568\nwl 13\nmm v,144,v,100,a,40\n"
"rt v,82,120,47616\nmm v,152,v,96,a,36\nrt v,84,120,53760\nsa m0,s50,151552\ns_mov_b32 s49, 614400\nbuffer_load_dwordx4 %13, s[56:59], s49 offen lds\n"
"mm v,152,v,100,a,44\nrt v,86,120,55808\nwl 14\nmm v,148,v,104,a,32\nrt v,88,121,45568\nwl 13\n"
"mm v,148,v,108,a,40\nrt v,90,121,47616\nmm v,156,v,104,a,36\nrt v,92,121,53760\nmm v,156,v,108,a,44\nrt v,94,121,55808\n"
"wl 14\nmm v,160,v,64,a,32\nrt v,96,112,57344\nwl 13\nmm v,160,v,68,a,40\nrt v,98,112,58368\n"
"sa m0,s50,155648\ns_mov_b32 s49, 614528\nbuffer_load_dwordx4 %13, s[56:59], s49 offen lds\nmm v,168,v,64,a,36\nrt v,100,112,59392\nmm v,168,v,68,a,44\n"
"rt v,102,112,60416\nwl 14\nmm v,164,v,72,a,32\nrt v,104,113,57344\nwl 13\nmm v,164,v,76,a,40\n"
"rt v,106,113,58368\nmm v,172,v,72,a,36\nrt v,108,113,59392\nmm v,172,v,76,a,44\nrt v,110,113,60416\ns_mov_b32 s49, 11264\n"
"buffer_load_dwordx4 a[88:91], %10, s[44:47], s49 offen sc1\nwl 14\nmm v,176,v,80,a,32\nrt v,64,120,61440\nsa m0,s50,0\ns_mov_b32 s49, 573440\n"
"dl 118,56,49\nwl 13\nmm v,176,v,84,a,40\nrt v,66,120,63488\ns_mov_b32 s49, 142336\nbuffer_load_dwordx4 a[92:95], %10, s[44:47], s49 offen sc1\n"
"mm v,184,v,80,a,36\nrt v,68,122,4096\nmm v,184,v,84,a,44\nrt v,70,122,6144\nwl 14\nmm v,180,v,88,a,32\n"
"rt v,72,121,61440\nwl 13\nmm v,180,v,92,a,40\nrt v,74,121,63488\nsa m0,s50,4096\ns_mov_b32 s49, 655360\n"
"buffer_load_dwordx4 %13, s[56:59], s49 offen lds\nmm v,188,v,88,a,36\nrt v,76,123,4096\nmm v,188,v,92,a,44\nrt v,78,123,6144\nwl 14\n"
"mm v,192,v,96,a,32\nrt v,80,120,61952\nwl 13\nmm v,192,v,100,a,40\nrt v,82,120,64000\nmm v,200,v,96,a,36\n"
"rt v,84,122,4608\nsa m0,s50,8192\ns_mov_b32 s49, 655488\nbuffer_load_dwordx4 %13, s[56:59], s49 offen lds\nmm v,200,v,100,a,44\nrt v,86,122,6656\n"
"wl 14\nmm v,196,v,104,a,32\nrt v,88,121,61952\nwl 13\nmm v,196,v,108,a,40\nrt v,90,121,64000\n"
"mm v,204,v,104,a,36\nrt v,92,123,4608\nmm v,204,v,108,a,44\nrt v,94,123,6656\nsa m0,s50,12288\ns_mov_b32 s49, 696320\n"
"buffer_load_dwordx4 %13, s[56:59], s49 offen lds\nwl 14\nmm v,128,v,64,a,48\nrt v,96,122,0\ns_waitcnt vmcnt(25) lgkmcnt(13)\nmm v,128,v,68,a,56\n"
"rt v,98,122,2048\nmm v,136,v,64,a,52\nrt v,100,122,8192\nwv 23\nmm v,136,v,68,a,60\nrt v,102,122,10240\n"
"s_mov_b32 s49, 12288\nbuffer_load_dwordx4 a[96:99], %10, s[44:47], s49 offen sc1\nwl 14\nmm v,132,v,72,a,48\nrt v,104,123,0\nsa m0,s50,16384\n"
"s_mov_b32 s49, 696448\nbuffer_load_dwordx4 %13, s[56:59], s49 offen lds\nwl 13\nmm v,132,v,76,a,56\nrt v,106,123,2048\ns_mov_b32 s49, 143360\n"
"buffer_load_dwordx4 a[100:103], %10, s[44:47], s49 offen sc1\nmm v,140,v,72,a,52\nrt v,108,123,8192\nmm v,140,v,76,a,60\nrt v,110,123,10240\nwl 14\n"
"mm v,144,v,80,a,48\nrt v,64,122,512\nwl 13\nmm v,144,v,84,a,56\nrt v,66,122,2560\nmm v,152,v,80,a,52\n"
"rt v,68,122,8704\nsa m0,s50,20480\ns_mov_b32 s49, 655360\ndl 118,56,49\nmm v,152,v,84,a,60\nrt v,70,122,10752\n"
"wl 14\nmm v,148,v,88,a,48\nrt v,72,123,512\nwl 13\nmm v,148,v,92,a,56\nrt v,74,123,2560\n"
"mm v,156,v,88,a,52\nrt v,76,123,8704\nmm v,156,v,92,a,60\nrt v,78,123,10752\nwl 14\nmm v,160,v,96,a,48\n"
"rt v,80,114,12288\nwl 13\nmm v,160,v,100,a,56\nrt v,82,114,13312\nsa m0,s50,24576\ns_mov_b32 s49, 737280\n"
"buffer_load_dwordx4 %13, s[56:59], s49 offen lds\nmm v,168,v,96,a,52\nrt v,84,114,14336\nmm v,168,v,100,a,60\nrt v,86,114,15360\nwl 14\n"
"mm v,164,v,104,a,48\ns_barrier\nrt v,88,115,12288\nwl 13\nmm v,164,v,108,a,56\nrt v,90,115,13312\n"
"mm v,172,v,104,a,52\nrt v,92,115,14336\nmm v,172,v,108,a,60\nrt v,94,115,15360\ns_mov_b32 s49, 13312\nbuffer_load_dwordx4 a[104:107], %10, s[44:47], s49 offen sc1\n"
"wl 14\nmm v,176,v,64,a,48\nrt v,96,122,16384\nsa m0,s50,28672\ns_mov_b32 s49, 737408\nbuffer_load_dwordx4 %13, s[56:59], s49 offen lds\n"
"wl 13\nmm v,176,v,68,a,56\nrt v,98,122,18432\ns_mov_b32 s49, 144384\nbuffer_load_dwordx4 a[108:111], %10, s[44:47], s49 offen sc1\nmm v,184,v,64,a,52\n"
"rt v,100,122,24576\nmm v,184,v,68,a,60\nrt v,102,122,26624\nwl 14\nmm v,180,v,72,a,48\nrt v,104,123,16384\n"
"wl 13\nmm v,180,v,76,a,56\nrt v,106,123,18432\nmm v,188,v,72,a,52\nrt v,108,123,24576\nsa m0,s50,32768\n"
"s_mov_b32 s49, 778240\nbuffer_load_dwordx4 %13, s[56:59], s49 offen lds\nmm v,188,v,76,a,60\nrt v,110,123,26624\nwl 14\nmm v,192,v,80,a,48\n"
"rt v,64,122,16896\nwl 13\nmm v,192,v,84,a,56\nrt v,66,122,18944\nmm v,200,v,80,a,52\nrt v,68,122,25088\n"
"mm v,200,v,84,a,60\nrt v,70,122,27136\nwl 14\nmm v,196,v,88,a,48\nrt v,72,123,16896\nwl 13\n"
"mm v,196,v,92,a,56\nrt v,74,123,18944\nsa m0,s50,36864\ns_mov_b32 s49, 778368\nbuffer_load_dwordx4 %13, s[56:59], s49 offen lds\nmm v,204,v,88,a,52\n"
"rt v,76,123,25088\nmm v,204,v,92,a,60\nrt v,78,123,27136\ns_waitcnt vmcnt(30) lgkmcnt(14)\nmm v,128,v,96,a,64\nrt v,80,122,20480\n"
"s_waitcnt vmcnt(25) lgkmcnt(13)\nmm v,128,v,100,a,72\nrt v,82,122,22528\nmm v,136,v,96,a,68\nrt v,84,122,28672\nwv 23\n"
"mm v,136,v,100,a,76\nrt v,86,122,30720\ns_mov_b32 s49, 14336\nbuffer_load_dwordx4 a[112:115], %10, s[44:47], s49 offen sc1\nwl 14\nmm v,132,v,104,a,64\n"
"rt v,88,123,20480\nsa m0,s50,40960\ns_mov_b32 s49, 737280\ndl 118,56,49\nwl 13\nmm v,132,v,108,a,72\n"
"rt v,90,123,22528\ns_mov_b32 s49, 145408\nbuffer_load_dwordx4 a[116:119], %10, s[44:47], s49 offen sc1\nmm v,140,v,104,a,68\nrt v,92,123,28672\nmm v,140,v,108,a,76\n"
"rt v,94,123,30720\nwl 14\nmm v,144,v,64,a,64\nrt v,96,122,20992\nwl 13\nmm v,144,v,68,a,72\n"
"rt v,98,122,23040\nmm v,152,v,64,a,68\nrt v,100,122,29184\nsa m0,s50,45056\ns_mov_b32 s49, 819200\nbuffer_load_dwordx4 %13, s[56:59], s49 offen lds\n"
"mm v,152,v,68,a,76\nrt v,102,122,31232\nwl 14\nmm v,148,v,72,a,64\nrt v,104,123,20992\nwl 13\n"
"mm v,148,v,76,a,72\nrt v,106,123,23040\nmm v,156,v,72,a,68\nrt v,108,123,29184\nmm v,156,v,76,a,76\nrt v,110,123,31232\n"
"wl 14\nmm v,160,v,80,a,64\nrt v,64,114,32768\nwl 13\nmm v,160,v,84,a,72\nrt v,66,114,33792\n"
"sa m0,s50,49152\ns_mov_b32 s49, 819328\nbuffer_load_dwordx4 %13, s[56:59], s49 offen lds\nmm v,168,v,80,a,68\nrt v,68,114,34816\nmm v,168,v,84,a,76\n"
"rt v,70,114,35840\nwl 14\nmm v,164,v,88,a,64\ns_barrier\nrt v,72,115,32768\nwl 13\n"
"mm v,164,v,92,a,72\nrt v,74,115,33792\nmm v,172,v,88,a,68\nrt v,76,115,34816\nmm v,172,v,92,a,76\nrt v,78,115,35840\n"
"s_mov_b32 s49, 15360\nbuffer_load_dwordx4 a[120:123], %10, s[44:47], s49 offen sc1\nwl 14\nmm v,176,v,96,a,64\nrt v,80,122,36864\nsa m0,s50,53248\n"
"s_mov_b32 s49, 860160\nbuffer_load_dwordx4 %13, s[56:59], s49 offen lds\nwl 13\nmm v,176,v,100,a,72\nrt v,82,122,38912\ns_mov_b32 s49, 146432\n"
"buffer_load_dwordx4 a[124:127], %10, s[44:47], s49 offen sc1\nmm v,184,v,96,a,68\nrt v,84,122,45056\nmm v,184,v,100,a,76\nrt v,86,122,47104\nwl 14\n"
"mm v,180,v,104,a,64\nrt v,88,123,36864\nwl 13\nmm v,180,v,108,a,72\nrt v,90,123,38912\nmm v,188,v,104,a,68\n"
"rt v,92,123,45056\nmm v,188,v,108,a,76\nrt v,94,123,47104\nwl 14\nmm v,192,v,64,a,64\nrt v,96,122,37376\n"
"wl 13\nmm v,192,v,68,a,72\nrt v,98,122,39424\nmm v,200,v,64,a,68\nrt v,100,122,45568\nsa m0,s50,57344\n"
"s_mov_b32 s49, 860288\nbuffer_load_dwordx4 %13, s[56:59], s49 offen lds\nmm v,200,v,68,a,76\nrt v,102,122,47616\nwl 14\nmm v,196,v,72,a,64\n"
"rt v,104,123,37376\nwl 13\nmm v,196,v,76,a,72\nrt v,106,123,39424\nmm v,204,v,72,a,68\nrt v,108,123,45568\n"
"mm v,204,v,76,a,76\nrt v,110,123,47616\ns_waitcnt vmcnt(29) lgkmcnt(14)\nmm v,128,v,80,a,80\nrt v,64,122,40960\ns_waitcnt vmcnt(24) lgkmcnt(13)\n"
"mm v,128,v,84,a,88\nrt v,66,122,43008\nmm v,136,v,80,a,84\nrt v,68,122,49152\nwv 22\nmm v,136,v,84,a,92\n"
"rt v,70,122,51200\ns_mov_b32 s49, 16384\nbuffer_load_dwordx4 a[128:131], %10, s[44:47], s49 offen sc1\nwl 14\nmm v,132,v,88,a,80\nrt v,72,123,40960\n"
"sa m0,s50,61440\ns_mov_b32 s49, 819200\ndl 118,56,49\nwl 13\nmm v,132,v,92,a,88\nrt v,74,123,43008\n"
"s_mov_b32 s49, 147456\nbuffer_load_dwordx4 a[132:135], %10, s[44:47], s49 offen sc1\nmm v,140,v,88,a,84\nrt v,76,123,49152\nmm v,140,v,92,a,92\nrt v,78,123,51200\n"
"wl 14\nmm v,144,v,96,a,80\nrt v,80,122,41472\nsa m0,s50,65536\ns_mov_b32 s49, 901120\nbuffer_load_dwordx4 %13, s[56:59], s49 offen lds\n"
"wl 13\nmm v,144,v,100,a,88\nrt v,82,122,43520\nmm v,152,v,96,a,84\nrt v,84,122,49664\nmm v,152,v,100,a,92\n"
"rt v,86,122,51712\nwl 14\nmm v,148,v,104,a,80\nrt v,88,123,41472\nsa m0,s50,69632\ns_mov_b32 s49, 901248\n"
"buffer_load_dwordx4 %13, s[56:59], s49 offen lds\nwl 13\nmm v,148,v,108,a,88\nrt v,90,123,43520\nmm v,156,v,104,a,84\nrt v,92,123,49664\n"
"mm v,156,v,108,a,92\nrt v,94,123,51712\nwl 14\nmm v,160,v,64,a,80\nrt v,96,114,53248\nsa m0,s50,73728\n"
"s_mov_b32 s49, 942080\nbuffer_load_dwordx4 %13, s[56:59], s49 offen lds\nwl 13\nmm v,160,v,68,a,88\nrt v,98,114,54272\nmm v,168,v,64,a,84\n"
"rt v,100,114,55296\nmm v,168,v,68,a,92\nrt v,102,114,56320\nwl 14\nmm v,164,v,72,a,80\ns_barrier\n"
"rt v,104,115,53248\nwl 13\nmm v,164,v,76,a,88\nrt v,106,115,54272\nsa m0,s50,77824\ns_mov_b32 s49, 942208\n"
"buffer_load_dwordx4 %13, s[56:59], s49 offen lds\nmm v,172,v,72,a,84\nrt v,108,115,55296\nmm v,172,v,76,a,92\nrt v,110,115,56320\ns_mov_b32 s49, 17408\n"
"buffer_load_dwordx4 a[136:139], %10, s[44:47], s49 offen sc1\nwl 14\nmm v,176,v,80,a,80\nrt v,64,122,57344\nwl 13\nmm v,176,v,84,a,88\n"
"rt v,66,122,59392\ns_mov_b32 s49, 148480\nbuffer_load_dwordx4 a[140:143], %10, s[44:47], s49 offen sc1\nmm v,184,v,80,a,84\nrt v,68,124,0\nmm v,184,v,84,a,92\n"
"rt v,70,124,2048\nwl 14\nmm v,180,v,88,a,80\nrt v,72,123,57344\nwl 13\nmm v,180,v,92,a,88\n"
"rt v,74,123,59392\nmm v,188,v,88,a,84\nrt v,76,125,0\nmm v,188,v,92,a,92\nrt v,78,125,2048\nwl 14\n"
"mm v,192,v,96,a,80\nrt v,80,122,57856\nwl 13\nmm v,192,v,100,a,88\nrt v,82,122,59904\nmm v,200,v,96,a,84\n"
"rt v,84,124,512\nmm v,200,v,100,a,92\nrt v,86,124,2560\nwl 14\nmm v,196,v,104,a,80\nrt v,88,123,57856\n"
"wl 13\nmm v,196,v,108,a,88\nrt v,90,123,59904\nmm v,204,v,104,a,84\nrt v,92,125,512\nmm v,204,v,108,a,92\n"
"rt v,94,125,2560\ns_waitcnt vmcnt(27) lgkmcnt(14)\nmm v,128,v,64,a,96\nrt v,96,122,61440\ns_waitcnt vmcnt(22) lgkmcnt(13)\nmm v,128,v,68,a,104\n"
"rt v,98,122,63488\nmm v,136,v,64,a,100\nrt v,100,124,4096\nwv 20\nmm v,136,v,68,a,108\nrt v,102,124,6144\n"
"s_mov_b32 s49, 18432\nbuffer_load_dwordx4 a[144:147], %10, s[44:47], s49 offen sc1\nwl 14\nmm v,132,v,72,a,96\nrt v,104,123,61440\nsa m0,s50,81920\n"
"s_mov_b32 s49, 901120\ndl 118,56,49\nwl 13\nmm v,132,v,76,a,104\nrt v,106,123,63488\ns_mov_b32 s49, 149504\n"
"buffer_load_dwordx4 a[148:151], %10, s[44:47], s49 offen sc1\nmm v,140,v,72,a,100\nrt v,108,125,4096\nmm v,140,v,76,a,108\nrt v,110,125,6144\nwl 14\n"
"mm v,144,v,80,a,96\nrt v,64,122,61952\nsa m0,s50,86016\ns_mov_b32 s49, 983040\nbuffer_load_dwordx4 %13, s[56:59], s49 offen lds\nwl 13\n"
"mm v,144,v,84,a,104\nrt v,66,122,64000\nmm v,152,v,80,a,100\nrt v,68,124,4608\nmm v,152,v,84,a,108\nrt v,70,124,6656\n"
"wl 14\nmm v,148,v,88,a,96\nrt v,72,123,61952\nsa m0,s50,90112\ns_mov_b32 s49, 983168\nbuffer_load_dwordx4 %13, s[56:59], s49 offen lds\n"
"wl 13\nmm v,148,v,92,a,104\nrt v,74,123,64000\nmm v,156,v,88,a,100\nrt v,76,125,4608\nmm v,156,v,92,a,108\n"
"rt v,78,125,6656\nwl 14\nmm v,160,v,96,a,96\nrt v,80,116,8192\nsa m0,s50,94208\ns_mov_b32 s49, 1024000\n"
"buffer_load_dwordx4 %13, s[56:59], s49 offen lds\nwl 13\nmm v,160,v,100,a,104\nrt v,82,116,9216\nmm v,168,v,96,a,100\nrt v,84,116,10240\n"
"mm v,168,v,100,a,108\nrt v,86,116,11264\nwl 14\nmm v,164,v,104,a,96\ns_barrier\nrt v,88,117,8192\n"
"wl 13\nmm v,164,v,108,a,104\nrt v,90,117,9216\nsa m0,s50,98304\ns_mov_b32 s49, 1024128\nbuffer_load_dwordx4 %13, s[56:59], s49 offen lds\n"
"mm v,172,v,104,a,100\nrt v,92,117,10240\nmm v,172,v,108,a,108\nrt v,94,117,11264\ns_mov_b32 s49, 19456\nbuffer_load_dwordx4 a[152:155], %10, s[44:47], s49 offen sc1\n"
"wl 14\nmm v,176,v,64,a,96\nrt v,96,124,12288\nwl 13\nmm v,176,v,68,a,104\nrt v,98,124,14336\n"
"s_mov_b32 s49, 150528\nbuffer_load_dwordx4 a[156:159], %10, s[44:47], s49 offen sc1\nmm v,184,v,64,a,100\nrt v,100,124,20480\nmm v,184,v,68,a,108\nrt v,102,124,22528\n"
"wl 14\nmm v,180,v,72,a,96\nrt v,104,125,12288\nwl 13\nmm v,180,v,76,a,104\nrt v,106,125,14336\n"
"mm v,188,v,72,a,100\nrt v,108,125,20480\nmm v,188,v,76,a,108\nrt v,110,125,22528\nwl 14\nmm v,192,v,80,a,96\n"
"rt v,64,124,12800\nwl 13\nmm v,192,v,84,a,104\nrt v,66,124,14848\nmm v,200,v,80,a,100\nrt v,68,124,20992\n"
"mm v,200,v,84,a,108\nrt v,70,124,23040\nwl 14\nmm v,196,v,88,a,96\nrt v,72,125,12800\nwl 13\n"
"mm v,196,v,92,a,104\nrt v,74,125,14848\nmm v,204,v,88,a,100\nrt v,76,125,20992\nmm v,204,v,92,a,108\nrt v,78,125,23040\n"
"s_waitcnt vmcnt(26) lgkmcnt(14)\nmm v,128,v,96,a,112\nrt v,80,124,16384\ns_waitcnt vmcnt(21) lgkmcnt(13)\nmm v,128,v,100,a,120\nrt v,82,124,18432\n"
"mm v,136,v,96,a,116\nrt v,84,124,24576\nwv 19\nmm v,136,v,100,a,124\nrt v,86,124,26624\ns_mov_b32 s49, 20480\n"
"buffer_load_dwordx4 a[160:163], %10, s[44:47], s49 offen sc1\nwl 14\nmm v,132,v,104,a,112\nrt v,88,125,16384\nv_lshlrev_b32 v57, 3, %0\ns_mov_b32 s51, 38912\n"
"buffer_load_dwordx2 v[62:63], v57, s[40:43], s51 offen sc1\nwl 13\nmm v,132,v,108,a,120\nrt v,90,125,18432\nsa m0,s50,102400\ns_mov_b32 s49, 983040\n"
"dl 118,56,49\nmm v,140,v,104,a,116\nrt v,92,125,24576\ns_mov_b32 s49, 151552\nbuffer_load_dwordx4 a[164:167], %10, s[44:47], s49 offen sc1\nmm v,140,v,108,a,124\n"
"rt v,94,125,26624\nwl 14\nmm v,144,v,64,a,112\nrt v,96,124,16896\nsa m0,s50,106496\ns_mov_b32 s49, 1064960\n"
"buffer_load_dwordx4 %13, s[56:59], s49 offen lds\nwl 13\nmm v,144,v,68,a,120\nrt v,98,124,18944\nmm v,152,v,64,a,116\nrt v,100,124,25088\n"
"mm v,152,v,68,a,124\nrt v,102,124,27136\nwl 14\nmm v,148,v,72,a,112\nrt v,104,125,16896\nsa m0,s50,110592\n"
"s_mov_b32 s49, 1065088\nbuffer_load_dwordx4 %13, s[56:59], s49 offen lds\nwl 13\nmm v,148,v,76,a,120\nrt v,106,125,18944\nmm v,156,v,72,a,116\n"
"rt v,108,125,25088\nmm v,156,v,76,a,124\nrt v,110,125,27136\nwl 14\nmm v,160,v,80,a,112\nrt v,64,112,0\n"
"sa m0,s50,114688\ns_mov_b32 s49, 1105920\nbuffer_load_dwordx4 %13, s[56:59], s49 offen lds\nwl 13\nmm v,160,v,84,a,120\nrt v,66,112,1024\n"
"mm v,168,v,80,a,116\nrt v,68,112,2048\nmm v,168,v,84,a,124\nrt v,70,112,3072\nwl 14\nmm v,164,v,88,a,112\n"
"s_barrier\nrt v,72,113,0\nwl 13\nmm v,164,v,92,a,120\nrt v,74,113,1024\nsa m0,s50,118784\n"
"s_mov_b32 s49, 1106048\nbuffer_load_dwordx4 %13, s[56:59], s49 offen lds\nmm v,172,v,88,a,116\nrt v,76,113,2048\nmm v,172,v,92,a,124\nrt v,78,113,3072\n"
"s_mov_b32 s49, 21504\nbuffer_load_dwordx4 a[168:171], %10, s[44:47], s49 offen sc1\nwl 14\nmm v,176,v,96,a,112\nrt v,80,120,4096\nwl 13\n"
"mm v,176,v,100,a,120\nrt v,82,120,6144\ns_mov_b32 s49, 152576\nbuffer_load_dwordx4 a[172:175], %10, s[44:47], s49 offen sc1\nmm v,184,v,96,a,116\nrt v,84,120,12288\n"
"mm v,184,v,100,a,124\nrt v,86,120,14336\nwl 14\nmm v,180,v,104,a,112\nrt v,88,121,4096\nwl 13\n"
"mm v,180,v,108,a,120\nrt v,90,121,6144\nmm v,188,v,104,a,116\nrt v,92,121,12288\nmm v,188,v,108,a,124\nrt v,94,121,14336\n"
"wl 14\nmm v,192,v,64,a,112\nrt v,96,120,4608\nwl 13\nmm v,192,v,68,a,120\nrt v,98,120,6656\n"
"mm v,200,v,64,a,116\nrt v,100,120,12800\nmm v,200,v,68,a,124\nrt v,102,120,14848\nwl 14\nmm v,196,v,72,a,112\n"
"rt v,104,121,4608\nwl 13\nmm v,196,v,76,a,120\nrt v,106,121,6656\nmm v,204,v,72,a,116\nrt v,108,121,12800\n"
"mm v,204,v,76,a,124\nrt v,110,121,14848\ns_waitcnt vmcnt(27) lgkmcnt(14)\nmm v,128,v,80,a,128\nrt v,64,120,8192\ns_waitcnt vmcnt(20) lgkmcnt(13)\n"
"mm v,128,v,84,a,136\nrt v,66,120,10240\nmm v,136,v,80,a,132\nrt v,68,120,16384\nwv 19\nmm v,136,v,84,a,140\n"
"rt v,70,120,18432\ns_mov_b32 s49, 22528\nbuffer_load_dwordx4 a[176:179], %10, s[44:47], s49 offen sc1\nwl 14\nmm v,132,v,88,a,128\nrt v,72,121,8192\n"
"sa m0,s50,122880\ns_mov_b32 s49, 1064960\ndl 118,56,49\nwl 13\nmm v,132,v,92,a,136\nrt v,74,121,10240\n"
"s_mov_b32 s49, 153600\nbuffer_load_dwordx4 a[180:183], %10, s[44:47], s49 offen sc1\nmm v,140,v,88,a,132\nrt v,76,121,16384\nmm v,140,v,92,a,140\nrt v,78,121,18432\n"
"wl 14\nmm v,144,v,96,a,128\nrt v,80,120,8704\nsa m0,s50,126976\ns_mov_b32 s49, 1146880\nbuffer_load_dwordx4 %13, s[56:59], s49 offen lds\n"
"wl 13\nmm v,144,v,100,a,136\nrt v,82,120,10752\nmm v,152,v,96,a,132\nrt v,84,120,16896\nmm v,152,v,100,a,140\n"
"rt v,86,120,18944\nwl 14\nmm v,148,v,104,a,128\nrt v,88,121,8704\nsa m0,s50,131072\ns_mov_b32 s49, 1147008\n"
"buffer_load_dwordx4 %13, s[56:59], s49 offen lds\nwl 13\nmm v,148,v,108,a,136\nrt v,90,121,10752\nmm v,156,v,104,a,132\nrt v,92,121,16896\n"
"mm v,156,v,108,a,140\nrt v,94,121,18944\nwl 14\nmm v,160,v,64,a,128\nrt v,96,112,20480\nsa m0,s50,135168\n"
"s_mov_b32 s49, 1187840\nbuffer_load_dwordx4 %13, s[56:59], s49 offen lds\nwl 13\nmm v,160,v,68,a,136\nrt v,98,112,21504\nmm v,168,v,64,a,132\n"
"rt v,100,112,22528\nmm v,168,v,68,a,140\nrt v,102,112,23552\nwl 14\nmm v,164,v,72,a,128\ns_barrier\n"
"rt v,104,113,20480\nwl 13\nmm v,164,v,76,a,136\nrt v,106,113,21504\nsa m0,s50,139264\ns_mov_b32 s49, 1187968\n"
"buffer_load_dwordx4 %13, s[56:59], s49 offen lds\nmm v,172,v,72,a,132\nrt v,108,113,22528\nmm v,172,v,76,a,140\nrt v,110,113,23552\ns_mov_b32 s49, 23552\n"
"buffer_load_dwordx4 a[184:187], %10, s[44:47], s49 offen sc1\nwl 14\nmm v,176,v,80,a,128\nrt v,64,120,24576\nwl 13\nmm v,176,v,84,a,136\n"
"rt v,66,120,26624\ns_mov_b32 s49, 154624\nbuffer_load_dwordx4 a[188:191], %10, s[44:47], s49 offen sc1\nmm v,184,v,80,a,132\nrt v,68,120,32768\nmm v,184,v,84,a,140\n"
"rt v,70,120,34816\nwl 14\nmm v,180,v,88,a,128\nrt v,72,121,24576\nwl 13\nmm v,180,v,92,a,136\n"
"rt v,74,121,26624\nmm v,188,v,88,a,132\nrt v,76,121,32768\nmm v,188,v,92,a,140\nrt v,78,121,34816\nwl 14\n"
"mm v,192,v,96,a,128\nrt v,80,120,25088\nwl 13\nmm v,192,v,100,a,136\nrt v,82,120,27136\nmm v,200,v,96,a,132\n"
"rt v,84,120,33280\nmm v,200,v,100,a,140\nrt v,86,120,35328\nwl 14\nmm v,196,v,104,a,128\nrt v,88,121,25088\n"
"wl 13\nmm v,196,v,108,a,136\nrt v,90,121,27136\nmm v,204,v,104,a,132\nrt v,92,121,33280\nmm v,204,v,108,a,140\n"
"rt v,94,121,35328\ns_waitcnt vmcnt(27) lgkmcnt(14)\nmm v,128,v,64,a,144\nrt v,96,120,28672\ns_waitcnt vmcnt(20) lgkmcnt(13)\nmm v,128,v,68,a,152\n"
"rt v,98,120,30720\nmm v,136,v,64,a,148\nrt v,100,120,36864\nwv 19\nmm v,136,v,68,a,156\nrt v,102,120,38912\n"
"wv 17\nv_cmp_ne_u64 vcc, %8, v[62:63]\ns_cbranch_vccnz SPIN_o\nSPUN_o:\nwl 14\nmm v,132,v,72,a,144\n"
"rt v,104,121,28672\ns_mov_b32 s49, 24576\nbuffer_load_dwordx4 a[192:195], %10, s[44:47], s49 offen sc1\nwl 13\nmm v,132,v,76,a,152\nrt v,106,121,30720\n"
"sa m0,s50,143360\ns_mov_b32 s49, 1146880\ndl 118,56,49\nmm v,140,v,72,a,148\nrt v,108,121,36864\ns_mov_b32 s49, 155648\n"
"buffer_load_dwordx4 a[196:199], %10, s[44:47], s49 offen sc1\nmm v,140,v,76,a,156\nrt v,110,121,38912\ns_mov_b32 s49, 25600\nbuffer_load_dwordx4 a[200:203], %10, s[44:47], s49 offen sc1\nwl 14\n"
"mm v,144,v,80,a,144\nrt v,64,120,29184\nsa m0,s50,147456\ns_mov_b32 s49, 1228800\nbuffer_load_dwordx4 %13, s[56:59], s49 offen lds\nwl 13\n"
"mm v,144,v,84,a,152\nrt v,66,120,31232\ns_mov_b32 s49, 156672\nbuffer_load_dwordx4 a[204:207], %10, s[44:47], s49 offen sc1\nmm v,152,v,80,a,148\nrt v,68,120,37376\n"
"mm v,152,v,84,a,156\nrt v,70,120,39424\nwl 14\nmm v,148,v,88,a,144\nrt v,72,121,29184\nsa m0,s50,151552\n"
"s_mov_b32 s49, 1228928\nbuffer_load_dwordx4 %13, s[56:59], s49 offen lds\nwl 13\nmm v,148,v,92,a,152\nrt v,74,121,31232\nmm v,156,v,88,a,148\n"
"rt v,76,121,37376\nmm v,156,v,92,a,156\nrt v,78,121,39424\nwl 14\nmm v,160,v,96,a,144\nrt v,80,112,40960\n"
"sa m0,s50,155648\ns_mov_b32 s49, 1269760\nbuffer_load_dwordx4 %13, s[56:59], s49 offen lds\nwl 13\nmm v,160,v,100,a,152\nrt v,82,112,41984\n"
"mm v,168,v,96,a,148\nrt v,84,112,43008\nmm v,168,v,100,a,156\nrt v,86,112,44032\nwl 14\nmm v,164,v,104,a,144\n"
"s_barrier\nrt v,88,113,40960\nwl 13\nmm v,164,v,108,a,152\nrt v,90,113,41984\nsa m0,s50,0\n"
"s_mov_b32 s49, 1269888\nbuffer_load_dwordx4 %13, s[56:59], s49 offen lds\nmm v,172,v,104,a,148\nrt v,92,113,43008\nmm v,172,v,108,a,156\nrt v,94,113,44032\n"
"s_mov_b32 s49, 26624\nbuffer_load_dwordx4 a[208:211], %10, s[44:47], s49 offen sc1\nwl 14\nmm v,176,v,64,a,144\nrt v,96,120,45056\nwl 13\n"
"mm v,176,v,68,a,152\nrt v,98,120,47104\ns_mov_b32 s49, 157696\nbuffer_load_dwordx4 a[212:215], %10, s[44:47], s49 offen sc1\nmm v,184,v,64,a,148\nrt v,100,120,53248\n"
"mm v,184,v,68,a,156\nrt v,102,120,55296\ns_mov_b32 s49, 27648\nbuffer_load_dwordx4 a[216:219], %10, s[44:47], s49 offen sc1\nwl 14\nmm v,180,v,72,a,144\n"
"rt v,104,121,45056\nwl 13\nmm v,180,v,76,a,152\nrt v,106,121,47104\ns_mov_b32 s49, 158720\nbuffer_load_dwordx4 a[220:223], %10, s[44:47], s49 offen sc1\n"
"mm v,188,v,72,a,148\nrt v,108,121,53248\nmm v,188,v,76,a,156\nrt v,110,121,55296\nwl 14\nmm v,192,v,80,a,144\n"
"rt v,64,120,45568\nwl 13\nmm v,192,v,84,a,152\nrt v,66,120,47616\nmm v,200,v,80,a,148\nrt v,68,120,53760\n"
"mm v,200,v,84,a,156\nrt v,70,120,55808\nwl 14\nmm v,196,v,88,a,144\nrt v,72,121,45568\nwl 13\n"
"mm v,196,v,92,a,152\nrt v,74,121,47616\nmm v,204,v,88,a,148\nrt v,76,121,53760\nmm v,204,v,92,a,156\nrt v,78,121,55808\n"
"wl 14\nmm v,128,v,96,a,160\nrt v,80,120,49152\ns_waitcnt vmcnt(23) lgkmcnt(13)\nmm v,128,v,100,a,168\nrt v,82,120,51200\n"
"mm v,136,v,96,a,164\nrt v,84,120,57344\nwv 22\nmm v,136,v,100,a,172\nrt v,86,120,59392\ns_mov_b32 s49, 28672\n"
"buffer_load_dwordx4 a[224:227], %10, s[44:47], s49 offen sc1\nwl 14\nmm v,132,v,104,a,160\nrt v,88,121,49152\nsa m0,s50,4096\ns_mov_b32 s49, 1228800\n"
"dl 118,56,49\nwl 13\nmm v,132,v,108,a,168\nrt v,90,121,51200\ns_mov_b32 s49, 159744\nbuffer_load_dwordx4 a[228:231], %10, s[44:47], s49 offen sc1\n"
"mm v,140,v,104,a,164\nrt v,92,121,57344\nmm v,140,v,108,a,172\nrt v,94,121,59392\ns_mov_b32 s49, 29696\nbuffer_load_dwordx4 a[232:235], %10, s[44:47], s49 offen sc1\n"
"wl 14\nmm v,144,v,64,a,160\nrt v,96,120,49664\nsa m0,s50,8192\nsa s49,%4,0\ndc 61,40,49\n"
"wl 13\nmm v,144,v,68,a,168\nrt v,98,120,51712\ns_mov_b32 s49, 160768\nbuffer_load_dwordx4 a[236:239], %10, s[44:47], s49 offen sc1\nmm v,152,v,64,a,164\n"
"rt v,100,120,57856\nmm v,152,v,68,a,172\nrt v,102,120,59904\nwl 14\nmm v,148,v,72,a,160\nrt v,104,121,49664\n"
"sa m0,s50,12288\nsa s49,%4,4096\ndc 61,40,49\nwl 13\nmm v,148,v,76,a,168\nrt v,106,121,51712\n"
"mm v,156,v,72,a,164\nrt v,108,121,57856\nmm v,156,v,76,a,172\nrt v,110,121,59904\nwl 14\nmm v,160,v,80,a,160\n"
"rt v,64,112,61440\nsa m0,s50,16384\nsa s49,%4,8192\ndc 61,40,49\nwl 13\nmm v,160,v,84,a,168\n"
"rt v,66,112,62464\nmm v,168,v,80,a,164\nrt v,68,112,63488\nmm v,168,v,84,a,172\nrt v,70,112,64512\nwl 14\n"
"mm v,164,v,88,a,160\ns_barrier\nrt v,72,113,61440\nwl 13\nmm v,164,v,92,a,168\nrt v,74,113,62464\n"
"sa m0,s50,20480\nsa s49,%4,12288\ndc 61,40,49\nmm v,172,v,88,a,164\nrt v,76,113,63488\nmm v,172,v,92,a,172\n"
"rt v,78,113,64512\ns_mov_b32 s49, 30720\nbuffer_load_dwordx4 a[240:243], %10, s[44:47], s49 offen sc1\nwl 14\nmm v,176,v,96,a,160\nrt v,80,122,0\n"
"wl 13\nmm v,176,v,100,a,168\nrt v,82,122,2048\ns_mov_b32 s49, 161792\nbuffer_load_dwordx4 a[244:247], %10, s[44:47], s49 offen sc1\nmm v,184,v,96,a,164\n"
"rt v,84,122,8192\nmm v,184,v,100,a,172\nrt v,86,122,10240\ns_mov_b32 s49, 31744\nbuffer_load_dwordx4 a[248:251], %10, s[44:47], s49 offen sc1\nwl 14\n"
"mm v,180,v,104,a,160\nrt v,88,123,0\nwl 13\nmm v,180,v,108,a,168\nrt v,90,123,2048\ns_mov_b32 s49, 162816\n"
"buffer_load_dwordx4 a[252:255], %10, s[44:47], s49 offen sc1\nmm v,188,v,104,a,164\nrt v,92,123,8192\nmm v,188,v,108,a,172\nrt v,94,123,10240\nwl 14\n"
"mm v,192,v,64,a,160\nrt v,96,122,512\nwl 13\nmm v,192,v,68,a,168\nrt v,98,122,2560\nmm v,200,v,64,a,164\n"
"rt v,100,122,8704\nmm v,200,v,68,a,172\nrt v,102,122,10752\nwl 14\nmm v,196,v,72,a,160\nrt v,104,123,512\n"
"wl 13\nmm v,196,v,76,a,168\nrt v,106,123,2560\nmm v,204,v,72,a,164\nrt v,108,123,8704\nmm v,204,v,76,a,172\n"
"rt v,110,123,10752\ns_waitcnt vmcnt(34) lgkmcnt(14)\nmm v,128,v,80,a,176\nrt v,64,122,4096\ns_waitcnt vmcnt(27) lgkmcnt(13)\nmm v,128,v,84,a,184\n"
"rt v,66,122,6144\nmm v,136,v,80,a,180\nrt v,68,122,12288\nwv 26\nmm v,136,v,84,a,188\nrt v,70,122,14336\n"
"wl 14\nmm v,132,v,88,a,176\nrt v,72,123,4096\nwl 13\nmm v,132,v,92,a,184\nrt v,74,123,6144\n"
"mm v,140,v,88,a,180\nrt v,76,123,12288\nmm v,140,v,92,a,188\nrt v,78,123,14336\nwl 14\nmm v,144,v,96,a,176\n"
"rt v,80,122,4608\nwl 13\nmm v,144,v,100,a,184\nrt v,82,122,6656\nmm v,152,v,96,a,180\nrt v,84,122,12800\n"
"mm v,152,v,100,a,188\nrt v,86,122,14848\nwl 14\nmm v,148,v,104,a,176\nrt v,88,123,4608\nwl 13\n"
"mm v,148,v,108,a,184\nrt v,90,123,6656\nmm v,156,v,104,a,180\nrt v,92,123,12800\nmm v,156,v,108,a,188\nrt v,94,123,14848\n"
"wl 14\nmm v,160,v,64,a,176\nrt v,96,114,16384\nwl 13\nmm v,160,v,68,a,184\nrt v,98,114,17408\n"
"mm v,168,v,64,a,180\nrt v,100,114,18432\nmm v,168,v,68,a,188\nrt v,102,114,19456\nwl 14\nmm v,164,v,72,a,176\n"
"rt v,104,115,16384\nwl 13\nmm v,164,v,76,a,184\nrt v,106,115,17408\nmm v,172,v,72,a,180\nrt v,108,115,18432\n"
"mm v,172,v,76,a,188\nrt v,110,115,19456\nwl 14\nmm v,176,v,80,a,176\ns_barrier\nrt v,64,122,20480\n"
"wl 13\nmm v,176,v,84,a,184\nrt v,66,122,22528\nmm v,184,v,80,a,180\nrt v,68,122,28672\nmm v,184,v,84,a,188\n"
"rt v,70,122,30720\nwl 14\nmm v,180,v,88,a,176\nrt v,72,122,40960\nwl 13\nmm v,180,v,92,a,184\n"
"rt v,74,122,43008\ns_barrier\nmm v,188,v,88,a,180\nrt v,76,122,49152\nmm v,188,v,92,a,188\nrt v,78,122,51200\n"
"wl 14\nmm v,192,v,96,a,176\nrt v,80,122,61440\nwl 13\nmm v,192,v,100,a,184\nrt v,82,122,63488\n"
"mm v,200,v,96,a,180\nrt v,84,124,4096\nmm v,200,v,100,a,188\nrt v,86,124,6144\nwl 14\nmm v,196,v,104,a,176\n"
"wv 8\ns_barrier\nrt v,88,124,16384\nwl 13\nmm v,196,v,108,a,184\nrt v,90,124,18432\n"
"mm v,204,v,104,a,180\nrt v,92,124,24576\nmm v,204,v,108,a,188\nrt v,94,124,26624\nwl 14\nmm v,128,v,64,a,192\n"
"rt v,96,123,20480\nwl 13\nmm v,128,v,68,a,200\nrt v,98,123,22528\nmm v,136,v,64,a,196\nrt v,100,123,28672\n"
"mm v,136,v,68,a,204\nrt v,102,123,30720\nwl 14\nmm v,128,v,72,a,208\nrt v,104,123,40960\nwl 13\n"
"mm v,128,v,76,a,216\nrt v,106,123,43008\nmm v,136,v,72,a,212\nrt v,108,123,49152\nmm v,136,v,76,a,220\nrt v,110,123,51200\n"
"wl 14\nmm v,128,v,80,a,224\nrt v,64,123,61440\nwl 13\nmm v,128,v,84,a,232\nrt v,66,123,63488\n"
"mm v,136,v,80,a,228\nrt v,68,125,4096\nwv 7\nmm v,136,v,84,a,236\nrt v,70,125,6144\ns_waitcnt vmcnt(3) lgkmcnt(14)\n"
"mm v,128,v,88,a,240\nrt v,72,125,16384\ns_waitcnt vmcnt(1) lgkmcnt(13)\nmm v,128,v,92,a,248\nrt v,74,125,18432\nmm v,136,v,88,a,244\n"
"rt v,76,125,24576\nwv 0\nmm v,136,v,92,a,252\nrt v,78,125,26624\nwl 14\nmm v,132,v,96,a,192\n"
"rt v,80,122,20992\nwl 13\nmm v,132,v,100,a,200\nrt v,82,122,23040\nmm v,140,v,96,a,196\nrt v,84,122,29184\n"
"mm v,140,v,100,a,204\nrt v,86,122,31232\nwl 14\nmm v,132,v,104,a,208\nrt v,88,122,41472\nwl 13\n"
"mm v,132,v,108,a,216\nrt v,90,122,43520\nmm v,140,v,104,a,212\nrt v,92,122,49664\nmm v,140,v,108,a,220\nrt v,94,122,51712\n"
"wl 14\nmm v,132,v,64,a,224\nrt v,96,122,61952\nwl 13\nmm v,132,v,68,a,232\nrt v,98,122,64000\n"
"mm v,140,v,64,a,228\nrt v,100,124,4608\nmm v,140,v,68,a,236\nrt v,102,124,6656\nwl 14\nmm v,132,v,72,a,240\n"
"rt v,104,124,16896\nwl 13\nmm v,132,v,76,a,248\nrt v,106,124,18944\nmm v,140,v,72,a,244\nrt v,108,124,25088\n"
"mm v,140,v,76,a,252\nrt v,110,124,27136\nwl 14\nmm v,144,v,80,a,192\nrt v,64,123,20992\nwl 13\n"
"mm v,144,v,84,a,200\nrt v,66,123,23040\nmm v,152,v,80,a,196\nrt v,68,123,29184\nmm v,152,v,84,a,204\nrt v,70,123,31232\n"
"wl 14\nmm v,144,v,88,a,208\nrt v,72,123,41472\ncv 128,128,129\ncv 129,130,131\ncv 130,132,133\n"
"cv 131,134,135\ns_mov_b32 s49, 0\nbuffer_store_dwordx4 v[128:131], %12, s[40:43], s49 offen sc1\ncv 136,136,137\nwl 13\nmm v,144,v,92,a,216\n"
"rt v,74,123,43520\ncv 137,138,139\nmm v,152,v,88,a,212\nrt v,76,123,49664\ncv 138,140,141\nmm v,152,v,92,a,220\n"
"rt v,78,123,51712\ncv 139,142,143\nwl 14\nmm v,144,v,96,a,224\nrt v,80,123,61952\ns_mov_b32 s49, 20480\n"
"buffer_store_dwordx4 v[136:139], %12, s[40:43], s49 offen sc1\nwl 13\nmm v,144,v,100,a,232\nrt v,82,123,64000\nmm v,152,v,96,a,228\nrt v,84,125,4608\n"
"mm v,152,v,100,a,236\nrt v,86,125,6656\nwl 14\nmm v,144,v,104,a,240\nrt v,88,125,16896\nsa m0,s50,24576\n"
"sa s49,%4,16384\ndc 61,40,49\nwl 13\nmm v,144,v,108,a,248\nrt v,90,125,18944\nmm v,152,v,104,a,244\n"
"rt v,92,125,25088\nsa m0,s50,28672\nsa s49,%4,20480\ndc 61,40,49\nmm v,152,v,108,a,252\nrt v,94,125,27136\n"
"wl 14\nmm v,148,v,64,a,192\nrt v,96,122,24576\nwl 13\nmm v,148,v,68,a,200\nrt v,98,122,26624\n"
"sa m0,s50,32768\nsa s49,%4,24576\ndc 61,40,49\nmm v,156,v,64,a,196\nrt v,100,122,32768\nmm v,156,v,68,a,204\n"
"rt v,102,122,34816\nwl 14\nmm v,148,v,72,a,208\nrt v,104,122,45056\nsa m0,s50,36864\nsa s49,%4,28672\n"
"dc 61,40,49\nwl 13\nmm v,148,v,76,a,216\nrt v,106,122,47104\nmm v,156,v,72,a,212\nrt v,108,122,53248\n"
"mm v,156,v,76,a,220\nrt v,110,122,55296\nsa m0,s50,40960\nsa s49,%4,32768\ndc 61,40,49\nwl 14\n"
"mm v,148,v,80,a,224\nrt v,64,124,0\nwl 13\nmm v,148,v,84,a,232\nrt v,66,124,2048\nmm v,156,v,80,a,228\n"
"rt v,68,124,8192\nsa m0,s50,45056\nsa s49,%4,36864\ndc 61,40,49\nmm v,156,v,84,a,236\nrt v,70,124,10240\n"
"wl 14\nmm v,148,v,88,a,240\ns_barrier\nrt v,72,124,20480\nwl 13\nmm v,148,v,92,a,248\n"
"rt v,74,124,22528\nsa m0,s50,49152\nsa s49,%4,40960\ndc 61,40,49\nmm v,156,v,88,a,244\nrt v,76,120,0\n"
"mm v,156,v,92,a,252\nrt v,78,120,2048\nwl 14\nmm v,160,v,96,a,192\nrt v,80,123,24576\nsa m0,s50,53248\n"
"sa s49,%4,45056\ndc 61,40,49\nwl 13\nmm v,160,v,100,a,200\nrt v,82,123,26624\nmm v,168,v,96,a,196\n"
"rt v,84,123,32768\nmm v,168,v,100,a,204\nrt v,86,123,34816\nwl 14\nmm v,160,v,104,a,208\nrt v,88,123,45056\n"
"cv 144,144,145\nwl 13\nmm v,160,v,108,a,216\nrt v,90,123,47104\ncv 145,146,147\nmm v,168,v,104,a,212\n"
"rt v,92,123,53248\nsa m0,s50,57344\nsa s49,%4,49152\ndc 61,40,49\nmm v,168,v,108,a,220\nrt v,94,123,55296\n"
"cv 146,148,149\nwl 14\nmm v,160,v,64,a,224\nrt v,96,125,0\ncv 147,150,151\nwl 13\n"
"mm v,160,v,68,a,232\nrt v,98,125,2048\ns_mov_b32 s49, 1024\nbuffer_store_dwordx4 v[144:147], %12, s[40:43], s49 offen sc1\nmm v,168,v,64,a,228\nrt v,100,125,8192\n"
"cv 152,152,153\nmm v,168,v,68,a,236\nrt v,102,125,10240\ncv 153,154,155\nwl 14\nmm v,160,v,72,a,240\n"
"rt v,104,125,20480\ncv 154,156,157\nwl 13\nmm v,160,v,76,a,248\nrt v,106,125,22528\nsa m0,s50,61440\n"
"sa s49,%4,53248\ndc 61,40,49\nmm v,168,v,72,a,244\nrt v,108,121,0\ncv 155,158,159\nmm v,168,v,76,a,252\n"
"rt v,110,121,2048\ns_mov_b32 s49, 21504\nbuffer_store_dwordx4 v[152:155], %12, s[40:43], s49 offen sc1\nwl 14\nmm v,164,v,80,a,192\nrt v,64,122,25088\n"
"wl 13\nmm v,164,v,84,a,200\nrt v,66,122,27136\nmm v,172,v,80,a,196\nrt v,68,122,33280\nmm v,172,v,84,a,204\n"
"rt v,70,122,35328\nwl 14\nmm v,164,v,88,a,208\nrt v,72,122,45568\nsa m0,s50,65536\nsa s49,%4,57344\n"
"dc 61,40,49\nwl 13\nmm v,164,v,92,a,216\nrt v,74,122,47616\nmm v,172,v,88,a,212\nrt v,76,122,53760\n"
"mm v,172,v,92,a,220\nrt v,78,122,55808\nwl 14\nmm v,164,v,96,a,224\nrt v,80,124,512\nsa m0,s50,69632\n"
"sa s49,%4,61440\ndc 61,40,49\nwl 13\nmm v,164,v,100,a,232\nrt v,82,124,2560\nmm v,172,v,96,a,228\n"
"rt v,84,124,8704\nmm v,172,v,100,a,236\nrt v,86,124,10752\nwl 14\nmm v,164,v,104,a,240\nrt v,88,124,20992\n"
"sa m0,s50,73728\nsa s49,%4,65536\ndc 61,40,49\nwl 13\nmm v,164,v,108,a,248\nrt v,90,124,23040\n"
"mm v,172,v,104,a,244\nrt v,92,120,512\nmm v,172,v,108,a,252\nrt v,94,120,2560\nwl 14\nmm v,176,v,64,a,192\n"
"rt v,96,123,25088\nsa m0,s50,77824\nsa s49,%4,69632\ndc 61,40,49\nwl 13\nmm v,176,v,68,a,200\n"
"rt v,98,123,27136\nmm v,184,v,64,a,196\nrt v,100,123,33280\nmm v,184,v,68,a,204\nrt v,102,123,35328\nwl 14\n"
"mm v,176,v,72,a,208\ns_barrier\nrt v,104,123,45568\nwl 13\nmm v,176,v,76,a,216\nrt v,106,123,47616\n"
"sa m0,s50,81920\nsa s49,%4,73728\ndc 61,40,49\nmm v,184,v,72,a,212\nrt v,108,123,53760\ncv 160,160,161\n"
"mm v,184,v,76,a,220\nrt v,110,123,55808\ncv 161,162,163\nwl 14\nmm v,176,v,80,a,224\nrt v,64,125,512\n"
"cv 162,164,165\nwl 13\nmm v,176,v,84,a,232\nrt v,66,125,2560\ncv 163,166,167\nmm v,184,v,80,a,228\n"
"rt v,68,125,8704\ns_mov_b32 s49, 2048\nbuffer_store_dwordx4 v[160:163], %12, s[40:43], s49 offen sc1\nmm v,184,v,84,a,236\nrt v,70,125,10752\ncv 168,168,169\n"
"wl 14\nmm v,176,v,88,a,240\nrt v,72,125,20992\ncv 169,170,171\nwl 13\nmm v,176,v,92,a,248\n"
"rt v,74,125,23040\ncv 170,172,173\nmm v,184,v,88,a,244\nrt v,76,121,512\ncv 171,174,175\nmm v,184,v,92,a,252\n"
"rt v,78,121,2560\ns_mov_b32 s49, 22528\nbuffer_store_dwordx4 v[168:171], %12, s[40:43], s49 offen sc1\nwl 14\nmm v,180,v,96,a,192\nrt v,80,114,36864\n"
"wl 13\nmm v,180,v,100,a,200\nrt v,82,114,37888\nmm v,188,v,96,a,196\nrt v,84,114,38912\nmm v,188,v,100,a,204\n"
"rt v,86,114,39936\nwl 14\nmm v,180,v,104,a,208\nrt v,88,114,57344\nwl 13\nmm v,180,v,108,a,216\n"
"rt v,90,114,58368\nmm v,188,v,104,a,212\nrt v,92,114,59392\nmm v,188,v,108,a,220\nrt v,94,114,60416\nwl 14\n"
"mm v,180,v,64,a,224\nrt v,96,116,12288\nwl 13\nmm v,180,v,68,a,232\nrt v,98,116,13312\nmm v,188,v,64,a,228\n"
"rt v,100,116,14336\nmm v,188,v,68,a,236\nrt v,102,116,15360\nwl 14\nmm v,180,v,72,a,240\nrt v,104,112,4096\n"
"wl 13\nmm v,180,v,76,a,248\nrt v,106,112,5120\nmm v,188,v,72,a,244\nrt v,108,112,6144\nmm v,188,v,76,a,252\n"
"rt v,110,112,7168\nwl 14\nmm v,192,v,80,a,192\nrt v,64,115,36864\nwl 13\nmm v,192,v,84,a,200\n"
"rt v,66,115,37888\nmm v,200,v,80,a,196\nrt v,68,115,38912\nmm v,200,v,84,a,204\nrt v,70,115,39936\nwl 14\n"
"mm v,192,v,88,a,208\nrt v,72,115,57344\ncv 176,176,177\nwl 13\nmm v,192,v,92,a,216\nrt v,74,115,58368\n"
"cv 177,178,179\nmm v,200,v,88,a,212\nrt v,76,115,59392\ncv 178,180,181\nmm v,200,v,92,a,220\nrt v,78,115,60416\n"
"cv 179,182,183\nwl 14\nmm v,192,v,96,a,224\nrt v,80,117,12288\ns_mov_b32 s49, 3072\nbuffer_store_dwordx4 v[176:179], %12, s[40:43], s49 offen sc1\n"
"wl 13\nmm v,192,v,100,a,232\nrt v,82,117,13312\ncv 184,184,185\nmm v,200,v,96,a,228\nrt v,84,117,14336\n"
"cv 185,186,187\nmm v,200,v,100,a,236\nrt v,86,117,15360\ncv 186,188,189\nwl 14\nmm v,192,v,104,a,240\n"
"wv 19\ns_barrier\nrt v,88,113,4096\nwl 13\nmm v,192,v,108,a,248\nrt v,90,113,5120\n"
"cv 187,190,191\nmm v,200,v,104,a,244\nrt v,92,113,6144\ns_mov_b32 s49, 23552\nbuffer_store_dwordx4 v[184:187], %12, s[40:43], s49 offen sc1\nmm v,200,v,108,a,252\n"
"rt v,94,113,7168\nwl 14\nmm v,196,v,64,a,192\nwl 12\nmm v,196,v,68,a,200\nmm v,204,v,64,a,196\n"
"mm v,204,v,68,a,204\nwl 10\nmm v,196,v,72,a,208\nwl 8\nmm v,196,v,76,a,216\nmm v,204,v,72,a,212\n"
"mm v,204,v,76,a,220\nwl 6\nmm v,196,v,80,a,224\nwl 4\nmm v,196,v,84,a,232\nmm v,204,v,80,a,228\n"
"mm v,204,v,84,a,236\nwl 2\nmm v,196,v,88,a,240\nwl 0\nmm v,196,v,92,a,248\nmm v,204,v,88,a,244\n"
"mm v,204,v,92,a,252\ns_barrier\ns_nop 5\ncv 192,192,193\ncv 193,194,195\ncv 194,196,197\n"
"cv 195,198,199\ns_mov_b32 s49, 0\nbuffer_store_dwordx4 v[192:195], %14, s[40:43], s49 offen sc1\ncv 200,200,201\ncv 201,202,203\ncv 202,204,205\n"
"cv 203,206,207\ns_mov_b32 s49, 20480\nbuffer_store_dwordx4 v[200:203], %14, s[40:43], s49 offen sc1\ns_branch DONE_o\nSPIN_o:\ns_mov_b32 s54, 0x10000\n"
"SPINL_o:\nbuffer_load_dwordx2 v[62:63], v57, s[40:43], s51 offen sc1\ns_sub_u32 s54, s54, 1\nwv 0\ns_cbranch_scc1 SPUN_o\nv_cmp_ne_u64 vcc, %8, v[62:63]\n"
"s_cbranch_vccnz SPINL_o\ns_branch SPUN_o\nDONE_o:\n"
    :: "v"(tid), "s"(ws), "s"(wop), "s"(wosz), "s"(o1), "s"(o2), "s"(ws + WS_ctx + (size_t)rbk * 1048576 + sp * 32768), "s"(1048576 - sp * 32768),
      "s"(tag), "s"(F_N + b * 8), "v"(vo), "v"(vso), "v"(vp + (c4 & 1) * 1024), "v"(vsw), "v"(vp + ((c4 & 1) ? 0 : 4096)) : "v57", "v58", "v59", "v60", "v61", "v62", "v63", "v64", "v65", "v66", "v67", "v68", "v69", "v70", "v71", "v72", "v73", "v74", "v75", "v76", "v77", "v78", "v79", "v80", "v81", "v82", "v83", "v84", "v85", "v86", "v87", "v88", "v89", "v90", "v91", "v92", "v93", "v94", "v95", "v96", "v97", "v98", "v99", "v100", "v101", "v102", "v103", "v104", "v105", "v106", "v107", "v108", "v109", "v110", "v111", "v112", "v113", "v114", "v115", "v116", "v117", "v118", "v119", "v120", "v121", "v122", "v123", "v124", "v125", "v128", "v129", "v130", "v131", "v132", "v133", "v134", "v135", "v136", "v137", "v138", "v139", "v140", "v141", "v142", "v143", "v144", "v145", "v146", "v147", "v148", "v149", "v150", "v151", "v152", "v153", "v154", "v155", "v156", "v157", "v158", "v159", "v160", "v161", "v162", "v163", "v164", "v165", "v166", "v167", "v168", "v169", "v170", "v171", "v172", "v173", "v174", "v175", "v176", "v177", "v178", "v179", "v180", "v181", "v182", "v183", "v184", "v185", "v186", "v187", "v188", "v189", "v190", "v191", "v192", "v193", "v194", "v195", "v196", "v197", "v198", "v199", "v200", "v201", "v202", "v203", "v204", "v205", "v206", "v207", "a0", "a1", "a2", "a3", "a4", "a5", "a6", "a7", "a8", "a9", "a10", "a11", "a12", "a13", "a14", "a15", "a16", "a17", "a18", "a19", "a20", "a21", "a22", "a23", "a24", "a25", "a26", "a27", "a28", "a29", "a30", "a31", "a32", "a33", "a34", "a35", "a36", "a37", "a38", "a39", "a40", "a41", "a42", "a43", "a44", "a45", "a46", "a47", "a48", "a49", "a50", "a51", "a52", "a53", "a54", "a55", "a56", "a57", "a58", "a59", "a60", "a61", "a62", "a63", "a64", "a65", "a66", "a67", "a68", "a69", "a70", "a71", "a72", "a73", "a74", "a75", "a76", "a77", "a78", "a79", "a80", "a81", "a82", "a83", "a84", "a85", "a86", "a87", "a88", "a89", "a90", "a91", "a92", "a93", "a94", "a95", "a96", "a97", "a98", "a99", "a100", "a101", "a102", "a103", "a104", "a105", "a106", "a107", "a108", "a109", "a110", "a111", "a112", "a113", "a114", "a115", "a116", "a117", "a118", "a119", "a120", "a121", "a122", "a123", "a124", "a125", "a126", "a127", "a128", "a129", "a130", "a131", "a132", "a133", "a134", "a135", "a136", "a137", "a138", "a139", "a140", "a141", "a142", "a143", "a144", "a145", "a146", "a147", "a148", "a149", "a150", "a151", "a152", "a153", "a154", "a155", "a156", "a157", "a158", "a159", "a160", "a161", "a162", "a163", "a164", "a165", "a166", "a167", "a168", "a169", "a170", "a171", "a172", "a173", "a174", "a175", "a176", "a177", "a178", "a179", "a180", "a181", "a182", "a183", "a184", "a185", "a186", "a187", "a188", "a189", "a190", "a191", "a192", "a193", "a194", "a195", "a196", "a197", "a198", "a199", "a200", "a201", "a202", "a203", "a204", "a205", "a206", "a207", "a208", "a209", "a210", "a211", "a212", "a213", "a214", "a215", "a216", "a217", "a218", "a219", "a220", "a221", "a222", "a223", "a224", "a225", "a226", "a227", "a228", "a229", "a230", "a231", "a232", "a233", "a234", "a235", "a236", "a237", "a238", "a239", "a240", "a241", "a242", "a243", "a244", "a245", "a246", "a247", "a248", "a249", "a250", "a251", "a252", "a253", "a254", "a255", "s40", "s41", "s42", "s43", "s44", "s45", "s46", "s47", "s48", "s49", "s50", "s51", "s52", "s53", "s54", "s55", "s56", "s57", "s58", "s59", "vcc", "scc", "m0", "memory");
    KEEP(k0);

    asm volatile("s_waitcnt vmcnt(0)\n s_barrier" ::: "memory");
    if (tid == 0) __hip_atomic_store((u64*)(ws + F_O + (rbk * 16 + sp * 4 + c4) * FS), tag, __ATOMIC_RELAXED, __HIP_MEMORY_SCOPE_AGENT);
  }
  {
    const int tok = (xc * 32 + i) * 8 + (tid & 7), c = tid >> 3;
    const bool third = c < 16;
    f32x4 xv[6], ga[6], sb[6], cb[6]; unsigned fo[3], mo[3];
#pragma unroll
    for (int k = 0; k < 3; ++k) { const int q = (k < 2 || third) ? c + 32 * k : c, col = (q >> 2) * 32 + (q & 3) * 4;
      fo[k] = ((tok >> 4) * 20 + (q >> 2)) * 1024 + ((q & 3) * 16 + (tok & 15)) * 16;
#pragma unroll
      for (int h = 0; h < 2; ++h) { xv[2 * k + h] = up4(*(const u32x2*)(x + (size_t)tok * 640 + col + 16 * h)); }
      mo[k] = WS_mod + col * 4; }
#pragma unroll
    for (int k = 0; k < 6; ++k) { ga[k] = ldf4(rs, mo[k >> 1] + 64 * (k & 1)); sb[k] = ldf4(rs, mo[k >> 1] + 2560 + 64 * (k & 1)); cb[k] = ldf4(rs, mo[k >> 1] + 5120 + 64 * (k & 1)); }
    WREQ(86016, o1, 77824); WREQ(90112, o1, 81920); WREQ(94208, o1, 86016); WREQ(98304, o1, 90112); WREQ(102400, o1, 94208); WREQ(106496, o1, 98304); WREQ(110592, o2, 0); WREQ(114688, o2, 4096); WREQ(118784, o2, 8192); WREQ(122880, o2, 12288);
    const Polls k1 = wait_tag(ws, F_O + ((tok >> 7) * 16 + (lane & 15)) * FS, tag, w1k, 0);
    asm volatile("s_barrier" ::: "memory");
    u32x4 pv[3][4];
#pragma unroll
    for (int k = 0; k < 3; ++k)
#pragma unroll
      for (int s = 0; s < 4; ++s) pv[k][s] = __builtin_amdgcn_raw_buffer_load_b128(rs, (unsigned)WS_part + s * 2621440 + fo[k], 0, 16);
    __builtin_amdgcn_sched_barrier(0);
#pragma unroll
    for (int k = 0; k < 6; ++k) {
      ga[k] = up4(pk4(ga[k])); sb[k] = up4(pk4(sb[k])); cb[k] = up4(pk4(cb[k])) + 1.f; asm volatile("" : "+v"(ga[k]), "+v"(sb[k]), "+v"(cb[k]), "+v"(xv[k])); }
    __builtin_amdgcn_sched_barrier(0);
    ldsf red = (ldsf)(156 * 1024);
    u32x2 q1[6], q2[6];
#pragma unroll
    for (int k = 0; k < 3; ++k)
#pragma unroll
      for (int h = 0; h < 2; ++h) { f32x4 at = up4((u32x2){pv[k][0][2 * h], pv[k][0][2 * h + 1]});
#pragma unroll
        for (int s = 1; s < 4; ++s) ACC4(at, pv[k][s][2 * h], pv[k][s][2 * h + 1]);
        q1[2 * k + h] = pk4(xv[2 * k + h] + ga[2 * k + h] * at); xv[2 * k + h] = up4(q1[2 * k + h]); }
    float mean, rstd;
    asm volatile("" :: "v"(xv[5])); KEEP(k1);

    WREQ(126976, o2, 16384); WREQ(131072, o2, 20480); WREQ(135168, o2, 24576);
    rowstat(tid, xv, third, red, mean, rstd);
    WREQ(139264, o2, 28672); WREQ(143360, o2, 32768); WREQ(147456, o2, 36864);
#pragma unroll
    for (int k = 0; k < 6; ++k) { const f32x4 e = cb[k] * rstd; q2[k] = pk4(__builtin_elementwise_fma(xv[k] + (f32x4)(-mean), e, sb[k])); xv[k] = up4(q2[k]); }
    WREQ(151552, o2, 40960); WREQ(155648, o2, 45056); WREQ(0, o2, 49152); WREQ(4096, o2, 53248);
    rowstat(tid, xv, third, red + 64, mean, rstd);
#pragma unroll
    for (int k = 0; k < 3; ++k) { const u32x2 a0 = pk4((xv[2 * k] + (f32x4)(-mean)) * rstd), a1 = pk4((xv[2 * k + 1] + (f32x4)(-mean)) * rstd); if (k == 2 && !third) fo[2] = 0x10000000u;
      __builtin_amdgcn_raw_buffer_store_b128((u32x4){a0[0], a0[1], a1[0], a1[1]}, rs, (unsigned)WS_af + fo[k], 0, 16); }
    asm volatile("" ::: "memory");
#pragma unroll
    for (int k = 0; k < 3; ++k) {
      __builtin_amdgcn_raw_buffer_store_b128((u32x4){q2[2 * k][0], q2[2 * k][1], q2[2 * k + 1][0], q2[2 * k + 1][1]}, rs, (unsigned)WS_h2 + fo[k], 0, 16);
      __builtin_amdgcn_raw_buffer_store_b128((u32x4){q1[2 * k][0], q1[2 * k][1], q1[2 * k + 1][0], q1[2 * k + 1][1]}, rs, (unsigned)WS_x1 + fo[k], 0, 16); }
  }
  const unsigned to = rb * 20480 + lane * 16;
  u32x2 rh[5], rx[5], ry[5];
  {
    f32x4 acc[5];
    asm volatile(
"s_mov_b64 s[52:53], %12\nv_and_b32 v58, 63, %5\nv_lshrrev_b32 v59, 6, %5\nv_lshlrev_b32 v58, 4, v58\nv_readfirstlane_b32 s48, v59\ns_mov_b64 s[40:41], %6\n"
"s_and_b32 s41, s41, 0xffff\ns_mov_b32 s42, 134217728\ns_mov_b32 s43, 0x20000\ns_mov_b64 s[44:45], %10\ns_and_b32 s45, s45, 0xffff\ns_mov_b32 s46, %11\n"
"s_mov_b32 s47, 0x20000\ns_lshl_b32 s50, s48, 10\nv_add_u32 v61, s50, v58\nv_add_u32 v59, 0x10000, v58\nv_add_u32 v60, 0x20000, v58\nrd a,80,58,8192\n"
"rd a,84,58,9216\nrd a,88,58,10240\nrd a,92,58,11264\nrd a,96,58,12288\nrd a,100,58,13312\nrd a,104,58,14336\n"
"rd a,108,58,15360\nrd a,112,58,16384\nrd a,116,58,17408\nrd a,120,58,18432\nrd a,124,58,19456\nrd a,128,58,20480\n"
"rd a,132,58,21504\nrd a,136,58,22528\nrd a,140,58,23552\nrd a,144,58,24576\nrd a,148,58,25600\nrd a,152,58,26624\n"
"rd a,156,58,27648\nrd a,160,58,28672\nrd a,164,58,29696\nrd a,168,58,30720\nrd a,172,58,31744\nrd a,176,58,32768\n"
"rd a,180,58,33792\nrd a,184,58,34816\nrd a,188,58,35840\nrd a,192,58,36864\nrd a,196,58,37888\nrd a,200,58,38912\n"
"rd a,204,58,39936\nrd a,208,58,40960\nrd a,212,58,41984\nrd a,216,58,43008\nrd a,220,58,44032\nrd a,224,58,45056\n"
"rd a,228,58,46080\nrd a,232,58,47104\nrd a,236,58,48128\nrd a,240,58,49152\nrd a,244,58,50176\nrd a,248,58,51200\n"
"rd a,252,58,52224\nrd v,64,58,53248\nrd v,68,58,54272\nrd v,72,58,55296\nrd v,76,58,56320\nrd v,80,58,57344\n"
"rd v,84,58,58368\nrd v,88,58,59392\nrd v,92,58,60416\nrd v,96,58,61440\nrd v,100,58,62464\ns_waitcnt vmcnt(6) lgkmcnt(0)\n"
"s_barrier\ns_cmp_eq_u32 s48, 0\ns_cbranch_scc0 NOPUB_w1\nv_mov_b32 v62, s52\nv_mov_b32 v63, s53\nv_mov_b32 v57, 0\n"
"s_mov_b64 exec, 1\nbuffer_store_dwordx2 v[62:63], v57, s[40:43], %13 offen sc1\ns_mov_b64 exec, -1\nNOPUB_w1:\nsa m0,s50,8192\nsa s49,%8,57344\n"
"dc 61,40,49\nsa m0,s50,12288\nsa s49,%8,61440\ndc 61,40,49\nsa m0,s50,16384\nsa s49,%8,65536\n"
"dc 61,40,49\nsa m0,s50,20480\nsa s49,%8,69632\ndc 61,40,49\nsa m0,s50,24576\nsa s49,%8,73728\n"
"dc 61,40,49\nsa m0,s50,28672\nsa s49,%8,77824\ndc 61,40,49\nsa m0,s50,32768\nsa s49,%8,81920\n"
"dc 61,40,49\nsa m0,s50,36864\nsa s49,%8,86016\ndc 61,40,49\nsa m0,s50,40960\nsa s49,%8,90112\n"
"dc 61,40,49\nsa m0,s50,45056\nsa s49,%8,94208\ndc 61,40,49\nsa m0,s50,49152\nsa s49,%8,98304\n"
"dc 61,40,49\ns_cmp_eq_u32 s48, 0\ns_cbranch_scc0 TRICKLE_w1\ns_mov_b32 s51, 0x10000\nSPINL_w1:\nbuffer_load_dwordx2 v[48:49], %14, s[40:43], 0 offen sc1\n"
"wv 0\nv_cmp_ne_u64 vcc, %12, v[48:49]\ns_cbranch_vccz POLLED_w1\ns_sub_u32 s51, s51, 1\ns_cbranch_scc0 SPINL_w1\ns_branch POLLED_w1\n"
"TRICKLE_w1:\nPOLLED_w1:\ns_barrier\ns_mov_b32 s49, 0\nbuffer_load_dwordx4 a[0:3], %15, s[44:47], s49 offen sc1\ns_mov_b32 s49, 1024\n"
"buffer_load_dwordx4 a[4:7], %15, s[44:47], s49 offen sc1\ns_mov_b32 s49, 2048\nbuffer_load_dwordx4 a[8:11], %15, s[44:47], s49 offen sc1\ns_mov_b32 s49, 3072\nbuffer_load_dwordx4 a[12:15], %15, s[44:47], s49 offen sc1\ns_mov_b32 s49, 4096\n"
"buffer_load_dwordx4 a[16:19], %15, s[44:47], s49 offen sc1\ns_mov_b32 s49, 5120\nbuffer_load_dwordx4 a[20:23], %15, s[44:47], s49 offen sc1\ns_mov_b32 s49, 6144\nbuffer_load_dwordx4 a[24:27], %15, s[44:47], s49 offen sc1\ns_mov_b32 s49, 7168\n"
"buffer_load_dwordx4 a[28:31], %15, s[44:47], s49 offen sc1\ns_mov_b32 s49, 8192\nbuffer_load_dwordx4 a[32:35], %15, s[44:47], s49 offen sc1\ns_mov_b32 s49, 9216\nbuffer_load_dwordx4 a[36:39], %15, s[44:47], s49 offen sc1\ns_mov_b32 s49, 10240\n"
"buffer_load_dwordx4 a[40:43], %15, s[44:47], s49 offen sc1\ns_mov_b32 s49, 11264\nbuffer_load_dwordx4 a[44:47], %15, s[44:47], s49 offen sc1\nwv 11\nmq %0,a,80,a,0\ns_mov_b32 s49, 12288\n"
"buffer_load_dwordx4 a[48:51], %15, s[44:47], s49 offen sc1\nmq %1,a,84,a,0\nmq %2,a,88,a,0\nmq %3,a,92,a,0\nmq %4,a,96,a,0\nwv 11\n"
"mp %0,a,100,a,4\ns_mov_b32 s49, 13312\nbuffer_load_dwordx4 a[52:55], %15, s[44:47], s49 offen sc1\nmp %1,a,104,a,4\nmp %2,a,108,a,4\nmp %3,a,112,a,4\n"
"mp %4,a,116,a,4\nwv 11\nmp %0,a,120,a,8\ns_mov_b32 s49, 14336\nbuffer_load_dwordx4 a[56:59], %15, s[44:47], s49 offen sc1\nmp %1,a,124,a,8\n"
"mp %2,a,128,a,8\nmp %3,a,132,a,8\nmp %4,a,136,a,8\nwv 11\nmp %0,a,140,a,12\ns_mov_b32 s49, 15360\n"
"buffer_load_dwordx4 a[60:63], %15, s[44:47], s49 offen sc1\nmp %1,a,144,a,12\nmp %2,a,148,a,12\nmp %3,a,152,a,12\nmp %4,a,156,a,12\nwv 11\n"
"mp %0,a,160,a,16\ns_mov_b32 s49, 16384\nbuffer_load_dwordx4 a[64:67], %15, s[44:47], s49 offen sc1\nmp %1,a,164,a,16\nmp %2,a,168,a,16\nmp %3,a,172,a,16\n"
"mp %4,a,176,a,16\nwv 11\nmp %0,a,180,a,20\ns_mov_b32 s49, 17408\nbuffer_load_dwordx4 a[68:71], %15, s[44:47], s49 offen sc1\nmp %1,a,184,a,20\n"
"mp %2,a,188,a,20\nmp %3,a,192,a,20\nmp %4,a,196,a,20\nwv 11\nmp %0,a,200,a,24\ns_mov_b32 s49, 18432\n"
"buffer_load_dwordx4 a[72:75], %15, s[44:47], s49 offen sc1\nmp %1,a,204,a,24\nmp %2,a,208,a,24\nmp %3,a,212,a,24\nmp %4,a,216,a,24\nwv 11\n"
"mp %0,a,220,a,28\ns_mov_b32 s49, 19456\nbuffer_load_dwordx4 a[76:79], %15, s[44:47], s49 offen sc1\nmp %1,a,224,a,28\nmp %2,a,228,a,28\nmp %3,a,232,a,28\n"
"mp %4,a,236,a,28\nwv 11\nmp %0,a,240,a,32\nmp %1,a,244,a,32\nmp %2,a,248,a,32\nmp %3,a,252,a,32\n"
"mp %4,v,64,a,32\nrd v,104,58,63488\nwv 10\nmp %0,v,68,a,36\nrd v,108,58,64512\nmp %1,v,72,a,36\n"
"rd v,64,59,0\nmp %2,v,76,a,36\nrd v,68,59,1024\nmp %3,v,80,a,36\nrd v,72,59,2048\nmp %4,v,84,a,36\n"
"rd v,76,59,3072\nwv 9\nmp %0,v,88,a,40\nrd v,80,59,4096\nmp %1,v,92,a,40\nrd v,84,59,5120\n"
"mp %2,v,96,a,40\nrd v,88,59,6144\nmp %3,v,100,a,40\nrd v,92,59,7168\nwl 9\nmp %4,v,104,a,40\n"
"rd v,96,59,8192\ns_waitcnt vmcnt(8) lgkmcnt(9)\nmp %0,v,108,a,44\nrd v,100,59,9216\nwl 9\nmp %1,v,64,a,44\n"
"rd v,104,59,10240\nwl 9\nmp %2,v,68,a,44\nrd v,108,59,11264\nwl 9\nmp %3,v,72,a,44\n"
"rd v,64,59,12288\nwl 9\nmp %4,v,76,a,44\nrd v,68,59,13312\ns_waitcnt vmcnt(7) lgkmcnt(9)\nmp %0,v,80,a,48\n"
"rd v,72,59,14336\nwl 9\nmp %1,v,84,a,48\nrd v,76,59,15360\nwl 9\nmp %2,v,88,a,48\n"
"rd v,80,59,16384\nwl 9\nmp %3,v,92,a,48\nrd v,84,59,17408\nwl 9\nmp %4,v,96,a,48\n"
"rd v,88,59,18432\ns_waitcnt vmcnt(6) lgkmcnt(9)\nmp %0,v,100,a,52\nrd v,92,59,19456\nwl 9\nmp %1,v,104,a,52\n"
"rd v,96,59,20480\nwl 9\nmp %2,v,108,a,52\nrd v,100,59,21504\nwl 9\nmp %3,v,64,a,52\n"
"rd v,104,59,22528\nwl 9\nmp %4,v,68,a,52\nrd v,108,59,23552\ns_waitcnt vmcnt(5) lgkmcnt(9)\nmp %0,v,72,a,56\n"
"rd v,64,59,24576\nwl 9\nmp %1,v,76,a,56\nrd v,68,59,25600\nwl 9\nmp %2,v,80,a,56\n"
"rd v,72,59,26624\nwl 9\nmp %3,v,84,a,56\nrd v,76,59,27648\nwl 9\nmp %4,v,88,a,56\n"
"rd v,80,59,28672\ns_waitcnt vmcnt(4) lgkmcnt(9)\nmp %0,v,92,a,60\nrd v,84,59,29696\nwl 9\nmp %1,v,96,a,60\n"
"rd v,88,59,30720\nwl 9\nmp %2,v,100,a,60\nrd v,92,59,31744\nwl 9\nmp %3,v,104,a,60\n"
"rd v,96,59,32768\nwl 9\nmp %4,v,108,a,60\nrd v,100,59,33792\ns_waitcnt vmcnt(3) lgkmcnt(9)\nmp %0,v,64,a,64\n"
"rd v,104,59,34816\nwl 9\nmp %1,v,68,a,64\nrd v,108,59,35840\nwl 9\nmp %2,v,72,a,64\n"
"rd v,64,59,36864\nwl 9\nmp %3,v,76,a,64\nrd v,68,59,37888\nwl 9\nmp %4,v,80,a,64\n"
"rd v,72,59,38912\ns_waitcnt vmcnt(2) lgkmcnt(9)\nmp %0,v,84,a,68\nrd v,76,59,39936\nwl 9\nmp %1,v,88,a,68\n"
"rd v,80,59,40960\nwl 9\nmp %2,v,92,a,68\nrd v,84,59,41984\nwl 9\nmp %3,v,96,a,68\n"
"rd v,88,59,43008\nwl 9\nmp %4,v,100,a,68\nrd v,92,59,44032\ns_waitcnt vmcnt(1) lgkmcnt(9)\nmp %0,v,104,a,72\n"
"wl 8\nmp %1,v,108,a,72\nwl 7\nmp %2,v,64,a,72\nwl 6\nmp %3,v,68,a,72\n"
"wl 5\nmp %4,v,72,a,72\ns_waitcnt vmcnt(0) lgkmcnt(4)\nmp %0,v,76,a,76\nwl 3\nmp %1,v,80,a,76\n"
"wl 2\nmp %2,v,84,a,76\nwl 1\nmp %3,v,88,a,76\nwl 0\nmp %4,v,92,a,76\n"
"s_barrier\ns_nop 6\n"
    : "=&v"(acc[0]), "=&v"(acc[1]), "=&v"(acc[2]), "=&v"(acc[3]), "=&v"(acc[4]) : "v"(tid), "s"(ws), "s"(o1), "s"(o2), "s"(o3), "s"(ws + WS_af), "s"(2621440), "s"(tag), "s"(F_R + (rb64 * 8 + cg) * FS), "v"(F_R + (rb64 * 8 + (lane & 7)) * FS), "v"(to), "s"(rot1(cg) * 1024) : "v48", "v49", "v57", "v58", "v59", "v60", "v61", "v62", "v63", "v64", "v65", "v66", "v67", "v68", "v69", "v70", "v71", "v72", "v73", "v74", "v75", "v76", "v77", "v78", "v79", "v80", "v81", "v82", "v83", "v84", "v85", "v86", "v87", "v88", "v89", "v90", "v91", "v92", "v93", "v94", "v95", "v96", "v97", "v98", "v99", "v100", "v101", "v102", "v103", "v104", "v105", "v106", "v107", "v108", "v109", "v110", "v111", "a0", "a1", "a2", "a3", "a4", "a5", "a6", "a7", "a8", "a9", "a10", "a11", "a12", "a13", "a14", "a15", "a16", "a17", "a18", "a19", "a20", "a21", "a22", "a23", "a24", "a25", "a26", "a27", "a28", "a29", "a30", "a31", "a32", "a33", "a34", "a35", "a36", "a37", "a38", "a39", "a40", "a41", "a42", "a43", "a44", "a45", "a46", "a47", "a48", "a49", "a50", "a51", "a52", "a53", "a54", "a55", "a56", "a57", "a58", "a59", "a60", "a61", "a62", "a63", "a64", "a65", "a66", "a67", "a68", "a69", "a70", "a71", "a72", "a73", "a74", "a75", "a76", "a77", "a78", "a79", "a80", "a81", "a82", "a83", "a84", "a85", "a86", "a87", "a88", "a89", "a90", "a91", "a92", "a93", "a94", "a95", "a96", "a97", "a98", "a99", "a100", "a101", "a102", "a103", "a104", "a105", "a106", "a107", "a108", "a109", "a110", "a111", "a112", "a113", "a114", "a115", "a116", "a117", "a118", "a119", "a120", "a121", "a122", "a123", "a124", "a125", "a126", "a127", "a128", "a129", "a130", "a131", "a132", "a133", "a134", "a135", "a136", "a137", "a138", "a139", "a140", "a141", "a142", "a143", "a144", "a145", "a146", "a147", "a148", "a149", "a150", "a151", "a152", "a153", "a154", "a155", "a156", "a157", "a158", "a159", "a160", "a161", "a162", "a163", "a164", "a165", "a166", "a167", "a168", "a169", "a170", "a171", "a172", "a173", "a174", "a175", "a176", "a177", "a178", "a179", "a180", "a181", "a182", "a183", "a184", "a185", "a186", "a187", "a188", "a189", "a190", "a191", "a192", "a193", "a194", "a195", "a196", "a197", "a198", "a199", "a200", "a201", "a202", "a203", "a204", "a205", "a206", "a207", "a208", "a209", "a210", "a211", "a212", "a213", "a214", "a215", "a216", "a217", "a218", "a219", "a220", "a221", "a222", "a223", "a224", "a225", "a226", "a227", "a228", "a229", "a230", "a231", "a232", "a233", "a234", "a235", "a236", "a237", "a238", "a239", "a240", "a241", "a242", "a243", "a244", "a245", "a246", "a247", "a248", "a249", "a250", "a251", "a252", "a253", "a254", "a255", "s40", "s41", "s42", "s43", "s44", "s45", "s46", "s47", "s48", "s49", "s50", "s51", "s52", "s53", "s54", "vcc", "scc", "m0", "memory");
    u32x2 y[5];
#define G4(v) (u32x2){gelu2((f32x2){v[0], v[1]}), gelu2((f32x2){v[2], v[3]})}
#define E1(n) y[n] = G4(acc[n]); asm volatile("" : "+v"(y[n]));

    WREQ(53248, o3, 0); E1(0)  WREQ(57344, o3, 4096); E1(1)  WREQ(61440, o3, 8192); E1(2)  WREQ(65536, o3, 12288); E1(3)  WREQ(69632, o3, 16384); WREQ(73728, o3, 20480); E1(4)
    store_tiles<5>(y, cg * 5, 5, rs, (unsigned)WS_g1 + to, lane * 8);
  }
  {
    f32x4 acc[5]; unsigned vo[8]; const int tok = rb * 16 + (lane & 15);
#pragma unroll
    for (int q = 0; q < 4; ++q) { const int qq = (q + rot2(cg) / 5) & 3, t = tok + (qq == 0 ? -1 : qq == 1 ? 1 : qq == 2 ? -2 : 2);
      vo[q] = (t < 0 || t >= 2048) ? 0x10000000u : (unsigned)((t >> 4) * 20480 + (g * 16 + (t & 15)) * 16);
      vo[4 + q] = vo[q] - (g * 16 + (t & 15)) * 8; }
    unsigned ro[5];
#pragma unroll
    for (int n = 0; n < 5; ++n) { const int nt = cg * 5 + n; ro[n] = (unsigned)WS_h2 + to + (nt >> 1) * 1024 + (nt & 1) * 8; }
    const int nb = min(max(rb64 + ((lane >> 3) == 1 ? -1 : (lane >> 3) == 2 ? 1 : 0), 0), 31);
    asm volatile(
"s_mov_b64 s[52:53], %17\nv_and_b32 v58, 63, %10\nv_lshrrev_b32 v59, 6, %10\nv_lshlrev_b32 v58, 4, v58\nv_readfirstlane_b32 s48, v59\ns_mov_b64 s[40:41], %11\n"
"s_and_b32 s41, s41, 0xffff\ns_mov_b32 s42, 134217728\ns_mov_b32 s43, 0x20000\ns_mov_b64 s[44:45], %15\ns_and_b32 s45, s45, 0xffff\ns_mov_b32 s46, %16\n"
"s_mov_b32 s47, 0x20000\ns_lshl_b32 s50, s48, 10\nv_add_u32 v61, s50, v58\nv_add_u32 v59, 0x10000, v58\nv_add_u32 v60, 0x20000, v58\nrd a,80,59,45056\n"
"rd a,84,59,46080\nrd a,88,59,47104\nrd a,92,59,48128\nrd a,96,59,49152\nrd a,100,59,50176\nrd a,104,59,51200\n"
"rd a,108,59,52224\nrd a,112,59,53248\nrd a,116,59,54272\nrd a,120,59,55296\nrd a,124,59,56320\nrd a,128,59,57344\n"
"rd a,132,59,58368\nrd a,136,59,59392\nrd a,140,59,60416\nrd a,144,59,61440\nrd a,148,59,62464\nrd a,152,59,63488\n"
"rd a,156,59,64512\nrd a,160,60,0\nrd a,164,60,1024\nrd a,168,60,2048\nrd a,172,60,3072\nrd a,176,60,4096\n"
"rd a,180,60,5120\nrd a,184,60,6144\nrd a,188,60,7168\nrd a,192,60,8192\nrd a,196,60,9216\nrd a,200,60,10240\n"
"rd a,204,60,11264\nrd a,208,60,12288\nrd a,212,60,13312\nrd a,216,60,14336\nrd a,220,60,15360\nrd a,224,60,16384\n"
"rd a,228,60,17408\nrd a,232,60,18432\nrd a,236,60,19456\nrd a,240,60,20480\nrd a,244,60,21504\nrd a,248,60,22528\n"
"rd a,252,60,23552\nrd v,64,60,24576\nrd v,68,60,25600\nrd v,72,60,26624\nrd v,76,60,27648\nrd v,80,58,0\n"
"rd v,84,58,1024\nrd v,88,58,2048\nrd v,92,58,3072\nrd v,96,58,4096\nrd v,100,58,5120\ns_waitcnt vmcnt(0) lgkmcnt(0)\n"
"s_barrier\ns_cmp_eq_u32 s48, 0\ns_cbranch_scc0 NOPUB_w2\nv_mov_b32 v62, s52\nv_mov_b32 v63, s53\nv_mov_b32 v57, 0\n"
"s_mov_b64 exec, 1\nbuffer_store_dwordx2 v[62:63], v57, s[40:43], %18 offen sc1\ns_mov_b64 exec, -1\nNOPUB_w2:\nsa m0,s50,77824\nsa s49,%13,24576\n"
"dc 61,40,49\nsa m0,s50,81920\nsa s49,%13,28672\ndc 61,40,49\nsa m0,s50,86016\nsa s49,%13,32768\n"
"dc 61,40,49\nsa m0,s50,90112\nsa s49,%13,36864\ndc 61,40,49\nsa m0,s50,94208\nsa s49,%13,40960\n"
"dc 61,40,49\nsa m0,s50,98304\nsa s49,%13,45056\ndc 61,40,49\nsa m0,s50,102400\nsa s49,%13,49152\n"
"dc 61,40,49\nsa m0,s50,106496\nsa s49,%13,53248\ndc 61,40,49\nsa m0,s50,110592\nsa s49,%13,57344\n"
"dc 61,40,49\nsa m0,s50,114688\nsa s49,%13,61440\ndc 61,40,49\nsa m0,s50,118784\nsa s49,%13,65536\n"
"dc 61,40,49\nsa m0,s50,122880\nsa s49,%13,69632\ndc 61,40,49\nsa m0,s50,126976\nsa s49,%13,73728\n"
"dc 61,40,49\ns_cmp_eq_u32 s48, 0\ns_cbranch_scc0 TRICKLE_w2\ns_mov_b32 s51, 0x10000\nSPINL_w2:\nbuffer_load_dwordx2 v[48:49], %19, s[40:43], 0 offen sc1\n"
"wv 0\nv_cmp_ne_u64 vcc, %17, v[48:49]\ns_cbranch_vccz POLLED_w2\ns_sub_u32 s51, s51, 1\ns_cbranch_scc0 SPINL_w2\ns_branch POLLED_w2\n"
"TRICKLE_w2:\nPOLLED_w2:\ns_barrier\ns_mov_b32 s49, 0\nbuffer_load_dwordx4 a[0:3], %20, s[44:47], s49 offen sc1\ns_mov_b32 s49, 1024\n"
"buffer_load_dwordx4 a[4:7], %20, s[44:47], s49 offen sc1\ns_mov_b32 s49, 2048\nbuffer_load_dwordx2 a[8:9], %24, s[44:47], s49 offen sc1\nbuffer_load_dwordx2 a[10:11], %24, s[44:47], s49 offen offset:512 sc1\ns_mov_b32 s49, 3072\nbuffer_load_dwordx4 a[12:15], %20, s[44:47], s49 offen sc1\n"
"s_mov_b32 s49, 4096\nbuffer_load_dwordx4 a[16:19], %20, s[44:47], s49 offen sc1\ns_mov_b32 s49, 5120\nbuffer_load_dwordx4 a[20:23], %21, s[44:47], s49 offen sc1\ns_mov_b32 s49, 6144\nbuffer_load_dwordx4 a[24:27], %21, s[44:47], s49 offen sc1\n"
"s_mov_b32 s49, 7168\nbuffer_load_dwordx2 a[28:29], %25, s[44:47], s49 offen sc1\nbuffer_load_dwordx2 a[30:31], %25, s[44:47], s49 offen offset:512 sc1\ns_mov_b32 s49, 8192\nbuffer_load_dwordx4 a[32:35], %21, s[44:47], s49 offen sc1\ns_mov_b32 s49, 9216\n"
"buffer_load_dwordx4 a[36:39], %21, s[44:47], s49 offen sc1\nbuffer_load_dwordx2 %5, %28, s[40:43], 0 offen sc1\nbuffer_load_dwordx2 %6, %29, s[40:43], 0 offen sc1\nbuffer_load_dwordx2 %7, %30, s[40:43], 0 offen sc1\nbuffer_load_dwordx2 %8, %31, s[40:43], 0 offen sc1\nbuffer_load_dwordx2 %9, %32, s[40:43], 0 offen sc1\n"
"wv 16\nmq %0,a,80,a,0\ns_mov_b32 s49, 10240\nbuffer_load_dwordx4 a[40:43], %22, s[44:47], s49 offen sc1\nmq %1,a,84,a,0\nmq %2,a,88,a,0\n"
"mq %3,a,92,a,0\nmq %4,a,96,a,0\nwv 16\nmp %0,a,100,a,4\ns_mov_b32 s49, 11264\nbuffer_load_dwordx4 a[44:47], %22, s[44:47], s49 offen sc1\n"
"mp %1,a,104,a,4\nmp %2,a,108,a,4\nmp %3,a,112,a,4\nmp %4,a,116,a,4\nwv 15\nmp %0,a,120,a,8\n"
"s_mov_b32 s49, 12288\nbuffer_load_dwordx2 a[48:49], %26, s[44:47], s49 offen sc1\nbuffer_load_dwordx2 a[50:51], %26, s[44:47], s49 offen offset:512 sc1\nmp %1,a,124,a,8\nmp %2,a,128,a,8\nmp %3,a,132,a,8\n"
"mp %4,a,136,a,8\nwv 16\nmp %0,a,140,a,12\ns_mov_b32 s49, 13312\nbuffer_load_dwordx4 a[52:55], %22, s[44:47], s49 offen sc1\nmp %1,a,144,a,12\n"
"mp %2,a,148,a,12\nmp %3,a,152,a,12\nmp %4,a,156,a,12\nwv 16\nmp %0,a,160,a,16\ns_mov_b32 s49, 14336\n"
"buffer_load_dwordx4 a[56:59], %22, s[44:47], s49 offen sc1\nmp %1,a,164,a,16\nmp %2,a,168,a,16\nmp %3,a,172,a,16\nmp %4,a,176,a,16\nwv 16\n"
"mp %0,a,180,a,20\ns_mov_b32 s49, 15360\nbuffer_load_dwordx4 a[60:63], %23, s[44:47], s49 offen sc1\nmp %1,a,184,a,20\nmp %2,a,188,a,20\nmp %3,a,192,a,20\n"
"mp %4,a,196,a,20\nwv 16\nmp %0,a,200,a,24\ns_mov_b32 s49, 16384\nbuffer_load_dwordx4 a[64:67], %23, s[44:47], s49 offen sc1\nmp %1,a,204,a,24\n"
"mp %2,a,208,a,24\nmp %3,a,212,a,24\nmp %4,a,216,a,24\nwv 15\nmp %0,a,220,a,28\ns_mov_b32 s49, 17408\n"
"buffer_load_dwordx2 a[68:69], %27, s[44:47], s49 offen sc1\nbuffer_load_dwordx2 a[70:71], %27, s[44:47], s49 offen offset:512 sc1\nmp %1,a,224,a,28\nmp %2,a,228,a,28\nmp %3,a,232,a,28\nmp %4,a,236,a,28\n"
"wv 16\nmp %0,a,240,a,32\ns_mov_b32 s49, 18432\nbuffer_load_dwordx4 a[72:75], %23, s[44:47], s49 offen sc1\nmp %1,a,244,a,32\nmp %2,a,248,a,32\n"
"mp %3,a,252,a,32\nmp %4,v,64,a,32\nrd v,104,58,6144\nwv 16\nmp %0,v,68,a,36\nrd v,108,58,7168\n"
"s_mov_b32 s49, 19456\nbuffer_load_dwordx4 a[76:79], %23, s[44:47], s49 offen sc1\nmp %1,v,72,a,36\nrd v,64,58,8192\nmp %2,v,76,a,36\nrd v,68,58,9216\n"
"mp %3,v,80,a,36\nrd v,72,58,10240\nmp %4,v,84,a,36\nrd v,76,58,11264\nwv 11\nmp %0,v,88,a,40\n"
"rd v,80,58,12288\nmp %1,v,92,a,40\nrd v,84,58,13312\nmp %2,v,96,a,40\nrd v,88,58,14336\nmp %3,v,100,a,40\n"
"rd v,92,58,15360\nwl 9\nmp %4,v,104,a,40\nrd v,96,58,16384\ns_waitcnt vmcnt(10) lgkmcnt(9)\nmp %0,v,108,a,44\n"
"rd v,100,58,17408\nwl 9\nmp %1,v,64,a,44\nrd v,104,58,18432\nwl 9\nmp %2,v,68,a,44\n"
"rd v,108,58,19456\nwl 9\nmp %3,v,72,a,44\nrd v,64,58,20480\nwl 9\nmp %4,v,76,a,44\n"
"rd v,68,58,21504\ns_waitcnt vmcnt(8) lgkmcnt(9)\nmp %0,v,80,a,48\nrd v,72,58,22528\nwl 9\nmp %1,v,84,a,48\n"
"rd v,76,58,23552\nwl 9\nmp %2,v,88,a,48\nrd v,80,58,24576\nwl 9\nmp %3,v,92,a,48\n"
"rd v,84,58,25600\nwl 9\nmp %4,v,96,a,48\nrd v,88,58,26624\ns_waitcnt vmcnt(7) lgkmcnt(9)\nmp %0,v,100,a,52\n"
"rd v,92,58,27648\nwl 9\nmp %1,v,104,a,52\nrd v,96,58,28672\nwl 9\nmp %2,v,108,a,52\n"
"rd v,100,58,29696\nwl 9\nmp %3,v,64,a,52\nrd v,104,58,30720\nwl 9\nmp %4,v,68,a,52\n"
"rd v,108,58,31744\ns_waitcnt vmcnt(6) lgkmcnt(9)\nmp %0,v,72,a,56\nrd v,64,58,32768\nwl 9\nmp %1,v,76,a,56\n"
"rd v,68,58,33792\nwl 9\nmp %2,v,80,a,56\nrd v,72,58,34816\nwl 9\nmp %3,v,84,a,56\n"
"rd v,76,58,35840\nwl 9\nmp %4,v,88,a,56\nrd v,80,58,36864\ns_waitcnt vmcnt(5) lgkmcnt(9)\nmp %0,v,92,a,60\n"
"rd v,84,58,37888\nwl 9\nmp %1,v,96,a,60\nrd v,88,58,38912\nwl 9\nmp %2,v,100,a,60\n"
"rd v,92,58,39936\nwl 9\nmp %3,v,104,a,60\nrd v,96,58,40960\nwl 9\nmp %4,v,108,a,60\n"
"rd v,100,58,41984\ns_waitcnt vmcnt(4) lgkmcnt(9)\nmp %0,v,64,a,64\nrd v,104,58,43008\nwl 9\nmp %1,v,68,a,64\n"
"rd v,108,58,44032\nwl 9\nmp %2,v,72,a,64\nrd v,64,58,45056\nwl 9\nmp %3,v,76,a,64\n"
"rd v,68,58,46080\nwl 9\nmp %4,v,80,a,64\nrd v,72,58,47104\ns_waitcnt vmcnt(2) lgkmcnt(9)\nmp %0,v,84,a,68\n"
"rd v,76,58,48128\nwl 9\nmp %1,v,88,a,68\nrd v,80,58,49152\nwl 9\nmp %2,v,92,a,68\n"
"rd v,84,58,50176\nwl 9\nmp %3,v,96,a,68\nrd v,88,58,51200\nwl 9\nmp %4,v,100,a,68\n"
"rd v,92,58,52224\ns_waitcnt vmcnt(1) lgkmcnt(9)\nmp %0,v,104,a,72\nwl 8\nmp %1,v,108,a,72\nwl 7\n"
"mp %2,v,64,a,72\nwl 6\nmp %3,v,68,a,72\nwl 5\nmp %4,v,72,a,72\ns_waitcnt vmcnt(0) lgkmcnt(4)\n"
"mp %0,v,76,a,76\nwl 3\nmp %1,v,80,a,76\nwl 2\nmp %2,v,84,a,76\nwl 1\n"
"mp %3,v,88,a,76\nwl 0\nmp %4,v,92,a,76\ns_barrier\nsa m0,s50,131072\nsa s49,%13,77824\n"
"dc 61,40,49\nsa m0,s50,135168\nsa s49,%13,81920\ndc 61,40,49\nsa m0,s50,139264\nsa s49,%13,86016\n"
"dc 61,40,49\nsa m0,s50,143360\nsa s49,%13,90112\ndc 61,40,49\nsa m0,s50,147456\nsa s49,%13,94208\n"
"dc 61,40,49\nsa m0,s50,151552\nsa s49,%13,98304\ndc 61,40,49\nsa m0,s50,155648\nsa s49,%13,102400\n"
"dc 61,40,49\nsa m0,s50,0\nsa s49,%13,106496\ndc 61,40,49\ns_nop 6\n"
    : "=&v"(acc[0]), "=&v"(acc[1]), "=&v"(acc[2]), "=&v"(acc[3]), "=&v"(acc[4]), "=&v"(rh[0]), "=&v"(rh[1]), "=&v"(rh[2]), "=&v"(rh[3]), "=&v"(rh[4]) : "v"(tid), "s"(ws), "s"(o2), "s"(o3), "s"(o4), "s"(ws + WS_g1), "s"(2621440), "s"(tag), "s"(F_1 + (rb64 * 8 + cg) * FS), "v"(F_1 + (nb * 8 + (lane & 7)) * FS),
      "v"(vo[0]), "v"(vo[1]), "v"(vo[2]), "v"(vo[3]), "v"(vo[4]), "v"(vo[5]), "v"(vo[6]), "v"(vo[7]), "v"(ro[0]), "v"(ro[1]), "v"(ro[2]), "v"(ro[3]), "v"(ro[4]), "s"(rot2(cg) * 1024) : "v48", "v49", "v57", "v58", "v59", "v60", "v61", "v62", "v63", "v64", "v65", "v66", "v67", "v68", "v69", "v70", "v71", "v72", "v73", "v74", "v75", "v76", "v77", "v78", "v79", "v80", "v81", "v82", "v83", "v84", "v85", "v86", "v87", "v88", "v89", "v90", "v91", "v92", "v93", "v94", "v95", "v96", "v97", "v98", "v99", "v100", "v101", "v102", "v103", "v104", "v105", "v106", "v107", "v108", "v109", "v110", "v111", "a0", "a1", "a2", "a3", "a4", "a5", "a6", "a7", "a8", "a9", "a10", "a11", "a12", "a13", "a14", "a15", "a16", "a17", "a18", "a19", "a20", "a21", "a22", "a23", "a24", "a25", "a26", "a27", "a28", "a29", "a30", "a31", "a32", "a33", "a34", "a35", "a36", "a37", "a38", "a39", "a40", "a41", "a42", "a43", "a44", "a45", "a46", "a47", "a48", "a49", "a50", "a51", "a52", "a53", "a54", "a55", "a56", "a57", "a58", "a59", "a60", "a61", "a62", "a63", "a64", "a65", "a66", "a67", "a68", "a69", "a70", "a71", "a72", "a73", "a74", "a75", "a76", "a77", "a78", "a79", "a80", "a81", "a82", "a83", "a84", "a85", "a86", "a87", "a88", "a89", "a90", "a91", "a92", "a93", "a94", "a95", "a96", "a97", "a98", "a99", "a100", "a101", "a102", "a103", "a104", "a105", "a106", "a107", "a108", "a109", "a110", "a111", "a112", "a113", "a114", "a115", "a116", "a117", "a118", "a119", "a120", "a121", "a122", "a123", "a124", "a125", "a126", "a127", "a128", "a129", "a130", "a131", "a132", "a133", "a134", "a135", "a136", "a137", "a138", "a139", "a140", "a141", "a142", "a143", "a144", "a145", "a146", "a147", "a148", "a149", "a150", "a151", "a152", "a153", "a154", "a155", "a156", "a157", "a158", "a159", "a160", "a161", "a162", "a163", "a164", "a165", "a166", "a167", "a168", "a169", "a170", "a171", "a172", "a173", "a174", "a175", "a176", "a177", "a178", "a179", "a180", "a181", "a182", "a183", "a184", "a185", "a186", "a187", "a188", "a189", "a190", "a191", "a192", "a193", "a194", "a195", "a196", "a197", "a198", "a199", "a200", "a201", "a202", "a203", "a204", "a205", "a206", "a207", "a208", "a209", "a210", "a211", "a212", "a213", "a214", "a215", "a216", "a217", "a218", "a219", "a220", "a221", "a222", "a223", "a224", "a225", "a226", "a227", "a228", "a229", "a230", "a231", "a232", "a233", "a234", "a235", "a236", "a237", "a238", "a239", "a240", "a241", "a242", "a243", "a244", "a245", "a246", "a247", "a248", "a249", "a250", "a251", "a252", "a253", "a254", "a255", "s40", "s41", "s42", "s43", "s44", "s45", "s46", "s47", "s48", "s49", "s50", "s51", "s52", "s53", "s54", "vcc", "scc", "m0", "memory");
    float s = 0.f;
#pragma unroll
    for (int n = 0; n < 5; ++n) { ry[n] = pk4(up4(rh[n]) + acc[n]); acc[n] = up4(ry[n]); s += sum4(acc[n]); }
    store_tiles<5>(ry, cg * 5, 5, rs, (unsigned)WS_y1 + to, lane * 8);


    const float mean = sumg(s) * (1.f / 80.f); float q = 0.f;
#pragma unroll
    for (int n = 0; n < 5; ++n) q += dot4(acc[n] - mean);
    q = sumg(q);
    __builtin_amdgcn_raw_buffer_store_b64((u32x2){__float_as_uint(mean), __float_as_uint(q)}, rs, lane < 16 ? (unsigned)WS_stats + (cg * 2048 + tok) * 8 : 0x10000000u, 0, 16);
  }
  {
    const int r128 = xc * 2 + (i >> 4), c16 = i & 15, r3 = r128 * 8 + w * 2, nt0 = c16 < 12 ? c16 * 7 : 84 + (c16 - 12) * 6, nv = c16 < 12 ? 7 : 6;
    f32x4 acc[14], cs[7]; f32x2 st_[4];
#pragma unroll
    for (int n = 0; n < 7; ++n) cs[n] = ldf4(rs, WS_cs + ((nt0 + n) * 16 + g * 4) * 4);
    const unsigned so = 2621440 + (g * 4096 + r3 * 16 + (lane & 15)) * 8;
    asm volatile(
"s_mov_b64 s[52:53], %25\nv_and_b32 v58, 63, %18\nv_lshrrev_b32 v59, 6, %18\nv_lshlrev_b32 v58, 4, v58\nv_readfirstlane_b32 s48, v59\ns_mov_b64 s[40:41], %19\n"
"s_and_b32 s41, s41, 0xffff\ns_mov_b32 s42, 134217728\ns_mov_b32 s43, 0x20000\ns_mov_b64 s[44:45], %23\ns_and_b32 s45, s45, 0xffff\ns_mov_b32 s46, %24\n"
"s_mov_b32 s47, 0x20000\ns_lshl_b32 s50, s48, 10\nv_add_u32 v61, s50, v58\nv_add_u32 v59, 0x10000, v58\nv_add_u32 v60, 0x20000, v58\nrd a,160,58,53248\n"
"rd a,164,58,54272\nrd a,168,58,55296\nrd a,172,58,56320\nrd a,176,58,57344\nrd a,180,58,58368\nrd a,184,58,59392\n"
"rd a,188,58,60416\nrd a,192,58,61440\nrd a,196,58,62464\nrd a,200,58,63488\nrd a,204,58,64512\nrd a,208,59,0\n"
"rd a,212,59,1024\nrd a,216,59,2048\nrd a,220,59,3072\nrd a,224,59,4096\nrd a,228,59,5120\nrd a,232,59,6144\n"
"rd a,236,59,7168\nrd a,240,59,8192\nrd a,244,59,9216\nrd a,248,59,10240\nrd a,252,59,11264\nrd v,64,59,12288\n"
"rd v,68,59,13312\nrd v,72,59,14336\nrd v,76,59,15360\nrd v,80,59,16384\nrd v,84,59,17408\nrd v,88,59,18432\n"
"rd v,92,59,19456\nrd v,96,59,20480\nrd v,100,59,21504\ns_waitcnt vmcnt(0) lgkmcnt(0)\ns_barrier\ns_cmp_eq_u32 s48, 0\n"
"s_cbranch_scc0 NOPUB_w3\nv_mov_b32 v62, s52\nv_mov_b32 v63, s53\nv_mov_b32 v57, 0\ns_mov_b64 exec, 1\nbuffer_store_dwordx2 v[62:63], v57, s[40:43], %26 offen sc1\n"
"s_mov_b64 exec, -1\nNOPUB_w3:\nsa m0,s50,4096\nsa s49,%20,110592\ndc 61,40,49\nsa m0,s50,8192\n"
"sa s49,%20,114688\ndc 61,40,49\nsa m0,s50,12288\nsa s49,%20,118784\ndc 61,40,49\nsa m0,s50,16384\n"
"sa s49,%20,122880\ndc 61,40,49\nsa m0,s50,20480\nsa s49,%20,126976\ndc 61,40,49\nsa m0,s50,24576\n"
"sa s49,%20,131072\ndc 61,40,49\nsa m0,s50,28672\nsa s49,%20,135168\ndc 61,40,49\nsa m0,s50,32768\n"
"sa s49,%20,139264\ndc 61,40,49\ns_cmp_eq_u32 s48, 0\ns_cbranch_scc0 TRICKLE_w3\ns_mov_b32 s51, 0x10000\nSPINL_w3:\n"
"buffer_load_dwordx2 v[48:49], %27, s[40:43], 0 offen sc1\nwv 0\nv_cmp_ne_u64 vcc, %25, v[48:49]\ns_cbranch_vccz POLLED_w3\ns_sub_u32 s51, s51, 1\ns_cbranch_scc0 SPINL_w3\n"
"s_branch POLLED_w3\nTRICKLE_w3:\nPOLLED_w3:\ns_barrier\ns_mov_b32 s49, 0\nbuffer_load_dwordx4 a[0:3], %28, s[44:47], s49 offen sc1\n"
"s_mov_b32 s49, 20480\nbuffer_load_dwordx4 a[4:7], %28, s[44:47], s49 offen sc1\ns_mov_b32 s49, 1024\nbuffer_load_dwordx4 a[8:11], %28, s[44:47], s49 offen sc1\ns_mov_b32 s49, 21504\nbuffer_load_dwordx4 a[12:15], %28, s[44:47], s49 offen sc1\n"
"s_mov_b32 s49, 2048\nbuffer_load_dwordx2 a[16:17], %29, s[44:47], s49 offen sc1\nbuffer_load_dwordx2 a[18:19], %29, s[44:47], s49 offen offset:512 sc1\ns_mov_b32 s49, 22528\nbuffer_load_dwordx2 a[20:21], %29, s[44:47], s49 offen sc1\nbuffer_load_dwordx2 a[22:23], %29, s[44:47], s49 offen offset:512 sc1\n"
"s_mov_b32 s49, 3072\nbuffer_load_dwordx4 a[24:27], %28, s[44:47], s49 offen sc1\ns_mov_b32 s49, 23552\nbuffer_load_dwordx4 a[28:31], %28, s[44:47], s49 offen sc1\ns_mov_b32 s49, 4096\nbuffer_load_dwordx4 a[32:35], %28, s[44:47], s49 offen sc1\n"
"s_mov_b32 s49, 24576\nbuffer_load_dwordx4 a[36:39], %28, s[44:47], s49 offen sc1\nbuffer_load_dwordx2 %14, %30, s[44:47], 0 offen sc1\nbuffer_load_dwordx2 %15, %31, s[44:47], 0 offen sc1\nbuffer_load_dwordx2 %16, %32, s[44:47], 0 offen sc1\nbuffer_load_dwordx2 %17, %33, s[44:47], 0 offen sc1\n"
"wv 15\nmq %0,a,160,a,0\nwv 14\nmq %1,a,160,a,4\ns_mov_b32 s49, 5120\nbuffer_load_dwordx4 a[40:43], %28, s[44:47], s49 offen sc1\n"
"mq %2,a,164,a,0\nmq %3,a,164,a,4\ns_mov_b32 s49, 25600\nbuffer_load_dwordx4 a[44:47], %28, s[44:47], s49 offen sc1\nmq %4,a,168,a,0\nmq %5,a,168,a,4\n"
"mq %6,a,172,a,0\nmq %7,a,172,a,4\nmq %8,a,176,a,0\nmq %9,a,176,a,4\nmq %10,a,180,a,0\nmq %11,a,180,a,4\n"
"mq %12,a,184,a,0\nmq %13,a,184,a,4\nwv 15\nmp %0,a,188,a,8\nwv 14\nmp %1,a,188,a,12\n"
"s_mov_b32 s49, 6144\nbuffer_load_dwordx4 a[48:51], %28, s[44:47], s49 offen sc1\nmp %2,a,192,a,8\nmp %3,a,192,a,12\ns_mov_b32 s49, 26624\nbuffer_load_dwordx4 a[52:55], %28, s[44:47], s49 offen sc1\n"
"mp %4,a,196,a,8\nmp %5,a,196,a,12\nmp %6,a,200,a,8\nmp %7,a,200,a,12\nmp %8,a,204,a,8\nmp %9,a,204,a,12\n"
"mp %10,a,208,a,8\nmp %11,a,208,a,12\nmp %12,a,212,a,8\nmp %13,a,212,a,12\nwv 14\nmp %0,a,216,a,16\n"
"wv 12\nmp %1,a,216,a,20\ns_mov_b32 s49, 7168\nbuffer_load_dwordx2 a[56:57], %29, s[44:47], s49 offen sc1\nbuffer_load_dwordx2 a[58:59], %29, s[44:47], s49 offen offset:512 sc1\nmp %2,a,220,a,16\n"
"mp %3,a,220,a,20\ns_mov_b32 s49, 27648\nbuffer_load_dwordx2 a[60:61], %29, s[44:47], s49 offen sc1\nbuffer_load_dwordx2 a[62:63], %29, s[44:47], s49 offen offset:512 sc1\nmp %4,a,224,a,16\nmp %5,a,224,a,20\n"
"mp %6,a,228,a,16\nmp %7,a,228,a,20\nmp %8,a,232,a,16\nmp %9,a,232,a,20\nmp %10,a,236,a,16\nmp %11,a,236,a,20\n"
"mp %12,a,240,a,16\nmp %13,a,240,a,20\nwv 15\nmp %0,a,244,a,24\nwv 14\nmp %1,a,244,a,28\n"
"s_mov_b32 s49, 8192\nbuffer_load_dwordx4 a[64:67], %28, s[44:47], s49 offen sc1\nmp %2,a,248,a,24\nmp %3,a,248,a,28\ns_mov_b32 s49, 28672\nbuffer_load_dwordx4 a[68:71], %28, s[44:47], s49 offen sc1\n"
"mp %4,a,252,a,24\nmp %5,a,252,a,28\nmp %6,v,64,a,24\nmp %7,v,64,a,28\nrd v,104,59,22528\nmp %8,v,68,a,24\n"
"mp %9,v,68,a,28\nrd v,108,59,23552\nmp %10,v,72,a,24\nmp %11,v,72,a,28\nrd v,64,59,24576\nmp %12,v,76,a,24\n"
"mp %13,v,76,a,28\nrd v,68,59,25600\nwv 15\nmp %0,v,80,a,32\nwv 14\nmp %1,v,80,a,36\n"
"rd v,72,59,26624\ns_mov_b32 s49, 9216\nbuffer_load_dwordx4 a[72:75], %28, s[44:47], s49 offen sc1\nmp %2,v,84,a,32\nmp %3,v,84,a,36\nrd v,76,59,27648\n"
"s_mov_b32 s49, 29696\nbuffer_load_dwordx4 a[76:79], %28, s[44:47], s49 offen sc1\nmp %4,v,88,a,32\nmp %5,v,88,a,36\nrd v,80,59,28672\nmp %6,v,92,a,32\n"
"mp %7,v,92,a,36\nrd v,84,59,29696\nmp %8,v,96,a,32\nmp %9,v,96,a,36\nrd v,88,59,30720\nmp %10,v,100,a,32\n"
"mp %11,v,100,a,36\nrd v,92,59,31744\nwl 9\nmp %12,v,104,a,32\nmp %13,v,104,a,36\nrd v,96,59,32768\n"
"s_waitcnt vmcnt(11) lgkmcnt(9)\nmp %0,v,108,a,40\nwv 10\nmp %1,v,108,a,44\nrd v,100,59,33792\ns_mov_b32 s49, 10240\n"
"buffer_load_dwordx4 a[80:83], %28, s[44:47], s49 offen sc1\nwl 9\nmp %2,v,64,a,40\nmp %3,v,64,a,44\nrd v,104,59,34816\ns_mov_b32 s49, 30720\n"
"buffer_load_dwordx4 a[84:87], %28, s[44:47], s49 offen sc1\nwl 9\nmp %4,v,68,a,40\nmp %5,v,68,a,44\nrd v,108,59,35840\nwl 9\n"
"mp %6,v,72,a,40\nmp %7,v,72,a,44\nrd v,64,59,36864\nwl 9\nmp %8,v,76,a,40\nmp %9,v,76,a,44\n"
"rd v,68,59,37888\nwl 9\nmp %10,v,80,a,40\nmp %11,v,80,a,44\nrd v,72,59,38912\nwl 9\n"
"mp %12,v,84,a,40\nmp %13,v,84,a,44\nrd v,76,59,39936\ns_waitcnt vmcnt(11) lgkmcnt(9)\nmp %0,v,88,a,48\nwv 10\n"
"mp %1,v,88,a,52\nrd v,80,59,40960\ns_mov_b32 s49, 11264\nbuffer_load_dwordx4 a[88:91], %28, s[44:47], s49 offen sc1\nwl 9\nmp %2,v,92,a,48\n"
"mp %3,v,92,a,52\nrd v,84,59,41984\ns_mov_b32 s49, 31744\nbuffer_load_dwordx4 a[92:95], %28, s[44:47], s49 offen sc1\nwl 9\nmp %4,v,96,a,48\n"
"mp %5,v,96,a,52\nrd v,88,59,43008\nwl 9\nmp %6,v,100,a,48\nmp %7,v,100,a,52\nrd v,92,59,44032\n"
"wl 9\nmp %8,v,104,a,48\nmp %9,v,104,a,52\nrd v,96,59,45056\nwl 9\nmp %10,v,108,a,48\n"
"mp %11,v,108,a,52\nrd v,100,59,46080\nwl 9\nmp %12,v,64,a,48\nmp %13,v,64,a,52\nrd v,104,59,47104\n"
"s_waitcnt vmcnt(10) lgkmcnt(9)\nmp %0,v,68,a,56\nwv 8\nmp %1,v,68,a,60\nrd v,108,59,48128\ns_mov_b32 s49, 12288\n"
"buffer_load_dwordx2 a[96:97], %29, s[44:47], s49 offen sc1\nbuffer_load_dwordx2 a[98:99], %29, s[44:47], s49 offen offset:512 sc1\nwl 9\nmp %2,v,72,a,56\nmp %3,v,72,a,60\nrd v,64,59,49152\n"
"s_mov_b32 s49, 32768\nbuffer_load_dwordx2 a[100:101], %29, s[44:47], s49 offen sc1\nbuffer_load_dwordx2 a[102:103], %29, s[44:47], s49 offen offset:512 sc1\nwl 9\nmp %4,v,76,a,56\nmp %5,v,76,a,60\n"
"rd v,68,59,50176\nwl 9\nmp %6,v,80,a,56\nmp %7,v,80,a,60\nrd v,72,59,51200\nwl 9\n"
"mp %8,v,84,a,56\nmp %9,v,84,a,60\nrd v,76,59,52224\nwl 9\nmp %10,v,88,a,56\nmp %11,v,88,a,60\n"
"rd v,80,59,53248\nwl 9\nmp %12,v,92,a,56\nmp %13,v,92,a,60\nrd v,84,59,54272\ns_waitcnt vmcnt(11) lgkmcnt(9)\n"
"mp %0,v,96,a,64\nwv 10\nmp %1,v,96,a,68\nrd v,88,59,55296\ns_mov_b32 s49, 13312\nbuffer_load_dwordx4 a[104:107], %28, s[44:47], s49 offen sc1\n"
"wl 9\nmp %2,v,100,a,64\nmp %3,v,100,a,68\nrd v,92,59,56320\ns_mov_b32 s49, 33792\nbuffer_load_dwordx4 a[108:111], %28, s[44:47], s49 offen sc1\n"
"wl 9\nmp %4,v,104,a,64\nmp %5,v,104,a,68\nrd v,96,59,57344\nwl 9\nmp %6,v,108,a,64\n"
"mp %7,v,108,a,68\nrd v,100,59,58368\nwl 9\nmp %8,v,64,a,64\nmp %9,v,64,a,68\nrd v,104,59,59392\n"
"wl 9\nmp %10,v,68,a,64\nmp %11,v,68,a,68\nrd v,108,59,60416\nwl 9\nmp %12,v,72,a,64\n"
"mp %13,v,72,a,68\nrd v,64,59,61440\ns_waitcnt vmcnt(11) lgkmcnt(9)\nmp %0,v,76,a,72\nwv 10\nmp %1,v,76,a,76\n"
"rd v,68,59,62464\ns_mov_b32 s49, 14336\nbuffer_load_dwordx4 a[112:115], %28, s[44:47], s49 offen sc1\nwl 9\nmp %2,v,80,a,72\nmp %3,v,80,a,76\n"
"rd v,72,59,63488\ns_mov_b32 s49, 34816\nbuffer_load_dwordx4 a[116:119], %28, s[44:47], s49 offen sc1\nwl 9\nmp %4,v,84,a,72\nmp %5,v,84,a,76\n"
"rd v,76,59,64512\nwl 9\nmp %6,v,88,a,72\nmp %7,v,88,a,76\nrd v,80,60,0\nwl 9\n"
"mp %8,v,92,a,72\nmp %9,v,92,a,76\nrd v,84,60,1024\nwl 9\nmp %10,v,96,a,72\nmp %11,v,96,a,76\n"
"rd v,88,60,2048\nwl 9\nmp %12,v,100,a,72\nmp %13,v,100,a,76\nrd v,92,60,3072\ns_waitcnt vmcnt(11) lgkmcnt(9)\n"
"mp %0,v,104,a,80\nwv 10\nmp %1,v,104,a,84\nrd v,96,60,4096\ns_mov_b32 s49, 15360\nbuffer_load_dwordx4 a[120:123], %28, s[44:47], s49 offen sc1\n"
"wl 9\nmp %2,v,108,a,80\nmp %3,v,108,a,84\nrd v,100,60,5120\ns_mov_b32 s49, 35840\nbuffer_load_dwordx4 a[124:127], %28, s[44:47], s49 offen sc1\n"
"wl 9\nmp %4,v,64,a,80\nmp %5,v,64,a,84\nrd v,104,60,6144\nwl 9\nmp %6,v,68,a,80\n"
"mp %7,v,68,a,84\nrd v,108,60,7168\nwl 9\nmp %8,v,72,a,80\nmp %9,v,72,a,84\nrd v,64,60,8192\n"
"wl 9\nmp %10,v,76,a,80\nmp %11,v,76,a,84\nrd v,68,60,9216\nwl 9\nmp %12,v,80,a,80\n"
"mp %13,v,80,a,84\nrd v,72,60,10240\ns_waitcnt vmcnt(11) lgkmcnt(9)\nmp %0,v,84,a,88\nwv 10\nmp %1,v,84,a,92\n"
"rd v,76,60,11264\ns_mov_b32 s49, 16384\nbuffer_load_dwordx4 a[128:131], %28, s[44:47], s49 offen sc1\nwl 9\nmp %2,v,88,a,88\nmp %3,v,88,a,92\n"
"rd v,80,60,12288\ns_mov_b32 s49, 36864\nbuffer_load_dwordx4 a[132:135], %28, s[44:47], s49 offen sc1\nwl 9\nmp %4,v,92,a,88\nmp %5,v,92,a,92\n"
"rd v,84,60,13312\nwl 9\nmp %6,v,96,a,88\nmp %7,v,96,a,92\nrd v,88,60,14336\nwl 9\n"
"mp %8,v,100,a,88\nmp %9,v,100,a,92\nrd v,92,60,15360\nwl 9\nmp %10,v,104,a,88\nmp %11,v,104,a,92\n"
"rd v,96,60,16384\nwl 9\nmp %12,v,108,a,88\nmp %13,v,108,a,92\nrd v,100,60,17408\ns_waitcnt vmcnt(10) lgkmcnt(9)\n"
"mp %0,v,64,a,96\nwv 8\nmp %1,v,64,a,100\nrd v,104,60,18432\ns_mov_b32 s49, 17408\nbuffer_load_dwordx2 a[136:137], %29, s[44:47], s49 offen sc1\n"
"buffer_load_dwordx2 a[138:139], %29, s[44:47], s49 offen offset:512 sc1\nwl 9\nmp %2,v,68,a,96\nmp %3,v,68,a,100\nrd v,108,60,19456\ns_mov_b32 s49, 37888\n"
"buffer_load_dwordx2 a[140:141], %29, s[44:47], s49 offen sc1\nbuffer_load_dwordx2 a[142:143], %29, s[44:47], s49 offen offset:512 sc1\nwl 9\nmp %4,v,72,a,96\nmp %5,v,72,a,100\nrd v,64,60,20480\n"
"wl 9\nmp %6,v,76,a,96\nmp %7,v,76,a,100\nrd v,68,60,21504\nwl 9\nmp %8,v,80,a,96\n"
"mp %9,v,80,a,100\nrd v,72,60,22528\nwl 9\nmp %10,v,84,a,96\nmp %11,v,84,a,100\nrd v,76,60,23552\n"
"wl 9\nmp %12,v,88,a,96\nmp %13,v,88,a,100\nrd v,80,60,24576\ns_waitcnt vmcnt(11) lgkmcnt(9)\nmp %0,v,92,a,104\n"
"wv 10\nmp %1,v,92,a,108\nrd v,84,60,25600\ns_mov_b32 s49, 18432\nbuffer_load_dwordx4 a[144:147], %28, s[44:47], s49 offen sc1\nwl 9\n"
"mp %2,v,96,a,104\nmp %3,v,96,a,108\nrd v,88,60,26624\ns_mov_b32 s49, 38912\nbuffer_load_dwordx4 a[148:151], %28, s[44:47], s49 offen sc1\nwl 9\n"
"mp %4,v,100,a,104\nmp %5,v,100,a,108\nrd v,92,60,27648\nwl 9\nmp %6,v,104,a,104\nmp %7,v,104,a,108\n"
"rd v,96,58,0\nwl 9\nmp %8,v,108,a,104\nmp %9,v,108,a,108\nrd v,100,58,1024\nwl 9\n"
"mp %10,v,64,a,104\nmp %11,v,64,a,108\nrd v,104,58,2048\nwl 9\nmp %12,v,68,a,104\nmp %13,v,68,a,108\n"
"rd v,108,58,3072\ns_waitcnt vmcnt(11) lgkmcnt(9)\nmp %0,v,72,a,112\nwv 10\nmp %1,v,72,a,116\ns_barrier\n"
"rd v,64,58,4096\ns_mov_b32 s49, 19456\nbuffer_load_dwordx4 a[152:155], %28, s[44:47], s49 offen sc1\nwl 9\nmp %2,v,76,a,112\nsa m0,s50,36864\n"
"sa s49,%21,0\ndc 61,40,49\nmp %3,v,76,a,116\nrd v,68,58,5120\ns_mov_b32 s49, 39936\nbuffer_load_dwordx4 a[156:159], %28, s[44:47], s49 offen sc1\n"
"wl 9\nmp %4,v,80,a,112\nsa m0,s50,40960\nsa s49,%21,4096\ndc 61,40,49\nmp %5,v,80,a,116\n"
"rd v,72,58,6144\nwl 9\nmp %6,v,84,a,112\nmp %7,v,84,a,116\nrd v,76,58,7168\nsa m0,s50,45056\n"
"sa s49,%21,8192\ndc 61,40,49\nwl 9\nmp %8,v,88,a,112\nmp %9,v,88,a,116\nrd v,80,58,8192\n"
"wl 9\nmp %10,v,92,a,112\nsa m0,s50,49152\nsa s49,%21,12288\ndc 61,40,49\nmp %11,v,92,a,116\n"
"rd v,84,58,9216\nwl 9\nmp %12,v,96,a,112\nmp %13,v,96,a,116\nrd v,88,58,10240\nsa m0,s50,53248\n"
"sa s49,%21,16384\ndc 61,40,49\ns_waitcnt vmcnt(16) lgkmcnt(9)\nmp %0,v,100,a,120\nwv 15\nmp %1,v,100,a,124\n"
"rd v,92,58,11264\nwl 9\nmp %2,v,104,a,120\nsa m0,s50,57344\nsa s49,%21,20480\ndc 61,40,49\n"
"mp %3,v,104,a,124\nrd v,96,58,12288\nwl 9\nmp %4,v,108,a,120\nsa m0,s50,61440\nsa s49,%21,24576\n"
"dc 61,40,49\nmp %5,v,108,a,124\nrd v,100,58,13312\nwl 9\nmp %6,v,64,a,120\nmp %7,v,64,a,124\n"
"rd v,104,58,14336\nsa m0,s50,65536\nsa s49,%21,28672\ndc 61,40,49\nwl 9\nmp %8,v,68,a,120\n"
"mp %9,v,68,a,124\nrd v,108,58,15360\nwl 9\nmp %10,v,72,a,120\nsa m0,s50,69632\nsa s49,%21,32768\n"
"dc 61,40,49\nmp %11,v,72,a,124\nrd v,64,58,16384\nwl 9\nmp %12,v,76,a,120\nmp %13,v,76,a,124\n"
"rd v,68,58,17408\nsa m0,s50,73728\nsa s49,%21,36864\ndc 61,40,49\ns_waitcnt vmcnt(19) lgkmcnt(9)\nmp %0,v,80,a,128\n"
"wv 18\nmp %1,v,80,a,132\nrd v,72,58,18432\nwl 9\nmp %2,v,84,a,128\nsa m0,s50,77824\n"
"sa s49,%21,40960\ndc 61,40,49\nmp %3,v,84,a,132\nrd v,76,58,19456\nwl 9\nmp %4,v,88,a,128\n"
"sa m0,s50,81920\nsa s49,%21,45056\ndc 61,40,49\nmp %5,v,88,a,132\nrd v,80,58,20480\nwl 9\n"
"mp %6,v,92,a,128\nmp %7,v,92,a,132\nrd v,84,58,21504\nsa m0,s50,86016\nsa s49,%21,49152\ndc 61,40,49\n"
"wl 9\nmp %8,v,96,a,128\nmp %9,v,96,a,132\nrd v,88,58,22528\nwl 9\nmp %10,v,100,a,128\n"
"sa m0,s50,90112\nsa s49,%21,53248\ndc 61,40,49\nmp %11,v,100,a,132\nrd v,92,58,23552\nwl 9\n"
"mp %12,v,104,a,128\nmp %13,v,104,a,132\nrd v,96,58,24576\nsa m0,s50,94208\nsa s49,%21,57344\ndc 61,40,49\n"
"s_waitcnt vmcnt(21) lgkmcnt(9)\nmp %0,v,108,a,136\nwv 19\nmp %1,v,108,a,140\nrd v,100,58,25600\nwl 9\n"
"mp %2,v,64,a,136\nsa m0,s50,98304\nsa s49,%21,61440\ndc 61,40,49\nmp %3,v,64,a,140\nrd v,104,58,26624\n"
"wl 9\nmp %4,v,68,a,136\nsa m0,s50,102400\nsa s49,%21,65536\ndc 61,40,49\nmp %5,v,68,a,140\n"
"rd v,108,58,27648\nwl 9\nmp %6,v,72,a,136\nmp %7,v,72,a,140\nrd v,64,58,28672\nsa m0,s50,106496\n"
"sa s49,%21,69632\ndc 61,40,49\nwl 9\nmp %8,v,76,a,136\nmp %9,v,76,a,140\nrd v,68,58,29696\n"
"wl 9\nmp %10,v,80,a,136\nsa m0,s50,110592\nsa s49,%21,73728\ndc 61,40,49\nmp %11,v,80,a,140\n"
"rd v,72,58,30720\nwl 9\nmp %12,v,84,a,136\nmp %13,v,84,a,140\nrd v,76,58,31744\nsa m0,s50,114688\n"
"sa s49,%21,77824\ndc 61,40,49\ns_waitcnt vmcnt(23) lgkmcnt(9)\nmp %0,v,88,a,144\nwv 22\nmp %1,v,88,a,148\n"
"rd v,80,58,32768\nwl 9\nmp %2,v,92,a,144\nsa m0,s50,118784\nsa s49,%21,81920\ndc 61,40,49\n"
"mp %3,v,92,a,148\nrd v,84,58,33792\nwl 9\nmp %4,v,96,a,144\nsa m0,s50,122880\nsa s49,%21,86016\n"
"dc 61,40,49\nmp %5,v,96,a,148\nrd v,88,58,34816\nwl 9\nmp %6,v,100,a,144\nmp %7,v,100,a,148\n"
"rd v,92,58,35840\nsa m0,s50,126976\nsa s49,%21,90112\ndc 61,40,49\nwl 9\nmp %8,v,104,a,144\n"
"mp %9,v,104,a,148\nwl 8\nmp %10,v,108,a,144\nsa m0,s50,131072\nsa s49,%21,94208\ndc 61,40,49\n"
"mp %11,v,108,a,148\nwl 7\nmp %12,v,64,a,144\nmp %13,v,64,a,148\nsa m0,s50,135168\nsa s49,%21,98304\n"
"dc 61,40,49\ns_waitcnt vmcnt(26) lgkmcnt(6)\nmp %0,v,68,a,152\nwv 24\nmp %1,v,68,a,156\nwl 5\n"
"mp %2,v,72,a,152\nsa m0,s50,139264\nsa s49,%21,102400\ndc 61,40,49\nmp %3,v,72,a,156\nwl 4\n"
"mp %4,v,76,a,152\nmp %5,v,76,a,156\nwl 3\nmp %6,v,80,a,152\nsa m0,s50,143360\nsa s49,%21,106496\n"
"dc 61,40,49\nmp %7,v,80,a,156\nwl 2\nmp %8,v,84,a,152\nmp %9,v,84,a,156\nwl 1\n"
"mp %10,v,88,a,152\nmp %11,v,88,a,156\nsa m0,s50,147456\nsa s49,%21,110592\ndc 61,40,49\nwl 0\n"
"mp %12,v,92,a,152\nmp %13,v,92,a,156\ns_barrier\nsa m0,s50,151552\nsa s49,%21,114688\ndc 61,40,49\n"
"sa m0,s50,155648\nsa s49,%21,118784\ndc 61,40,49\nsa m0,s50,0\nsa s49,%21,122880\ndc 61,40,49\n"
"sa m0,s50,4096\nsa s49,%21,126976\ndc 61,40,49\ns_nop 6\n"
    : "=&v"(acc[0]), "=&v"(acc[1]), "=&v"(acc[2]), "=&v"(acc[3]), "=&v"(acc[4]), "=&v"(acc[5]), "=&v"(acc[6]), "=&v"(acc[7]), "=&v"(acc[8]), "=&v"(acc[9]), "=&v"(acc[10]), "=&v"(acc[11]), "=&v"(acc[12]), "=&v"(acc[13]), "=&v"(st_[0]), "=&v"(st_[1]), "=&v"(st_[2]), "=&v"(st_[3]) : "v"(tid), "s"(ws), "s"(o3), "s"(o4), "s"(o4), "s"(ws + WS_y1), "s"(2621440 + 131072), "s"(tag), "s"(F_2 + (rb64 * 8 + cg) * FS), "v"(F_2 + ((r128 * 2 + ((lane >> 3) & 1)) * 8 + (lane & 7)) * FS),
      "v"(lane * 16 + r3 * 20480), "v"(lane * 8 + r3 * 20480), "v"(so), "v"(so + 16384), "v"(so + 128), "v"(so + 16512), "s"(rot3(c16) * 1024) : "v48", "v49", "v57", "v58", "v59", "v60", "v61", "v62", "v63", "v64", "v65", "v66", "v67", "v68", "v69", "v70", "v71", "v72", "v73", "v74", "v75", "v76", "v77", "v78", "v79", "v80", "v81", "v82", "v83", "v84", "v85", "v86", "v87", "v88", "v89", "v90", "v91", "v92", "v93", "v94", "v95", "v96", "v97", "v98", "v99", "v100", "v101", "v102", "v103", "v104", "v105", "v106", "v107", "v108", "v109", "v110", "v111", "a0", "a1", "a2", "a3", "a4", "a5", "a6", "a7", "a8", "a9", "a10", "a11", "a12", "a13", "a14", "a15", "a16", "a17", "a18", "a19", "a20", "a21", "a22", "a23", "a24", "a25", "a26", "a27", "a28", "a29", "a30", "a31", "a32", "a33", "a34", "a35", "a36", "a37", "a38", "a39", "a40", "a41", "a42", "a43", "a44", "a45", "a46", "a47", "a48", "a49", "a50", "a51", "a52", "a53", "a54", "a55", "a56", "a57", "a58", "a59", "a60", "a61", "a62", "a63", "a64", "a65", "a66", "a67", "a68", "a69", "a70", "a71", "a72", "a73", "a74", "a75", "a76", "a77", "a78", "a79", "a80", "a81", "a82", "a83", "a84", "a85", "a86", "a87", "a88", "a89", "a90", "a91", "a92", "a93", "a94", "a95", "a96", "a97", "a98", "a99", "a100", "a101", "a102", "a103", "a104", "a105", "a106", "a107", "a108", "a109", "a110", "a111", "a112", "a113", "a114", "a115", "a116", "a117", "a118", "a119", "a120", "a121", "a122", "a123", "a124", "a125", "a126", "a127", "a128", "a129", "a130", "a131", "a132", "a133", "a134", "a135", "a136", "a137", "a138", "a139", "a140", "a141", "a142", "a143", "a144", "a145", "a146", "a147", "a148", "a149", "a150", "a151", "a152", "a153", "a154", "a155", "a156", "a157", "a158", "a159", "a160", "a161", "a162", "a163", "a164", "a165", "a166", "a167", "a168", "a169", "a170", "a171", "a172", "a173", "a174", "a175", "a176", "a177", "a178", "a179", "a180", "a181", "a182", "a183", "a184", "a185", "a186", "a187", "a188", "a189", "a190", "a191", "a192", "a193", "a194", "a195", "a196", "a197", "a198", "a199", "a200", "a201", "a202", "a203", "a204", "a205", "a206", "a207", "a208", "a209", "a210", "a211", "a212", "a213", "a214", "a215", "a216", "a217", "a218", "a219", "a220", "a221", "a222", "a223", "a224", "a225", "a226", "a227", "a228", "a229", "a230", "a231", "a232", "a233", "a234", "a235", "a236", "a237", "a238", "a239", "a240", "a241", "a242", "a243", "a244", "a245", "a246", "a247", "a248", "a249", "a250", "a251", "a252", "a253", "a254", "a255", "s40", "s41", "s42", "s43", "s44", "s45", "s46", "s47", "s48", "s49", "s50", "s51", "s52", "s53", "s54", "vcc", "scc", "m0", "memory");
#pragma unroll
    for (int n = 0; n < 5; ++n) { const int nt = cg * 5 + n; rx[n] = __builtin_amdgcn_raw_buffer_load_b64(rs, (unsigned)WS_x1 + to + (nt >> 1) * 1024 + (nt & 1) * 8, 0, 16); }
    float mean[2], rstd[2];
#pragma unroll
    for (int m = 0; m < 2; ++m) { const f32x4 st = {st_[2 * m][0], st_[2 * m][1], st_[2 * m + 1][0], st_[2 * m + 1][1]};
      mean[m] = sumg(st[0] + st[2]) * 0.125f; const float d0 = st[0] - mean[m], d1 = st[2] - mean[m];
      rstd[m] = __builtin_amdgcn_rsqf(sumg(st[1] + st[3] + 80.f * (d0 * d0 + d1 * d1)) * (1.f / 640.f) + 1e-5f); }
    u32x2 y3[2][7];
#define E3(n, m) { const f32x4 z = __builtin_elementwise_fma(cs[n], (f32x4)(-mean[m]), acc[n * 2 + m]) * rstd[m]; y3[m][n] = G4(z); asm volatile("" : "+v"(y3[m][n])); \
      if (n == 6) store_tiles<7>(y3[m], nt0, nv, rs, (unsigned)WS_g3 + (r3 + m) * 55296 + lane * 16, lane * 8); }
    E3(0, 0)  E3(1, 0)  E3(2, 0)  E3(3, 0)  E3(4, 0)  E3(5, 0)  E3(6, 0)  E3(0, 1)  E3(1, 1)  E3(2, 1)  E3(3, 1)  E3(4, 1)  E3(5, 1)  E3(6, 1)
  }
  {
    f32x4 acc[5], gb[5]; const __amdgpu_buffer_rsrc_t ro = __builtin_amdgcn_make_buffer_rsrc(out, 0, 2621440, 0x00020000);
#pragma unroll
    for (int n = 0; n < 5; ++n) gb[n] = ldf4(rs, WS_mod + (1920 + (cg * 5 + n) * 16 + g * 4) * 4);
    asm volatile(
"s_mov_b64 s[52:53], %12\nv_and_b32 v58, 63, %5\nv_lshrrev_b32 v59, 6, %5\nv_lshlrev_b32 v58, 4, v58\nv_readfirstlane_b32 s48, v59\ns_mov_b64 s[40:41], %6\n"
"s_and_b32 s41, s41, 0xffff\ns_mov_b32 s42, 134217728\ns_mov_b32 s43, 0x20000\ns_mov_b64 s[44:45], %10\ns_and_b32 s45, s45, 0xffff\ns_mov_b32 s46, %11\n"
"s_mov_b32 s47, 0x20000\ns_lshl_b32 s50, s48, 10\nv_add_u32 v61, s50, v58\nv_add_u32 v59, 0x10000, v58\nv_add_u32 v60, 0x20000, v58\nwv 0\n"
"s_barrier\ns_cmp_eq_u32 s48, 0\ns_cbranch_scc0 NOPUB_w4\nv_mov_b32 v62, s52\nv_mov_b32 v63, s53\nv_mov_b32 v57, 0\n"
"s_mov_b64 exec, 1\nbuffer_store_dwordx2 v[62:63], v57, s[40:43], %13 offen sc1\ns_mov_b64 exec, -1\nNOPUB_w4:\nsa m0,s50,8192\nsa s49,%7,131072\n"
"dc 61,40,49\nsa m0,s50,12288\nsa s49,%7,135168\ndc 61,40,49\nsa m0,s50,16384\nsa s49,%7,139264\n"
"dc 61,40,49\nsa m0,s50,20480\nsa s49,%7,143360\ndc 61,40,49\nsa m0,s50,24576\nsa s49,%7,147456\n"
"dc 61,40,49\nsa m0,s50,28672\nsa s49,%7,151552\ndc 61,40,49\nsa m0,s50,32768\nsa s49,%7,155648\n"
"dc 61,40,49\nrd a,216,58,36864\nrd a,220,58,37888\nrd a,224,58,38912\nrd a,228,58,39936\nrd a,232,58,40960\n"
"rd a,236,58,41984\nrd a,240,58,43008\nrd a,244,58,44032\nrd a,248,58,45056\nrd a,252,58,46080\nrd v,64,58,47104\n"
"rd v,68,58,48128\nrd v,72,58,49152\nrd v,76,58,50176\nrd v,80,58,51200\nrd v,84,58,52224\nrd v,88,58,53248\n"
"rd v,92,58,54272\nrd v,96,58,55296\nrd v,100,58,56320\ns_cmp_eq_u32 s48, 0\ns_cbranch_scc0 TRICKLE_w4\ns_mov_b32 s51, 0x10000\n"
"SPINL_w4:\nbuffer_load_dwordx2 v[48:49], %14, s[40:43], 0 offen sc1\nwv 0\nv_cmp_ne_u64 vcc, %12, v[48:49]\ns_cbranch_vccz POLLED_w4\ns_sub_u32 s51, s51, 1\n"
"s_cbranch_scc0 SPINL_w4\ns_branch POLLED_w4\nTRICKLE_w4:\nPOLLED_w4:\nwl 0\ns_barrier\n"
"s_mov_b32 s49, 0\nbuffer_load_dwordx4 a[0:3], %15, s[44:47], s49 offen sc1\ns_mov_b32 s49, 1024\nbuffer_load_dwordx4 a[4:7], %15, s[44:47], s49 offen sc1\ns_mov_b32 s49, 2048\nbuffer_load_dwordx4 a[8:11], %15, s[44:47], s49 offen sc1\n"
"s_mov_b32 s49, 3072\nbuffer_load_dwordx2 a[12:13], %16, s[44:47], s49 offen sc1\nbuffer_load_dwordx2 a[14:15], %16, s[44:47], s49 offen offset:512 sc1\ns_mov_b32 s49, 4096\nbuffer_load_dwordx4 a[16:19], %15, s[44:47], s49 offen sc1\ns_mov_b32 s49, 5120\n"
"buffer_load_dwordx4 a[20:23], %15, s[44:47], s49 offen sc1\ns_mov_b32 s49, 6144\nbuffer_load_dwordx4 a[24:27], %15, s[44:47], s49 offen sc1\ns_mov_b32 s49, 7168\nbuffer_load_dwordx4 a[28:31], %15, s[44:47], s49 offen sc1\ns_mov_b32 s49, 8192\n"
"buffer_load_dwordx4 a[32:35], %15, s[44:47], s49 offen sc1\ns_mov_b32 s49, 9216\nbuffer_load_dwordx4 a[36:39], %15, s[44:47], s49 offen sc1\nwv 10\nmq %0,a,216,a,0\ns_mov_b32 s49, 10240\n"
"buffer_load_dwordx2 a[40:41], %16, s[44:47], s49 offen sc1\nbuffer_load_dwordx2 a[42:43], %16, s[44:47], s49 offen offset:512 sc1\nmq %1,a,220,a,0\nsa m0,s50,36864\nsa s49,%7,159744\ndc 61,40,49\n"
"mq %2,a,224,a,0\nmq %3,a,228,a,0\nmq %4,a,232,a,0\nwv 12\nmp %0,a,236,a,4\ns_mov_b32 s49, 11264\n"
"buffer_load_dwordx4 a[44:47], %15, s[44:47], s49 offen sc1\nmp %1,a,240,a,4\nsa m0,s50,40960\nsa s49,%7,163840\ndc 61,40,49\nmp %2,a,244,a,4\n"
"mp %3,a,248,a,4\nmp %4,a,252,a,4\nwv 13\nmp %0,v,64,a,8\nrd v,104,58,57344\ns_mov_b32 s49, 12288\n"
"buffer_load_dwordx4 a[48:51], %15, s[44:47], s49 offen sc1\nmp %1,v,68,a,8\nrd v,108,58,58368\nsa m0,s50,45056\nsa s49,%7,167936\ndc 61,40,49\n"
"mp %2,v,72,a,8\nrd v,64,58,59392\nmp %3,v,76,a,8\nrd v,68,58,60416\nmp %4,v,80,a,8\nrd v,72,58,61440\n"
"wv 13\nmp %0,v,84,a,12\ns_barrier\nrd v,76,58,62464\ns_mov_b32 s49, 13312\nbuffer_load_dwordx4 a[52:55], %15, s[44:47], s49 offen sc1\n"
"mp %1,v,88,a,12\nrd v,80,58,63488\nsa m0,s50,49152\nsa s49,%7,172032\ndc 61,40,49\nmp %2,v,92,a,12\n"
"rd v,84,58,64512\nmp %3,v,96,a,12\nrd v,88,59,0\nsa m0,s50,53248\nsa s49,%7,176128\ndc 61,40,49\n"
"mp %4,v,100,a,12\nrd v,92,59,1024\ns_waitcnt vmcnt(15) lgkmcnt(9)\nmp %0,v,104,a,16\nrd v,96,59,2048\ns_mov_b32 s49, 14336\n"
"buffer_load_dwordx4 a[56:59], %15, s[44:47], s49 offen sc1\nwl 9\nmp %1,v,108,a,16\nrd v,100,59,3072\nwl 9\nmp %2,v,64,a,16\n"
"rd v,104,59,4096\nwl 9\nmp %3,v,68,a,16\nrd v,108,59,5120\nwl 9\nmp %4,v,72,a,16\n"
"rd v,64,59,6144\ns_waitcnt vmcnt(15) lgkmcnt(9)\nmp %0,v,76,a,20\nrd v,68,59,7168\ns_mov_b32 s49, 15360\nbuffer_load_dwordx4 a[60:63], %15, s[44:47], s49 offen sc1\n"
"wl 9\nmp %1,v,80,a,20\nrd v,72,59,8192\nwl 9\nmp %2,v,84,a,20\nrd v,76,59,9216\n"
"wl 9\nmp %3,v,88,a,20\nrd v,80,59,10240\nwl 9\nmp %4,v,92,a,20\nrd v,84,59,11264\n"
"s_waitcnt vmcnt(15) lgkmcnt(9)\nmp %0,v,96,a,24\nrd v,88,59,12288\ns_mov_b32 s49, 16384\nbuffer_load_dwordx4 a[64:67], %15, s[44:47], s49 offen sc1\nwl 9\n"
"mp %1,v,100,a,24\nrd v,92,59,13312\nwl 9\nmp %2,v,104,a,24\nrd v,96,59,14336\nwl 9\n"
"mp %3,v,108,a,24\nrd v,100,59,15360\nwl 9\nmp %4,v,64,a,24\nrd v,104,59,16384\ns_waitcnt vmcnt(15) lgkmcnt(9)\n"
"mp %0,v,68,a,28\nrd v,108,59,17408\ns_mov_b32 s49, 17408\nbuffer_load_dwordx2 a[68:69], %16, s[44:47], s49 offen sc1\nbuffer_load_dwordx2 a[70:71], %16, s[44:47], s49 offen offset:512 sc1\nwl 9\n"
"mp %1,v,72,a,28\nrd v,64,59,18432\nwl 9\nmp %2,v,76,a,28\nrd v,68,59,19456\nwl 9\n"
"mp %3,v,80,a,28\nrd v,72,59,20480\nwl 9\nmp %4,v,84,a,28\nrd v,76,59,21504\ns_waitcnt vmcnt(16) lgkmcnt(9)\n"
"mp %0,v,88,a,32\nrd v,80,59,22528\ns_mov_b32 s49, 18432\nbuffer_load_dwordx4 a[72:75], %15, s[44:47], s49 offen sc1\nwl 9\nmp %1,v,92,a,32\n"
"rd v,84,59,23552\nwl 9\nmp %2,v,96,a,32\nrd v,88,59,24576\nwl 9\nmp %3,v,100,a,32\n"
"rd v,92,59,25600\nwl 9\nmp %4,v,104,a,32\nrd v,96,59,26624\ns_waitcnt vmcnt(16) lgkmcnt(9)\nmp %0,v,108,a,36\n"
"s_barrier\nrd v,100,59,27648\ns_mov_b32 s49, 19456\nbuffer_load_dwordx4 a[76:79], %15, s[44:47], s49 offen sc1\nwl 9\nmp %1,v,64,a,36\n"
"rd v,104,59,28672\nwl 9\nmp %2,v,68,a,36\nrd v,108,59,29696\nwl 9\nmp %3,v,72,a,36\n"
"rd v,64,59,30720\nwl 9\nmp %4,v,76,a,36\nrd v,68,59,31744\ns_waitcnt vmcnt(15) lgkmcnt(9)\nmp %0,v,80,a,40\n"
"rd v,72,59,32768\ns_mov_b32 s49, 20480\nbuffer_load_dwordx4 a[80:83], %15, s[44:47], s49 offen sc1\nwl 9\nmp %1,v,84,a,40\nrd v,76,59,33792\n"
"sa m0,s50,57344\nsa s49,%7,180224\ndc 61,40,49\nwl 9\nmp %2,v,88,a,40\nrd v,80,59,34816\n"
"sa m0,s50,61440\nsa s49,%7,184320\ndc 61,40,49\nwl 9\nmp %3,v,92,a,40\nrd v,84,59,35840\n"
"sa m0,s50,65536\nsa s49,%7,188416\ndc 61,40,49\nwl 9\nmp %4,v,96,a,40\nrd v,88,59,36864\n"
"sa m0,s50,69632\nsa s49,%7,192512\ndc 61,40,49\ns_waitcnt vmcnt(18) lgkmcnt(9)\nmp %0,v,100,a,44\nrd v,92,59,37888\n"
"sa m0,s50,73728\nsa s49,%7,196608\ndc 61,40,49\nwl 9\nmp %1,v,104,a,44\nrd v,96,59,38912\n"
"s_mov_b32 s49, 21504\nbuffer_load_dwordx4 a[84:87], %15, s[44:47], s49 offen sc1\nwl 9\nmp %2,v,108,a,44\nrd v,100,59,39936\nsa m0,s50,77824\n"
"sa s49,%7,200704\ndc 61,40,49\nwl 9\nmp %3,v,64,a,44\nrd v,104,59,40960\nwl 9\n"
"mp %4,v,68,a,44\nrd v,108,59,41984\ns_waitcnt vmcnt(19) lgkmcnt(9)\nmp %0,v,72,a,48\nrd v,64,59,43008\ns_mov_b32 s49, 22528\n"
"buffer_load_dwordx4 a[88:91], %15, s[44:47], s49 offen sc1\nwl 9\nmp %1,v,76,a,48\nrd v,68,59,44032\nwl 9\nmp %2,v,80,a,48\n"
"rd v,72,59,45056\nwl 9\nmp %3,v,84,a,48\nrd v,76,59,46080\nwl 9\nmp %4,v,88,a,48\n"
"rd v,80,59,47104\ns_waitcnt vmcnt(18) lgkmcnt(9)\nmp %0,v,92,a,52\nrd v,84,59,48128\ns_mov_b32 s49, 23552\nbuffer_load_dwordx4 a[92:95], %15, s[44:47], s49 offen sc1\n"
"wl 9\nmp %1,v,96,a,52\nrd v,88,59,49152\nwl 9\nmp %2,v,100,a,52\nrd v,92,59,50176\n"
"wl 9\nmp %3,v,104,a,52\nrd v,96,59,51200\nwl 9\nmp %4,v,108,a,52\nrd v,100,59,52224\n"
"s_waitcnt vmcnt(16) lgkmcnt(9)\nmp %0,v,64,a,56\nrd v,104,59,53248\ns_mov_b32 s49, 24576\nbuffer_load_dwordx2 a[96:97], %16, s[44:47], s49 offen sc1\nbuffer_load_dwordx2 a[98:99], %16, s[44:47], s49 offen offset:512 sc1\n"
"wl 9\nmp %1,v,68,a,56\nrd v,108,59,54272\nwl 9\nmp %2,v,72,a,56\nrd v,64,59,55296\n"
"wl 9\nmp %3,v,76,a,56\nrd v,68,59,56320\nwl 9\nmp %4,v,80,a,56\nrd v,72,59,57344\n"
"s_waitcnt vmcnt(17) lgkmcnt(9)\nmp %0,v,84,a,60\ns_barrier\nrd v,76,59,58368\nwl 9\nmp %1,v,88,a,60\n"
"rd v,80,59,59392\ns_mov_b32 s49, 25600\nbuffer_load_dwordx4 a[100:103], %15, s[44:47], s49 offen sc1\nwl 9\nmp %2,v,92,a,60\nrd v,84,59,60416\n"
"wl 9\nmp %3,v,96,a,60\nrd v,88,59,61440\nwl 9\nmp %4,v,100,a,60\nrd v,92,59,62464\n"
"s_waitcnt vmcnt(17) lgkmcnt(9)\nmp %0,v,104,a,64\nrd v,96,59,63488\ns_mov_b32 s49, 26624\nbuffer_load_dwordx4 a[104:107], %15, s[44:47], s49 offen sc1\nwl 9\n"
"mp %1,v,108,a,64\nrd v,100,59,64512\nsa m0,s50,81920\nsa s49,%7,204800\ndc 61,40,49\nwl 9\n"
"mp %2,v,64,a,64\nrd v,104,60,0\nsa m0,s50,86016\nsa s49,%7,208896\ndc 61,40,49\nwl 9\n"
"mp %3,v,68,a,64\nrd v,108,60,1024\nsa m0,s50,90112\nsa s49,%7,212992\ndc 61,40,49\nwl 9\n"
"mp %4,v,72,a,64\nrd v,64,60,2048\nsa m0,s50,94208\nsa s49,%7,217088\ndc 61,40,49\ns_waitcnt vmcnt(20) lgkmcnt(9)\n"
"mp %0,v,76,a,68\nrd v,68,60,3072\nsa m0,s50,98304\nsa s49,%7,221184\ndc 61,40,49\nwl 9\n"
"mp %1,v,80,a,68\nrd v,72,60,4096\ns_mov_b32 s49, 27648\nbuffer_load_dwordx4 a[108:111], %15, s[44:47], s49 offen sc1\nwl 9\nmp %2,v,84,a,68\n"
"rd v,76,60,5120\nsa m0,s50,102400\nsa s49,%7,225280\ndc 61,40,49\nwl 9\nmp %3,v,88,a,68\n"
"rd v,80,60,6144\nwl 9\nmp %4,v,92,a,68\nrd v,84,60,7168\ns_waitcnt vmcnt(22) lgkmcnt(9)\nmp %0,v,96,a,72\n"
"rd v,88,60,8192\ns_mov_b32 s49, 28672\nbuffer_load_dwordx4 a[112:115], %15, s[44:47], s49 offen sc1\nwl 9\nmp %1,v,100,a,72\nrd v,92,60,9216\n"
"sa m0,s50,106496\nsa s49,%7,229376\ndc 61,40,49\nwl 9\nmp %2,v,104,a,72\nrd v,96,60,10240\n"
"wl 9\nmp %3,v,108,a,72\nrd v,100,60,11264\nwl 9\nmp %4,v,64,a,72\nrd v,104,60,12288\n"
"s_waitcnt vmcnt(23) lgkmcnt(9)\nmp %0,v,68,a,76\nrd v,108,60,13312\ns_mov_b32 s49, 29696\nbuffer_load_dwordx4 a[116:119], %15, s[44:47], s49 offen sc1\nwl 9\n"
"mp %1,v,72,a,76\nrd v,64,60,14336\nsa m0,s50,110592\nsa s49,%7,233472\ndc 61,40,49\nwl 9\n"
"mp %2,v,76,a,76\nrd v,68,60,15360\nwl 9\nmp %3,v,80,a,76\nrd v,72,60,16384\nwl 9\n"
"mp %4,v,84,a,76\nrd v,76,60,17408\ns_waitcnt vmcnt(24) lgkmcnt(9)\nmp %0,v,88,a,80\nrd v,80,60,18432\ns_mov_b32 s49, 30720\n"
"buffer_load_dwordx4 a[120:123], %15, s[44:47], s49 offen sc1\nwl 9\nmp %1,v,92,a,80\nrd v,84,60,19456\nwl 9\nmp %2,v,96,a,80\n"
"rd v,88,60,20480\nwl 9\nmp %3,v,100,a,80\nrd v,92,60,21504\nwl 9\nmp %4,v,104,a,80\n"
"rd v,96,60,22528\ns_waitcnt vmcnt(19) lgkmcnt(9)\nmp %0,v,108,a,84\ns_barrier\nrd v,100,60,23552\nwl 9\n"
"mp %1,v,64,a,84\nrd v,104,60,24576\ns_mov_b32 s49, 31744\nbuffer_load_dwordx2 a[124:125], %16, s[44:47], s49 offen sc1\nbuffer_load_dwordx2 a[126:127], %16, s[44:47], s49 offen offset:512 sc1\nwl 9\n"
"mp %2,v,68,a,84\nrd v,108,60,25600\nwl 9\nmp %3,v,72,a,84\nrd v,64,60,26624\nwl 9\n"
"mp %4,v,76,a,84\nrd v,68,60,27648\ns_waitcnt vmcnt(19) lgkmcnt(9)\nmp %0,v,80,a,88\nrd v,72,58,0\ns_mov_b32 s49, 32768\n"
"buffer_load_dwordx4 a[128:131], %15, s[44:47], s49 offen sc1\nwl 9\nmp %1,v,84,a,88\nrd v,76,58,1024\nsa m0,s50,114688\nsa s49,%7,237568\n"
"dc 61,40,49\nwl 9\nmp %2,v,88,a,88\nrd v,80,58,2048\nsa m0,s50,118784\nsa s49,%7,241664\n"
"dc 61,40,49\nwl 9\nmp %3,v,92,a,88\nrd v,84,58,3072\nsa m0,s50,122880\nsa s49,%7,245760\n"
"dc 61,40,49\nwl 9\nmp %4,v,96,a,88\nrd v,88,58,4096\nsa m0,s50,126976\nsa s49,%7,249856\n"
"dc 61,40,49\ns_waitcnt vmcnt(23) lgkmcnt(9)\nmp %0,v,100,a,92\nrd v,92,58,5120\ns_mov_b32 s49, 33792\nbuffer_load_dwordx4 a[132:135], %15, s[44:47], s49 offen sc1\n"
"wl 9\nmp %1,v,104,a,92\nrd v,96,58,6144\nsa m0,s50,131072\nsa s49,%7,253952\ndc 61,40,49\n"
"wl 9\nmp %2,v,108,a,92\nrd v,100,58,7168\nwl 9\nmp %3,v,64,a,92\nrd v,104,58,8192\n"
"sa m0,s50,135168\nsa s49,%7,258048\ndc 61,40,49\nwl 9\nmp %4,v,68,a,92\nrd v,108,58,9216\n"
"s_waitcnt vmcnt(24) lgkmcnt(9)\nmp %0,v,72,a,96\nrd v,64,58,10240\ns_mov_b32 s49, 34816\nbuffer_load_dwordx4 a[136:139], %15, s[44:47], s49 offen sc1\nwl 9\n"
"mp %1,v,76,a,96\nrd v,68,58,11264\nsa m0,s50,139264\nsa s49,%7,262144\ndc 61,40,49\nwl 9\n"
"mp %2,v,80,a,96\nrd v,72,58,12288\nwl 9\nmp %3,v,84,a,96\nrd v,76,58,13312\nwl 9\n"
"mp %4,v,88,a,96\nrd v,80,58,14336\ns_waitcnt vmcnt(25) lgkmcnt(9)\nmp %0,v,92,a,100\nrd v,84,58,15360\ns_mov_b32 s49, 35840\n"
"buffer_load_dwordx4 a[140:143], %15, s[44:47], s49 offen sc1\nwl 9\nmp %1,v,96,a,100\nrd v,88,58,16384\nwl 9\nmp %2,v,100,a,100\n"
"rd v,92,58,17408\nwl 9\nmp %3,v,104,a,100\nrd v,96,58,18432\nwl 9\nmp %4,v,108,a,100\n"
"rd v,100,58,19456\ns_waitcnt vmcnt(25) lgkmcnt(9)\nmp %0,v,64,a,104\nrd v,104,58,20480\ns_mov_b32 s49, 36864\nbuffer_load_dwordx4 a[144:147], %15, s[44:47], s49 offen sc1\n"
"wl 9\nmp %1,v,68,a,104\nrd v,108,58,21504\nwl 9\nmp %2,v,72,a,104\nrd v,64,58,22528\n"
"wl 9\nmp %3,v,76,a,104\nrd v,68,58,23552\nwl 9\nmp %4,v,80,a,104\nrd v,72,58,24576\n"
"s_waitcnt vmcnt(20) lgkmcnt(9)\nmp %0,v,84,a,108\ns_barrier\nrd v,76,58,25600\nwl 9\nmp %1,v,88,a,108\n"
"rd v,80,58,26624\ns_mov_b32 s49, 37888\nbuffer_load_dwordx4 a[148:151], %15, s[44:47], s49 offen sc1\nwl 9\nmp %2,v,92,a,108\nrd v,84,58,27648\n"
"wl 9\nmp %3,v,96,a,108\nrd v,88,58,28672\nwl 9\nmp %4,v,100,a,108\nrd v,92,58,29696\n"
"s_waitcnt vmcnt(19) lgkmcnt(9)\nmp %0,v,104,a,112\nrd v,96,58,30720\ns_mov_b32 s49, 38912\nbuffer_load_dwordx2 a[152:153], %16, s[44:47], s49 offen sc1\nbuffer_load_dwordx2 a[154:155], %16, s[44:47], s49 offen offset:512 sc1\n"
"wl 9\nmp %1,v,108,a,112\nrd v,100,58,31744\nsa m0,s50,143360\nsa s49,%7,266240\ndc 61,40,49\n"
"wl 9\nmp %2,v,64,a,112\nrd v,104,58,32768\nsa m0,s50,147456\nsa s49,%7,270336\ndc 61,40,49\n"
"wl 9\nmp %3,v,68,a,112\nrd v,108,58,33792\nwl 9\nmp %4,v,72,a,112\nrd v,64,58,34816\n"
"sa m0,s50,151552\nsa s49,%7,274432\ndc 61,40,49\ns_waitcnt vmcnt(22) lgkmcnt(9)\nmp %0,v,76,a,116\nrd v,68,58,35840\n"
"s_mov_b32 s49, 39936\nbuffer_load_dwordx4 a[156:159], %15, s[44:47], s49 offen sc1\nwl 9\nmp %1,v,80,a,116\nrd v,72,58,36864\nwl 9\n"
"mp %2,v,84,a,116\nrd v,76,58,37888\nwl 9\nmp %3,v,88,a,116\nrd v,80,58,38912\nwl 9\n"
"mp %4,v,92,a,116\nrd v,84,58,39936\ns_waitcnt vmcnt(21) lgkmcnt(9)\nmp %0,v,96,a,120\nrd v,88,58,40960\ns_mov_b32 s49, 40960\n"
"buffer_load_dwordx4 a[160:163], %15, s[44:47], s49 offen sc1\nwl 9\nmp %1,v,100,a,120\nrd v,92,58,41984\nwl 9\nmp %2,v,104,a,120\n"
"rd v,96,58,43008\nwl 9\nmp %3,v,108,a,120\nrd v,100,58,44032\nwl 9\nmp %4,v,64,a,120\n"
"rd v,104,58,45056\ns_waitcnt vmcnt(20) lgkmcnt(9)\nmp %0,v,68,a,124\nrd v,108,58,46080\ns_mov_b32 s49, 41984\nbuffer_load_dwordx4 a[164:167], %15, s[44:47], s49 offen sc1\n"
"wl 9\nmp %1,v,72,a,124\nrd v,64,58,47104\nwl 9\nmp %2,v,76,a,124\nrd v,68,58,48128\n"
"wl 9\nmp %3,v,80,a,124\nrd v,72,58,49152\nwl 9\nmp %4,v,84,a,124\nrd v,76,58,50176\n"
"s_waitcnt vmcnt(20) lgkmcnt(9)\nmp %0,v,88,a,128\nrd v,80,58,51200\ns_mov_b32 s49, 43008\nbuffer_load_dwordx4 a[168:171], %15, s[44:47], s49 offen sc1\nwl 9\n"
"mp %1,v,92,a,128\nrd v,84,58,52224\nwl 9\nmp %2,v,96,a,128\nrd v,88,58,53248\nwl 9\n"
"mp %3,v,100,a,128\nrd v,92,58,54272\nwl 9\nmp %4,v,104,a,128\nrd v,96,58,55296\ns_waitcnt vmcnt(16) lgkmcnt(9)\n"
"mp %0,v,108,a,132\nrd v,100,58,56320\ns_mov_b32 s49, 44032\nbuffer_load_dwordx4 a[172:175], %15, s[44:47], s49 offen sc1\nwl 9\nmp %1,v,64,a,132\n"
"s_barrier\nrd v,104,58,57344\nwl 9\nmp %2,v,68,a,132\nrd v,108,58,58368\nwl 9\n"
"mp %3,v,72,a,132\nrd v,64,58,59392\nwl 9\nmp %4,v,76,a,132\nrd v,68,58,60416\ns_waitcnt vmcnt(14) lgkmcnt(9)\n"
"mp %0,v,80,a,136\nrd v,72,58,61440\ns_mov_b32 s49, 45056\nbuffer_load_dwordx4 a[176:179], %15, s[44:47], s49 offen sc1\nwl 9\nmp %1,v,84,a,136\n"
"rd v,76,58,62464\nwl 9\nmp %2,v,88,a,136\nrd v,80,58,63488\nwl 9\nmp %3,v,92,a,136\n"
"rd v,84,58,64512\nwl 9\nmp %4,v,96,a,136\nrd v,88,59,0\ns_waitcnt vmcnt(13) lgkmcnt(9)\nmp %0,v,100,a,140\n"
"rd v,92,59,1024\ns_mov_b32 s49, 46080\nbuffer_load_dwordx4 a[180:183], %15, s[44:47], s49 offen sc1\nwl 9\nmp %1,v,104,a,140\nrd v,96,59,2048\n"
"wl 9\nmp %2,v,108,a,140\nrd v,100,59,3072\nwl 9\nmp %3,v,64,a,140\nrd v,104,59,4096\n"
"wl 9\nmp %4,v,68,a,140\nrd v,108,59,5120\ns_waitcnt vmcnt(13) lgkmcnt(9)\nmp %0,v,72,a,144\nrd v,64,59,6144\n"
"s_mov_b32 s49, 47104\nbuffer_load_dwordx4 a[184:187], %15, s[44:47], s49 offen sc1\nwl 9\nmp %1,v,76,a,144\nrd v,68,59,7168\nwl 9\n"
"mp %2,v,80,a,144\nrd v,72,59,8192\nwl 9\nmp %3,v,84,a,144\nrd v,76,59,9216\nwl 9\n"
"mp %4,v,88,a,144\nrd v,80,59,10240\ns_waitcnt vmcnt(13) lgkmcnt(9)\nmp %0,v,92,a,148\nrd v,84,59,11264\ns_mov_b32 s49, 48128\n"
"buffer_load_dwordx4 a[188:191], %15, s[44:47], s49 offen sc1\nwl 9\nmp %1,v,96,a,148\nrd v,88,59,12288\nwl 9\nmp %2,v,100,a,148\n"
"rd v,92,59,13312\nwl 9\nmp %3,v,104,a,148\nrd v,96,59,14336\nwl 9\nmp %4,v,108,a,148\n"
"rd v,100,59,15360\ns_waitcnt vmcnt(12) lgkmcnt(9)\nmp %0,v,64,a,152\nrd v,104,59,16384\ns_mov_b32 s49, 49152\nbuffer_load_dwordx4 a[192:195], %15, s[44:47], s49 offen sc1\n"
"wl 9\nmp %1,v,68,a,152\nrd v,108,59,17408\nwl 9\nmp %2,v,72,a,152\nrd v,64,59,18432\n"
"wl 9\nmp %3,v,76,a,152\nrd v,68,59,19456\nwl 9\nmp %4,v,80,a,152\nrd v,72,59,20480\n"
"s_waitcnt vmcnt(9) lgkmcnt(9)\nmp %0,v,84,a,156\nrd v,76,59,21504\ns_mov_b32 s49, 50176\nbuffer_load_dwordx4 a[196:199], %15, s[44:47], s49 offen sc1\nwl 9\n"
"mp %1,v,88,a,156\nrd v,80,59,22528\nwl 9\nmp %2,v,92,a,156\nrd v,84,59,23552\nwl 9\n"
"mp %3,v,96,a,156\ns_barrier\nrd v,88,59,24576\nwl 9\nmp %4,v,100,a,156\nrd v,92,59,25600\n"
"s_waitcnt vmcnt(9) lgkmcnt(9)\nmp %0,v,104,a,160\nrd v,96,59,26624\ns_mov_b32 s49, 51200\nbuffer_load_dwordx4 a[200:203], %15, s[44:47], s49 offen sc1\nwl 9\n"
"mp %1,v,108,a,160\nrd v,100,59,27648\nwl 9\nmp %2,v,64,a,160\nrd v,104,59,28672\nwl 9\n"
"mp %3,v,68,a,160\nrd v,108,59,29696\nwl 9\nmp %4,v,72,a,160\nrd v,64,59,30720\ns_waitcnt vmcnt(9) lgkmcnt(9)\n"
"mp %0,v,76,a,164\nrd v,68,59,31744\ns_mov_b32 s49, 52224\nbuffer_load_dwordx4 a[204:207], %15, s[44:47], s49 offen sc1\nwl 9\nmp %1,v,80,a,164\n"
"rd v,72,59,32768\nwl 9\nmp %2,v,84,a,164\nrd v,76,59,33792\nwl 9\nmp %3,v,88,a,164\n"
"rd v,80,59,34816\nwl 9\nmp %4,v,92,a,164\nrd v,84,59,35840\ns_waitcnt vmcnt(9) lgkmcnt(9)\nmp %0,v,96,a,168\n"
"rd v,88,59,36864\ns_mov_b32 s49, 53248\nbuffer_load_dwordx4 a[208:211], %15, s[44:47], s49 offen sc1\nwl 9\nmp %1,v,100,a,168\nrd v,92,59,37888\n"
"wl 9\nmp %2,v,104,a,168\nrd v,96,59,38912\nwl 9\nmp %3,v,108,a,168\nrd v,100,59,39936\n"
"wl 9\nmp %4,v,64,a,168\nrd v,104,59,40960\ns_waitcnt vmcnt(9) lgkmcnt(9)\nmp %0,v,68,a,172\nrd v,108,59,41984\n"
"s_mov_b32 s49, 54272\nbuffer_load_dwordx4 a[212:215], %15, s[44:47], s49 offen sc1\nwl 9\nmp %1,v,72,a,172\nrd v,64,59,43008\nwl 9\n"
"mp %2,v,76,a,172\nrd v,68,59,44032\nwl 9\nmp %3,v,80,a,172\nrd v,72,59,45056\nwl 9\n"
"mp %4,v,84,a,172\nrd v,76,59,46080\ns_waitcnt vmcnt(9) lgkmcnt(9)\nmp %0,v,88,a,176\nrd v,80,59,47104\nwl 9\n"
"mp %1,v,92,a,176\nrd v,84,59,48128\nwl 9\nmp %2,v,96,a,176\nrd v,88,59,49152\nwl 9\n"
"mp %3,v,100,a,176\nrd v,92,59,50176\nwl 9\nmp %4,v,104,a,176\nrd v,96,59,51200\ns_waitcnt vmcnt(8) lgkmcnt(9)\n"
"mp %0,v,108,a,180\nrd v,100,59,52224\nwl 9\nmp %1,v,64,a,180\nrd v,104,59,53248\nwl 9\n"
"mp %2,v,68,a,180\nrd v,108,59,54272\nwl 9\nmp %3,v,72,a,180\nrd v,64,59,55296\nwl 9\n"
"mp %4,v,76,a,180\nrd v,68,59,56320\ns_waitcnt vmcnt(7) lgkmcnt(9)\nmp %0,v,80,a,184\ns_barrier\nrd v,72,59,57344\n"
"wl 9\nmp %1,v,84,a,184\nrd v,76,59,58368\nwl 9\nmp %2,v,88,a,184\nrd v,80,59,59392\n"
"wl 9\nmp %3,v,92,a,184\nrd v,84,59,60416\nwl 9\nmp %4,v,96,a,184\nrd v,88,59,61440\n"
"s_waitcnt vmcnt(6) lgkmcnt(9)\nmp %0,v,100,a,188\nrd v,92,59,62464\nwl 9\nmp %1,v,104,a,188\nrd v,96,59,63488\n"
"wl 9\nmp %2,v,108,a,188\nrd v,100,59,64512\nwl 9\nmp %3,v,64,a,188\nrd v,104,60,0\n"
"wl 9\nmp %4,v,68,a,188\nrd v,108,60,1024\ns_waitcnt vmcnt(5) lgkmcnt(9)\nmp %0,v,72,a,192\nrd v,64,60,2048\n"
"wl 9\nmp %1,v,76,a,192\nrd v,68,60,3072\nwl 9\nmp %2,v,80,a,192\nrd v,72,60,4096\n"
"wl 9\nmp %3,v,84,a,192\nrd v,76,60,5120\nwl 9\nmp %4,v,88,a,192\nrd v,80,60,6144\n"
"s_waitcnt vmcnt(4) lgkmcnt(9)\nmp %0,v,92,a,196\nrd v,84,60,7168\nwl 9\nmp %1,v,96,a,196\nrd v,88,60,8192\n"
"wl 9\nmp %2,v,100,a,196\nrd v,92,60,9216\nwl 9\nmp %3,v,104,a,196\nrd v,96,60,10240\n"
"wl 9\nmp %4,v,108,a,196\nrd v,100,60,11264\ns_waitcnt vmcnt(3) lgkmcnt(9)\nmp %0,v,64,a,200\nrd v,104,60,12288\n"
"wl 9\nmp %1,v,68,a,200\nrd v,108,60,13312\nwl 9\nmp %2,v,72,a,200\nrd v,64,60,14336\n"
"wl 9\nmp %3,v,76,a,200\nrd v,68,60,15360\nwl 9\nmp %4,v,80,a,200\nrd v,72,60,16384\n"
"s_waitcnt vmcnt(2) lgkmcnt(9)\nmp %0,v,84,a,204\nrd v,76,60,17408\nwl 9\nmp %1,v,88,a,204\nrd v,80,60,18432\n"
"wl 9\nmp %2,v,92,a,204\nrd v,84,60,19456\nwl 9\nmp %3,v,96,a,204\nrd v,88,60,20480\n"
"wl 9\nmp %4,v,100,a,204\nrd v,92,60,21504\ns_waitcnt vmcnt(1) lgkmcnt(9)\nmp %0,v,104,a,208\nwl 8\n"
"mp %1,v,108,a,208\nwl 7\nmp %2,v,64,a,208\nwl 6\nmp %3,v,68,a,208\nwl 5\n"
"mp %4,v,72,a,208\ns_waitcnt vmcnt(0) lgkmcnt(4)\nmp %0,v,76,a,212\nwl 3\nmp %1,v,80,a,212\nwl 2\n"
"mp %2,v,84,a,212\nwl 1\nmp %3,v,88,a,212\nwl 0\nmp %4,v,92,a,212\ns_barrier\n"
"s_nop 6\n""CODE_END:\n"
    : "=&v"(acc[0]), "=&v"(acc[1]), "=&v"(acc[2]), "=&v"(acc[3]), "=&v"(acc[4]) : "v"(tid), "s"(ws), "s"(o4), "s"(o4), "s"(o4), "s"(ws + WS_g3), "s"(7077888), "s"(tag), "s"(F_3 + ((xc * 2 + (i >> 4)) * 16 + (i & 15)) * FS), "v"(F_3 + ((rb64 >> 1) * 16 + (lane & 15)) * FS),
      "v"(lane * 16 + rb * 55296), "v"(lane * 8 + rb * 55296), "s"(rot4a(cg) * 1024), "s"((42 + rot4b(cg)) * 1024) : "v48", "v49", "v57", "v58", "v59", "v60", "v61", "v62", "v63", "v64", "v65", "v66", "v67", "v68", "v69", "v70", "v71", "v72", "v73", "v74", "v75", "v76", "v77", "v78", "v79", "v80", "v81", "v82", "v83", "v84", "v85", "v86", "v87", "v88", "v89", "v90", "v91", "v92", "v93", "v94", "v95", "v96", "v97", "v98", "v99", "v100", "v101", "v102", "v103", "v104", "v105", "v106", "v107", "v108", "v109", "v110", "v111", "a0", "a1", "a2", "a3", "a4", "a5", "a6", "a7", "a8", "a9", "a10", "a11", "a12", "a13", "a14", "a15", "a16", "a17", "a18", "a19", "a20", "a21", "a22", "a23", "a24", "a25", "a26", "a27", "a28", "a29", "a30", "a31", "a32", "a33", "a34", "a35", "a36", "a37", "a38", "a39", "a40", "a41", "a42", "a43", "a44", "a45", "a46", "a47", "a48", "a49", "a50", "a51", "a52", "a53", "a54", "a55", "a56", "a57", "a58", "a59", "a60", "a61", "a62", "a63", "a64", "a65", "a66", "a67", "a68", "a69", "a70", "a71", "a72", "a73", "a74", "a75", "a76", "a77", "a78", "a79", "a80", "a81", "a82", "a83", "a84", "a85", "a86", "a87", "a88", "a89", "a90", "a91", "a92", "a93", "a94", "a95", "a96", "a97", "a98", "a99", "a100", "a101", "a102", "a103", "a104", "a105", "a106", "a107", "a108", "a109", "a110", "a111", "a112", "a113", "a114", "a115", "a116", "a117", "a118", "a119", "a120", "a121", "a122", "a123", "a124", "a125", "a126", "a127", "a128", "a129", "a130", "a131", "a132", "a133", "a134", "a135", "a136", "a137", "a138", "a139", "a140", "a141", "a142", "a143", "a144", "a145", "a146", "a147", "a148", "a149", "a150", "a151", "a152", "a153", "a154", "a155", "a156", "a157", "a158", "a159", "a160", "a161", "a162", "a163", "a164", "a165", "a166", "a167", "a168", "a169", "a170", "a171", "a172", "a173", "a174", "a175", "a176", "a177", "a178", "a179", "a180", "a181", "a182", "a183", "a184", "a185", "a186", "a187", "a188", "a189", "a190", "a191", "a192", "a193", "a194", "a195", "a196", "a197", "a198", "a199", "a200", "a201", "a202", "a203", "a204", "a205", "a206", "a207", "a208", "a209", "a210", "a211", "a212", "a213", "a214", "a215", "a216", "a217", "a218", "a219", "a220", "a221", "a222", "a223", "a224", "a225", "a226", "a227", "a228", "a229", "a230", "a231", "a232", "a233", "a234", "a235", "a236", "a237", "a238", "a239", "a240", "a241", "a242", "a243", "a244", "a245", "a246", "a247", "a248", "a249", "a250", "a251", "a252", "a253", "a254", "a255", "s40", "s41", "s42", "s43", "s44", "s45", "s46", "s47", "s48", "s49", "s50", "s51", "s52", "s53", "s54", "vcc", "scc", "m0", "memory");
#pragma unroll
    for (int n = 0; n < 5; ++n) __builtin_amdgcn_raw_buffer_store_b64(pk4(up4(rx[n]) + up4(pk4(gb[n])) * (up4(ry[n]) + acc[n])), ro, ((rb * 16 + (lane & 15)) * 640 + (cg * 5 + n) * 16 + g * 4) * 2, 0, 0);
  }
  if (b == 0 && tid == 0) __hip_atomic_store((u64*)(ws + F_E), tag, __ATOMIC_RELAXED, __HIP_MEMORY_SCOPE_AGENT);
}

extern "C" void kernel_launch(void* const* d_in, const int* in_sizes, int n_in, void* d_out, int out_size, void* d_ws, size_t ws_size, hipStream_t stream) {
  char* ws = (char*)d_ws;
  (void)hipFuncSetAttribute((const void*)k_all, hipFuncAttributeMaxDynamicSharedMemorySize, 163840);
  Args A;
  A.ws = ws; A.ckv = (const char*)d_in[1]; A.wuk = (const char*)d_in[3]; A.wuv = (const char*)d_in[4]; A.wq = (const char*)d_in[2]; A.x = (const u16*)d_in[0]; A.cvec = (const u16*)d_in[11];
  A.wmod = (const u16*)d_in[12]; A.wo = (const u16*)d_in[5]; A.w1 = (const u16*)d_in[7]; A.w2 = (const u16*)d_in[8]; A.w3 = (const u16*)d_in[9]; A.w4 = (const u16*)d_in[10]; A.out = (u16*)d_out;
  k_all<<<256, 256, 163840, stream>>>(A);
}
```
